```python
import torch

def _softmax(x, axis=-1):
    return torch.softmax(x, dim=axis)

def _gelu(x):
    return torch.nn.functional.gelu(x, approximate='tanh')

def _silu(x):
    return torch.nn.functional.silu(x)

def _sigmoid(x):
    return torch.sigmoid(x)

def _layernorm(x, axis=-1, eps=1e-05):
    m = x.mean(dim=axis, keepdim=True)
    v = x.var(dim=axis, keepdim=True, unbiased=False)
    return (x - m) / torch.sqrt(v + eps)

def _scatter_add(tgt, idx, src, axis=0):
    return tgt.clone().index_add_(axis, idx, src)

def _segment_sum(src, seg, n):
    out = torch.zeros((n,) + src.shape[1:], dtype=src.dtype)
    return out.index_add_(0, seg, src)

def _pad(x, pw):
    flat = tuple((v for pair in reversed(pw) for v in pair))
    return torch.nn.functional.pad(x, flat)
kernel_name = 'interleave_layers__linear_attn_block__memory_aug_recurrent_mem_tr__prompt'

def setup_inputs():

    _w = lambda a: (lambda t: t.to(torch.bfloat16) if t.is_floating_point() else t)(torch.as_tensor(a))

    def _A_setup():
        B, S, D = (1, 2048, 640)
        return {'x': _w((lambda *s: torch.randn(*s, dtype=torch.bfloat16) * 0.05)(B, S, D)), 'wqkv': _w((lambda *s: torch.randn(*s, dtype=torch.bfloat16) * 0.05)(D, 3 * D)), 'wo': _w((lambda *s: torch.randn(*s, dtype=torch.bfloat16) * 0.05)(D, D))}
    _A_in = _A_setup()

    def _B_setup():
        B, S, Dm = (1, 2048, 640)
        H, Dh = (32, 192)
        M, seg = (8, 32)
        seg = min(seg, S)
        S = S // seg * seg
        return {'x': _w((lambda *s: torch.randn(*s, dtype=torch.bfloat16) * 0.05)(B, S, Dm)), 'mem0': _w((lambda *s: torch.randn(*s, dtype=torch.bfloat16) * 0.8)(B, M, Dm)), 'Wq': _w((lambda *s: torch.randn(*s, dtype=torch.bfloat16) * 1.6)(Dm, H, Dh) * Dm ** (-0.5)), 'Wk': _w((lambda *s: torch.randn(*s, dtype=torch.bfloat16) * 1.6)(Dm, H, Dh) * Dm ** (-0.5)), 'Wv': _w((lambda *s: torch.randn(*s, dtype=torch.bfloat16) * 0.8)(Dm, H, Dh) * Dm ** (-0.5)), 'Wo': _w((lambda *s: torch.randn(*s, dtype=torch.bfloat16) * 0.8)(H, Dh, Dm) * (H * Dh) ** (-0.5)), 'seg_len': seg}
    _B_in = _B_setup()
    return {'A_x': _A_in['x'], 'A_wqkv': _A_in['wqkv'], 'A_wo': _A_in['wo'], 'B_x': _B_in['x'], 'B_mem0': _B_in['mem0'], 'B_Wq': _B_in['Wq'], 'B_Wk': _B_in['Wk'], 'B_Wv': _B_in['Wv'], 'B_Wo': _B_in['Wo'], 'B_seg_len': _B_in['seg_len']}

def reference(A_x, A_wqkv, A_wo, B_x, B_mem0, B_Wq, B_Wk, B_Wv, B_Wo, B_seg_len):

    def _A_ref(x, wqkv, wo):
        B, S, D = x.shape
        H = 32
        Dh = D // H
        qkv = (lambda x, *d: x.permute(*d))((x @ wqkv).reshape(B, S, 3, H, Dh), 2, 0, 3, 1, 4)
        q, k, v = (qkv[0], qkv[1], qkv[2])
        phi = lambda t: torch.where(t > 0, t + 1, torch.exp(t)) + 1e-06
        pq, pk = (phi(q), phi(k))
        kv = torch.einsum('bhsd,bhse->bhde', pk, v)
        z = torch.einsum('bhsd->bhd', pk)
        num = torch.einsum('bhqd,bhde->bhqe', pq, kv)
        den = torch.einsum('bhqd,bhd->bhq', pq, z)[..., None] + 1e-06
        o = num / den
        return (lambda x, *d: x.permute(*d))(o, 0, 2, 1, 3).reshape(B, S, D) @ wo

    def _B_ref(x, mem0, Wq, Wk, Wv, Wo, seg_len):
        B, S, Dm = x.shape
        H, Dh = (Wq.shape[1], Wq.shape[2])
        M = mem0.shape[1]
        scale = Dh ** (-0.5)
        n_seg = S // seg_len
        L = M + seg_len
        idx = torch.arange(L)
        mask = (idx[:, None] < M) | (idx[:, None] >= idx[None, :])
        mem = mem0
        outs = []
        for s in range(n_seg):
            xs = x[:, s * seg_len:(s + 1) * seg_len, :].clone()
            h = torch.concatenate([mem, xs], axis=1)
            q = torch.einsum('btd,dhe->bhte', h, Wq)
            k = torch.einsum('btd,dhe->bhte', h, Wk)
            v = torch.einsum('btd,dhe->bhte', h, Wv)
            scores = torch.einsum('bhqe,bhke->bhqk', q, k) * scale
            scores = torch.where(mask[None, None], scores, -1000000000.0)
            p = _softmax(scores, axis=-1)
            o = torch.einsum('bhqk,bhke->bhqe', p, v)
            o = torch.einsum('bhte,hed->btd', o, Wo)
            mem = o[:, :M, :].clone()
            outs.append(o[:, M:, :])
        return torch.concatenate(outs, axis=1)
    x = A_x
    x = x + _A_ref(_layernorm(x, axis=-1), A_wqkv, A_wo)
    x = x + _B_ref(_layernorm(x, axis=-1), B_mem0, B_Wq, B_Wk, B_Wv, B_Wo, B_seg_len)
    x = x + _A_ref(_layernorm(x, axis=-1), A_wqkv, A_wo)
    x = x + _B_ref(_layernorm(x, axis=-1), B_mem0, B_Wq, B_Wk, B_Wv, B_Wo, B_seg_len)
    return x
```

```cpp
#include <hip/hip_runtime.h>
#include <hip/hip_bf16.h>
#include <stdint.h>
#include <type_traits>

typedef __attribute__((ext_vector_type(8))) short bf16x8;
typedef __attribute__((ext_vector_type(4))) float f32x4;
typedef __attribute__((ext_vector_type(4))) unsigned u32x4;
typedef __attribute__((ext_vector_type(2))) unsigned u32x2;
typedef uint16_t bf16_t;

typedef __attribute__((ext_vector_type(2))) __bf16 bf16x2_t;
typedef __attribute__((ext_vector_type(2))) float f32x2;
__device__ __forceinline__ unsigned pk2(float lo, float hi) { f32x2 v = {lo, hi}; return __builtin_bit_cast(unsigned, __builtin_convertvector(v, bf16x2_t)); }
__device__ __forceinline__ float rbf(float f) { return __uint_as_float(pk2(f, f) << 16); }
__device__ __forceinline__ float lo2f(unsigned p) { return __uint_as_float(p << 16); }
__device__ __forceinline__ float hi2f(unsigned p) { return __uint_as_float(p & 0xffff0000u); }

constexpr int S_ = 2048, D_ = 640, H_ = 32, E_ = 192, NSEG_ = 64;
constexpr int HE_ = H_ * E_;

constexpr int NR_ = 80;
constexpr int NXP_ = 48;
constexpr size_t al(size_t x) { return (x + 255) & ~(size_t)255; }
constexpr size_t WS_WF_AQKV = 0;
constexpr size_t WS_WF_AO   = WS_WF_AQKV + al((size_t)640 * 1920 * 2);
constexpr size_t WS_PQ      = WS_WF_AO + al((size_t)640 * 640 * 2);
constexpr unsigned XI_X = 10240, XI_S8 = XI_X + 40960, XI_PITCH = XI_S8 + 2048, XI_BYTES = NSEG_ * XI_PITCH;
constexpr size_t WS_X       = WS_PQ + al((size_t)S_ * D_ * 2);
constexpr size_t WS_KVP     = WS_X + al(XI_BYTES);
constexpr size_t WS_KVF     = WS_KVP + al((size_t)32 * 32 * 420 * 4);
constexpr size_t WS_ST      = WS_KVF + al((size_t)32 * 1024 * 2);
#define KXR 32
#define PR 32
#define URING 32
constexpr unsigned ST_KX = 0;
constexpr unsigned ST_VX = ST_KX + KXR * 32u * 12288;
constexpr unsigned ST_KM = ST_VX + KXR * 32u * 12288;
constexpr unsigned ST_PX = ST_KM + PR * 32u * 3072;
constexpr unsigned ST_U  = ST_PX + PR * 2u * 40960;
constexpr unsigned ST_BYTES = ST_U + (unsigned)URING * 40 * 32768;
constexpr size_t WS_FR      = WS_ST + al(ST_BYTES);
constexpr unsigned FR_FILL = 40u << 14, FR_UM = FR_FILL, FR_USED = FR_UM + (160u << 14);
__device__ __forceinline__ unsigned char* al2m(unsigned char* p) { return (unsigned char*)(((uintptr_t)p + (((uintptr_t)1 << 21) - 1)) & ~(((uintptr_t)1 << 21) - 1)); }
constexpr unsigned M_STAMP = 16384 + 2048;
constexpr unsigned FLP = 128;
constexpr unsigned F_AF = 0, F_POL = F_AF + 64 * 16 * FLP, F_AQ = F_POL + 256, F_SD = F_AQ + 256, F_XP = F_SD + 256, F_PX = F_XP + 64 * NXP_ * FLP, F_KM = F_PX + 70 * 64 * FLP, F_UX = F_KM + 64 * 32 * FLP,
    F_OF = F_UX + 65 * 32 * FLP, F_RD = F_OF + 64 * NR_ * FLP, F_ERR = F_RD + 64 * NR_ * FLP, HOP_BYTES = F_ERR + 256;
constexpr size_t WS_HOP     = WS_FR + ((size_t)6 << 20);
constexpr size_t WS_DBG     = WS_HOP + al(2 * HOP_BYTES);
constexpr size_t WS_END     = WS_DBG + al((size_t)2 << 20);
static_assert(WS_END <= (size_t)256 * 1024 * 1024, "ws too big");
static_assert(FR_USED <= (4u << 20), "hand-off pages");

__device__ __forceinline__ unsigned phi2(float x0, float x1) {
    const unsigned t = pk2(x0, x1); const float t0 = lo2f(t), t1 = hi2f(t);
    const unsigned a = pk2(t0 > 0.f ? t0 + 1.0f : __expf(t0), t1 > 0.f ? t1 + 1.0f : __expf(t1));
    return pk2(lo2f(a) + 1e-6f, hi2f(a) + 1e-6f);
}
#define CBAR() asm volatile("" ::: "memory")
#define VMDRAIN() asm volatile("s_waitcnt vmcnt(0)" ::: "memory")
#define PIN(x) asm volatile("" : "+v"(x))
#define VMKNOWN() __builtin_amdgcn_s_waitcnt(0x0F70)
#define LBAR() do { asm volatile("s_waitcnt lgkmcnt(0)" ::: "memory"); __builtin_amdgcn_s_barrier(); asm volatile("" ::: "memory"); } while (0)
typedef __amdgpu_buffer_rsrc_t rsrc_t;
__device__ __forceinline__ u32x4 ld16c(rsrc_t r, unsigned off) { return __builtin_amdgcn_raw_buffer_load_b128(r, off, 0, 16); }
__device__ __forceinline__ u32x2 ld8c(rsrc_t r, unsigned off) { return __builtin_amdgcn_raw_buffer_load_b64(r, off, 0, 16); }
__device__ __forceinline__ unsigned ld4c(rsrc_t r, unsigned off) { return __builtin_amdgcn_raw_buffer_load_b32(r, off, 0, 16); }
__device__ __forceinline__ void st16c(rsrc_t r, unsigned off, u32x4 v) { __builtin_amdgcn_raw_buffer_store_b128(v, r, off, 0, 16); }
__device__ __forceinline__ void st8c(rsrc_t r, unsigned off, u32x2 v) { __builtin_amdgcn_raw_buffer_store_b64(v, r, off, 0, 16); }
__device__ __forceinline__ void st4c(rsrc_t r, unsigned off, unsigned v) { __builtin_amdgcn_raw_buffer_store_b32(v, r, off, 0, 16); }
template <int CTRL> __device__ __forceinline__ unsigned dppu(unsigned x) { return (unsigned)__builtin_amdgcn_mov_dpp((int)x, CTRL, 0xf, 0xf, true); }
template <int CTRL> __device__ __forceinline__ float dppf(float x) { return __uint_as_float(dppu<CTRL>(__float_as_uint(x))); }
__device__ __forceinline__ float xg_max(float x) {
    auto s = __builtin_amdgcn_permlane16_swap(__float_as_uint(x), __float_as_uint(x), false, false); x = fmaxf(__uint_as_float(s[0]), __uint_as_float(s[1]));
    auto t = __builtin_amdgcn_permlane32_swap(__float_as_uint(x), __float_as_uint(x), false, false); return fmaxf(__uint_as_float(t[0]), __uint_as_float(t[1])); }
__device__ __forceinline__ float xg_sum(float x) {
    auto s = __builtin_amdgcn_permlane16_swap(__float_as_uint(x), __float_as_uint(x), false, false); x = __uint_as_float(s[0]) + __uint_as_float(s[1]);
    auto t = __builtin_amdgcn_permlane32_swap(__float_as_uint(x), __float_as_uint(x), false, false); return __uint_as_float(t[0]) + __uint_as_float(t[1]); }

typedef __attribute__((ext_vector_type(8))) unsigned u32x8;
typedef __attribute__((ext_vector_type(8))) __bf16 bfv8;
typedef __attribute__((ext_vector_type(16))) __bf16 bfv16;
typedef short v4i16_t __attribute__((ext_vector_type(4)));
typedef __attribute__((address_space(3))) volatile unsigned lds_u32;
#define SBAR() __builtin_amdgcn_sched_barrier(0)
__device__ __forceinline__ int lane_id() { int l; asm volatile("v_mbcnt_lo_u32_b32 %0, -1, 0\n\tv_mbcnt_hi_u32_b32 %0, -1, %0" : "=v"(l)); return l; }
#define Z4 ((f32x4){0.f, 0.f, 0.f, 0.f})
__device__ __forceinline__ f32x4 smf(u32x4 A, u32x8 B, f32x4 c, unsigned idx) { return __builtin_amdgcn_smfmac_f32_16x16x64_bf16(__builtin_bit_cast(bfv8, A), __builtin_bit_cast(bfv16, B), c, (int)idx, 0, 0); }
__device__ __forceinline__ f32x4 dmf(u32x4 A, u32x4 B, f32x4 c) { return __builtin_amdgcn_mfma_f32_16x16x32_bf16(__builtin_bit_cast(bfv8, A), __builtin_bit_cast(bfv8, B), c, 0, 0, 0); }
__device__ __forceinline__ u32x4 lo4(u32x8 v) { return (u32x4){v[0], v[1], v[2], v[3]}; }
__device__ __forceinline__ u32x4 hi4(u32x8 v) { return (u32x4){v[4], v[5], v[6], v[7]}; }
__device__ __forceinline__ u32x8 cat8(u32x4 a, u32x4 b) { return (u32x8){a.x, a.y, a.z, a.w, b.x, b.y, b.z, b.w}; }
__device__ __forceinline__ u32x4 lds16(const unsigned char* p) { return *reinterpret_cast<const u32x4*>(p); }
__device__ __forceinline__ u32x4 lds16a(int ad) { return *(const __attribute__((address_space(3))) u32x4*)(uintptr_t)(unsigned)ad; }
__device__ __forceinline__ unsigned lds4a(int ad) { return *(const __attribute__((address_space(3))) unsigned*)(uintptr_t)(unsigned)ad; }
__device__ __forceinline__ void sts16a(int ad, u32x4 v) { *(__attribute__((address_space(3))) u32x4*)(uintptr_t)(unsigned)ad = v; }
__device__ __forceinline__ u32x2 ldstr(const unsigned char* p) { return __builtin_bit_cast(u32x2, __builtin_amdgcn_ds_read_tr16_b64_v4i16((__attribute__((address_space(3))) v4i16_t*)p)); }
__device__ __forceinline__ unsigned umax4(u32x4 v) { return max(max(v.x, v.y), max(v.z, v.w)); }
__device__ __forceinline__ void dma16(rsrc_t r, unsigned voff, unsigned soff, unsigned ldsd) {
    unsigned keep;
    asm volatile("s_mov_b32 %0, m0\n\ts_mov_b32 m0, %1\n\ts_nop 2\n\tbuffer_load_dwordx4 %2, %3, %4 offen lds\n\ts_mov_b32 m0, %0" : "=&s"(keep) : "s"(ldsd), "v"(voff), "s"(r), "s"(soff) : "memory");
}
__device__ __forceinline__ void dma16c(rsrc_t r, unsigned voff, unsigned soff, unsigned ldsd) {
    unsigned keep;
    asm volatile("s_mov_b32 %0, m0\n\ts_mov_b32 m0, %1\n\ts_nop 2\n\tbuffer_load_dwordx4 %2, %3, %4 offen sc1 lds\n\ts_mov_b32 m0, %0" : "=&s"(keep) : "s"(ldsd), "v"(voff), "s"(r), "s"(soff) : "memory");
}
__device__ __forceinline__ void dma4c(rsrc_t r, unsigned voff, unsigned soff, unsigned ldsd) {
    unsigned keep;
    asm volatile("s_mov_b32 %0, m0\n\ts_mov_b32 m0, %1\n\ts_nop 2\n\tbuffer_load_dword %2, %3, %4 offen sc1 lds\n\ts_mov_b32 m0, %0" : "=&s"(keep) : "s"(ldsd), "v"(voff), "s"(r), "s"(soff) : "memory");
}
template <int N> __device__ __forceinline__ void dma16cn(rsrc_t r, unsigned voff, unsigned soff, unsigned ldsd) {
    unsigned keep;
    if (N >= 4) asm volatile("s_mov_b32 %0, m0\n\ts_mov_b32 m0, %1\n\ts_nop 2\n\tbuffer_load_dwordx4 %2, %3, %4 offen sc1 lds\n\tbuffer_load_dwordx4 %2, %3, %4 offen offset:1024 sc1 lds\n\t"
        "buffer_load_dwordx4 %2, %3, %4 offen offset:2048 sc1 lds\n\tbuffer_load_dwordx4 %2, %3, %4 offen offset:3072 sc1 lds\n\ts_mov_b32 m0, %0" : "=&s"(keep) : "s"(ldsd), "v"(voff), "s"(r), "s"(soff) : "memory");
    else if (N == 3) asm volatile("s_mov_b32 %0, m0\n\ts_mov_b32 m0, %1\n\ts_nop 2\n\tbuffer_load_dwordx4 %2, %3, %4 offen sc1 lds\n\tbuffer_load_dwordx4 %2, %3, %4 offen offset:1024 sc1 lds\n\t"
        "buffer_load_dwordx4 %2, %3, %4 offen offset:2048 sc1 lds\n\ts_mov_b32 m0, %0" : "=&s"(keep) : "s"(ldsd), "v"(voff), "s"(r), "s"(soff) : "memory");
    else if (N >= 2) asm volatile("s_mov_b32 %0, m0\n\ts_mov_b32 m0, %1\n\ts_nop 2\n\tbuffer_load_dwordx4 %2, %3, %4 offen sc1 lds\n\tbuffer_load_dwordx4 %2, %3, %4 offen offset:1024 sc1 lds\n\ts_mov_b32 m0, %0"
        : "=&s"(keep) : "s"(ldsd), "v"(voff), "s"(r), "s"(soff) : "memory");
    else dma16c(r, voff, soff, ldsd);
    constexpr int D = N >= 4 ? 4 : N;
    if constexpr (N > D) dma16cn<N - D>(r, voff, soff + D * 1024, ldsd + D * 1024);
}
__device__ __forceinline__ void st16s(u32x4 v, rsrc_t r, unsigned voff, unsigned soff) { __builtin_amdgcn_raw_buffer_store_b128(v, r, voff, soff, 16); asm volatile("s_nop 1" :: "v"(v) : "memory"); }
#define VMW(n) asm volatile("s_waitcnt vmcnt(%0)" :: "i"(n) : "memory")

struct MegaArgs {
    const bf16_t* wq; const bf16_t* wk; const bf16_t* wv; const bf16_t* wo; const bf16_t* wfq; const bf16_t* wfo; const float* kvp; bf16_t* kvt;
    const bf16_t* x0; bf16_t* pq; unsigned char* xi; bf16_t* out;
    unsigned char* st; unsigned char* hop; unsigned char* hx;
};
#define STAMP(idx) do { } while (0)
#define KATTR
#define ATI() do { } while (0)
#define AT(n) do { } while (0)
#define ATV do { } while (0)
#define ATD(role, on) do { } while (0)
#define SPINB 17
constexpr unsigned SPIN_CAP = 2u << SPINB;
constexpr unsigned SPIN_CAP_W = 1u << SPINB;
constexpr int MG_LDS = 163840;

#define PARAM(n, v) constexpr int n = v
#define TT_M1  14
#define TT_M2  14
#define TT_V   95
#define TT_V1 TT_V
#define TT_DEC 2
#define TT_BUMP 2
#define TT_DCY 1
#define TT_XS 5
#define TT_XD 1
#define TT_RT 34
#define TT_UM 8
#define TT_TW 12
#define TT_MD 60
#define TT_BD 0
#define TT_KD 0
#define TT_QD 20
#define TT_QN -40
#define TT_AD 90
#define XQD 0
#define SPRIO 2
#define PFD 6
#define DPS 5
#define PF0 2
#define KIM 6
#define XLD 9
#define NEAR 60
#define ROT 1
#define TF 58
#define RTS 30
#define RTL 8
#define R0S 4
#define VPRIO 0
#define XPD 4
#define TT_FP 6
#define TT_OD 30
#define TT_VD 0
#define YP1 0
#define QA 3
#define OIM 63
#define OLAG 3
constexpr int TT_E1 = 142, TT_E2 = 50;
__device__ __forceinline__ unsigned rt_now() { return (unsigned)__builtin_amdgcn_s_memrealtime(); }
__device__ __forceinline__ void rt_wait(unsigned t) {
    unsigned d;
    asm volatile("s_branch 2f\n"
        "1:\n\ts_cmp_lt_u32 %0, 12\n\ts_cbranch_scc1 2f\n\ts_sleep 2\n\ts_cmp_lt_u32 %0, 30\n\ts_cbranch_scc1 2f\n\ts_sleep 5\n"
        "2:\n\ts_memrealtime vcc\n\ts_waitcnt lgkmcnt(0)\n\ts_sub_u32 %0, %1, vcc_lo\n\ts_cmp_lt_u32 %0, 20000\n\ts_cbranch_scc1 1b" : "=&s"(d) : "s"(t - 1u) : "scc", "vcc", "memory");
}
__device__ __forceinline__ const void* psel(const void* p0, const void* p1, int l) { return (const void*)((uintptr_t)p0 + (((uintptr_t)p1 - (uintptr_t)p0) & (uintptr_t)(-(long)l))); }
#define MKR(p, n) __builtin_amdgcn_make_buffer_rsrc((void*)(p), 0, (int)(n), 0x00020000)
__device__ __forceinline__ unsigned my_side() { return (__builtin_amdgcn_s_getreg((3 << 11) | 20) & 15u) >= 4u ? 1u : 0u; }
__device__ __forceinline__ void tt_step(unsigned tw, int s, unsigned t_poll, unsigned& tb, unsigned& cC1, unsigned& cP) {
    const unsigned tgt0 = tb + cP, pc2 = cP - cC1;
    int d = (int)(tw + pc2 - tgt0);
    d = max(d, (int)dppu<0x128>((unsigned)d)); d = max(d, (int)dppu<0x124>((unsigned)d)); d = max(d, (int)dppu<0x122>((unsigned)d)); d = max(d, (int)dppu<0x121>((unsigned)d));
    d = __builtin_amdgcn_readlane(d, 0);
    tb = s == 0 ? t_poll : ((s == 1 || d > 0) ? tgt0 + (unsigned)d - pc2 + (s == 1 ? TT_V1 : TT_V) : tgt0);
    cC1 = __builtin_amdgcn_readlane(tw, 16); cP = __builtin_amdgcn_readlane(tw, 17);
}
__device__ __forceinline__ void set_err(unsigned char* hop) { unsigned z, v; asm volatile("v_mov_b32 %0, 0\n\tv_mov_b32 %1, 1\n\ts_nop 4\n\tglobal_store_dword %0, %1, %2\n\ts_nop 1" : "=&v"(z), "=&v"(v) : "s"(hop + F_ERR) : "memory"); }
__device__ __forceinline__ void wait_set(rsrc_t rfl, unsigned off, unsigned char* hop, int slp) {
    for (unsigned sp = 0;; ++sp) { CBAR();
        const unsigned v = ld4c(rfl, off);
        if (__all(v != 0u)) break;
        for (int q = 0; q < slp; ++q) __builtin_amdgcn_s_sleep(8);
        if (sp > SPIN_CAP_W) { set_err(hop); break; } }
}
__device__ __forceinline__ void pol_vote(const MegaArgs& a, const int v) {
    const rsrc_t r = MKR(al2m(a.hx), 1 << 21);
    const unsigned lane = lane_id(), b0 = (48u + v) << 14;
    if (lane < 16) st4c(r, b0 + (lane & 1) * 8192 + (lane >> 1) * 896, 0u);
    VMDRAIN();
    unsigned best[2] = {~0u, ~0u}, acc = 0;
#pragma unroll 1
    for (int q = 0; q < 12; ++q) {
        const unsigned long long t0 = __builtin_amdgcn_s_memtime();
        unsigned x = ld4c(r, b0 + ((q ^ v) & 1) * 8192 + (q >> 1) * 896 + acc);
        asm volatile("s_waitcnt vmcnt(0)" : "+v"(x) :: "memory");
        const unsigned dt = (unsigned)(__builtin_amdgcn_s_memtime() - t0);
        acc += x; if ((q ^ v) & 1) best[1] = min(best[1], dt); else best[0] = min(best[0], dt);
    }
    const int df = max(-300, min(300, (int)best[0] - (int)best[1]));
    if (lane == 0) st4c(MKR(a.hop, F_ERR), F_POL + v * 4, (unsigned)((my_side() ? -df : df) | 1));
}
__device__ __forceinline__ unsigned page_pol(const MegaArgs& a) {
    const rsrc_t rfl = MKR(a.hop, F_ERR); const unsigned off = F_POL + lane_id() * 4;
    wait_set(rfl, off, a.hop, 2);
    return __popcll(__ballot((int)ld4c(rfl, off) > 0)) > 32 ? 1u : 0u;
}
__device__ __forceinline__ unsigned wait_timed(rsrc_t rfl, unsigned off, unsigned char* hop, unsigned& tnext, int nper, int dly, bool fast) {
    if (!fast && (int)(tnext - rt_now()) < 4500) rt_wait(tnext);
    for (unsigned sp = 0;; ++sp) { CBAR();
        const u32x4 v = ld16c(rfl, off);
        if (__all(v.x != 0u)) { tnext = __builtin_amdgcn_readfirstlane(v.w) + (unsigned)nper * __builtin_amdgcn_readfirstlane(v.x) + (unsigned)dly; return __builtin_amdgcn_readfirstlane(v.x); }
        if (fast) __builtin_amdgcn_s_sleep(2); else __builtin_amdgcn_s_sleep(40);
        if (sp > SPIN_CAP_W) { set_err(hop); return 0u; } }
}
template <int NRND, class F> __device__ __forceinline__ void load_w(rsrc_t r0, rsrc_t r1, unsigned row1, unsigned col0, unsigned char* lds, unsigned ldsb, int wave, int lane, F&& put) {
    unsigned vo[3];
#pragma unroll
    for (int i = 0; i < 3; ++i) { const int c = 64 * i + lane, row = c / 24, ch = c - row * 24; vo[i] = (unsigned)((8 * wave + row) * HE_ + col0 + ch * 8) * 2; }
    auto issue = [&](int rd) {
#pragma unroll
        for (int i = 0; i < 3; ++i) { dma16(r0, vo[i], (unsigned)rd * (64 * HE_ * 2), ldsb + (rd % 3) * 49152 + wave * 3072 + i * 1024);
            dma16(r1, vo[i], ((unsigned)rd * 64 + row1) * (HE_ * 2), ldsb + (rd % 3) * 49152 + 24576 + wave * 3072 + i * 1024); }
    };
    issue(0); issue(1); issue(2);
    const unsigned char* rp = lds + (wave >> 2) * 24576 + (8 * (lane >> 4) + ((lane & 15) >> 2)) * 384 + (wave & 3) * 96 + (lane & 3) * 8;
#pragma unroll
    for (int rd = 0; rd < NRND; ++rd) {
        if (rd + 2 < NRND) VMW(12); else if (rd + 1 < NRND) VMW(6); else VMW(0);
        LBAR();
#pragma unroll
        for (int u = 0; u < 3; ++u) { const unsigned char* p = rp + (rd % 3) * 49152 + u * 32;
            const u32x2 a0 = ldstr(p), a1 = ldstr(p + 4 * 384), b0 = ldstr(p + 32 * 384), b1 = ldstr(p + 36 * 384);
            put(rd, u, (u32x8){a0.x, a0.y, a1.x, a1.y, b0.x, b0.y, b1.x, b1.y}); }
        LBAR();
        if (rd + 3 < NRND) issue(rd + 3);
    }
}

__device__ __forceinline__ void w_prefetch(const MegaArgs& a, unsigned char* lds, const int h, const int wave) {
    const int lane = lane_id();
    const unsigned ldsb = __builtin_amdgcn_readfirstlane((unsigned)(uintptr_t)(__attribute__((address_space(3))) unsigned char*)lds);
    const rsrc_t rq = MKR(a.wq, D_ * HE_ * 2), rk = MKR(a.wk, D_ * HE_ * 2);
    const unsigned vo = (lane >> 3) * (HE_ * 2) + h * 384 + (lane & 7) * 16;
#pragma unroll 1
    for (int q = PF0 * 24 + wave; q < 240; q += 8) { const int rp = q / 3; const unsigned so = (unsigned)rp * (8 * HE_ * 2) + (q - rp * 3) * 128;
        dma16(rq, vo, so, ldsb + wave * 2048); dma16(rk, vo, so, ldsb + wave * 2048 + 1024); VMW(PFD); }
    VMDRAIN(); LBAR();
}

__device__ __forceinline__ unsigned wsw(unsigned row) { return ((row >> 1) & 1u) | ((((row >> 2) ^ (row >> 3)) & 1u) << 1); }
template <int NB, int RB, int NRND, int LB, int R0, class G, class F, class H> __device__ __forceinline__ void load_b(unsigned ldsb, int wave, G&& src, F&& take, H&& back) {
    auto issue = [&](int rd) {
#pragma unroll
        for (int j = 0; j < NB; ++j) src(rd, j, ldsb + LB + ((rd + R0) % RB) * (NB * 8192) + j * 8192 + wave * 1024); };
#pragma unroll
    for (int rd = 0; rd < RB; ++rd) issue(rd);
#pragma unroll
    for (int rd = 0; rd < NRND; ++rd) {
        VMW((NRND - 1 - rd < RB - 1 ? NRND - 1 - rd : RB - 1) * NB);
        LBAR();
        take(rd, LB + ((rd + R0) % RB) * (NB * 8192));
        LBAR();
        back(rd);
        if (rd + RB < NRND) issue(rd + RB);
    }
}
__device__ __forceinline__ u32x4 ld_frag(const unsigned char* blk, unsigned r0, unsigned dr, unsigned win, unsigned l15) {
    const unsigned ra = r0 + (l15 >> 2), rb = ra + dr;
    const u32x2 x = ldstr(blk + ra * 128 + ((win ^ wsw(ra)) << 5) + (l15 & 3) * 8), y = ldstr(blk + rb * 128 + ((win ^ wsw(rb)) << 5) + (l15 & 3) * 8);
    return (u32x4){x.x, x.y, y.x, y.y};
}
__device__ __forceinline__ void ln_consts(float s1, float s2, float& mb, float& inv) {
    const float mean = s1 * (1.0f / D_), var = fmaxf(__builtin_fmaf(-mean, mean, s2 * (1.0f / D_)), 0.f);
    mb = rbf(mean); inv = __builtin_amdgcn_rcpf(rbf(__builtin_amdgcn_sqrtf(rbf(rbf(var) + 1e-5f))));
}
__device__ __forceinline__ unsigned m_idx(int ln) { return (ln & 2) ? 0xEEEEu : 0x4444u; }
__device__ __forceinline__ f32x4 zero4() { f32x2 a, b; asm volatile("v_mov_b64 %0, 0\n\tv_mov_b64 %1, 0\n\ts_nop 0" : "=v"(a), "=v"(b)); return (f32x4){a[0], a[1], b[0], b[1]}; }
__device__ __forceinline__ u32x4 ones4() { u32x2 a, b; asm volatile("v_mov_b64 %0, -1\n\tv_mov_b64 %1, -1" : "=v"(a), "=v"(b)); return (u32x4){a[0], a[1], b[0], b[1]}; }
__device__ __forceinline__ f32x2 fold2(f32x4 v) { return (f32x2){v[0], v[1]} + (f32x2){v[2], v[3]}; }
__device__ __forceinline__ unsigned fold_pk(f32x4 v) { float x = v[0] + v[2]; PIN(x); return pk2(x, v[1] + v[3]); }
__device__ __forceinline__ float muls(float x, float y) { float r; asm("v_mul_f32 %0, %1, %2" : "=v"(r) : "v"(x), "v"(y)); return r; }
#define LDSB() __builtin_amdgcn_readfirstlane((unsigned)(uintptr_t)(__attribute__((address_space(3))) unsigned char*)lds)
__device__ __forceinline__ int xp_of(int h) { return 3 * (h >> 1) + (h & 1); }
__device__ __forceinline__ unsigned um_base(unsigned long long sd, unsigned pol, unsigned c) {
    const unsigned s = (unsigned)(sd >> c) & 1u; const unsigned long long same = (s ? sd : ~sd) & ((1ull << c) - 1ull);
    return FR_UM + ((unsigned)__popcll(same) << 16) + ((s ^ pol) << 13);
}
__device__ __forceinline__ unsigned long long um_sides(const MegaArgs& a) {
    const rsrc_t rfl = MKR(a.hop, F_ERR); const unsigned l = lane_id(), off = F_SD + (l < 40 ? l : 0) * 4;
    wait_set(rfl, off, a.hop, 2);
    return __ballot(ld4c(rfl, off) == 2u);
}

#define ROW2(op, a, b) asm volatile("s_nop 1\n\t" op " %0, %0, %0 row_ror:8 row_mask:0xf bank_mask:0xf\n\t" op " %1, %1, %1 row_ror:8 row_mask:0xf bank_mask:0xf\n\ts_nop 0\n\t" \
    op " %0, %0, %0 row_ror:4 row_mask:0xf bank_mask:0xf\n\t" op " %1, %1, %1 row_ror:4 row_mask:0xf bank_mask:0xf\n\ts_nop 0\n\t" \
    op " %0, %0, %0 row_ror:2 row_mask:0xf bank_mask:0xf\n\t" op " %1, %1, %1 row_ror:2 row_mask:0xf bank_mask:0xf\n\ts_nop 0\n\t" \
    op " %0, %0, %0 row_ror:1 row_mask:0xf bank_mask:0xf\n\t" op " %1, %1, %1 row_ror:1 row_mask:0xf bank_mask:0xf" : "+v"(a), "+v"(b))
template <int W> __device__ __forceinline__ void s_run(const MegaArgs& a, unsigned char* lds, const int h, const u32x8 (&wr)[8][3]) {
    constexpr int wave = W; const int lane = lane_id();
    const unsigned ldsb = LDSB();
    const rsrc_t rhx = MKR(al2m(a.hx), FR_USED);
    constexpr int L_MEM = 0, L_Q = 10240, L_K = 13312, L_KX = 16384, L_TIM = 28672, L_W = 32768;
    lds_u32* tim = (lds_u32*)(lds + L_TIM + 256);
    if (W == 0) { tim[0] = 0; tim[1] = 0; tim[2] = 0; tim[4] = 0; }
    const unsigned pol = page_pol(a), sx = (my_side() ^ pol) << 13;
    unsigned po = (2 + (lane >> 5)) * 16384 + h * 256 + ((lane >> 4) & 1) * 128 + (lane & 8) * 4 + (lane & 2) * 8 + (lane & 4) * 2 + (lane & 1) * 8192;
    unsigned pm = 4 * 16384 + (lane >> 5) * 2048 + (h >> 4) * 1024 + ((h >> 1) & 3) * 256 + ((lane >> 3) & 3) * 64 + (h & 8) * 4 + (lane & 4) * 4 + (h & 1) * 8 + (lane & 2) * 2 + (lane & 1) * 8192;
    const int xj = xp_of(h);
    unsigned idx = m_idx(lane);
    PIN(idx); if (W < 2) { PIN(po); PIN(pm); }
#pragma unroll 1
    for (int layer = 0; layer < 2; ++layer) {
    unsigned char* hop = a.hop + layer * HOP_BYTES;
    const rsrc_t rfl = MKR(hop, F_ERR), rst = MKR(a.st, ST_BYTES);
    const unsigned sb0 = (unsigned)layer * (20u << 14);
    auto kx_fetch = [&](int t, bool wait) {
        int ln = lane_id();
        if (wait) wait_set(rfl, F_XP + (t * NXP_ + xj) * FLP, hop, 0);
        dma16cn<12>(rst, ln * 16, ST_KX + ((t & (KXR - 1)) * 32 + h) * 12288, ldsb + L_KX);
    };
    if (W == 3 && lane_id() == 0) STAMP(100 + h);
    bool early = false;
    if (W == 2) { early = __all(ld4c(rfl, F_XP + xj * FLP) != 0u); if (early) kx_fetch(0, false); }
    VMKNOWN();
    unsigned tb = 0, cC1 = 0, cP = 0, kdur = 0, krt = 0;
#pragma unroll 1
    for (int s = 0; s < NSEG_; ++s) {
        int ln = lane_id();
        ATV;
        const unsigned sb = sb0 + (unsigned)(s & 3) * (5u << 14);
        unsigned qo = (W < 4 ? L_Q + ((ln >> 3) & 1) * 256 + (ln >> 4) * 64 + ((ln >> 1) & 1) * 32 + (ln & 1) * 16 + ((ln >> 2) & 1) * 8
                             : L_K + ((ln & 15) >> 2) * 128 + (((ln >> 4) * 2 + (ln & 1) + (ln & 4)) & 7) * 16 + (ln & 2) * 2);
        unsigned sel = (ln & 1) ? 0x03020706u : 0x05040100u, w2o = L_W + 65536 + wave * 2048 + ln * 16;
        PIN(qo); PIN(sel); PIN(w2o);
        unsigned char* qp = lds + qo;
        u32x8 wl[3];
        const unsigned char* wp = lds + L_W + wave * 2048 + ln * 16;
        auto w_lds = [&](int f) { const unsigned char* p = f < 4 ? wp + f * 16384 : lds + w2o + (f - 4) * 16384; return cat8(lds16(p), lds16(p + 1024)); };
#pragma unroll
        for (int u = 0; u < 3; ++u) wl[u] = w_lds(1 + u);
        f32x4 acc[3] = {zero4(), zero4(), zero4()};
        unsigned tp = s == 0 ? rt_now() : tb + cP;
#define G3(A, Wt) do { acc[0] = smf(A, Wt[0], acc[0], idx); acc[1] = smf(A, Wt[1], acc[1], idx); acc[2] = smf(A, Wt[2], acc[2], idx); PIN(acc[0]); PIN(acc[1]); PIN(acc[2]); SBAR(); } while (0)
#define S1(u, A, B) do { acc[u] = smf(A, B, acc[u], idx); PIN(acc[u]); SBAR(); } while (0)
        u32x4 m0, n0, n1; unsigned nw = 0;
        unsigned char* mp = lds + L_MEM + ln * 16;
        if (W != 1) {
            constexpr int B0 = W == 7 ? 8 : W, B1 = W == 3 ? 7 : (W == 4 ? 1 : 9); constexpr bool TWO = W >= 3 && W <= 5;
            unsigned mo = sx + ln * 16;
            const unsigned to = sx + M_STAMP + min(ln, 17) * 64 + (s >> 2) * 4;
            PIN(wl[0]); PIN(wl[1]); PIN(wl[2]); PIN(mo);
            u32x4 m1; unsigned tw = 0;
            auto fetch = [&](u32x4& x0, u32x4& x1, unsigned& xw) { CBAR();
                x0 = __builtin_amdgcn_raw_buffer_load_b128(rhx, mo, sb + (B0 < 8 ? B0 * 1024 : 16384), 16);
                if (TWO) x1 = __builtin_amdgcn_raw_buffer_load_b128(rhx, mo, sb + (B1 < 8 ? B1 * 1024 : 17408), 16);
                if (W == 2) xw = __builtin_amdgcn_raw_buffer_load_b32(rhx, to, sb, 16);
                CBAR(); };
            auto take = [&](const u32x4& x0, const u32x4& x1, unsigned xw) {
                *reinterpret_cast<u32x4*>(mp + B0 * 1024) = x0;
                if (TWO) *reinterpret_cast<u32x4*>(mp + B1 * 1024) = x1;
                if (W == 2) *reinterpret_cast<unsigned*>(lds + L_TIM + ln * 4) = xw;
                return __all(max(max(umax4(x0), TWO ? umax4(x1) : 0u), xw) != 0xffffffffu); };
            rt_wait(tp); fetch(m0, m1, tw); __builtin_amdgcn_s_sleep(DPS); fetch(n0, n1, nw);
            if (__builtin_expect(!take(m0, m1, tw), 0)) {
                tim[2] = (unsigned)s + 1u;
                for (unsigned sp = 0;; ++sp) {
                    fetch(m0, m1, tw);
                    if (take(n0, n1, nw)) { m0 = n0; break; }
                    if ((sp & 63u) == 63u && (sp > SPIN_CAP || tim[0])) { tim[0] = 1; break; }
                    if (sp > RTS) __builtin_amdgcn_s_sleep(RTL);
                    fetch(n0, n1, nw);
                    if (take(m0, m1, tw)) break;
                }
                VMKNOWN();
            }
        }
        if (W == 0) __builtin_amdgcn_s_setprio(SPRIO);
        AT(2);
        LBAR();
        AT(3);
        {
            constexpr int F = W == 7 ? 0 : W, R1 = F == 0 ? 1 : 0, R2 = F <= 1 ? 2 : 1, R3 = F <= 2 ? 3 : 2, R4 = F <= 3 ? 4 : 3, R5 = F <= 4 ? 5 : 4, R6 = F <= 5 ? 6 : 5;
            if (W == 1) { m0 = lds16(mp + 1024); SBAR(); }
            u32x4 A0 = lds16(mp + (W == 7 ? 0 : 8) * 1024); SBAR(); u32x4 A1 = lds16(mp + R1 * 1024); SBAR(); u32x4 A2 = lds16(mp + R2 * 1024); SBAR();
            u32x4 A3;
            if (W != 7) { G3(m0, wr[F]); A3 = lds16(mp + 9 * 1024); SBAR(); }
            { const u32x4 a8 = W == 7 ? m0 : A0;
              S1(0, a8, wl[0]); wl[0] = w_lds(4); SBAR(); S1(1, a8, wl[1]); wl[1] = w_lds(5); SBAR(); S1(2, a8, wl[2]); wl[2] = w_lds(6); SBAR(); }
            if (W == 7) { A3 = lds16(mp + 9 * 1024); SBAR(); G3(A0, wr[F]); }
            A0 = lds16(mp + R3 * 1024); SBAR();
            G3(A1, wr[R1]); A1 = lds16(mp + R4 * 1024); SBAR();
            if (W != 1) asm volatile("" :: "v"(n0)); if (W >= 3 && W <= 5) asm volatile("" :: "v"(n1)); if (W == 2) asm volatile("" :: "v"(nw));
            SBAR();
            G3(A2, wr[R2]); A2 = lds16(mp + R5 * 1024); SBAR();
            S1(1, A3, wl[1]); wl[1] = w_lds(0); SBAR(); S1(2, A3, wl[2]); wl[2] = w_lds(7); SBAR(); S1(0, A3, wl[0]); A3 = lds16(mp + R6 * 1024); SBAR();
            G3(A0, wr[R3]); A0 = lds16(mp + 7 * 1024); SBAR();
            G3(A1, wr[R4]); G3(A2, wr[R5]);
            auto put = [&](int u) { const int T = 3 * (W & 3) + u; const f32x2 v = fold2(acc[u]); const unsigned o = pk2(v[0], v[1]);
                *reinterpret_cast<unsigned*>(qp + (T >> 1) * 512 + (T & 1) * (W < 4 ? 4 : 8)) = __builtin_amdgcn_perm(dppu<0xB1>(o), o, sel); };
            acc[0] = smf(A3, wr[R6][0], acc[0], idx); acc[1] = smf(A3, wr[R6][1], acc[1], idx); acc[0] = smf(A0, wr[7][0], acc[0], idx);
            acc[2] = smf(A3, wr[R6][2], acc[2], idx); acc[1] = smf(A0, wl[1], acc[1], idx); PIN(acc[0]); PIN(acc[1]); PIN(acc[2]); SBAR();
            put(0); SBAR();
            acc[2] = smf(A0, wl[2], acc[2], idx); PIN(acc[2]); SBAR();
            put(1); SBAR(); put(2); SBAR(); AT(4);
        }
        if (s == 0) { if (W == 2 && !early) { kx_fetch(0, true); VMDRAIN(); } LBAR(); }
        if (W == 0 && s + 1 < NSEG_) {
            PIN(ln);
            unsigned kp = ldsb + L_KX + ln * 16, mk = ldsb + L_K + (ln >> 4) * 128 + ((ln + ((ln >> 2) & 4)) & 7) * 16;
            u32x8 ka = cat8(lds16a(kp), lds16a(kp + 1024)), kb = cat8(lds16a(kp + 6144), lds16a(kp + 7168)), kc = cat8(lds16a(kp + 2048), lds16a(kp + 3072)), kd = cat8(lds16a(kp + 8192), lds16a(kp + 9216));
            f32x4 sc[3] = {zero4(), zero4(), zero4()};
            const bool up = (ln & 8) != 0;
            PIN(ka); PIN(kb); PIN(kc); PIN(kd); PIN(sc[0]); PIN(sc[1]); PIN(sc[2]); PIN(kp); PIN(mk);
            __builtin_amdgcn_s_setprio(3);
            LBAR();
            AT(5);
            const u32x4 q0 = lds16a(kp + (L_Q - L_KX)); SBAR(); u32x8 ke = cat8(lds16a(mk), lds16a(mk + 512)); SBAR(); const u32x4 q1 = lds16a(kp + (L_Q - L_KX + 1024)); SBAR(); const u32x4 q2 = lds16a(kp + (L_Q - L_KX + 2048)); SBAR();
            sc[0] = smf(q0, ka, sc[0], idx); sc[1] = smf(q0, kb, sc[1], idx); PIN(sc[0]); PIN(sc[1]); SBAR();
            ka = cat8(lds16a(mk + 1024), lds16a(mk + 1536)); SBAR(); kb = cat8(lds16a(kp + 4096), lds16a(kp + 5120)); SBAR();
            sc[2] = smf(q0, ke, sc[2], idx); PIN(sc[2]); SBAR();
            ke = cat8(lds16a(kp + 10240), lds16a(kp + 11264)); SBAR();
            sc[0] = smf(q1, kc, sc[0], idx); PIN(sc[0]); SBAR();
            kc = cat8(lds16a(mk + 2048), lds16a(mk + 2560)); SBAR();
            sc[1] = smf(q1, kd, sc[1], idx); sc[2] = smf(q1, ka, sc[2], idx); sc[0] = smf(q2, kb, sc[0], idx); sc[1] = smf(q2, ke, sc[1], idx); sc[2] = smf(q2, kc, sc[2], idx); SBAR();
            AT(6);
            const f32x2 s0 = fold2(sc[0]), s1 = fold2(sc[1]), s2 = fold2(sc[2]);
            float ma = __builtin_fmaxf(__builtin_fmaxf(s0[0], s1[0]), s2[0]), mb = __builtin_fmaxf(__builtin_fmaxf(s0[1], s1[1]), s2[1]);
            ROW2("v_max_f32_dpp", ma, mb);
            const float c1 = 0.07216878364870322f * 1.4426950408889634f;
            ma *= -c1; mb *= -c1;
            const float a0 = __builtin_amdgcn_exp2f(__builtin_fmaf(s0[0], c1, ma)), b0 = __builtin_amdgcn_exp2f(__builtin_fmaf(s0[1], c1, mb));
            const float a1 = __builtin_amdgcn_exp2f(__builtin_fmaf(s1[0], c1, ma)), b1 = __builtin_amdgcn_exp2f(__builtin_fmaf(s1[1], c1, mb));
            const float a2 = __builtin_amdgcn_exp2f(__builtin_fmaf(s2[0], c1, ma)), b2 = __builtin_amdgcn_exp2f(__builtin_fmaf(s2[1], c1, mb));
            float za = __builtin_fmaf(a2, 0.5f, a0 + a1), zb = __builtin_fmaf(b2, 0.5f, b0 + b1);
            ROW2("v_add_f32_dpp", za, zb);
            za = __builtin_amdgcn_rcpf(za); zb = __builtin_amdgcn_rcpf(zb);
            const unsigned da = pk2(a0 * za, a1 * za), db = pk2(b0 * zb, b1 * zb);
            const float vm = up ? b2 * zb : a2 * za;
            u32x2 oa = (u32x2){dppu<0xA0>(da), dppu<0xF5>(da)}, ob = (u32x2){dppu<0xA0>(db), dppu<0xF5>(db)}; unsigned om = pk2(dppf<0xA0>(vm), dppf<0xF5>(vm));
            PIN(oa); PIN(ob); PIN(om);
            __builtin_amdgcn_raw_buffer_store_b64(oa, rhx, po, sb, 16);
            __builtin_amdgcn_raw_buffer_store_b64(ob, rhx, po + 64, sb, 16);
            __builtin_amdgcn_raw_buffer_store_b32(om, rhx, pm, sb, 16);
            SBAR(); AT(7);
            tim[1] = rt_now() - (tb + cP);
            tim[4] = (unsigned)s + 1u;
            __builtin_amdgcn_s_setprio(0);
            PIN(ln);
            tt_step(*reinterpret_cast<const unsigned*>(lds + L_TIM + ln * 4), s, s == 0 ? rt_now() - (W == 0 ? TT_E1 : TT_E1 - 35) : 0u, tb, cC1, cP);
        } else {
            if (W == 1) VMDRAIN();
            LBAR();
            AT(5);
            tt_step(*reinterpret_cast<const unsigned*>(lds + L_TIM + ln * 4), s, s == 0 ? rt_now() - (W == 0 ? TT_E1 : TT_E1 - 35) : 0u, tb, cC1, cP);
            if (W == 2 && s + 1 < NSEG_) wait_set(rfl, F_XP + ((s + 1) * NXP_ + xj) * FLP, hop, 0);
            if ((W == 1 || W == 2) && s + 1 < NSEG_) { for (unsigned sp = 0; tim[4] != (unsigned)s + 1u && sp < SPIN_CAP; ++sp) __builtin_amdgcn_s_sleep(2); CBAR(); }
            if (W == 1) {
                kdur = min(__builtin_amdgcn_readfirstlane(tim[1]), 4095u); krt = __builtin_amdgcn_readfirstlane(tim[2]) == (unsigned)s + 1u ? 1u : 0u;
#pragma unroll
                for (int m = 0; m < 3; ++m) st16s(lds16(lds + L_K + m * 1024 + ln * 16), rst, ln * 16, ST_KM + ((s & (PR - 1)) * 32 + h) * 3072 + m * 1024);
                {
                    const unsigned sr = sb0 + (unsigned)((s + 2) & 3) * (5u << 14);
                    __builtin_amdgcn_raw_buffer_store_b64((u32x2){~0u, ~0u}, rhx, po, sr, 16); __builtin_amdgcn_raw_buffer_store_b64((u32x2){~0u, ~0u}, rhx, po, sr + 64, 16);
                    __builtin_amdgcn_raw_buffer_store_b32(~0u, rhx, pm, sr, 16);
                }
                VMW(3); if (ln == 0) st16c(rfl, F_KM + (s * 32 + h) * FLP, (u32x4){cP, kdur, krt, tb}); if (ln == 0 && h == 0 && s >= 56) STAMP(370 + s - 56);
            }
            if (W == 2 && s + 1 < NSEG_) dma16cn<12>(rst, ln * 16, ST_KX + (((s + 1) & (KXR - 1)) * 32 + h) * 12288, ldsb + L_KX);
        }
        ATD(0, s == 41 && h == 0);
    }
    if (W == 0) { tim[2] = 0; tim[4] = 0; }
    if (W == 1 && lane_id() == 0) STAMP(512 + blockIdx.x);
    }
    if (W == 0 && tim[0]) set_err(a.hop);
}

__device__ __forceinline__ void s_part(const MegaArgs& a, unsigned char* lds, const int h, const int wave) {
    const int lane = lane_id();
    const unsigned ldsb = LDSB();
    u32x8 wr[8][3];
    auto keep = [&](int f, u32x8 v) { unsigned char* p = lds + 32768 + f * 16384 + wave * 2048 + lane * 16; *reinterpret_cast<u32x4*>(p) = lo4(v); *reinterpret_cast<u32x4*>(p + 1024) = hi4(v); };
    load_w<10>(MKR(a.wq, D_ * HE_ * 2), MKR(a.wk, D_ * HE_ * 2), 0u, h * 192, lds, ldsb, wave, lane, [&](int rd, int u, u32x8 v) { if (rd >= 8) keep(3 * rd - 23 + u, v); else if (rd == 7 && u == 2) keep(7, v); else wr[rd][u] = v; });
    keep(0, wr[7][1]);
    if (wave == 7) s_run<7>(a, lds, h, wr); else if (wave == 6) s_run<6>(a, lds, h, wr); else if (wave == 5) s_run<5>(a, lds, h, wr); else if (wave == 4) s_run<4>(a, lds, h, wr);
    else if (wave == 3) s_run<3>(a, lds, h, wr); else if (wave == 2) s_run<2>(a, lds, h, wr); else if (wave == 1) s_run<1>(a, lds, h, wr); else s_run<0>(a, lds, h, wr);
}

template <bool XW> __device__ __forceinline__ void v_run(const MegaArgs& a, unsigned char* lds, const int h, const int wave) {
    const int lane = lane_id(), tid = wave * 64 + lane;
    const unsigned ldsb = LDSB();
    constexpr int L_MEM = 0, L_V = 10240, L_VX = 13312, L_TIM = 25600, L_E = 26624, L_F = L_E + 1024, L_T = L_F + 2048, L_W = 16384, NO = XW ? 4 : 6, NV = XW ? 2 : 1, KR = XW ? 5 : 4;
    constexpr int NBK = NO - 1;
    u32x8 wo[NO][3], wv[KR][NV];
    const int vt = XW ? 2 * wave : 4 + wave;
    {
        const rsrc_t rv = MKR(a.wv, D_ * HE_ * 2), ro = MKR(a.wo, HE_ * D_ * 2);
        const unsigned l15 = lane & 15, lg = lane >> 4, row = 8 * wave + (lane >> 3), ch = ((lane & 7) ^ (wsw(row) << 1)) * 16;
        const unsigned vo = row * (HE_ * 2) + h * 384 + ch, oo = (h * 192 + row) * (D_ * 2) + ch;
        u32x8 tmp[2][NV];
        load_b<6, 3, 10, L_W, 2>(ldsb, wave,
            [&](int rd, int j, unsigned dst) { if (rd < 5) dma16(ro, oo, (j >> 1) * (64 * D_ * 2) + rd * 256 + (j & 1) * 128, dst); else dma16(rv, vo, ((rd - 5) * 2 + j / 3) * (64 * HE_ * 2) + (j % 3) * 128, dst); },
            [&](int rd, int base) {
                if (rd < 5) {
#pragma unroll
                    for (int i = 0; i < (rd != 3 ? 1 : (XW ? 0 : 2)); ++i) { const int tl = rd != 3 ? wave : 2 * (wave - 4) + i;
#pragma unroll
                        for (int k = 0; k < 3; ++k) { const unsigned char* p = lds + base + (2 * k + (tl >> 2)) * 8192; wo[rd < 3 ? rd : (rd == 3 ? 4 + i : 3)][k] = cat8(ld_frag(p, 4 * lg, 16, tl & 3, l15), ld_frag(p, 32 + 4 * lg, 16, tl & 3, l15)); } }
                } else {
#pragma unroll
                    for (int k = 0; k < 2; ++k)
#pragma unroll
                        for (int u = 0; u < NV; ++u) { const unsigned char* p = lds + base + (3 * k + (vt >> 2)) * 8192; const u32x8 v = cat8(ld_frag(p, 8 * lg, 4, (vt & 3) + u, l15), ld_frag(p, 32 + 8 * lg, 4, (vt & 3) + u, l15));
                            if (2 * (rd - 5) + k < KR) wv[2 * (rd - 5) + k][u] = v; else tmp[k][u] = v; } } },
            [&](int rd) {
                if (rd >= 7) {
#pragma unroll
                    for (int k = 0; k < 2; ++k)
#pragma unroll
                        for (int u = 0; u < NV; ++u) if (2 * (rd - 5) + k >= KR) { unsigned char* p = lds + L_W + (2 * (rd - 5) + k - 4) * 24576 + (vt + u) * 2048 + lane * 16; *reinterpret_cast<u32x4*>(p) = lo4(tmp[k][u]); *reinterpret_cast<u32x4*>(p + 1024) = hi4(tmp[k][u]); } } });
    }
    const rsrc_t rhx = MKR(al2m(a.hx), FR_USED), rst = MKR(a.st, ST_BYTES);
    lds_u32* tim = (lds_u32*)(lds + L_TIM);
    if (tid == 0) { tim[0] = 0; tim[2] = 0; }
    const unsigned pol = page_pol(a), sx = (my_side() ^ pol) << 13;
    unsigned uo0, uo1 = 0, l16 = lane * 16;
    { const unsigned long long sd = um_sides(a);
      uo0 = um_base(sd, pol, 8 * (lane >> 4) + (lane < 48 ? 0 : 8) + wave) + h * 256 + (lane & 15) * 16;
      if (!XW) uo1 = um_base(sd, pol, 16 + 2 * wave + (lane >> 5)) + h * 256 + (lane & 15) * 16 + (lane & 16) / 2; }
    auto arm = [&](unsigned so) { const u32x4 on = ones4(); st16s(on, rhx, uo0, so); if (!XW) __builtin_amdgcn_raw_buffer_store_b64((u32x2){on.x, on.y}, rhx, uo1, so, 16); SBAR(); };
    arm(16384);
    const int xj = xp_of(h), xj2 = 3 * (h >> 1) + 2;
    if (XW) uo1 = wave < 2 ? l16 + 16384 + wave * 1024 : M_STAMP + min(lane, 17) * 64;
    PIN(uo0); PIN(uo1); PIN(l16);
    *reinterpret_cast<u32x4*>(lds + L_T + l16) = (u32x4){L_V + ((lane >> 3) & 1) * 256u + (lane >> 4) * 64u + ((lane >> 1) & 1) * 32u + (lane & 1) * 16u + ((lane >> 2) & 1) * 8u,
        (lane & 1) ? 0x03020706u : 0x05040100u, m_idx(lane), F_XP + ((lane & 1) ? xj : xj2) * FLP};
    VMKNOWN();
    ATV;
#define la ((int)l16)
    auto u_so = [&](int t, int i) { return ST_U + (((t & (URING - 1)) * 40 + (i < 3 ? 8 * i + wave : 13 + 2 * wave + i)) * 32 + h) * 1024; };
    u32x4 xf[6][2];
    auto back0 = [&](int q) { xf[q >> 1][q & 1] = lds16a(la + L_VX + (q & 1) * 6144 + (q >> 1) * 1024); SBAR(); };
    auto back = [&](int t, auto&& side) {
        f32x4 acc[2][2]; u32x4 o; unsigned so;
#pragma unroll
        for (int i = 0; i < NBK; ++i) {
#pragma unroll
            for (int j = 0; j < 12; ++j) { const int k = j >> 1, r = j & 1;
                acc[i & 1][r] = dmf(xf[k][r], (k & 1) ? hi4(wo[i + (i > 2)][k >> 1]) : lo4(wo[i + (i > 2)][k >> 1]), k ? acc[i & 1][r] : Z4); PIN(acc[i & 1][r]); SBAR();
                if (i == 0) { side(j); if (j + 4 < 12) back0(j + 4); }
                else { const f32x4 c0 = acc[(i - 1) & 1][0], c1 = acc[(i - 1) & 1][1];
                    if (j == 2) { so = u_so(t, i - 1); asm volatile("" : "+s"(so)); }
                    if (j == 3) { o.x = pk2(c0[0], c1[0]); PIN(o.x); } if (j == 4) { o.y = pk2(c0[2], c1[2]); PIN(o.y); } if (j == 5) { o.z = pk2(c0[1], c1[1]); PIN(o.z); } if (j == 6) { o.w = pk2(c0[3], c1[3]); PIN(o); }
                    if (j == 7) __builtin_amdgcn_raw_buffer_store_b128(o, rst, l16, so, 16);
                    if (j == 9) asm volatile("" :: "v"(o));
                    if (j == 10 && i == NBK - 1) { so = u_so(t, i); asm volatile("" : "+s"(so)); }
                }
                SBAR(); }
        }
        const f32x4 c0 = acc[(NBK - 1) & 1][0], c1 = acc[(NBK - 1) & 1][1];
        st16s((u32x4){pk2(c0[0], c1[0]), pk2(c0[2], c1[2]), pk2(c0[1], c1[1]), pk2(c0[3], c1[3])}, rst, l16, so); };
    auto v_so = [&](int t) { return ST_VX + ((t & (KXR - 1)) * 32 + h) * 12288 + (wave - 4) * 3072; };
#pragma unroll 1
    for (int layer = 0; layer < 2; ++layer) {
    unsigned char* hop = a.hop + layer * HOP_BYTES;
    const rsrc_t rfl = MKR(hop, F_ERR);
    const unsigned sb0 = (unsigned)layer * (20u << 14) + sx;
    if (wave == 0 && l16 == 0) STAMP(140 + h);
    unsigned tb = rt_now(), cC1 = 0, cP = 0, tn = tb, krt = 0, te = 0;
#pragma unroll 1
    for (int s = 0; s < NSEG_; ++s) {
        PIN(l16);
        int w0o = la + L_W + vt * 2048, w1o = w0o + 3 * 24576; unsigned us = (s & 3) << 14;
        PIN(w0o); PIN(w1o); asm volatile("" : "+s"(us));
        const u32x4 lc = lds16a(la + L_T);
        unsigned fw = 1u;
        if (!XW) fw = lds4a(L_F + wave * 256 + (l16 >> 2));
        SBAR();
        auto w_h = [&](int kb, int u) { return lds16a((kb < 7 ? w0o + (kb - 4) * 24576 : w1o + (kb - 7) * 24576) + u * 1024); };
        constexpr int NL = 4;
        u32x4 wl[NL][2];
#pragma unroll
        for (int u = 0; u < 2 * NL; ++u) wl[u >> 1][u & 1] = XW ? w_h(5 + (u >> 2), u & 3) : w_h(4 + (u >> 1), u & 1);
        f32x4 acc[NV];
#pragma unroll
        for (int u = 0; u < NV; ++u) acc[u] = zero4();
        const unsigned sel = lc.y, idx = lc.z;
        unsigned d0 = 0, d1 = 0;
        SBAR();
        if (XW) {
            unsigned s0 = sb0 + (unsigned)(s & 3) * (5u << 14), s1 = s0 + wave * 1024, s2 = s1 + 4096;
            int mw = la + wave * 1024, me = mw + (wave < 2 ? 8192 : L_E - 2048);
            if (wave >= 2) s0 += (s >> 2) * 4;
            const unsigned em = wave < 2 ? ~0u : 0u;
            PIN(mw); PIN(me); asm volatile("" : "+s"(s0), "+s"(s1), "+s"(s2));
            u32x4 m0, m1, m2;
            auto fetch = [&]() { CBAR();
                m0 = __builtin_amdgcn_raw_buffer_load_b128(rhx, l16, s1, 16); SBAR(); m1 = __builtin_amdgcn_raw_buffer_load_b128(rhx, l16, s2, 16); SBAR(); m2 = __builtin_amdgcn_raw_buffer_load_b128(rhx, uo1, s0, 16);
                CBAR(); SBAR(); };
            auto take = [&]() { sts16a(mw, m0); sts16a(mw + 4096, m1); sts16a(me, m2); SBAR();
                return __all(max(max(umax4(m0), umax4(m1)), max(m2.x, max(max(m2.y, m2.z), m2.w) & em)) != 0xffffffffu); };
            AT(0);
            rt_wait(tb + cP + TT_VD);
            AT(6);
            fetch();
            if (__builtin_expect(!take(), 0)) {
                tim[2] = (unsigned)s + 1u;
                for (unsigned sp = 0;; ++sp) {
                    fetch();
                    if (take()) break;
                    if ((sp & 63u) == 63u && (sp > SPIN_CAP || tim[0])) { tim[0] = 1; break; }
                    if (sp > 4) __builtin_amdgcn_s_sleep(RTL);
                }
            }
        } else {
            if (__builtin_expect(s > 0 && !__all(fw != 0u), 0)) { wait_set(rfl, lc.w + min(s + 2, NSEG_ - 1) * (NXP_ * FLP), hop, 0); VMKNOWN(); }
            d0 = v_so(s + 2); d1 = min(s + 3, NSEG_ - 1) * (NXP_ * FLP); asm volatile("" : "+s"(d0), "+s"(d1));
        }
        AT(1);
        if (XW) { LBAR(); __builtin_amdgcn_s_setprio(0); } else { __builtin_amdgcn_s_setprio(YP1); CBAR(); __builtin_amdgcn_s_barrier(); CBAR(); }
        AT(2);
        f32x4 um[NO]; unsigned kdur = 0;
        {
#define VS(u, A, B) do { acc[(u) % NV] = smf(A, B, acc[(u) % NV], idx); PIN(acc[(u) % NV]); SBAR(); } while (0)
#define VL(u) cat8(wl[(u) % NL][0], wl[(u) % NL][1])
#define VA(k) lds16a(la + L_MEM + (k) * 1024); SBAR()
#define VW(u, kb, q) wl[(u) % NL][(q) & 1] = w_h(kb, q); SBAR()
            unsigned tw, tr; int qo;
            if (XW) {
                u32x4 A0 = VA(5); u32x4 A1 = VA(6); u32x4 A2 = VA(0); u32x4 A3 = VA(1);
                VS(0, A0, VL(0)); VS(1, A0, VL(1)); VW(0, 7, 0);
                VS(0, A1, VL(2)); VW(0, 7, 1); A0 = VA(2); VS(1, A1, VL(3)); VW(1, 7, 2);
                VS(0, A2, wv[0][0]); VW(1, 7, 3); A1 = VA(7); VS(1, A2, wv[0][NV - 1]); VW(2, 8, 0);
                VS(0, A3, wv[1][0]); VW(2, 8, 1); A2 = VA(8); VS(1, A3, wv[1][NV - 1]); VW(3, 8, 2);
                VS(0, A0, wv[2][0]); VW(3, 8, 3); A3 = VA(3); VS(1, A0, wv[2][NV - 1]);
                VS(0, A1, VL(0)); A0 = VA(4); VS(1, A1, VL(1)); VW(0, 9, 0);
                VS(0, A2, VL(2)); VW(0, 9, 1); A1 = VA(9); VS(1, A2, VL(3)); VW(1, 9, 2);
                VS(0, A3, wv[3][0]); VW(1, 9, 3); VS(1, A3, wv[3][NV - 1]); um[0] = zero4(); tw = lds4a(la + L_E); tr = tim[2]; SBAR();
                VS(0, A0, wv[KR - 1][0]); um[1] = zero4(); qo = lc.x + (vt >> 1) * 512; PIN(qo); SBAR(); VS(0, A1, VL(0)); um[2] = zero4(); SBAR(); VS(1, A0, wv[KR - 1][NV - 1]); um[3] = zero4(); SBAR(); VS(1, A1, VL(1));
            } else {
                u32x4 A[3];
                constexpr int ord[10] = {0, 4, 5, 6, 7, 1, 2, 3, 8, 9};
#pragma unroll
                for (int k = 0; k < 3; ++k) { A[k] = VA(ord[k]); }
                dma16cn<3>(rst, l16, d0, ldsb + L_VX + (wave - 4) * 3072); dma4c(rfl, lc.w, d1, ldsb + L_F + wave * 256); SBAR();
#pragma unroll
                for (int k = 0; k < 10; ++k) { VS(0, A[k % 3], (ord[k] < 4 ? wv[ord[k] & 3][0] : VL(ord[k]))); if (k >= 2 && k < 6) { VW((k - 2) >> 1, 8 + ((k - 2) >> 1), k & 1); }
                    if (k + 3 < 10) { A[k % 3] = VA(ord[(k + 3) % 10]); }
                    if (k == 0) { qo = lc.x + ((vt >> 1) * 512 + (vt & 1) * 4); PIN(qo); SBAR(); }
                    if (k >= 6 && k < 9) { um[(2 * k - 12) % NO] = zero4(); um[(2 * k - 11) % NO] = zero4(); SBAR(); } }
            }
            unsigned o[NV];
#pragma unroll
            for (int u = 0; u < NV; ++u) { o[u] = fold_pk(acc[u]); o[u] = __builtin_amdgcn_perm(dppu<0xB1>(o[u]), o[u], sel); }
            if (XW) { *(__attribute__((address_space(3))) u32x2*)(uintptr_t)(unsigned)qo = (u32x2){o[0], o[NV - 1]}; SBAR();
                kdur = min(te - tn, 4095u) + (krt == (unsigned)s ? 4096u : 0u);
                tn = tb + cP; krt = __builtin_amdgcn_readfirstlane(tr);
                tt_step(tw, s, tb, tb, cC1, cP); asm volatile("" : "+s"(tb), "+s"(cC1), "+s"(cP));
            } else { *(__attribute__((address_space(3))) unsigned*)(uintptr_t)(unsigned)qo = o[0]; __builtin_amdgcn_s_setprio(2); }
        }
        AT(3);
        VMDRAIN(); LBAR();
        AT(4);
        {
            const u32x4 q0 = lds16a(la + L_V), q1 = lds16a(la + L_V + 1024), q2 = lds16a(la + L_V + 2048); SBAR();
            if (XW && wave == 3 && s > 0) { if (l16 == 0) st16c(rfl, F_UX + ((s + 1) * 32 + h) * FLP, (u32x4){idx, (kdur + l16) & 4095u, (kdur + l16) >> 12, idx}); SBAR(); }
#define VU(u, q, k) do { um[u] = smf(q, wo[u][k], um[u], idx); PIN(um[u]); SBAR(); } while (0)
#define VO(u) min(fold_pk(um[u]), 0xfffffffeu)
            if (!XW) { VU(NO - 2, q0, 0); VU(NO - 1, q0, 0); VU(NO - 2, q1, 1); VU(NO - 1, q1, 1); VU(NO - 2, q2, 2); VU(NO - 1, q2, 2); }
            VU(0, q0, 0); VU(2, q0, 0);
            unsigned e0 = 0, e1 = 0;
            if (!XW) { e0 = VO(NO - 2); PIN(e0); SBAR(); }
            VU(0, q1, 1);
            if (!XW) { e1 = VO(NO - 1); PIN(e1); SBAR(); }
            VU(2, q1, 1);
            if (!XW) { const auto z = __builtin_amdgcn_permlane32_swap(e0, e1, false, false); __builtin_amdgcn_raw_buffer_store_b64((u32x2){z[0], z[1]}, rhx, uo1, us, 16); SBAR(); }
            VU(0, q2, 2); back0(0); VU(2, q2, 2); back0(1); VU(1, q0, 0); back0(2); VU(1, q1, 1); back0(3);
            e0 = VO(0); PIN(e0); SBAR();
            VU(1, q2, 2);
            e1 = VO(2); PIN(e1); SBAR();
            VU(3, q0, 0);
            const auto x02 = __builtin_amdgcn_permlane32_swap(e0, e1, false, false); SBAR();
            VU(3, q1, 1); VU(3, q2, 2);
            e0 = VO(1); PIN(e0); SBAR();
            const auto x13 = __builtin_amdgcn_permlane32_swap(e0, VO(3), false, false);
            const auto y01 = __builtin_amdgcn_permlane16_swap(x02[0], x13[0], false, false), y23 = __builtin_amdgcn_permlane16_swap(x02[1], x13[1], false, false);
            st16s((u32x4){y01[0], y23[0], y01[1], y23[1]}, rhx, uo0, us);
            SBAR();
            AT(5);
            __builtin_amdgcn_s_setprio(XW ? 2 : 0);
        }
        if (__builtin_expect(s == 0, 0)) {
            arm(2u << 14);
#pragma unroll 1
            for (int t = 0; t < 3; ++t) {
                if (!XW) { wait_set(rfl, lds4a(la + L_T + 12) + t * (NXP_ * FLP), hop, t ? 0 : 1); dma16cn<3>(rst, l16, v_so(t), ldsb + L_VX + (wave - 4) * 3072); VMDRAIN(); }
                LBAR();
                back0(0); back0(1); back0(2); back0(3);
                back(t, [](int) {});
                VMDRAIN(); LBAR();
                if (wave == 0 && l16 == 0) st4c(rfl, F_UX + (t * 32 + h) * FLP, 1u);
                if (wave == 0 && l16 == 0 && h == 0) STAMP(10 + t);
            }
        } else back(s + 2, [&](int j) { if (XW && j == 0) { te = rt_now(); asm volatile("" : "+s"(te)); } if (j == 9) arm(us ^ (2u << 14)); });
        AT(7);
        ATD(1, s == 41 && h == 0);
    }
    if (wave == 0 && l16 == 0) { tim[2] = 0; STAMP(512 + blockIdx.x); }
    }
    if (wave == 0 && l16 == 0 && tim[0]) set_err(a.hop);
#undef la
}

constexpr int RO_UM = 131072, RO_TIM = RO_UM + 3 * 8192, RO_END = NSEG_ + OLAG;
__device__ __forceinline__ void ro_state(const MegaArgs& a, unsigned char* lds, const int r, const unsigned pol) {
    constexpr int wave = 0;
    const int lane = lane_id();
    const rsrc_t rhx = MKR(al2m(a.hx), FR_USED);
    lds_u32* tim = (lds_u32*)(lds + RO_TIM + 256);
    const unsigned sx = (my_side() ^ pol) << 13;
    unsigned po = sx + (2 + (r & 1)) * 16384 + lane * 16, uo = um_base(um_sides(a), pol, r >> 1) + lane * 16;
    unsigned ho = (r < 64 ? 0 : 8192) + r * 128 + (lane >> 5) * 64 + (lane & 2) * 16 + (lane & 16) + (lane & 12) + (lane & 1) * 8192;
    unsigned idx = (lane & 1) ? 0xEEEEu : 0x4444u;
    const unsigned pmo = (2 - (r & 1)) * 16384 + (r & 1) * 2048;
    PIN(po); PIN(uo); PIN(ho); PIN(idx);
#pragma unroll 1
    for (int layer = 0; layer < 2; ++layer) {
    const unsigned sb0 = (unsigned)layer * (20u << 14);
    unsigned tb = 0, cC1 = 0, cP = 0; int u3 = 0;
    u32x8 ux[16];
#pragma unroll 1
    for (int t = 0; t < RO_END; ++t) {
        const int s = min(t, NSEG_) - 1;
        ATV;
        if ((t > 0 && t < NSEG_) || t == RO_END - 1) {
            unsigned sb = sb0 + (unsigned)(t & 3) * (5u << 14), sp0 = sb0 + (unsigned)(s & 3) * (5u << 14), sp1 = sp0 + 4096, sp2 = sp0 + pmo;
            const unsigned sr = sb0 + (unsigned)((t + 2) & 3) * (5u << 14);
            asm volatile("" : "+s"(sb), "+s"(sp0), "+s"(sp1), "+s"(sp2));
            u32x4 um[8], p[10];
            auto u_load = [&]() { CBAR();
#pragma unroll
                for (int q = 0; q < 8; ++q) { um[q] = __builtin_amdgcn_raw_buffer_load_b128(rhx, uo + (q & 3) * 1024, ((s & 3) << 14) + (q >> 2) * 4096, 16); SBAR(); }
                CBAR(); };
            auto u_bad = [&]() { unsigned m = 0;
#pragma unroll
                for (int q = 0; q < 8; ++q) m = max(m, umax4(um[q]));
                return m; };
            unsigned tp = tb + cC1;
            if (t < NSEG_) {
                auto p_load = [&]() { CBAR();
#pragma unroll
                    for (int q = 0; q < 10; ++q) { p[q] = __builtin_amdgcn_raw_buffer_load_b128(rhx, po + (q & 3) * 1024, q < 4 ? sp0 : (q < 8 ? sp1 : sp2), 16); SBAR(); }
                    CBAR(); };
                unsigned o;
                auto p_use = [&](f32x4 c0, f32x4 c1) {
#pragma unroll
                    for (int q = 0; q < 8; ++q) { c0 = smf(p[q], ux[2 * q], c0, idx); c1 = smf(p[q], ux[2 * q + 1], c1, idx); SBAR(); }
#pragma unroll
                    for (int q = 0; q < 2; ++q) { c0 = smf(p[8 + q], cat8(um[4 * q], um[4 * q + 1]), c0, idx); c1 = smf(p[8 + q], cat8(um[4 * q + 2], um[4 * q + 3]), c1, idx); }
                    SBAR();
                    float v0 = c0[0] + c0[1]; PIN(v0);
                    const float v = v0 + (c1[2] + c1[3]);
                    o = pk2(dppf<0xA0>(v), dppf<0xF5>(v));
                    return !__any(v != v); };
                f32x4 c0 = zero4(), c1 = zero4();
                { const unsigned n = rt_now(); if ((int)(n - tp) > 4) { tp = n; tim[2] = (unsigned)t; } }
                AT(0);
                rt_wait(tp - TT_UM); u_load(); rt_wait(tp); AT(1); p_load();
                if (__builtin_expect(!p_use(c0, c1), 0)) {
                    tim[2] = (unsigned)t;
                    for (unsigned sp = 0;; ++sp) {
                        u_load(); p_load();
                        unsigned m = u_bad();
#pragma unroll
                        for (int q = 0; q < 10; ++q) m = max(m, umax4(p[q]));
                        if (__all(m != 0xffffffffu)) break;
                        if ((sp & 63u) == 63u && (sp > SPIN_CAP || tim[0])) { tim[0] = 1; break; }
                        if (sp > 3 || s == 0) __builtin_amdgcn_s_sleep(R0S);
                    }
                    p_use(zero4(), zero4());
                    o = min(o, 0xfffffffeu);
                    tp = rt_now() - TT_RT;
                }
                __builtin_amdgcn_raw_buffer_store_b32(o, rhx, ho, sb, 16);
                SBAR(); AT(2);
                tim[1] = rt_now() - (tb + cC1);
                if (r % 5 == 0 && lane < 2) { __builtin_amdgcn_raw_buffer_store_b32(min(tp, 0xfffffffeu), rhx, M_STAMP + (r / 5) * 64 + lane * 8192, sb + (t >> 2) * 4, 16);
                    if (t + 2 < NSEG_) __builtin_amdgcn_raw_buffer_store_b32(~0u, rhx, M_STAMP + (r / 5) * 64 + lane * 8192, sr + ((t + 2) >> 2) * 4, 16); }
                __builtin_amdgcn_raw_buffer_store_b32(~0u, rhx, ho, sr, 16);
            } else {
                rt_wait(tp - TT_UM);
                for (unsigned sp = 0;; ++sp) {
                    u_load();
                    if (__all(u_bad() != 0xffffffffu)) break;
                    if ((sp & 63u) == 63u && (sp > SPIN_CAP || tim[0])) { tim[0] = 1; break; }
                    __builtin_amdgcn_s_sleep(R0S);
                }
            }
            LBAR();
            unsigned char* up = lds + RO_UM + u3 * 8192 + lane_id() * 16;
#pragma unroll
            for (int q = 0; q < 8; ++q) *reinterpret_cast<u32x4*>(up + q * 1024) = um[q];
            u3 = u3 == 2 ? 0 : u3 + 1;
        } else LBAR();
        AT(3);
        LBAR();
        AT(4);
        if (t < NSEG_) {
            const int ln = lane_id();
            const unsigned tpv = *reinterpret_cast<const unsigned*>(lds + RO_TIM + ln * 4);
            const unsigned char* xp = lds + (t & 3) * 32768 + (ln >> 5) * 1024 + (ln & 31) * 16;
#pragma unroll
            for (int q = 0; q < 16; ++q) ux[q] = cat8(lds16(xp + (q >> 1) * 4096 + (q & 1) * 512), lds16(xp + (q >> 1) * 4096 + (q & 1) * 512 + 2048));
            tb = __builtin_amdgcn_readlane(tpv, 0); cC1 = __builtin_amdgcn_readlane(tpv, 1); cP = __builtin_amdgcn_readlane(tpv, 2);
            if (t == 0) tb = rt_now() - cC1;
#pragma unroll
            for (int q = 0; q < 16; ++q) PIN(ux[q]);
        }
        ATD(2, t == 42 && r == 0);
    }
    if (lane == 0) tim[2] = 0;
    }
    if (lane == 0 && tim[0]) set_err(a.hop);
}
__device__ __forceinline__ void ro_time(const MegaArgs& a, unsigned char* lds, const int r, const unsigned pol) {
    const int lane = lane_id();
    const rsrc_t rhx = MKR(al2m(a.hx), FR_USED);
    lds_u32* tim = (lds_u32*)(lds + RO_TIM + 256);
    const unsigned sx = (my_side() ^ pol) << 13, to = sx + M_STAMP + min(lane, 17) * 64;
#pragma unroll 1
    for (int layer = 0; layer < 2; ++layer) {
    const rsrc_t rfl = MKR(a.hop + layer * HOP_BYTES, F_ERR);
    const unsigned sb0 = (unsigned)layer * (20u << 14);
    unsigned tb = 0, cC1 = 0, cP = 0;
#pragma unroll 1
    for (int s = 0; s < RO_END; ++s) {
        LBAR();
        if (s < NSEG_) {
            if (s > 1) rt_wait(tb + cP + TT_TW);
            unsigned tw;
            for (unsigned sp = 0;; ++sp) { CBAR();
                tw = __builtin_amdgcn_raw_buffer_load_b32(rhx, to, sb0 + (unsigned)(s & 3) * (5u << 14) + (s >> 2) * 4, 16);
                if (__all(tw != 0xffffffffu)) break;
                __builtin_amdgcn_s_sleep(6);
                if (sp > SPIN_CAP_W || tim[0]) { tim[0] = 1; break; } }
            tt_step(tw, s, rt_now(), tb, cC1, cP);
            if (lane == 0) *reinterpret_cast<u32x4*>(lds + RO_TIM) = (u32x4){tb, cC1, cP, 0u};
            if (lane == 0 && s > 1) st16c(rfl, F_RD + (s * NR_ + r) * FLP, (u32x4){1u, min((unsigned)tim[1], 4095u), tim[2] == (unsigned)s ? 1u : 0u, 0u});
        }
        LBAR();
    }
    }
}
__device__ __forceinline__ void ro_master(const MegaArgs& a) {
    const int lane = lane_id();
    const rsrc_t rhx = MKR(al2m(a.hx), FR_USED);
    int e1 = TT_E1 * 16, e2 = TT_E2 * 16, x1 = 0, x2 = 0; bool pr2 = true;
#pragma unroll 1
    for (int layer = 0; layer < 2; ++layer) {
    const rsrc_t rfl = MKR(a.hop + layer * HOP_BYTES, F_ERR);
    const unsigned sb0 = (unsigned)layer * (20u << 14);
#pragma unroll 1
    for (int s = 0; s < RO_END; ++s) {
        LBAR();
        if (s + 1 < NSEG_) {
            const int q = max(s, 2);
            const u32x4 f = ld16c(rfl, lane < 32 ? F_KM + ((q - 2) * 32 + lane) * FLP : F_UX + (q * 32 + lane - 32) * FLP), g = ld16c(rfl, F_RD + ((q - 1) * NR_ + lane) * FLP), k = ld16c(rfl, F_RD + ((q - 1) * NR_ + 64 + (lane & 15)) * FLP);
            auto wmax = [&](unsigned v) { v = max(v, dppu<0x128>(v)); v = max(v, dppu<0x124>(v)); v = max(v, dppu<0x122>(v)); v = max(v, dppu<0x121>(v));
                return (int)max(max(__builtin_amdgcn_readlane(v, 0), __builtin_amdgcn_readlane(v, 16)), max(__builtin_amdgcn_readlane(v, 32), __builtin_amdgcn_readlane(v, 48))); };
            auto upd = [&](int& e, int m) { if (m > 0) e += (min(m * 16, e + 16 * 12) - e) >> TT_DEC; };
            const bool rs = __any(f.z != 0u), r2 = __any((g.z | k.z) != 0u);
            if (s > 3) {
                if (!rs) upd(e1, wmax(f.y + (lane < 32 ? 0u : TT_UM)));
                if (!r2) upd(e2, wmax(max(g.y, k.y)));
                if (rs && !pr2) x2 += TT_BUMP; else if (r2 && !rs) x1 += TT_BUMP;
                if (!rs && !r2 && (s & TT_DCY) == 0) { x1 -= x1 > 0; x2 -= x2 > 0; }
            }
            pr2 = r2 || s < 3;
            const int xs = max(TT_XS - s * TT_XD, 0);
            const unsigned c1 = (e1 >> 4) + TT_M1 + x1 + xs, c2 = c1 + (e2 >> 4) + TT_M2 + x2 + xs;
            if (lane < 4) __builtin_amdgcn_raw_buffer_store_b32((lane & 2) ? c2 : c1, rhx, M_STAMP + 1024 + (lane & 2) * 32 + (lane & 1) * 8192, sb0 + (unsigned)((s + 1) & 3) * (5u << 14) + ((s + 1) >> 2) * 4, 16);
        }
        LBAR();
    }
    }
}
__device__ __forceinline__ void ro_out(const MegaArgs& a, unsigned char* lds, const int r) {
    constexpr int wave = 2;
    const int lane = lane_id(), l15 = lane & 15, lg = lane >> 4, c = r >> 1, rt = r & 1;
    const rsrc_t rst = MKR(a.st, ST_BYTES), rx = MKR(a.xi, XI_BYTES), ro = MKR(a.out, S_ * D_ * 2);
    const unsigned xo = XI_X + rt * 20480 + (c >> 1) * 1024 + ((c & 1) * 2 + (lg >> 1)) * 256 + l15 * 16 + (lg & 1) * 8;
#pragma unroll 1
    for (int layer = 0; layer < 2; ++layer) {
    unsigned char* hop = a.hop + layer * HOP_BYTES;
    const rsrc_t rfl = MKR(hop, F_ERR);
    unsigned tb = 0, cC1 = 0, cP = 0; int u3 = 0;
    u32x2 o = (u32x2){0u, 0u}, sq = o;
    auto put = [&](int s) {
        if (layer) __builtin_amdgcn_raw_buffer_store_b64(o, ro, ((rt * 16 + l15) * D_ + 16 * c + 4 * lg) * 2, s * (32 * D_ * 2), 0);
        else { __builtin_amdgcn_raw_buffer_store_b64(o, rx, xo, s * XI_PITCH, 16); if (lane < 16) __builtin_amdgcn_raw_buffer_store_b64(sq, rx, (rt * 40 + c) * 128 + lane * 8, s * XI_PITCH, 16); } };
#pragma unroll 1
    for (int t = 0; t < RO_END; ++t) {
        const int s = t - OLAG;
        ATV;
        if (s >= 0) {
            if (t < NSEG_) rt_wait(tb + cC1 + TT_FP);
            wait_set(rfl, F_PX + ((s + QA) * 64 + (lane & 31) * 2 + rt) * FLP, hop, 0);
        }
        LBAR();
        u32x4 p[40]; u32x2 rr;
        if (s >= 0) {
            if (lane == 0 && !layer && s > 1 && t < OIM + 2) st16c(rfl, F_OF + ((s - 2) * NR_ + r) * FLP, (u32x4){cP, 1u, 1u, tb + cP});
            if (s > 0 && t <= OIM) put(s - 1);
            if (t < NSEG_) rt_wait(max((int)(tb + cP + TT_BD - rt_now()), 0) + rt_now() + TT_OD);
            AT(0);
            const int ln = lane_id();
#pragma unroll
            for (int q = 0; q < 40; ++q) p[q] = __builtin_amdgcn_raw_buffer_load_b128(rst, ln * 16 + (q & 3) * 1024, ST_PX + ((s & (PR - 1)) * 2 + rt) * 40960 + (q >> 2) * 4096, 16);
            SBAR();
            rr = __builtin_amdgcn_raw_buffer_load_b64(rx, xo, s * XI_PITCH, 16);
            CBAR();
            AT(1);
        }
        LBAR();
        if (t < NSEG_) { const unsigned tpv = *reinterpret_cast<const unsigned*>(lds + RO_TIM + lane * 4); tb = __builtin_amdgcn_readlane(tpv, 0); cC1 = __builtin_amdgcn_readlane(tpv, 1); cP = __builtin_amdgcn_readlane(tpv, 2); }
        if (s >= 0) {
            const int ln = lane_id();
            const unsigned char* xp = lds + (s & 3) * 32768 + ln * 16; const unsigned char* mp = lds + RO_UM + u3 * 8192 + ln * 16;
            u3 = u3 == 2 ? 0 : u3 + 1;
            f32x4 acc[2] = {Z4, Z4}; u32x4 uf[4];
#pragma unroll
            for (int q = 0; q < 4; ++q) uf[q] = lds16(xp + q * 1024);
            SBAR();
#pragma unroll
            for (int q = 0; q < 40; ++q) { acc[q & 1] = dmf(uf[q & 3], p[q], acc[q & 1]); PIN(acc[q & 1]); SBAR();
                if (q + 4 < 40) { uf[q & 3] = q + 4 < 32 ? lds16(xp + (q + 4) * 1024) : lds16(mp + (q - 28) * 1024); SBAR(); } }
            const f32x4 v = acc[0] + acc[1];
            const unsigned p0 = pk2(v[0], v[1]), p1 = pk2(v[2], v[3]);
            o = (u32x2){pk2(lo2f(rr.x) + lo2f(p0), hi2f(rr.x) + hi2f(p0)), pk2(lo2f(rr.y) + lo2f(p1), hi2f(rr.y) + hi2f(p1))};
            const float y0 = lo2f(o.x), y1 = hi2f(o.x), y2 = lo2f(o.y), y3 = hi2f(o.y);
            sq = (u32x2){__float_as_uint(xg_sum((y0 + y1) + (y2 + y3))), __float_as_uint(xg_sum(__builtin_fmaf(y0, y0, y1 * y1) + __builtin_fmaf(y2, y2, y3 * y3)))};
            AT(2);
            if (t >= OIM) { put(s); if (!layer) { VMDRAIN(); if (lane == 0) st16c(rfl, F_OF + (s * NR_ + r) * FLP, (u32x4){cP, 1u, 1u, tb}); } }
            if (lane == 0 && r == 0 && s >= 56) STAMP(300 + s - 56);
        }
        ATD(2, t == 42 && r == 0);
    }
    }
}
__device__ __forceinline__ void ro_fill(const MegaArgs& a, unsigned char* lds, const int r) {
    constexpr int wave = 3;
    const int lane = lane_id();
    const unsigned ldsb = LDSB();
    const rsrc_t rst = MKR(a.st, ST_BYTES);
#pragma unroll 1
    for (int layer = 0; layer < 2; ++layer) {
    unsigned char* hop = a.hop + layer * HOP_BYTES;
    const rsrc_t rfl = MKR(hop, F_ERR);
    unsigned tb = 0, cC1 = 0, cP = 0;
#pragma unroll 1
    for (int t = 0; t < RO_END; ++t) {
        ATV;
        if (t < NSEG_) {
            if (t == 0) wait_set(rfl, F_XP + (r % NXP_) * FLP, hop, 4);
            if (t > 1) rt_wait(tb + cC1 + TT_FP);
            wait_set(rfl, (r < 64 || (t == 0 && lane >= 32)) ? F_UX + (t * 32 + (lane & 31)) * FLP : F_PX + (t * 64 + (lane & 31) * 2 + ((r >> 3) & 1)) * FLP, hop, t > 2 ? 0 : 1);
        }
        LBAR();
        if (t < NSEG_) {
            if (t > 1) rt_wait(tb + cP + TT_BD);
            AT(0);
            dma16cn<32>(rst, lane * 16, ST_U + ((t & (URING - 1)) * 40 + (r >> 1)) * 32768, ldsb + (t & 3) * 32768);
            AT(1);
            VMDRAIN();
            AT(2);
            if (lane == 0 && r == 0 && t == 0) STAMP(13);
        }
        LBAR();
        if (t < NSEG_) { const unsigned tpv = *reinterpret_cast<const unsigned*>(lds + RO_TIM + lane * 4); tb = __builtin_amdgcn_readlane(tpv, 0); cC1 = __builtin_amdgcn_readlane(tpv, 1); cP = __builtin_amdgcn_readlane(tpv, 2); }
        ATD(2, t == 42 && r == 0);
    }
    if (lane == 0) STAMP(512 + blockIdx.x);
    }
}
__device__ __forceinline__ void ro_part(const MegaArgs& a, unsigned char* lds, const int r, const int wave) {
    if (wave == 0) { lds_u32* tim = (lds_u32*)(lds + RO_TIM + 256); tim[0] = 0; tim[1] = 0; tim[2] = 0; }
    if (wave == 0) { __builtin_amdgcn_s_setprio(3); ro_state(a, lds, r, page_pol(a)); }
    else if (wave == 1) ro_time(a, lds, r, page_pol(a));
    else if (wave == 2) ro_out(a, lds, r);
    else if (wave == 3) ro_fill(a, lds, r);
    else if (wave == 5 && r == 0) ro_master(a);
    else { for (int q = 0; q < 4 * RO_END; ++q) __builtin_amdgcn_s_barrier(); }
}

#define PIM 60
#define XNP 3
constexpr int XB = 40960;
struct XWt { u32x4 w[2][20]; f32x4 cs[2]; };
template <int NB, int NR, class G, class H> __device__ __forceinline__ void x_load(XWt& x, unsigned char* lds, const int wave, const int pw, G&& src, H&& more) {
    const unsigned ldsb = LDSB(), lane = lane_id(), l15 = lane & 15, lg = lane >> 4;
    load_b<NB, 4, NR, 0, 0>(ldsb, wave, src, [&](int rd, int base) { if (rd >= 10) more(rd, base); else if (pw >= 0) {
#pragma unroll
        for (int u = 0; u < 2; ++u)
#pragma unroll
            for (int k = 0; k < 2; ++k) x.w[u][(2 * rd + k) % 20] = ld_frag(lds + base + (pw >> 1) * 8192, 32 * k + 8 * lg, 4, (pw & 1) * 2 + u, l15); } }, [](int) {});
    if (pw >= 0) {
        const u32x4 one = (u32x4){0x3F803F80u, 0x3F803F80u, 0x3F803F80u, 0x3F803F80u};
        x.cs[0] = Z4; x.cs[1] = Z4;
#pragma unroll
        for (int k = 0; k < 20; ++k) { x.cs[0] = dmf(x.w[0][k], one, x.cs[0]); x.cs[1] = dmf(x.w[1][k], one, x.cs[1]); }
        PIN(x.cs[0]); PIN(x.cs[1]);
    }
}
__device__ __forceinline__ void c_load(u32x2 (&sv)[4], rsrc_t rx, int t) {
    const int lane = lane_id();
#pragma unroll
    for (int q = 0; q < 4; ++q) sv[q] = __builtin_amdgcn_raw_buffer_load_b64(rx, (lane >> 4) * 512 + q * 128 + (lane & 15) * 8, t * XI_PITCH + XI_S8, 16);
    CBAR();
}
__device__ __forceinline__ void c_put(const u32x2 (&sv)[4], unsigned char* dst) {
    const f32x2 s = (__builtin_bit_cast(f32x2, sv[0]) + __builtin_bit_cast(f32x2, sv[1])) + (__builtin_bit_cast(f32x2, sv[2]) + __builtin_bit_cast(f32x2, sv[3]));
    const auto a1 = __builtin_amdgcn_permlane16_swap(__float_as_uint(s[0]), __float_as_uint(s[0]), false, false), a2 = __builtin_amdgcn_permlane16_swap(__float_as_uint(s[1]), __float_as_uint(s[1]), false, false);
    float m, i; ln_consts(__uint_as_float(a1[0]) + __uint_as_float(a1[1]), __uint_as_float(a2[0]) + __uint_as_float(a2[1]), m, i);
    const int lane = lane_id();
    *reinterpret_cast<f32x2*>(dst + (lane >> 5) * 128 + (lane & 15) * 8) = (f32x2){-m, i};
}
template <class F> __device__ __forceinline__ void x_proj(const XWt& x, const unsigned char* xb, const unsigned char* cb, u32x4 (&o)[2], F&& mid) {
    const int lane = lane_id();
    unsigned l16 = lane * 16, c8 = (lane & 15) * 8; PIN(l16); PIN(c8);
    const unsigned char* xp = xb + l16;
    f32x4 acc[2][2] = {{Z4, Z4}, {Z4, Z4}};
    u32x4 xf[3][2]; f32x2 ct[2];
#pragma unroll
    for (int k = 0; k < 2; ++k) { xf[k][0] = lds16(xp + k * 1024); SBAR(); xf[k][1] = lds16(xp + 20480 + k * 1024); SBAR(); }
#pragma unroll
    for (int k = 0; k < 20; ++k) {
#pragma unroll
        for (int j = 0; j < 4; ++j) { acc[j >> 1][j & 1] = dmf(x.w[j >> 1][k], xf[k % 3][j & 1], acc[j >> 1][j & 1]); PIN(acc[j >> 1][j & 1]); SBAR();
            if ((j == 1 || j == 2) && k + 2 < 20) { xf[(k + 2) % 3][j - 1] = lds16(xp + (j - 1) * 20480 + (k + 2) * 1024); SBAR(); }
            if (j == 1 && k == 18) { ct[0] = *reinterpret_cast<const f32x2*>(cb + c8); ct[1] = *reinterpret_cast<const f32x2*>(cb + c8 + 128); SBAR(); } }
        mid(k, l16);
    }
#pragma unroll
    for (int rt = 0; rt < 2; ++rt)
#pragma unroll
        for (int q = 0; q < 4; ++q) { const int u = q >> 1, e = 2 * (q & 1);
            o[rt][q] = pk2(__builtin_fmaf(ct[rt][0], x.cs[u][e], acc[u][rt][e]) * ct[rt][1], __builtin_fmaf(ct[rt][0], x.cs[u][e + 1], acc[u][rt][e + 1]) * ct[rt][1]); }
}
__device__ __forceinline__ void xp_part(const MegaArgs& a, unsigned char* lds, const int j, const int wave) {
    const int lane = lane_id(), tid = wave * 64 + lane;
    const unsigned ldsb = LDSB();
    const int hp = j / 3, kind = j - 3 * hp;
    XWt x;
    {
        const rsrc_t rk = MKR(psel(a.wk, a.wv, kind >> 1), D_ * HE_ * 2), rv = MKR(a.wv, D_ * HE_ * 2);
        const unsigned row = 8 * wave + (lane >> 3), vo = row * (HE_ * 2) + ((lane & 7) ^ (wsw(row) << 1)) * 16 + hp * 768;
        x_load<4, 10>(x, lds, wave, wave, [&](int rd, int q, unsigned dst) { dma16(q < 3 ? rk : rv, vo, rd * (64 * HE_ * 2) + (kind < 2 ? kind * 384 + (q % 3) * 128 : (q >> 1) * 384 + 128 + (q & 1) * 128), dst); }, [](int, int) {});
    }
    const unsigned oo = kind < 2 ? (wave < 6 ? ST_KX + wave * 1024 : ST_VX + (wave - 6) * 1024) + (2 * hp + kind) * 12288 : ST_VX + (2 * hp + (wave >> 2)) * 12288 + (2 + (wave & 3)) * 1024;
    const rsrc_t rst = MKR(a.st, ST_BYTES), rx = MKR(a.xi, XI_BYTES);
    const int wr_ = (wave + (blockIdx.x >> 3) * ROT) & 7;
#pragma unroll 1
    for (int layer = 0; layer < 2; ++layer) {
    unsigned char* hop = a.hop + layer * HOP_BYTES;
    const rsrc_t rfl = MKR(hop, F_ERR);
    auto piece = [&](int t, int b, int k, unsigned l16) { const unsigned o = (wr_ * 5 + k) * 1024; dma16c(rx, l16, (unsigned)t * XI_PITCH + XI_X + o, ldsb + b * XB + o); };
    auto x_dma = [&](int t, int b) {
#pragma unroll
        for (int k = 0; k < 5; ++k) piece(t, b, k, lane * 16); };
    if (tid == 0 && j == 0) STAMP(7);
    wait_set(rfl, F_AF + (lane & 15) * FLP, hop, 1);
    if (tid == 0 && j == 0) STAMP(8);
    if (wave == 1) { u32x2 sv[4]; c_load(sv, rx, 0); x_dma(0, 0); c_put(sv, lds + 3 * XB); } else x_dma(0, 0);
    wait_set(rfl, F_AF + (16 + (lane & 15)) * FLP, hop, 0);
    VMKNOWN();
    x_dma(1, 1);
    unsigned tnext = rt_now(); int b = 0;
#pragma unroll 1
    for (int t = 0; t < NSEG_; ++t) {
        ATV;
        LBAR();
        AT(0);
        if (tid == 0 && t >= KIM + 2) st4c(rfl, F_XP + ((t - 2) * NXP_ + j) * FLP, 1u);
        u32x4 fl;
        const unsigned fo = (lane < 16 && t >= XLD) ? F_KM + ((t - XLD) * 32 + 2 * hp) * FLP : F_AF + (min(t + 3, NSEG_ - 1) * 16 + (lane & 15)) * FLP;
        if (wave == 2 && t > 0) asm volatile("buffer_load_dwordx4 %0, %1, %2, 0 offen sc1" : "=&v"(fl) : "v"(fo), "s"(rfl) : "memory");
        u32x2 sv[4];
        if (wave == 1) c_load(sv, rx, min(t + 1, NSEG_ - 1));
        const int bn = b == 0 ? 2 : b - 1;
        const bool go = t > 1 && t + 2 < NSEG_;
        u32x4 o[2];
        x_proj(x, lds + b * XB, lds + 3 * XB + (t & 1) * 256, o, [&](int k, unsigned l16) { if ((k & 3) == 1 && go) { piece(t + 2, bn, k >> 2, l16); SBAR(); } });
        const int ln = lane_id();
        st16s(o[0], rst, ln * 16, oo + (t & (KXR - 1)) * (32 * 12288)); st16s(o[1], rst, ln * 16, oo + (t & (KXR - 1)) * (32 * 12288) + 6144);
        AT(1);
        if (wave == 1) c_put(sv, lds + 3 * XB + ((t + 1) & 1) * 256);
        if (t < KIM || t + 2 >= NSEG_) { VMDRAIN(); LBAR();
            if (tid == 0) { st4c(rfl, F_XP + (t * NXP_ + j) * FLP, 1u); if (t + 2 == NSEG_) st4c(rfl, F_XP + ((t - 1) * NXP_ + j) * FLP, 1u); } if (tid == 0 && j == 0 && t < 3) STAMP(1 + t);
            if (t < 2) { wait_set(rfl, F_AF + ((t + 2) * 16 + (ln & 15)) * FLP, hop, 0); VMKNOWN(); x_dma(t + 2, bn); }
        } else VMW(7);
        if (wave == 2 && t > 0) {
            asm volatile("" : "+v"(fl));
            if (__all(fl.x != 0u)) tnext = __builtin_amdgcn_readfirstlane(fl.w) + XNP * __builtin_amdgcn_readfirstlane(fl.x) + TT_KD; else wait_timed(rfl, fo, hop, tnext, XNP, TT_KD, t < XLD);
            if (t >= XLD && (int)(tnext - rt_now()) < 4500) rt_wait(tnext);
        }
        AT(2);
        ATD(3, t == 48 && j == 0);
        b = b == 2 ? 0 : b + 1;
    }
    LBAR();
    if (tid == 0) STAMP(512 + blockIdx.x);
    }
}

template <int AT_> __device__ __forceinline__ void xq_run(const MegaArgs& a, unsigned char* lds, const int h, const int wave) {
    constexpr bool ATT = AT_ >= 0; constexpr int qt = ATT ? AT_ : 0;
    const int lane = lane_id(), tid = wave * 64 + lane;
    const unsigned ldsb = LDSB();
    constexpr int L_C = 2 * XB, L_Q = L_C + 512, L_VX = L_Q + (QA + 1) * 12288;
    rt_wait(rt_now() + XQD);
    XWt x; u32x4 wo[4][6];
    {
        const rsrc_t rq = MKR(a.wq, D_ * HE_ * 2), ro = MKR(a.wo, HE_ * D_ * 2);
        const unsigned row = 8 * wave + (lane >> 3), ch = ((lane & 7) ^ (wsw(row) << 1)) * 16, vo = row * (HE_ * 2) + ch + h * 384, oo = (h * 192 + row) * (D_ * 2) + ch;
        x_load<3, 12>(x, lds, wave, ATT ? -1 : wave - 2, [&](int rd, int q, unsigned dst) { if (rd < 10) dma16(rq, vo, rd * (64 * HE_ * 2) + q * 128, dst); else dma16(ro, oo, q * (64 * D_ * 2) + (rd - 2) * 128, dst); },
            [&](int rd, int base) { if (rd == 10 + AT_) {
#pragma unroll
                for (int q = 0; q < 24; ++q) wo[q / 6][q % 6] = ld_frag(lds + base + ((q % 6) >> 1) * 8192, (q & 1) * 32 + 4 * (lane >> 4), 16, q / 6, lane & 15); } });
    }
    const rsrc_t rst = MKR(a.st, ST_BYTES), rx = MKR(a.xi, XI_BYTES);
    const int wr_ = (wave - 2 + (blockIdx.x >> 3)) % 6, xj = xp_of(h), xj2 = 3 * (h >> 1) + 2;
    const int l15 = lane & 15, lg = lane >> 4;
    const bool cm[4] = {4 * lg <= l15, 4 * lg + 1 <= l15, 4 * lg + 2 <= l15, 4 * lg + 3 <= l15};
#pragma unroll 1
    for (int layer = 0; layer < 2; ++layer) {
    unsigned char* hop = a.hop + layer * HOP_BYTES;
    const rsrc_t rfl = MKR(hop, F_ERR);
    auto piece = [&](int t, int k, unsigned l16) { const unsigned o = min(wr_ * 7 + k, 39) * 1024; dma16c(rx, l16, (unsigned)t * XI_PITCH + XI_X + o, ldsb + (t & 1) * XB + o); };
    if (wave == 0) wait_set(rfl, F_AF + (lane & 31) * FLP, hop, 2);
    LBAR();
    if (!ATT) {
#pragma unroll
        for (int k = 0; k < 7; ++k) piece(0, k, lane * 16); }
    u32x4 kf[2][6];
    if (AT_ == 0) { u32x2 sv[4]; c_load(sv, rx, 0); c_put(sv, lds + L_C); }
    VMKNOWN();
    unsigned tnext = rt_now(); int qs = 0;
#pragma unroll 1
    for (int i = 0; i < NSEG_ + QA; ++i) {
        ATV;
        if (i <= NSEG_) LBAR();
        AT(0);
        if (!ATT) {
            if (i < NSEG_) {
                u32x4 o[2];
                x_proj(x, lds + (i & 1) * XB, lds + L_C + (i & 1) * 256, o, [&](int k, unsigned l16) { if ((k & 1) && k < 14 && i + 1 < NSEG_) { piece(i + 1, k >> 1, l16); SBAR(); } });
                unsigned char* qp = lds + L_Q + qs * 12288 + (wave - 2) * 1024 + lane_id() * 16;
                *reinterpret_cast<u32x4*>(qp) = o[0]; *reinterpret_cast<u32x4*>(qp + 6144) = o[1];
                AT(1);
                VMDRAIN();
            }
        } else {
            const int t = i - QA;
            const unsigned fa = F_AF + (min(i + 2, NSEG_ - 1) * 16 + l15) * FLP, fx = F_XP + ((lane & 1) ? xj : xj2) * FLP, ub = ST_U + (((i & (URING - 1)) * 40 + 32 + 4 * qt) * 32 + h) * 1024;
            unsigned l16 = lane_id() * 16; PIN(l16);
            auto v_dma = [&]() { dma16cn<6>(rst, l16, ST_VX + (((i + 1) & (KXR - 1)) * 32 + h) * 12288 + qt * 6144, ldsb + L_VX + ((i + 1) & 1) * 12288 + qt * 6144); };
            auto next = [&]() {
                const unsigned kb = ST_KX + (((t + 1) & (KXR - 1)) * 32 + h) * 12288;
#pragma unroll
                for (int m = 0; m < (qt ? 12 : 6); ++m) asm volatile("buffer_load_dwordx4 %0, %1, %2, %3 offen offset:%4 sc1" : "=&v"(kf[m / 6][m % 6]) : "v"(l16), "s"(rst), "s"(kb + (m >> 2) * 4096), "i"((m & 3) * 1024) : "memory"); };
            auto got = [&]() {
#pragma unroll
                for (int m = 0; m < (qt ? 12 : 6); ++m) PIN(kf[m / 6][m % 6]); };
            u32x4 vf[12]; u32x2 sv[4];
            auto u_two = [&](int u0) {
                f32x4 acc[2][2] = {{Z4, Z4}, {Z4, Z4}};
#pragma unroll
                for (int j = 0; j < 24; ++j) { const int k = j >> 2, u = (j >> 1) & 1, r = j & 1; acc[u][r] = dmf(vf[r * 6 + k], wo[u0 + u][k], acc[u][r]); }
                SBAR();
#pragma unroll
                for (int u = 0; u < 2; ++u) st16s((u32x4){pk2(acc[u][0][0], acc[u][1][0]), pk2(acc[u][0][2], acc[u][1][2]), pk2(acc[u][0][1], acc[u][1][1]), pk2(acc[u][0][3], acc[u][1][3])}, rst, l16, ub + (u0 + u) * 32768);
                SBAR(); };
            if (t < 0) {
                if (!qt) { c_load(sv, rx, i + 1); c_put(sv, lds + L_C + ((i + 1) & 1) * 256); }
                wait_set(rfl, fx + i * (NXP_ * FLP), hop, 1);
#pragma unroll
                for (int m = 0; m < 12; ++m) vf[m] = __builtin_amdgcn_raw_buffer_load_b128(rst, l16 + (m & 3) * 1024, ST_VX + (i * 32 + h) * 12288 + (m >> 2) * 4096, 16);
                SBAR();
                u_two(0); u_two(2);
                VMDRAIN();
                if (l16 == 0) st4c(rfl, F_PX + (i * 64 + h * 2 + qt) * FLP, 1u);
                wait_set(rfl, (lane < 16 && t == -1) ? fx + (i + 1) * (NXP_ * FLP) : fa, hop, 1); v_dma(); next(); VMDRAIN(); got();
            } else {
                const unsigned char* qp = lds + L_Q + (qs == QA ? 0 : qs + 1) * 12288 + qt * 6144 + l16;
                u32x4 qf[6], km[6], fl;
                const bool uu = i < NSEG_;
#pragma unroll
                for (int m = 0; m < 12; ++m) vf[m] = lds16(lds + L_VX + (i & 1) * 12288 + l16 + m * 1024);
                SBAR();
#pragma unroll
                for (int q = 0; q < (qt ? 0 : 4); ++q) asm volatile("buffer_load_dwordx2 %0, %1, %2, %3 offen offset:%4 sc1" : "=&v"(sv[q]) : "v"((l16 >> 8) * 512 + (l16 & 255) / 2), "s"(rx), "s"(min(i + 1, NSEG_ - 1) * XI_PITCH + XI_S8), "i"(q * 128) : "memory");
                if (uu && t > 0) { v_dma(); u_two(0); VMW(8); } else { if (uu) u_two(0); VMDRAIN(); }
                got();
#pragma unroll
                for (int q = 0; q < (qt ? 0 : 4); ++q) PIN(sv[q]);
                if (l16 == 0 && t <= PIM) st4c(rfl, F_PX + ((i - 1) * 64 + h * 2 + qt) * FLP, 1u);
                AT(1);
                const unsigned fo = lane < 16 ? F_KM + (t * 32 + h) * FLP : (lane < 32 ? fx + min(i + 2, NSEG_ - 1) * (NXP_ * FLP) : fa);
                const int dl = t + 2 == NSEG_ ? TT_QN : TT_QD;
#pragma unroll
                for (int m = 0; m < 6; ++m) qf[m] = lds16(qp + m * 1024);
                rt_wait(tnext);
                AT(5);
                asm volatile("buffer_load_dwordx4 %0, %1, %2, 0 offen sc1" : "=&v"(fl) : "v"(fo), "s"(rfl) : "memory");
                f32x4 sc[3] = {Z4, Z4, Z4};
#pragma unroll
                for (int m = 0; m < 6; ++m) { sc[0] = dmf(kf[0][m], qf[m], sc[0]); if (qt) sc[1] = dmf(kf[1][m], qf[m], sc[1]); }
                float s2[10]; float mx = -3.0e38f, scl = 0.07216878364870322f;
                auto cvt = [&](int q) {
                    const unsigned r0 = pk2(sc[q >> 1][2 * (q & 1)], sc[q >> 1][2 * (q & 1) + 1]), r1 = pk2(muls(lo2f(r0), scl), muls(hi2f(r0), scl));
                    s2[2 * q] = (q >> 1) != qt || cm[2 * (q & 1)] ? lo2f(r1) : -3.0e38f; s2[2 * q + 1] = (q >> 1) != qt || cm[2 * (q & 1) + 1] ? hi2f(r1) : -3.0e38f;
                    mx = fmaxf(mx, fmaxf(s2[2 * q], s2[2 * q + 1])); PIN(s2[2 * q]); PIN(s2[2 * q + 1]); };
#pragma unroll
                for (int q = 0; q < 4; ++q) { if (qt == 0 && q >= 2) { s2[2 * q] = 0.f; s2[2 * q + 1] = 0.f; } else cvt(q); }
                PIN(mx);
                if (!qt && uu) c_put(sv, lds + L_C + ((i + 1) & 1) * 256);
                asm volatile("s_waitcnt vmcnt(0)" : "+v"(fl) :: "memory");
                if (__all(fl.x != 0u)) tnext = __builtin_amdgcn_readfirstlane(fl.w) + 2u * __builtin_amdgcn_readfirstlane(fl.x) + dl; else wait_timed(rfl, fo, hop, tnext, 2, dl, t + 1 == NSEG_ || t < 2);
                AT(2);
                if (l16 == 0 && h == 0 && qt == 0 && t >= 56) STAMP(360 + t - 56);
                const unsigned mo = (lg * 8 + (((l15 >> 2) * 2 + (l15 & 1) + (lg & 1) * 4) & 7)) * 16;
                if (t == 0) v_dma();
#pragma unroll
                for (int m = 0; m < 6; ++m) asm volatile("buffer_load_dwordx4 %0, %1, %2, %3 offen offset:%4 sc1" : "=&v"(km[m]) : "v"(mo), "s"(rst), "s"(ST_KM + ((t & (PR - 1)) * 32 + h) * 3072), "i"(m * 512) : "memory");
                if (uu) { u_two(2); VMW(2); } else VMDRAIN();
                asm volatile("" : "+v"(km[0]), "+v"(km[1]), "+v"(km[2]), "+v"(km[3]), "+v"(km[4]), "+v"(km[5]));
#pragma unroll
                for (int m = 0; m < 6; ++m) sc[2] = dmf(km[m], qf[m], sc[2]);
                cvt(4);
                mx = xg_max(mx) * -1.4426950408889634f;
                float sum = 0.f, su1 = 0.f;
#pragma unroll
                for (int q = 0; q < 10; ++q) { if (qt == 0 && q >= 4 && q < 8) continue; s2[q] = __builtin_amdgcn_exp2f(__builtin_fmaf(s2[q], 1.4426950408889634f, mx)); if (q & 1) su1 += s2[q]; else sum += s2[q]; }
                float inv = __builtin_amdgcn_rcpf(xg_sum(sum + su1));
                asm volatile("s_nop 0" : "+v"(inv));
#pragma unroll
                for (int q = 0; q < 10; ++q) { if (qt == 0 && q >= 4 && q < 8) continue; s2[q] = muls(s2[q], inv); }
                const unsigned pb = ST_PX + ((t & (PR - 1)) * 2 + qt) * 40960;
                st16s((u32x4){pk2(s2[0], s2[4]), pk2(s2[2], s2[6]), pk2(s2[1], s2[5]), pk2(s2[3], s2[7])}, rst, l16, pb + h * 1024);
                __builtin_amdgcn_raw_buffer_store_b32(pk2(s2[8], s2[9]), rst, l15 * 16 + (lg & 1) * 8 + (lg >> 1) * 4, pb + 32768 + h * 256, 16);
                AT(3);
                if (t >= PIM) { VMDRAIN(); if (l16 == 0) st4c(rfl, F_PX + (i * 64 + h * 2 + qt) * FLP, 1u); }
                next();
                AT(4);
                if (l16 == 0 && h == 0 && qt == 1 && t >= 56) STAMP(340 + t - 56);
            }
        }
        ATD(4, i == 44 && h == 0);
        qs = qs == QA ? 0 : qs + 1;
    }
    VMDRAIN(); LBAR();
    if (tid == 0) STAMP(512 + blockIdx.x);
    }
}
__device__ __forceinline__ void xq_part(const MegaArgs& a, unsigned char* lds, const int h, const int wave) {
    if (wave < 2) __builtin_amdgcn_s_setprio(1);
    if (wave == 0) xq_run<0>(a, lds, h, wave); else if (wave == 1) xq_run<1>(a, lds, h, wave); else xq_run<-1>(a, lds, h, wave);
}

#define AIM 12
__device__ __forceinline__ void a_part(const MegaArgs& a, unsigned char* lds, const int ai, const int wave) {
    const int lane = lane_id(), tid = wave * 64 + lane;
    const unsigned ldsb = LDSB();
    constexpr int PITCH = 1296, L_ST = 16 * PITCH + 64, L_Y = L_ST + 704, YB = 51200, L_SK = L_Y + 2 * YB, KP = 48;
    const int cb = ai & 7, rep = ai >> 3, h = ai;
    const int nt = wave < 5 ? wave : 0;
    u32x4 b[20], wq[20];
    {
        const f32x4* p = reinterpret_cast<const f32x4*>(a.kvp + ((long)h * 32 + 4 * wave) * 420);
        f32x4 v[4][2];
#pragma unroll
        for (int q = 0; q < 4; ++q) { v[q][0] = p[q * 105 + lane]; v[q][1] = p[q * 105 + 64 + (lane < 41 ? lane : 0)]; }
        f32x4* red = reinterpret_cast<f32x4*>(lds + L_Y + wave * 2048);
        red[lane] = (v[0][0] + v[1][0]) + (v[2][0] + v[3][0]); red[64 + lane] = (v[0][1] + v[1][1]) + (v[2][1] + v[3][1]);
        LBAR();
        if (wave >= 4) {
            const int i = (tid - 256) * 2, e = i / 24, d = i - e * 24; const bool live = d < 20 && e <= 20;
            const float* rp = reinterpret_cast<const float*>(lds + L_Y) + (live ? (e < 20 ? d * 20 + e : 400 + d) : 0);
            const int st = e < 20 ? 20 : 1;
            float s0 = 0.f, s1 = 0.f;
#pragma unroll
            for (int q = 0; q < 8; ++q) { s0 += rp[q * 512]; s1 += rp[q * 512 + st]; }
            st4c(MKR(a.kvt, 65536), h * 1024 + i * 2, live ? min(pk2(s0, s1), 0xfffffffeu) : 0u);
        }
        CBAR();
        if (wave < 5) {
            const bf16_t* wb = a.wfo + ((long)(5 * cb + nt) * 20 * 64 + lane) * 8;
#pragma unroll
            for (int q = 0; q < 20; ++q) b[q] = *reinterpret_cast<const u32x4*>(wb + q * 512);
        }
    }
#pragma unroll 1
    for (int layer = 0; layer < 2; ++layer) {
        int lane = lane_id();
        int l15 = lane & 15, lg = lane >> 4;
        unsigned char* hop = a.hop + layer * HOP_BYTES;
        const rsrc_t rfl = MKR(hop, F_ERR), rp = MKR(a.pq, S_ * D_ * 2 + 16), rr_ = MKR(psel(a.x0, a.xi, layer), layer ? XI_BYTES : S_ * D_ * 2), rxa = MKR(a.xi, XI_BYTES);
        u32x4 tq[4]; u32x2 rres;
        auto fetch = [&](int u) {
#pragma unroll
            for (int q = 0; q < 4; ++q) tq[q] = __builtin_amdgcn_raw_buffer_load_b128(rp, (lg < 2 ? l15 * 32 + lg * 16 : 512 + l15 * 8) + q * 640, u * 20480 + wave * 2560, 16); };
        const int ct = 5 * cb + nt;
        const unsigned oo = XI_X + (ct >> 1) * 1024 + ((ct & 1) * 2 + (lg >> 1)) * 256 + l15 * 16 + (lg & 1) * 8, ro = layer ? oo : (unsigned)(l15 * D_ + 16 * ct + 4 * lg) * 2;
        auto res = [&](int u) { rres = __builtin_amdgcn_raw_buffer_load_b64(rr_, ro, (u >> 1) * (layer ? XI_PITCH : 40960) + (u & 1) * 20480, 16); };
        fetch(rep); res(rep);
        u32x4 k0[4], k1[4], kz[4];
        {
            const rsrc_t rkv = MKR(a.kvt + layer * 16384, 32768);
            const unsigned ko = (lg < 3 ? lg : 0) * 16;
            for (unsigned sp = 0; sp < SPIN_CAP_W; ++sp) { CBAR();
                unsigned mx = 0u;
#pragma unroll
                for (int q = 0; q < 4; ++q) { k0[q] = __builtin_amdgcn_raw_buffer_load_b128(rkv, ko + l15 * 48 + q * 1024, wave * 4096, 16);
                    k1[q] = __builtin_amdgcn_raw_buffer_load_b128(rkv, ko + (16 + (l15 & 3)) * 48 + q * 1024, wave * 4096, 16); kz[q] = __builtin_amdgcn_raw_buffer_load_b128(rkv, ko + 960 + q * 1024, wave * 4096, 16); }
#pragma unroll
                for (int q = 0; q < 4; ++q) mx = max(mx, max(umax4(k0[q]), max(umax4(k1[q]), umax4(kz[q]))));
                if (__all(mx != 0xffffffffu)) break;
                __builtin_amdgcn_s_sleep(2);
            }
            if (tid == 0 && ai == 0) STAMP(4);
        }
#pragma unroll 1
        for (int u = rep; u < 2 * NSEG_; u += 4) {
            if (layer && u >= 8 && u < 12) wait_set(rfl, F_AQ + (lane & 31) * 4, hop, 1);
            {
                unsigned char* sr = lds + l15 * PITCH + wave * 160;
                f32x4 dz[4], d0[4], d1[4];
#pragma unroll
                for (int q = 0; q < 4; ++q) {
                    u32x4 f; f.x = lg < 3 ? tq[q].x : 0u; f.y = lg < 3 ? tq[q].y : 0u; f.z = lg < 2 ? tq[q].z : 0u; f.w = lg < 2 ? tq[q].w : 0u;
                    dz[q] = dmf(kz[q], f, Z4); d0[q] = dmf(k0[q], f, Z4); d1[q] = dmf(k1[q], f, Z4);
                }
                SBAR(); fetch(min(u + 4, 2 * NSEG_ - 1)); SBAR();
                float inv[4];
#pragma unroll
                for (int q = 0; q < 4; ++q) {
                    inv[q] = __builtin_amdgcn_rcpf(rbf(rbf(dz[q][0]) + 1e-6f));
                    const unsigned n0 = pk2(d0[q][0], d0[q][1]), n1 = pk2(d0[q][2], d0[q][3]);
                    *reinterpret_cast<u32x2*>(sr + q * 40 + 8 * lg) = (u32x2){pk2(lo2f(n0) * inv[q], hi2f(n0) * inv[q]), pk2(lo2f(n1) * inv[q], hi2f(n1) * inv[q])};
                }
                const f32x4 dd = lg == 0 ? d1[0] : (lg == 1 ? d1[1] : (lg == 2 ? d1[2] : d1[3]));
                const float iv = lg == 0 ? inv[0] : (lg == 1 ? inv[1] : (lg == 2 ? inv[2] : inv[3]));
                const unsigned n0 = pk2(dd[0], dd[1]), n1 = pk2(dd[2], dd[3]);
                *reinterpret_cast<u32x2*>(sr + lg * 40 + 32) = (u32x2){pk2(lo2f(n0) * iv, hi2f(n0) * iv), pk2(lo2f(n1) * iv, hi2f(n1) * iv)};
            }
            LBAR();
            if (layer && u == rep && tid == 448) st4c(rfl, F_AQ + h * 4, 1u);
            if (wave < 5) {
                f32x4 acc[2] = {Z4, Z4};
                const unsigned char* ar = lds + l15 * PITCH + 16 * lg;
                u32x4 af[8];
#pragma unroll
                for (int q = 0; q < 8; ++q) af[q] = lds16(ar + 64 * q);
                SBAR();
#pragma unroll
                for (int q = 0; q < 20; ++q) { acc[q & 1] = dmf(b[q], af[q & 7], acc[q & 1]); PIN(acc[q & 1]); SBAR();
                    if (q + 8 < 20) { af[q & 7] = lds16(ar + 64 * (q + 8)); SBAR(); } }
                const f32x4 v = acc[0] + acc[1]; const unsigned p0 = pk2(v[0], v[1]), p1 = pk2(v[2], v[3]);
                const u32x2 o = (u32x2){pk2(lo2f(rres.x) + lo2f(p0), hi2f(rres.x) + hi2f(p0)), pk2(lo2f(rres.y) + lo2f(p1), hi2f(rres.y) + hi2f(p1))};
                __builtin_amdgcn_raw_buffer_store_b64(o, rxa, oo, (u >> 1) * XI_PITCH + (u & 1) * 20480, 16);
                const float y0 = lo2f(o.x), y1 = hi2f(o.x), y2 = lo2f(o.y), y3 = hi2f(o.y);
                const float s1 = xg_sum((y0 + y1) + (y2 + y3)), s2 = xg_sum(__builtin_fmaf(y0, y0, y1 * y1) + __builtin_fmaf(y2, y2, y3 * y3));
                if (lane < 16) *reinterpret_cast<f32x2*>(lds + L_ST + wave * 128 + lane * 8) = (f32x2){s1, s2};
            }
            LBAR();
            if (wave == 0) {
                if (lane < 16) { const f32x2* sp = reinterpret_cast<const f32x2*>(lds + L_ST + lane * 8); const f32x2 v = ((sp[0] + sp[16]) + (sp[32] + sp[48])) + sp[64];
                    __builtin_amdgcn_raw_buffer_store_b64(__builtin_bit_cast(u32x2, v), rxa, XI_S8 + cb * 128 + lane * 8, (u >> 1) * XI_PITCH + (u & 1) * 1024, 16); }
                if (lane == 0 && u >= AIM + 4) st4c(rfl, F_AF + ((u - 4) * 8 + cb) * FLP, 1u); }
            if (u < AIM || u + 4 >= 2 * NSEG_) { VMKNOWN(); LBAR(); if (tid == 0) st4c(rfl, F_AF + (u * 8 + cb) * FLP, 1u); }
            if (tid == 0 && ai == 0 && u == 0) STAMP(5);
            if (tid == 0 && ai == 0 && u == 124) STAMP(6);
            CBAR(); res(min(u + 4, 2 * NSEG_ - 1));
        }
        if (layer) break;
        const int T = wave & 3, rt = wave >> 2;
        PIN(lane); l15 = lane & 15; lg = lane >> 4;
        {
            const int c = 20 * h + (T == 1 ? min(l15 >> 2, 2) * 640 + 16 + (l15 & 3) : (T == 0 ? 0 : (T - 1) * 640) + l15);
            const rsrc_t rw = MKR(a.wfq, 640 * 1920 * 2);
#pragma unroll
            for (int q = 0; q < 20; ++q) wq[q] = __builtin_amdgcn_raw_buffer_load_b128(rw, ((c >> 4) * 20 * 64 + lg * 16 + (c & 15)) * 16, q * 1024, 0);
        }
        f32x4 d4[2][2] = {{Z4, Z4}, {Z4, Z4}}, cs = Z4;
#pragma unroll
        for (int q = 0; q < 20; ++q) cs = dmf(wq[q], (u32x4){0x3F803F80u, 0x3F803F80u, 0x3F803F80u, 0x3F803F80u}, cs);
        unsigned tnext = rt_now();
        VMKNOWN();
        const unsigned fo = F_OF + ((wave & 1) * 40 + (lane < 40 ? lane : 0)) * FLP;
        auto y_wait = [&](int t) { if (wave >= 6) wait_timed(rfl, fo + t * (NR_ * FLP), hop, tnext, 1, TT_AD, false); };
        const int wr_ = ((wave + (blockIdx.x >> 3) * ROT) & 7);
        auto y_dma = [&](int t) { const unsigned o = wr_ * 6144; dma16cn<6>(rxa, lane * 16, (unsigned)t * XI_PITCH + o, ldsb + L_Y + (t & 1) * YB + o);
            if (wave == 2 || wave == 3) dma16c(rxa, lane * 16, (unsigned)t * XI_PITCH + (46 + wave) * 1024, ldsb + L_Y + (t & 1) * YB + (46 + wave) * 1024); };
        y_wait(0); LBAR(); y_dma(0); y_wait(1); VMDRAIN(); LBAR();
        auto comp = [&](const int t, auto&& mid) {
            if (tid == 128 && ai == 0 && t >= 56) STAMP(330 + t - 56);
            f32x4 acc = Z4, ac1 = Z4;
            const unsigned char* yp = lds + L_Y + (t & 1) * YB + XI_X + rt * 20480 + lane * 16;
            {
                const f32x2* sp = reinterpret_cast<const f32x2*>(lds + L_Y + (t & 1) * YB + rt * 5120 + lane * 8);
                f32x2 sv[10]; u32x4 yf[6]; float x1, x2;
#pragma unroll
                for (int q = 0; q < 4; ++q) yf[q] = lds16(yp + q * 1024);
                SBAR();
#pragma unroll
                for (int q = 0; q < 10; ++q) sv[q] = sp[q * 64];
                SBAR();
#pragma unroll
                for (int q = 0; q < 20; ++q) { if (q & 1) { ac1 = dmf(wq[q], yf[q % 6], ac1); PIN(ac1); } else { acc = dmf(wq[q], yf[q % 6], acc); PIN(acc); } SBAR(); if (q + 4 < 20) { yf[(q + 4) % 6] = lds16(yp + (q + 4) * 1024); SBAR(); }
                    if (q == 2) { x1 = sv[0][0]; x2 = sv[0][1]; }
                    if (q >= 2 && q < 11) { x1 += sv[q - 1][0]; PIN(x1); x2 += sv[q - 1][1]; PIN(x2); }
                    if (q == 11) { const auto e = __builtin_amdgcn_permlane16_swap(__float_as_uint(x1), __float_as_uint(x2), false, false); x1 = __uint_as_float(e[0]) + __uint_as_float(e[1]); PIN(x1); }
                    if (q == 12) { const auto e = __builtin_amdgcn_permlane32_swap(__float_as_uint(x1), __float_as_uint(x1), false, false); x1 = (__uint_as_float(e[0]) + __uint_as_float(e[1])) * (1.0f / D_); PIN(x1); }
                    if (q == 13) { const auto e = __builtin_amdgcn_permlane16_swap(__float_as_uint(x1), __float_as_uint(x1), false, false); x1 = __uint_as_float(e[0]); x2 = fmaxf(__builtin_fmaf(-x1, x1, __uint_as_float(e[1])), 0.f); PIN(x1); PIN(x2); }
                    if (q == 14) { x2 = rbf(x2) + 1e-5f; PIN(x2); } if (q == 15) { x2 = __builtin_amdgcn_sqrtf(rbf(x2)); PIN(x2); } if (q == 16) { x2 = __builtin_amdgcn_rcpf(rbf(x2)); PIN(x2); } if (q == 17) { x1 = rbf(x1); PIN(x1); }
                    SBAR(); }
                acc = (acc + ac1 - x1 * cs) * x2;
            }
            const bool isv = T == 3 || (T == 1 && lg >= 2), isq = T == 0 || (T == 1 && lg == 0);
            const u32x2 o = isv ? (u32x2){pk2(acc[0], acc[1]), pk2(acc[2], acc[3])} : (u32x2){phi2(acc[0], acc[1]), phi2(acc[2], acc[3])};
            const int row = rt * 16 + l15, cc = T == 1 ? 16 : 4 * lg;
            if (isq) __builtin_amdgcn_raw_buffer_store_b64(o, rp, T ? 512 + l15 * 8 : l15 * 32 + lg * 8, (t * 2 + rt) * 20480 + h * 640, 16);
            else *reinterpret_cast<u32x2*>(lds + L_SK + (t & 1) * 64 * KP + (isv ? 32 * KP : 0) + row * KP + cc * 2) = o;
            mid();
            LBAR();
            if (wave == 0) {
                u32x4 A[2], B[2];
#pragma unroll
                for (int q = 0; q < 2; ++q) { const unsigned char* p = lds + L_SK + (t & 1) * 64 * KP + (8 * lg + (l15 >> 2)) * KP + 32 * q + 8 * (l15 & 3);
                    const u32x2 a0 = ldstr(p), a1 = ldstr(p + 4 * KP), b0 = ldstr(p + 32 * KP), b1 = ldstr(p + 36 * KP);
                    A[q] = (u32x4){a0.x, a0.y, a1.x, a1.y}; B[q] = (u32x4){b0.x, b0.y, b1.x, b1.y}; }
                if (l15 >= 4) { A[1] = (u32x4){0u, 0u, 0u, 0u}; B[1] = (u32x4){0x3F803F80u, 0x3F803F80u, 0x3F803F80u, 0x3F803F80u}; }
#pragma unroll
                for (int x = 0; x < 2; ++x)
#pragma unroll
                    for (int y = 0; y < 2; ++y) d4[x][y] = dmf(A[x], B[y], d4[x][y]);
            }
        };
#pragma unroll 1
        for (int t = 0; t < TF; ++t) {
            u32x4 fl; bool early = false;
            if (wave >= 6 && (int)(tnext - rt_now()) < 0) { early = true; asm volatile("s_nop 4\n\tbuffer_load_dwordx4 %0, %1, %2, %3 offen sc1" : "=&v"(fl) : "v"(fo), "s"(rfl), "s"(min(t + 2, TF - 1) * (NR_ * FLP)) : "memory"); }
            if (t + 1 < TF) y_dma(t + 1);
            comp(t, [&]() { if (T < 2) VMW(1); else VMDRAIN();
                if (early) { asm volatile("" : "+v"(fl)); early = __all(fl.x != 0u); if (early) tnext = __builtin_amdgcn_readfirstlane(fl.w) + __builtin_amdgcn_readfirstlane(fl.x) + TT_AD; }
                if (t + 2 < TF && !early) y_wait(t + 2); });
            if (t == 8) { VMDRAIN(); LBAR(); if (tid == 0) st4c(rfl, F_AQ + h * 4, 1u); }
            if (t == 40) { PIN(lane); wait_set(rfl, F_AQ + (lane & 31) * 4, hop, 4); }
        }
        {
            lds_u32* rdy = (lds_u32*)(lds + 16 * PITCH);
            int nx = TF;
#pragma unroll 1
            for (int t = TF; t < NSEG_; ++t) {
                PIN(lane);
                if (nx == t) { if (wave >= 6) wait_set(rfl, fo + t * (NR_ * FLP), hop, 0); LBAR(); y_dma(t); nx = t + 1; }
                if (wave >= 6) rdy[(t & 1) * 2 + (wave & 1)] = __all(ld4c(rfl, fo + min(nx, NSEG_ - 1) * (NR_ * FLP)) != 0u) ? 1u : 0u;
                VMDRAIN(); LBAR();
                if (nx < NSEG_ && __builtin_amdgcn_readfirstlane(rdy[(t & 1) * 2] & rdy[(t & 1) * 2 + 1])) { y_dma(nx); ++nx; }
                comp(t, []() {});
            }
        }
        PIN(lane); l15 = lane & 15; lg = lane >> 4;
        if (wave == 0) {
            const rsrc_t rkv = MKR(a.kvt + 16384, 32768);
#pragma unroll
            for (int y = 0; y < 2; ++y)
#pragma unroll
                for (int x = 0; x < 2; ++x) { const int e = 16 * y + l15, d = 16 * x + 4 * lg;
                    const u32x2 o = (u32x2){min(pk2(d4[x][y][0], d4[x][y][1]), 0xfffffffeu), min(pk2(d4[x][y][2], d4[x][y][3]), 0xfffffffeu)};
                    if (d < 24 && (e <= 20 || (e == 21 && d < 8))) st8c(rkv, h * 1024 + e * 48 + d * 2, e <= 20 ? o : (u32x2){0u, 0u}); }
        }
        if (tid == 0) STAMP(512 + blockIdx.x);
    }
}

__global__ void __launch_bounds__(512, 2) KATTR bmega_kernel(MegaArgs a) {
    ATI();
    extern __shared__ __attribute__((aligned(16))) unsigned char lds[];
    const int c = blockIdx.x, x = c & 7, rr = c >> 3, q = x >> 1, wave = __builtin_amdgcn_readfirstlane(threadIdx.x >> 6);
    if (LDSB() != 0u) return;
#define MAPV 1
    int role, id;
    const bool ev = (x & 1) == 0;
    if (MAPV == 1) { if (rr >= 28) { role = 5; id = (rr - 28) * 8 + x; } else if (rr < 8) { role = ev ? 0 : 1; id = q * 8 + rr; } else if (ev) { role = 2; id = q * 20 + rr - 8; } else if (rr < 20) { role = 3; id = q * 12 + rr - 8; } else { role = 4; id = q * 8 + rr - 20; } }
    if (MAPV == 2) { if (ev) { if (rr < 16) { role = rr >> 3; id = q * 8 + (rr & 7); } else { role = 2; id = q * 20 + rr - 16; } }
        else if (rr < 12) { role = 3; id = q * 12 + rr; } else if (rr < 20) { role = 4; id = q * 8 + rr - 12; } else if (rr < 24) { role = 2; id = q * 20 + rr - 4; } else { role = 5; id = (rr - 24) * 4 + q; } }
    if (MAPV == 3) { if (ev) { role = rr < 16 ? rr >> 3 : (rr >> 3) + 2; id = role == 5 ? (rr & 7) * 4 + q : q * 8 + (rr & 7); } else if (rr < 12) { role = 3; id = q * 12 + rr; } else { role = 2; id = q * 20 + rr - 12; } }
    if (role == 0) s_part(a, lds, id, wave);
    else if (role == 1) { if (wave < 4) v_run<true>(a, lds, id, wave); else v_run<false>(a, lds, id, wave); }
    else if (role == 2) { const int k = id % 20;
        if (wave == 0 && !(id & 1) && lane_id() == 0) st4c(MKR(a.hop, F_ERR), F_SD + (id >> 1) * 4, 1u + my_side());
        if (k < 8) { if (MAPV != 3) w_prefetch(a, lds, q * 8 + k, wave); } else if (k < 10) pol_vote(a, (q * 2 + k - 8) * 8 + wave);
        ro_part(a, lds, id, wave); }
    else if (role == 3) xp_part(a, lds, id, wave);
    else if (role == 4) xq_part(a, lds, id, wave);
    else a_part(a, lds, id, wave);
}

struct PrepDesc { const bf16_t* wqkv; const bf16_t* wao; bf16_t* fqkv; bf16_t* fao; unsigned char* zptr; unsigned char* fptr; const bf16_t* in; bf16_t* pq; float* kvp; unsigned char* kvt; const bf16_t* mem0; };
constexpr int FQ_STAGE = 19456, FP_LDS = 4 * FQ_STAGE + 512;
__global__ void __launch_bounds__(512, 4) front_kernel(PrepDesc d) {
    extern __shared__ __attribute__((aligned(16))) unsigned char lds[];
    const int lane = threadIdx.x & 63, wave = __builtin_amdgcn_readfirstlane(threadIdx.x >> 6), l15 = lane & 15, lg = lane >> 4;
    const unsigned ldsb = LDSB();
    if (blockIdx.x < 256) {
        const int rb = blockIdx.x >> 3, g = blockIdx.x & 7;
        const rsrc_t rin = MKR(d.in, S_ * D_ * 2);
        f32x2* st = reinterpret_cast<f32x2*>(lds + 4 * FQ_STAGE);
        const int rp = wave & 1, cg = wave >> 1;
        const rsrc_t rw = MKR(d.wqkv, D_ * 1920 * 2);
        unsigned vo[2];
#pragma unroll
        for (int u = 0; u < 2; ++u) { const int c = min(wave + 8 * u, 14) * 64 + lane, row = c / 30, e = c - row * 30, sg = e / 10; vo[u] = (unsigned)(row * 1920 + sg * 640 + 80 * g + (e - sg * 10) * 8) * 2; }
        auto issue = [&](int it) { dma16(rw, vo[0], it * (32 * 1920 * 2), ldsb + (it & 3) * FQ_STAGE + wave * 1024); dma16(rw, vo[1], it * (32 * 1920 * 2), ldsb + (it & 3) * FQ_STAGE + min(wave + 8, 14) * 1024); };
        u32x4 xa[3];
        const unsigned xo = ((rb * 64 + (wave & 3) * 16 + l15) * D_ + 8 * lg + (wave >> 2) * 32) * 2;
        auto xl = [&](int q) { asm volatile("buffer_load_dwordx4 %0, %1, %2, 0 offen offset:%3" : "=&v"(xa[q % 3]) : "v"(xo), "s"(rin), "i"(q * 128) : "memory"); };
        {
            u32x4 x[2][5];
#pragma unroll
            for (int z = 0; z < 2; ++z)
#pragma unroll
                for (int i = 0; i < 5; ++i) x[z][i] = __builtin_amdgcn_raw_buffer_load_b128(rin, (rb * 64 + wave * 8 + z * 4 + lg) * (D_ * 2) + l15 * 16 + i * 256, 0, 0);
            xl(0); xl(1); xl(2);
            CBAR();
            issue(0); issue(1); issue(2);
            asm volatile("s_waitcnt vmcnt(6)" : "+v"(xa[0]), "+v"(xa[1]), "+v"(xa[2]), "+v"(x[0][0]), "+v"(x[0][1]), "+v"(x[0][2]), "+v"(x[0][3]), "+v"(x[0][4]), "+v"(x[1][0]), "+v"(x[1][1]), "+v"(x[1][2]), "+v"(x[1][3]), "+v"(x[1][4]) :: "memory");
#pragma unroll
            for (int z = 0; z < 2; ++z) {
                float s0 = 0.f, s1 = 0.f;
#pragma unroll
                for (int i = 0; i < 5; ++i)
#pragma unroll
                    for (int e = 0; e < 4; ++e) { s0 += lo2f(x[z][i][e]); s1 += hi2f(x[z][i][e]); }
                float sm = s0 + s1;
                sm += dppf<0x128>(sm); sm += dppf<0x124>(sm); sm += dppf<0x122>(sm); sm += dppf<0x121>(sm);
                const float mean = sm * (1.0f / D_);
                s0 = 0.f; s1 = 0.f;
#pragma unroll
                for (int i = 0; i < 5; ++i)
#pragma unroll
                    for (int e = 0; e < 4; ++e) { const float d0 = lo2f(x[z][i][e]) - mean, d1 = hi2f(x[z][i][e]) - mean; s0 = __builtin_fmaf(d0, d0, s0); s1 = __builtin_fmaf(d1, d1, s1); }
                float q = s0 + s1;
                q += dppf<0x128>(q); q += dppf<0x124>(q); q += dppf<0x122>(q); q += dppf<0x121>(q);
                if (l15 == 0) st[wave * 8 + z * 4 + lg] = (f32x2){rbf(mean), 1.0f / rbf(sqrtf(rbf(rbf(q * (1.0f / D_)) + 1e-5f)))};
            }
            LBAR();
        }
        const f32x2 sr = st[(wave & 3) * 16 + l15];
        auto put = [&](int q) { u32x4 o;
#pragma unroll
            for (int e = 0; e < 4; ++e) { const unsigned v = pk2(lo2f(xa[q % 3][e]) - sr[0], hi2f(xa[q % 3][e]) - sr[0]); o[e] = pk2(lo2f(v) * sr[1], hi2f(v) * sr[1]); }
            *reinterpret_cast<u32x4*>(lds + ((2 * q + (wave >> 2)) & 3) * FQ_STAGE + 15360 + (wave & 3) * 1024 + lane * 16) = o; };
        put(0);
        f32x4 acc[2][4];
#pragma unroll
        for (int i = 0; i < 2; ++i)
#pragma unroll
            for (int u = 0; u < 4; ++u) acc[i][u] = Z4;
        const unsigned char* wp = lds + (8 * lg + (l15 >> 2)) * 480 + (l15 & 3) * 8 + cg * 32;
#pragma unroll
        for (int it = 0; it < 20; ++it) {
            asm volatile("s_waitcnt vmcnt(%3)" : "+v"(xa[0]), "+v"(xa[1]), "+v"(xa[2]) : "i"(it + 2 < 20 ? 4 : (it + 1 < 20 ? 2 : 0)) : "memory");
            LBAR();
            if (!(it & 1) && it / 2 + 3 < 10) xl(it / 2 + 3);
            if (it + 3 < 20) issue(it + 3);
            const unsigned char* sp = lds + (it & 3) * FQ_STAGE;
            u32x4 af[2], bfr[4];
#pragma unroll
            for (int i = 0; i < 2; ++i) af[i] = lds16(sp + 15360 + (2 * rp + i) * 1024 + lane * 16);
#pragma unroll
            for (int u = 0; u < 4; ++u) { const unsigned char* p = wp + (it & 3) * FQ_STAGE + (u == 3 && cg == 3 ? 2 : u) * 128; const u32x2 x0 = ldstr(p), x1 = ldstr(p + 4 * 480); bfr[u] = (u32x4){x0.x, x0.y, x1.x, x1.y}; }
            SBAR();
#pragma unroll
            for (int u = 0; u < 4; ++u)
#pragma unroll
                for (int i = 0; i < 2; ++i) acc[i][u] = dmf(bfr[u], af[i], acc[i][u]);
            if ((it & 1) && it / 2 + 1 < 10) put(it / 2 + 1);
            if (rb == it && rp == 0) {
#pragma unroll
                for (int u = 0; u < 4; ++u) { const int T = 4 * u + cg; if (T < 15) *reinterpret_cast<u32x4*>(d.fqkv + ((long)(((T / 5) * 40 + 5 * g + T % 5) * 20 + it) * 64 + lane) * 8) = bfr[u]; } }
        }
        LBAR();
        constexpr int KP = 176;
        unsigned char* sk = lds; unsigned char* sv = lds + 64 * KP;
#pragma unroll
        for (int u = 0; u < 4; ++u) {
            const int T = 4 * u + cg;
            if (T > 14) break;
#pragma unroll
            for (int i = 0; i < 2; ++i) {
                const int row = (2 * rp + i) * 16 + l15;
                u32x2 o;
                if (T < 10) { o.x = phi2(acc[i][u][0], acc[i][u][1]); o.y = phi2(acc[i][u][2], acc[i][u][3]); }
                else { o.x = pk2(acc[i][u][0], acc[i][u][1]); o.y = pk2(acc[i][u][2], acc[i][u][3]); }
                if (T < 5) { const int gi = 4 * T + lg, hd = gi / 5, pt = gi - 5 * hd; *reinterpret_cast<u32x2*>(d.pq + ((rb * 4 + 2 * rp + i) * 32 + 4 * g + hd) * 320 + (pt < 4 ? l15 * 16 + pt * 4 : 256 + l15 * 4)) = o; }
                else *reinterpret_cast<u32x2*>((T < 10 ? sk + (T - 5) * 32 : sv + (T - 10) * 32) + row * KP + 8 * lg) = o;
            }
        }
        LBAR();
        if (wave >= 4) return;
        {
            f32x4 d4[2][2] = {{Z4, Z4}, {Z4, Z4}};
#pragma unroll
            for (int ks = 0; ks < 2; ++ks) {
                u32x4 A[2], B[2];
#pragma unroll
                for (int t = 0; t < 2; ++t) {
                    const int off = (32 * ks + 8 * lg + (l15 >> 2)) * KP + 40 * wave + 32 * t + 8 * (l15 & 3);
                    const u32x2 a0 = ldstr(sk + off), a1 = ldstr(sk + off + 4 * KP), b0 = ldstr(sv + off), b1 = ldstr(sv + off + 4 * KP);
                    A[t] = (u32x4){a0.x, a0.y, a1.x, a1.y}; B[t] = (u32x4){b0.x, b0.y, b1.x, b1.y};
                }
                if (l15 == 4) B[1] = (u32x4){0x3F803F80u, 0x3F803F80u, 0x3F803F80u, 0x3F803F80u};
#pragma unroll
                for (int x = 0; x < 2; ++x)
#pragma unroll
                    for (int y = 0; y < 2; ++y) d4[x][y] = dmf(A[x], B[y], d4[x][y]);
            }
            float* op = d.kvp + ((long)(4 * g + wave) * 32 + rb) * 420;
#pragma unroll
            for (int x = 0; x < 2; ++x)
#pragma unroll
                for (int y = 0; y < 2; ++y)
#pragma unroll
                    for (int r = 0; r < 4; ++r) { const int dd = 16 * x + 4 * lg + r, e = 16 * y + l15;
                        if (dd < 20) { if (e < 20) op[dd * 20 + e] = d4[x][y][r]; else if (e == 20) op[400 + dd] = d4[x][y][r]; } }
        }
        return;
    }
    {
        const int u = (blockIdx.x - 256) * 8 + wave;
            if (u < 200) { const int kk = u / 10, n0 = (u - kk * 10) * 64;
                const bf16_t* W = d.wao + (long)(kk * 32 + (lane >> 3)) * D_ + n0 + (lane & 7) * 8;
                unsigned char* t = lds + wave * 4096;
                u32x4 v[4];
#pragma unroll
                for (int q = 0; q < 4; ++q) v[q] = *reinterpret_cast<const u32x4*>(W + (long)q * 8 * D_);
#pragma unroll
                for (int q = 0; q < 4; ++q) *reinterpret_cast<u32x4*>(t + q * 1024 + lane * 16) = v[q];
                CBAR();
#pragma unroll
                for (int q = 0; q < 4; ++q) { const unsigned char* p = t + (8 * lg + (l15 >> 2)) * 128 + q * 32 + (l15 & 3) * 8; const u32x2 x0 = ldstr(p), y0 = ldstr(p + 512);
                    *reinterpret_cast<u32x4*>(d.fao + ((long)((n0 / 16 + q) * 20 + kk) * 64 + lane) * 8) = (u32x4){x0.x, x0.y, y0.x, y0.y}; } }
    {
        const unsigned g0 = ((blockIdx.x - 256) * 8 + wave) * 64 + lane; constexpr unsigned NL = 49152;
        const u32x4 z = (u32x4){0u, 0u, 0u, 0u};
        for (unsigned q = g0; q < 2 * HOP_BYTES / 16; q += NL) reinterpret_cast<u32x4*>(d.zptr)[q] = z;
        if (g0 < 4096) reinterpret_cast<u32x4*>(d.kvt)[g0] = (u32x4){~0u, ~0u, ~0u, ~0u};
        const unsigned fi = NL - 1 - g0;
        if (fi < FR_FILL / 16) {
            const unsigned blk = fi >> 10, c = fi & 511u;
            u32x4 v = (u32x4){~0u, ~0u, ~0u, ~0u};
            if (blk % 20u < 2u) {
                const unsigned b = (blk % 20u) * 8 + (c >> 6), m = c & 15u;
                if (b < 10u) { const unsigned* mp = reinterpret_cast<const unsigned*>(d.mem0 + (2 * (m >> 2) + (m & 1)) * D_ + b * 64 + ((c >> 4) & 3u) * 16 + (m & 2));
                    v = (u32x4){min(mp[0], 0xfffffffeu), min(mp[2], 0xfffffffeu), min(mp[4], 0xfffffffeu), min(mp[6], 0xfffffffeu)}; }
                else if (!(c & 3u)) v.x = c == 196u ? TT_E1 + TT_E2 + 40u : TT_E1 + 20u;
            }
            reinterpret_cast<u32x4*>(al2m(d.fptr))[fi] = v;
        }
    }
    }
}

extern "C" void kernel_launch(void* const* d_in, const int* in_sizes, int n_in, void* d_out, int out_size, void* d_ws, size_t ws_size, hipStream_t stream) {
    const bf16_t* A_x = (const bf16_t*)d_in[0]; const bf16_t* A_wqkv = (const bf16_t*)d_in[1]; const bf16_t* A_wo = (const bf16_t*)d_in[2];
    const bf16_t* B_mem0 = (const bf16_t*)d_in[4]; const bf16_t* Wq = (const bf16_t*)d_in[5]; const bf16_t* Wk = (const bf16_t*)d_in[6]; const bf16_t* Wv = (const bf16_t*)d_in[7]; const bf16_t* Wo = (const bf16_t*)d_in[8];
    unsigned char* ws = (unsigned char*)d_ws;
    (void)hipFuncSetAttribute((const void*)bmega_kernel, hipFuncAttributeMaxDynamicSharedMemorySize, MG_LDS);
    (void)hipFuncSetAttribute((const void*)front_kernel, hipFuncAttributeMaxDynamicSharedMemorySize, FP_LDS);
    bf16_t* wfAqkv = (bf16_t*)(ws + WS_WF_AQKV); bf16_t* wfAo = (bf16_t*)(ws + WS_WF_AO);
    float* kvp = (float*)(ws + WS_KVP);
    unsigned char* fr = ws + WS_FR;
    PrepDesc pd;
    pd.wqkv = A_wqkv; pd.wao = A_wo; pd.fqkv = wfAqkv; pd.fao = wfAo; pd.zptr = ws + WS_HOP; pd.fptr = fr;
    pd.in = A_x; pd.pq = (bf16_t*)(ws + WS_PQ); pd.kvp = kvp; pd.kvt = ws + WS_KVF; pd.mem0 = B_mem0;
    front_kernel<<<352, 512, FP_LDS, stream>>>(pd);
    MegaArgs ma; ma.wq = Wq; ma.wk = Wk; ma.wv = Wv; ma.wo = Wo; ma.wfq = wfAqkv; ma.wfo = wfAo; ma.kvp = kvp; ma.kvt = (bf16_t*)(ws + WS_KVF);
    ma.x0 = A_x; ma.pq = (bf16_t*)(ws + WS_PQ); ma.xi = ws + WS_X; ma.out = (bf16_t*)d_out;
    ma.st = ws + WS_ST; ma.hop = ws + WS_HOP; ma.hx = fr;
    bmega_kernel<<<256, 512, MG_LDS, stream>>>(ma);
}
```

```cpp
#include <hip/hip_runtime.h>
#include <hip/hip_bf16.h>
#include <stdint.h>
#include <type_traits>

typedef __attribute__((ext_vector_type(8))) short bf16x8;
typedef __attribute__((ext_vector_type(4))) float f32x4;
typedef __attribute__((ext_vector_type(4))) unsigned u32x4;
typedef __attribute__((ext_vector_type(2))) unsigned u32x2;
typedef uint16_t bf16_t;

typedef __attribute__((ext_vector_type(2))) __bf16 bf16x2_t;
typedef __attribute__((ext_vector_type(2))) float f32x2;
__device__ __forceinline__ unsigned pk2(float lo, float hi) { f32x2 v = {lo, hi}; return __builtin_bit_cast(unsigned, __builtin_convertvector(v, bf16x2_t)); }
__device__ __forceinline__ float rbf(float f) { return __uint_as_float(pk2(f, f) << 16); }
__device__ __forceinline__ float lo2f(unsigned p) { return __uint_as_float(p << 16); }
__device__ __forceinline__ float hi2f(unsigned p) { return __uint_as_float(p & 0xffff0000u); }

constexpr int S_ = 2048, D_ = 640, H_ = 32, E_ = 192, NSEG_ = 64;
constexpr int HE_ = H_ * E_;

constexpr int NR_ = 80;
constexpr int NXP_ = 48;
constexpr size_t al(size_t x) { return (x + 255) & ~(size_t)255; }
constexpr size_t WS_WF_AQKV = 0;
constexpr size_t WS_WF_AO   = WS_WF_AQKV + al((size_t)640 * 1920 * 2);
constexpr size_t WS_PQ      = WS_WF_AO + al((size_t)640 * 640 * 2);
constexpr unsigned XI_X = 10240, XI_S8 = XI_X + 40960, XI_PITCH = XI_S8 + 2048, XI_BYTES = NSEG_ * XI_PITCH;
constexpr size_t WS_X       = WS_PQ + al((size_t)S_ * D_ * 2);
constexpr size_t WS_KVP     = WS_X + al(XI_BYTES);
constexpr size_t WS_KVF     = WS_KVP + al((size_t)32 * 16 * 4096);
constexpr size_t WS_ST      = WS_KVF + al((size_t)32 * 1024 * 2);
#define KXR 32
#define PR 32
#define URING 32
constexpr unsigned ST_KX = 0;
constexpr unsigned ST_VX = ST_KX + KXR * 32u * 12288;
constexpr unsigned ST_KM = ST_VX + KXR * 32u * 12288;
constexpr unsigned ST_PX = ST_KM + PR * 32u * 3072;
constexpr unsigned ST_U  = ST_PX + PR * 2u * 40960;
constexpr unsigned ST_BYTES = ST_U + (unsigned)URING * 40 * 32768;
constexpr size_t WS_FR      = WS_ST + al(ST_BYTES);
constexpr unsigned FR_FILL = 40u << 14, FR_UM = FR_FILL, FR_USED = FR_UM + (160u << 14);
__device__ __forceinline__ unsigned char* al2m(unsigned char* p) { return (unsigned char*)(((uintptr_t)p + (((uintptr_t)1 << 21) - 1)) & ~(((uintptr_t)1 << 21) - 1)); }
constexpr unsigned M_STAMP = 16384 + 2048;
constexpr unsigned FLP = 128;
constexpr unsigned F_POL = 0, F_AQ = F_POL + 256, F_SD = F_AQ + 256, F_WO = F_SD + 256, F_IN = F_WO + 8 * FLP, F_QK = F_IN + 8 * FLP, F_PQ = F_QK + 128 * FLP, F_AF = 36864, F_XP = F_AF + 64 * 16 * FLP, F_PX = F_XP + 64 * NXP_ * FLP, F_KM = F_PX + 70 * 64 * FLP, F_UX = F_KM + 64 * 32 * FLP,
    F_OF = F_UX + 65 * 32 * FLP, F_RD = F_OF + 64 * NR_ * FLP, F_ERR = F_RD + 64 * NR_ * FLP, HOP_BYTES = F_ERR + 256;
constexpr size_t WS_BOOT    = WS_FR + ((size_t)6 << 20);
constexpr size_t WS_HOP     = WS_BOOT + 2048;
constexpr size_t WS_DBG     = WS_HOP + al(2 * HOP_BYTES);
constexpr size_t WS_END     = WS_DBG + al((size_t)2 << 20);
static_assert(WS_END <= (size_t)256 * 1024 * 1024, "ws too big");
static_assert(FR_USED <= (4u << 20), "hand-off pages");

__device__ __forceinline__ float phi1(float x) { return (__builtin_amdgcn_exp2f(fminf(x, 0.f) * 1.4426950408889634f) + 1e-6f) + fmaxf(x, 0.f); }
__device__ __forceinline__ unsigned phi2(float x0, float x1) { return pk2(phi1(x0), phi1(x1)); }
constexpr unsigned OOB = 0x40000000u;
#define CBAR() asm volatile("" ::: "memory")
#define VMDRAIN() asm volatile("s_waitcnt vmcnt(0)" ::: "memory")
#define PIN(x) asm volatile("" : "+v"(x))
#define VMKNOWN() __builtin_amdgcn_s_waitcnt(0x0F70)
#define LBAR() do { asm volatile("s_waitcnt lgkmcnt(0)" ::: "memory"); __builtin_amdgcn_s_barrier(); asm volatile("" ::: "memory"); } while (0)
typedef __amdgpu_buffer_rsrc_t rsrc_t;
__device__ __forceinline__ u32x4 ld16c(rsrc_t r, unsigned off) { return __builtin_amdgcn_raw_buffer_load_b128(r, off, 0, 16); }
__device__ __forceinline__ u32x2 ld8c(rsrc_t r, unsigned off) { return __builtin_amdgcn_raw_buffer_load_b64(r, off, 0, 16); }
__device__ __forceinline__ unsigned ld4c(rsrc_t r, unsigned off) { return __builtin_amdgcn_raw_buffer_load_b32(r, off, 0, 16); }
__device__ __forceinline__ void st16c(rsrc_t r, unsigned off, u32x4 v) { __builtin_amdgcn_raw_buffer_store_b128(v, r, off, 0, 16); }
__device__ __forceinline__ void st8c(rsrc_t r, unsigned off, u32x2 v) { __builtin_amdgcn_raw_buffer_store_b64(v, r, off, 0, 16); }
__device__ __forceinline__ void st4c(rsrc_t r, unsigned off, unsigned v) { __builtin_amdgcn_raw_buffer_store_b32(v, r, off, 0, 16); }
template <int CTRL> __device__ __forceinline__ unsigned dppu(unsigned x) { return (unsigned)__builtin_amdgcn_mov_dpp((int)x, CTRL, 0xf, 0xf, true); }
template <int CTRL> __device__ __forceinline__ float dppf(float x) { return __uint_as_float(dppu<CTRL>(__float_as_uint(x))); }
__device__ __forceinline__ float xg_max(float x) {
    auto s = __builtin_amdgcn_permlane16_swap(__float_as_uint(x), __float_as_uint(x), false, false); x = fmaxf(__uint_as_float(s[0]), __uint_as_float(s[1]));
    auto t = __builtin_amdgcn_permlane32_swap(__float_as_uint(x), __float_as_uint(x), false, false); return fmaxf(__uint_as_float(t[0]), __uint_as_float(t[1])); }
__device__ __forceinline__ float xg_sum(float x) {
    auto s = __builtin_amdgcn_permlane16_swap(__float_as_uint(x), __float_as_uint(x), false, false); x = __uint_as_float(s[0]) + __uint_as_float(s[1]);
    auto t = __builtin_amdgcn_permlane32_swap(__float_as_uint(x), __float_as_uint(x), false, false); return __uint_as_float(t[0]) + __uint_as_float(t[1]); }

typedef __attribute__((ext_vector_type(8))) unsigned u32x8;
typedef __attribute__((ext_vector_type(8))) __bf16 bfv8;
typedef __attribute__((ext_vector_type(16))) __bf16 bfv16;
typedef short v4i16_t __attribute__((ext_vector_type(4)));
typedef __attribute__((address_space(3))) volatile unsigned lds_u32;
#define SBAR() __builtin_amdgcn_sched_barrier(0)
__device__ __forceinline__ int lane_id() { int l; asm volatile("v_mbcnt_lo_u32_b32 %0, -1, 0\n\tv_mbcnt_hi_u32_b32 %0, -1, %0" : "=v"(l)); return l; }
#define Z4 ((f32x4){0.f, 0.f, 0.f, 0.f})
__device__ __forceinline__ f32x4 smf(u32x4 A, u32x8 B, f32x4 c, unsigned idx) { return __builtin_amdgcn_smfmac_f32_16x16x64_bf16(__builtin_bit_cast(bfv8, A), __builtin_bit_cast(bfv16, B), c, (int)idx, 0, 0); }
__device__ __forceinline__ f32x4 dmf(u32x4 A, u32x4 B, f32x4 c) { return __builtin_amdgcn_mfma_f32_16x16x32_bf16(__builtin_bit_cast(bfv8, A), __builtin_bit_cast(bfv8, B), c, 0, 0, 0); }
__device__ __forceinline__ u32x4 lo4(u32x8 v) { return (u32x4){v[0], v[1], v[2], v[3]}; }
__device__ __forceinline__ u32x4 hi4(u32x8 v) { return (u32x4){v[4], v[5], v[6], v[7]}; }
__device__ __forceinline__ u32x8 cat8(u32x4 a, u32x4 b) { return (u32x8){a.x, a.y, a.z, a.w, b.x, b.y, b.z, b.w}; }
__device__ __forceinline__ u32x4 lds16(const unsigned char* p) { return *reinterpret_cast<const u32x4*>(p); }
__device__ __forceinline__ u32x4 lds16a(int ad) { return *(const __attribute__((address_space(3))) u32x4*)(uintptr_t)(unsigned)ad; }
__device__ __forceinline__ unsigned lds4a(int ad) { return *(const __attribute__((address_space(3))) unsigned*)(uintptr_t)(unsigned)ad; }
__device__ __forceinline__ void sts16a(int ad, u32x4 v) { *(__attribute__((address_space(3))) u32x4*)(uintptr_t)(unsigned)ad = v; }
__device__ __forceinline__ u32x2 ldstr(const unsigned char* p) { return __builtin_bit_cast(u32x2, __builtin_amdgcn_ds_read_tr16_b64_v4i16((__attribute__((address_space(3))) v4i16_t*)p)); }
__device__ __forceinline__ unsigned umax4(u32x4 v) { return max(max(v.x, v.y), max(v.z, v.w)); }
__device__ __forceinline__ void dma16(rsrc_t r, unsigned voff, unsigned soff, unsigned ldsd) {
    unsigned keep;
    asm volatile("s_mov_b32 %0, m0\n\ts_mov_b32 m0, %1\n\ts_nop 2\n\tbuffer_load_dwordx4 %2, %3, %4 offen lds\n\ts_mov_b32 m0, %0" : "=&s"(keep) : "s"(ldsd), "v"(voff), "s"(r), "s"(soff) : "memory");
}
__device__ __forceinline__ void dma16c(rsrc_t r, unsigned voff, unsigned soff, unsigned ldsd) {
    unsigned keep;
    asm volatile("s_mov_b32 %0, m0\n\ts_mov_b32 m0, %1\n\ts_nop 2\n\tbuffer_load_dwordx4 %2, %3, %4 offen sc1 lds\n\ts_mov_b32 m0, %0" : "=&s"(keep) : "s"(ldsd), "v"(voff), "s"(r), "s"(soff) : "memory");
}
__device__ __forceinline__ void dma4c(rsrc_t r, unsigned voff, unsigned soff, unsigned ldsd) {
    unsigned keep;
    asm volatile("s_mov_b32 %0, m0\n\ts_mov_b32 m0, %1\n\ts_nop 2\n\tbuffer_load_dword %2, %3, %4 offen sc1 lds\n\ts_mov_b32 m0, %0" : "=&s"(keep) : "s"(ldsd), "v"(voff), "s"(r), "s"(soff) : "memory");
}
template <int N> __device__ __forceinline__ void dma16cn(rsrc_t r, unsigned voff, unsigned soff, unsigned ldsd) {
    unsigned keep;
    if (N >= 4) asm volatile("s_mov_b32 %0, m0\n\ts_mov_b32 m0, %1\n\ts_nop 2\n\tbuffer_load_dwordx4 %2, %3, %4 offen sc1 lds\n\tbuffer_load_dwordx4 %2, %3, %4 offen offset:1024 sc1 lds\n\t"
        "buffer_load_dwordx4 %2, %3, %4 offen offset:2048 sc1 lds\n\tbuffer_load_dwordx4 %2, %3, %4 offen offset:3072 sc1 lds\n\ts_mov_b32 m0, %0" : "=&s"(keep) : "s"(ldsd), "v"(voff), "s"(r), "s"(soff) : "memory");
    else if (N == 3) asm volatile("s_mov_b32 %0, m0\n\ts_mov_b32 m0, %1\n\ts_nop 2\n\tbuffer_load_dwordx4 %2, %3, %4 offen sc1 lds\n\tbuffer_load_dwordx4 %2, %3, %4 offen offset:1024 sc1 lds\n\t"
        "buffer_load_dwordx4 %2, %3, %4 offen offset:2048 sc1 lds\n\ts_mov_b32 m0, %0" : "=&s"(keep) : "s"(ldsd), "v"(voff), "s"(r), "s"(soff) : "memory");
    else if (N >= 2) asm volatile("s_mov_b32 %0, m0\n\ts_mov_b32 m0, %1\n\ts_nop 2\n\tbuffer_load_dwordx4 %2, %3, %4 offen sc1 lds\n\tbuffer_load_dwordx4 %2, %3, %4 offen offset:1024 sc1 lds\n\ts_mov_b32 m0, %0"
        : "=&s"(keep) : "s"(ldsd), "v"(voff), "s"(r), "s"(soff) : "memory");
    else dma16c(r, voff, soff, ldsd);
    constexpr int D = N >= 4 ? 4 : N;
    if constexpr (N > D) dma16cn<N - D>(r, voff, soff + D * 1024, ldsd + D * 1024);
}
__device__ __forceinline__ void st16s(u32x4 v, rsrc_t r, unsigned voff, unsigned soff) { __builtin_amdgcn_raw_buffer_store_b128(v, r, voff, soff, 16); asm volatile("s_nop 1" :: "v"(v) : "memory"); }
#define VMW(n) asm volatile("s_waitcnt vmcnt(%0)" :: "i"(n) : "memory")

struct MegaArgs { const bf16_t* wq; const bf16_t* wk; const bf16_t* wv; const bf16_t* wo; const bf16_t* wqkv; const bf16_t* wao; const bf16_t* mem0; const bf16_t* x0; bf16_t* out; unsigned char* ws; };
typedef const __attribute__((address_space(4))) MegaArgs KA;
__device__ __forceinline__ KA& args() { KA* p = (KA*)__builtin_amdgcn_kernarg_segment_ptr(); asm volatile("" : "+s"(p)); return *p; }
#define STAMP(idx) do { } while (0)
#define KATTR
#define ATI() do { } while (0)
#define AT(n) do { } while (0)
#define ATV do { } while (0)
#define ATD(role, on) do { } while (0)
#define SPINB 17
constexpr unsigned SPIN_CAP = 2u << SPINB;
constexpr unsigned SPIN_CAP_W = 1u << SPINB;
constexpr int MG_LDS = 163840;

#define PARAM(n, v) constexpr int n = v
#define TT_M1  17
#define TT_M2  17
#define TT_V   85
#define TT_V1 TT_V
#define TT_DEC 2
#define TT_BUMP 2
#define TT_DCY 1
#define TT_XS 5
#define TT_XD 1
#define TT_RT 34
#define TT_UM -5
#define TT_TW 12
#define TT_MD 60
#define TT_BD 0
#define TT_KD 0
#define TT_QD 20
#define TT_QN -40
#define TT_AD 250
#define XQD 0
#define SPRIO 2
#define PFD 6
#define DPS 5
#define PF0 2
#define KIM 6
#define XLD 9
#define NEAR 60
#define ROT 1
#define TF 60
#define RTS 30
#define RTL 8
#define R0S 4
#define VPRIO 0
#define XPD 4
#define TT_FP 6
#define TT_OD 30
#define TT_VD 5
#define YP1 0
#define QA 3
#define OIM 63
#define TT_UX 0
#define RLATE 0
#define PLATE 0
#define SLATE1 0
#define SLATE2 0
#define TT_TL 250
#define TT_AN 110
#define RROT 0
#define AROT 0
#define OLAG 3
constexpr int TT_E1 = 142, TT_E2 = 50;
__device__ __forceinline__ unsigned rt_now() { return (unsigned)__builtin_amdgcn_s_memrealtime(); }
__device__ __forceinline__ void rt_wait(unsigned t) {
    unsigned d;
    asm volatile("s_branch 2f\n"
        "1:\n\ts_cmp_lt_u32 %0, 12\n\ts_cbranch_scc1 2f\n\ts_sleep 2\n\ts_cmp_lt_u32 %0, 30\n\ts_cbranch_scc1 2f\n\ts_sleep 5\n"
        "2:\n\ts_memrealtime vcc\n\ts_waitcnt lgkmcnt(0)\n\ts_sub_u32 %0, %1, vcc_lo\n\ts_cmp_lt_u32 %0, 20000\n\ts_cbranch_scc1 1b" : "=&s"(d) : "s"(t - 1u) : "scc", "vcc", "memory");
}
#define FINE 1
#define FC 100
template <int K> __device__ __forceinline__ void rt_fine(unsigned t) {
    if (!FINE) { rt_wait(t); return; }
    asm volatile("s_branch 2f\n"
        "1:\n\ts_cmp_lt_u32 s70, 12\n\ts_cbranch_scc1 2f\n\ts_sleep 2\n\ts_cmp_lt_u32 s70, 30\n\ts_cbranch_scc1 2f\n\ts_sleep 5\n"
        "2:\n\ts_memrealtime vcc\n\ts_waitcnt lgkmcnt(0)\n\ts_sub_u32 s70, %0, vcc_lo\n\ts_cmp_lt_u32 s70, 20000\n\ts_cbranch_scc1 1b\n\t"
        "s_cmp_lt_u32 s70, -8\n\ts_cbranch_scc1 5f\n\t"
        "s_memrealtime s[70:71]\n\ts_nop 4\n\ts_memrealtime s[72:73]\n\ts_nop 4\n\ts_memrealtime s[74:75]\n\ts_nop 4\n\ts_memrealtime s[76:77]\n\ts_nop 4\n\ts_memrealtime vcc\n\ts_waitcnt lgkmcnt(0)\n\t"
        "s_add_u32 s72, s72, s74\n\ts_add_u32 s76, s76, vcc_lo\n\ts_add_u32 s72, s72, s76\n\ts_lshl_b32 s71, s70, 2\n\ts_sub_u32 s72, s72, s71\n\ts_mul_i32 s72, s72, 6\n\t"
        "s_sub_u32 s70, %0, s70\n\ts_mul_i32 s70, s70, %1\n\ts_sub_i32 s70, s70, s72\n\ts_add_i32 s70, s70, %2\n\ts_max_i32 s70, s70, 0\n\ts_min_i32 s70, s70, 255\n\ts_lshr_b32 s70, s70, 3\n\ts_lshl_b32 s70, s70, 2\n\t"
        "s_getpc_b64 vcc\n3:\n\ts_sub_u32 s70, 4f-3b, s70\n\ts_add_u32 vcc_lo, vcc_lo, s70\n\ts_addc_u32 vcc_hi, vcc_hi, 0\n\ts_setpc_b64 vcc\n"
        "5:\n\ts_add_u32 s70, s70, 18\n\ts_cmp_gt_u32 s70, 18\n\ts_cbranch_scc1 4f\n\ts_memrealtime vcc\n\ts_waitcnt lgkmcnt(0)\n\ts_sub_u32 s70, %0, vcc_lo\n\ts_branch 5b\n"
        "\t.rept 32\n\ts_nop 1\n\t.endr\n4:"
        :: "s"(t - 19u), "i"(K), "i"(15 * K - FC) : "scc", "vcc", "s70", "s71", "s72", "s73", "s74", "s75", "s76", "s77", "memory");
}
__device__ __forceinline__ const void* psel(const void* p0, const void* p1, int l) { return (const void*)((uintptr_t)p0 + (((uintptr_t)p1 - (uintptr_t)p0) & (uintptr_t)(-(long)l))); }
#define MKR(p, n) __builtin_amdgcn_make_buffer_rsrc((void*)(p), 0, (int)(n), 0x00020000)
__device__ __forceinline__ unsigned my_side() { return (__builtin_amdgcn_s_getreg((3 << 11) | 20) & 15u) >= 4u ? 1u : 0u; }
__device__ __forceinline__ void tt_step(unsigned tw, int s, unsigned t_poll, unsigned& tb, unsigned& cC1, unsigned& cP) {
    const unsigned tgt0 = tb + cP, pc2 = cP - cC1;
    int d = (int)(tw + pc2 - tgt0);
    d = max(d, (int)dppu<0x128>((unsigned)d)); d = max(d, (int)dppu<0x124>((unsigned)d)); d = max(d, (int)dppu<0x122>((unsigned)d)); d = max(d, (int)dppu<0x121>((unsigned)d));
    d = __builtin_amdgcn_readlane(d, 0);
    tb = s == 0 ? t_poll : ((s == 1 || d > 0) ? tgt0 + (unsigned)d - pc2 + (s == 1 ? TT_V1 : TT_V) : tgt0);
    cC1 = __builtin_amdgcn_readlane(tw, 16); cP = __builtin_amdgcn_readlane(tw, 17);
}
__device__ __forceinline__ void set_err(unsigned char* hop) { unsigned z, v; asm volatile("v_mov_b32 %0, 0\n\tv_mov_b32 %1, 1\n\ts_nop 4\n\tglobal_store_dword %0, %1, %2\n\ts_nop 1" : "=&v"(z), "=&v"(v) : "s"(hop + F_ERR) : "memory"); }
__device__ __forceinline__ void wait_set(rsrc_t rfl, unsigned off, unsigned char* hop, int slp) {
    for (unsigned sp = 0;; ++sp) { CBAR();
        const unsigned v = ld4c(rfl, off);
        if (__all(v != 0u)) break;
        for (int q = 0; q < slp; ++q) __builtin_amdgcn_s_sleep(8);
        if (sp > SPIN_CAP_W) { set_err(hop); break; } }
}
__device__ __forceinline__ void boot_wait(KA& a) {
    const rsrc_t r = MKR((a.ws + WS_HOP) - 2048, 2048); const unsigned off = 128 + (lane_id() & 7) * 128;
    for (unsigned sp = 0; sp < SPIN_CAP_W; ++sp) { CBAR();
        const u32x2 t = ld8c(r, 0), v = ld8c(r, off);
        if (__all(v.x == t.x + 1u && v.y == ~t.y)) break;
        __builtin_amdgcn_s_sleep(16); }
}
__device__ __forceinline__ void init_wait(KA& a) { boot_wait(a); wait_set(MKR(a.ws + WS_HOP, F_ERR), F_IN + (blockIdx.x & 7) * FLP, a.ws + WS_HOP, 3); }
__device__ __forceinline__ void pol_vote(KA& a, const int v) {
    const rsrc_t r = MKR(al2m((a.ws + WS_FR)), 1 << 22);
    const unsigned lane = lane_id(), b0 = (48u + v + (v & 1) * 128u) << 14;
    if (lane < 16) st4c(r, b0 + (lane & 1) * 8192 + (lane >> 1) * 896, 0u);
    VMDRAIN();
    unsigned best[2] = {~0u, ~0u}, acc = 0;
#pragma unroll 1
    for (int q = 0; q < 12; ++q) {
        const unsigned long long t0 = __builtin_amdgcn_s_memtime();
        unsigned x = ld4c(r, b0 + ((q ^ (v >> 1)) & 1) * 8192 + (q >> 1) * 896 + acc);
        asm volatile("s_waitcnt vmcnt(0)" : "+v"(x) :: "memory");
        const unsigned dt = (unsigned)(__builtin_amdgcn_s_memtime() - t0);
        acc += x; if ((q ^ (v >> 1)) & 1) best[1] = min(best[1], dt); else best[0] = min(best[0], dt);
    }
    const int df = max(-300, min(300, (int)best[0] - (int)best[1]));
    if (lane == 0) st4c(MKR((a.ws + WS_HOP), F_ERR), F_POL + v * 4, (unsigned)((my_side() ? -df : df) | 1));
}
__device__ __forceinline__ unsigned page_pol(KA& a) {
    const rsrc_t rfl = MKR((a.ws + WS_HOP), F_ERR); const unsigned off = F_POL + lane_id() * 4;
    init_wait(a); wait_set(rfl, off, (a.ws + WS_HOP), 2);
    const unsigned long long b = __ballot((int)ld4c(rfl, off) > 0);
    return (__popcll(b & 0x5555555555555555ull) > 16 ? 1u : 0u) | (__popcll(b & 0xAAAAAAAAAAAAAAAAull) > 16 ? 2u : 0u);
}
__device__ __forceinline__ unsigned wait_timed(rsrc_t rfl, unsigned off, unsigned char* hop, unsigned& tnext, int nper, int dly, bool fast) {
    if (!fast && (int)(tnext - rt_now()) < 4500) rt_wait(tnext);
    for (unsigned sp = 0;; ++sp) { CBAR();
        const u32x4 v = ld16c(rfl, off);
        if (__all(v.x != 0u)) { tnext = __builtin_amdgcn_readfirstlane(v.w) + (unsigned)nper * __builtin_amdgcn_readfirstlane(v.x) + (unsigned)dly; return __builtin_amdgcn_readfirstlane(v.x); }
        if (fast) __builtin_amdgcn_s_sleep(2); else __builtin_amdgcn_s_sleep(40);
        if (sp > SPIN_CAP_W) { set_err(hop); return 0u; } }
}
template <int NRND, class F> __device__ __forceinline__ void load_w(rsrc_t r0, rsrc_t r1, unsigned row1, unsigned col0, unsigned char* lds, unsigned ldsb, int wave, int lane, F&& put) {
    unsigned vo[3];
#pragma unroll
    for (int i = 0; i < 3; ++i) { const int c = 64 * i + lane, row = c / 24, ch = c - row * 24; vo[i] = (unsigned)((8 * wave + row) * HE_ + col0 + ch * 8) * 2; }
    auto issue = [&](int rd) {
#pragma unroll
        for (int i = 0; i < 3; ++i) { dma16(r0, vo[i], (unsigned)rd * (64 * HE_ * 2), ldsb + (rd % 3) * 49152 + wave * 3072 + i * 1024);
            dma16(r1, vo[i], ((unsigned)rd * 64 + row1) * (HE_ * 2), ldsb + (rd % 3) * 49152 + 24576 + wave * 3072 + i * 1024); }
    };
    issue(0); issue(1); issue(2);
    const unsigned char* rp = lds + (wave >> 2) * 24576 + (8 * (lane >> 4) + ((lane & 15) >> 2)) * 384 + (wave & 3) * 96 + (lane & 3) * 8;
#pragma unroll
    for (int rd = 0; rd < NRND; ++rd) {
        if (rd + 2 < NRND) VMW(12); else if (rd + 1 < NRND) VMW(6); else VMW(0);
        LBAR();
#pragma unroll
        for (int u = 0; u < 3; ++u) { const unsigned char* p = rp + (rd % 3) * 49152 + u * 32;
            const u32x2 a0 = ldstr(p), a1 = ldstr(p + 4 * 384), b0 = ldstr(p + 32 * 384), b1 = ldstr(p + 36 * 384);
            put(rd, u, (u32x8){a0.x, a0.y, a1.x, a1.y, b0.x, b0.y, b1.x, b1.y}); }
        LBAR();
        if (rd + 3 < NRND) issue(rd + 3);
    }
}

__device__ __forceinline__ unsigned wsw(unsigned row) { return ((row >> 1) & 1u) | ((((row >> 2) ^ (row >> 3)) & 1u) << 1); }
template <int NB, int RB, int NRND, int LB, int R0, class G, class F, class H> __device__ __forceinline__ void load_b(unsigned ldsb, int wave, G&& src, F&& take, H&& back) {
    auto issue = [&](int rd) {
#pragma unroll
        for (int j = 0; j < NB; ++j) src(rd, j, ldsb + LB + ((rd + R0) % RB) * (NB * 8192) + j * 8192 + wave * 1024); };
#pragma unroll
    for (int rd = 0; rd < RB; ++rd) issue(rd);
#pragma unroll
    for (int rd = 0; rd < NRND; ++rd) {
        VMW((NRND - 1 - rd < RB - 1 ? NRND - 1 - rd : RB - 1) * NB);
        LBAR();
        take(rd, LB + ((rd + R0) % RB) * (NB * 8192));
        LBAR();
        back(rd);
        if (rd + RB < NRND) issue(rd + RB);
    }
}
__device__ __forceinline__ u32x4 ld_frag(const unsigned char* blk, unsigned r0, unsigned dr, unsigned win, unsigned l15) {
    const unsigned ra = r0 + (l15 >> 2), rb = ra + dr;
    const u32x2 x = ldstr(blk + ra * 128 + ((win ^ wsw(ra)) << 5) + (l15 & 3) * 8), y = ldstr(blk + rb * 128 + ((win ^ wsw(rb)) << 5) + (l15 & 3) * 8);
    return (u32x4){x.x, x.y, y.x, y.y};
}
__device__ __forceinline__ void ln_consts(float s1, float s2, float& mb, float& inv) {
    const float mean = s1 * (1.0f / D_), var = fmaxf(__builtin_fmaf(-mean, mean, s2 * (1.0f / D_)), 0.f);
    mb = rbf(mean); inv = __builtin_amdgcn_rcpf(rbf(__builtin_amdgcn_sqrtf(rbf(rbf(var) + 1e-5f))));
}
__device__ __forceinline__ float row_sums(u32x2 o) {
    const float y0 = lo2f(o.x), y1 = hi2f(o.x), y2 = lo2f(o.y), y3 = hi2f(o.y);
    const auto e = __builtin_amdgcn_permlane16_swap(__float_as_uint((y0 + y1) + (y2 + y3)), __float_as_uint(__builtin_fmaf(y0, y0, y1 * y1) + __builtin_fmaf(y2, y2, y3 * y3)), false, false);
    const float x = __uint_as_float(e[0]) + __uint_as_float(e[1]);
    const auto f = __builtin_amdgcn_permlane32_swap(__float_as_uint(x), __float_as_uint(x), false, false);
    return __uint_as_float(f[0]) + __uint_as_float(f[1]);
}
__device__ __forceinline__ unsigned m_idx(int ln) { return (ln & 2) ? 0xEEEEu : 0x4444u; }
__device__ __forceinline__ f32x4 zero4() { f32x2 a, b; asm volatile("v_mov_b64 %0, 0\n\tv_mov_b64 %1, 0\n\ts_nop 0" : "=v"(a), "=v"(b)); return (f32x4){a[0], a[1], b[0], b[1]}; }
__device__ __forceinline__ u32x4 ones4() { u32x2 a, b; asm volatile("v_mov_b64 %0, -1\n\tv_mov_b64 %1, -1" : "=v"(a), "=v"(b)); return (u32x4){a[0], a[1], b[0], b[1]}; }
__device__ __forceinline__ f32x2 fold2(f32x4 v) { return (f32x2){v[0], v[1]} + (f32x2){v[2], v[3]}; }
__device__ __forceinline__ unsigned fold_pk(f32x4 v) { float x = v[0] + v[2]; PIN(x); return pk2(x, v[1] + v[3]); }
__device__ __forceinline__ float muls(float x, float y) { float r; asm("v_mul_f32 %0, %1, %2" : "=v"(r) : "v"(x), "v"(y)); return r; }
#define LDSB() __builtin_amdgcn_readfirstlane((unsigned)(uintptr_t)(__attribute__((address_space(3))) unsigned char*)lds)
__device__ __forceinline__ int xp_of(int h) { return 3 * (h >> 1) + (h & 1); }
__device__ __forceinline__ unsigned um_base(unsigned long long sd, unsigned pol, unsigned c) {
    const unsigned s = (unsigned)(sd >> c) & 1u; const unsigned long long same = (s ? sd : ~sd) & ((1ull << c) - 1ull);
    const unsigned o = FR_UM + ((unsigned)__popcll(same) << 16);
    return o + (((s ^ (pol >> (o >> 21))) & 1u) << 13);
}
__device__ __forceinline__ unsigned long long um_sides(KA& a) {
    const rsrc_t rfl = MKR((a.ws + WS_HOP), F_ERR); const unsigned l = lane_id(), off = F_SD + (l < 40 ? l : 0) * 4;
    wait_set(rfl, off, (a.ws + WS_HOP), 2);
    return __ballot(ld4c(rfl, off) == 2u);
}

#define ROW2(op, a, b) asm volatile("s_nop 1\n\t" op " %0, %0, %0 row_ror:8 row_mask:0xf bank_mask:0xf\n\t" op " %1, %1, %1 row_ror:8 row_mask:0xf bank_mask:0xf\n\ts_nop 0\n\t" \
    op " %0, %0, %0 row_ror:4 row_mask:0xf bank_mask:0xf\n\t" op " %1, %1, %1 row_ror:4 row_mask:0xf bank_mask:0xf\n\ts_nop 0\n\t" \
    op " %0, %0, %0 row_ror:2 row_mask:0xf bank_mask:0xf\n\t" op " %1, %1, %1 row_ror:2 row_mask:0xf bank_mask:0xf\n\ts_nop 0\n\t" \
    op " %0, %0, %0 row_ror:1 row_mask:0xf bank_mask:0xf\n\t" op " %1, %1, %1 row_ror:1 row_mask:0xf bank_mask:0xf" : "+v"(a), "+v"(b))
template <int W> __device__ __forceinline__ void s_run(KA& a, unsigned char* lds, const int h, const u32x8 (&wr)[8][3]) {
    constexpr int wave = W; const int lane = lane_id();
    const unsigned ldsb = LDSB();
    const rsrc_t rhx = MKR(al2m((a.ws + WS_FR)), FR_USED);
    constexpr int L_MEM = 0, L_Q = 10240, L_K = 13312, L_KX = 16384, L_TIM = 28672, L_W = 32768;
    lds_u32* tim = (lds_u32*)(lds + L_TIM + 256);
    if (W == 0) { tim[0] = 0; tim[1] = 0; tim[2] = 0; tim[4] = 0; }
    const unsigned pol = page_pol(a), sx = ((my_side() ^ pol) & 1u) << 13;
    unsigned po = (2 + (lane >> 5)) * 16384 + h * 256 + ((lane >> 4) & 1) * 128 + (lane & 8) * 4 + (lane & 2) * 8 + (lane & 4) * 2 + (lane & 1) * 8192;
    unsigned pm = 4 * 16384 + (lane >> 5) * 2048 + (h >> 4) * 1024 + ((h >> 1) & 3) * 256 + ((lane >> 3) & 3) * 64 + (h & 8) * 4 + (lane & 4) * 4 + (h & 1) * 8 + (lane & 2) * 2 + (lane & 1) * 8192;
    const int xj = xp_of(h);
    unsigned idx = m_idx(lane);
    PIN(idx); if (W < 2) { PIN(po); PIN(pm); }
#pragma unroll 1
    for (int layer = 0; layer < 2; ++layer) {
    unsigned char* hop = (a.ws + WS_HOP) + layer * HOP_BYTES;
    const rsrc_t rfl = MKR(hop, F_ERR), rst = MKR((a.ws + WS_ST), ST_BYTES);
    const unsigned sb0 = (unsigned)layer * (20u << 14);
    auto kx_fetch = [&](int t, bool wait) {
        int ln = lane_id();
        if (wait) wait_set(rfl, F_XP + (t * NXP_ + xj) * FLP, hop, 0);
        dma16cn<12>(rst, ln * 16, ST_KX + ((t & (KXR - 1)) * 32 + h) * 12288, ldsb + L_KX);
    };
    if (W == 3 && lane_id() == 0) STAMP(100 + h);
    bool early = false;
    if (W == 2) { early = __all(ld4c(rfl, F_XP + xj * FLP) != 0u); if (early) kx_fetch(0, false); }
    VMKNOWN();
    unsigned tb = 0, cC1 = 0, cP = 0, kdur = 0, krt = 0;
#pragma unroll 1
    for (int s = 0; s < NSEG_; ++s) {
        int ln = lane_id();
        ATV;
        const unsigned sb = sb0 + (unsigned)(s & 3) * (5u << 14);
        unsigned qo = (W < 4 ? L_Q + ((ln >> 3) & 1) * 256 + (ln >> 4) * 64 + ((ln >> 1) & 1) * 32 + (ln & 1) * 16 + ((ln >> 2) & 1) * 8
                             : L_K + ((ln & 15) >> 2) * 128 + (((ln >> 4) * 2 + (ln & 1) + (ln & 4)) & 7) * 16 + (ln & 2) * 2);
        unsigned sel = (ln & 1) ? 0x03020706u : 0x05040100u, w2o = L_W + 65536 + wave * 2048 + ln * 16;
        PIN(qo); PIN(sel); PIN(w2o);
        unsigned char* qp = lds + qo;
        u32x8 wl[3];
        const unsigned char* wp = lds + L_W + wave * 2048 + ln * 16;
        auto w_lds = [&](int f) { const unsigned char* p = f < 4 ? wp + f * 16384 : lds + w2o + (f - 4) * 16384; return cat8(lds16(p), lds16(p + 1024)); };
#pragma unroll
        for (int u = 0; u < 3; ++u) wl[u] = w_lds(1 + u);
        f32x4 acc[3] = {zero4(), zero4(), zero4()};
        unsigned tp = s == 0 ? rt_now() : tb + cP;
#define G3(A, Wt) do { acc[0] = smf(A, Wt[0], acc[0], idx); acc[1] = smf(A, Wt[1], acc[1], idx); acc[2] = smf(A, Wt[2], acc[2], idx); PIN(acc[0]); PIN(acc[1]); PIN(acc[2]); SBAR(); } while (0)
#define S1(u, A, B) do { acc[u] = smf(A, B, acc[u], idx); PIN(acc[u]); SBAR(); } while (0)
        u32x4 m0, n0, n1; unsigned nw = 0;
        unsigned char* mp = lds + L_MEM + ln * 16;
        if (W != 1) {
            constexpr int B0 = W == 7 ? 8 : W, B1 = W == 3 ? 7 : (W == 4 ? 1 : 9); constexpr bool TWO = W >= 3 && W <= 5;
            unsigned mo = sx + ln * 16;
            const unsigned to = sx + M_STAMP + min(ln, 17) * 64 + (s >> 2) * 4;
            PIN(wl[0]); PIN(wl[1]); PIN(wl[2]); PIN(mo);
            u32x4 m1; unsigned tw = 0;
            auto fetch = [&](u32x4& x0, u32x4& x1, unsigned& xw) { CBAR();
                x0 = __builtin_amdgcn_raw_buffer_load_b128(rhx, mo, sb + (B0 < 8 ? B0 * 1024 : 16384), 16);
                if (TWO) x1 = __builtin_amdgcn_raw_buffer_load_b128(rhx, mo, sb + (B1 < 8 ? B1 * 1024 : 17408), 16);
                if (W == 2) xw = __builtin_amdgcn_raw_buffer_load_b32(rhx, to, sb, 16);
                CBAR(); };
            auto take = [&](const u32x4& x0, const u32x4& x1, unsigned xw) {
                *reinterpret_cast<u32x4*>(mp + B0 * 1024) = x0;
                if (TWO) *reinterpret_cast<u32x4*>(mp + B1 * 1024) = x1;
                if (W == 2) *reinterpret_cast<unsigned*>(lds + L_TIM + ln * 4) = xw;
                return __all(max(max(umax4(x0), TWO ? umax4(x1) : 0u), xw) != 0xffffffffu); };
            rt_fine<23>(tp); fetch(m0, m1, tw); __builtin_amdgcn_s_sleep(DPS); fetch(n0, n1, nw);
            if (__builtin_expect(!take(m0, m1, tw), 0)) {
                tim[2] = (unsigned)s + 1u;
                for (unsigned sp = 0;; ++sp) {
                    fetch(m0, m1, tw);
                    if (take(n0, n1, nw)) { m0 = n0; break; }
                    if ((sp & 63u) == 63u && (sp > SPIN_CAP || tim[0])) { tim[0] = 1; break; }
                    if (sp > RTS) __builtin_amdgcn_s_sleep(RTL);
                    fetch(n0, n1, nw);
                    if (take(m0, m1, tw)) break;
                }
                VMKNOWN();
            }
        }
        if (W == 0) __builtin_amdgcn_s_setprio(SPRIO);
        AT(2);
        LBAR();
        AT(3);
        {
            constexpr int F = W == 7 ? 0 : W, R1 = F == 0 ? 1 : 0, R2 = F <= 1 ? 2 : 1, R3 = F <= 2 ? 3 : 2, R4 = F <= 3 ? 4 : 3, R5 = F <= 4 ? 5 : 4, R6 = F <= 5 ? 6 : 5;
            if (W == 1) { m0 = lds16(mp + 1024); SBAR(); }
            u32x4 A0 = lds16(mp + (W == 7 ? 0 : 8) * 1024); SBAR(); u32x4 A1 = lds16(mp + R1 * 1024); SBAR(); u32x4 A2 = lds16(mp + R2 * 1024); SBAR();
            u32x4 A3;
            if (W != 7) { G3(m0, wr[F]); A3 = lds16(mp + 9 * 1024); SBAR(); }
            { const u32x4 a8 = W == 7 ? m0 : A0;
              S1(0, a8, wl[0]); wl[0] = w_lds(4); SBAR(); S1(1, a8, wl[1]); wl[1] = w_lds(5); SBAR(); S1(2, a8, wl[2]); wl[2] = w_lds(6); SBAR(); }
            if (W == 7) { A3 = lds16(mp + 9 * 1024); SBAR(); G3(A0, wr[F]); }
            A0 = lds16(mp + R3 * 1024); SBAR();
            G3(A1, wr[R1]); A1 = lds16(mp + R4 * 1024); SBAR();
            if (W != 1) asm volatile("" :: "v"(n0)); if (W >= 3 && W <= 5) asm volatile("" :: "v"(n1)); if (W == 2) asm volatile("" :: "v"(nw));
            SBAR();
            G3(A2, wr[R2]); A2 = lds16(mp + R5 * 1024); SBAR();
            S1(1, A3, wl[1]); wl[1] = w_lds(0); SBAR(); S1(2, A3, wl[2]); wl[2] = w_lds(7); SBAR(); S1(0, A3, wl[0]); A3 = lds16(mp + R6 * 1024); SBAR();
            G3(A0, wr[R3]); A0 = lds16(mp + 7 * 1024); SBAR();
            G3(A1, wr[R4]); G3(A2, wr[R5]);
            auto put = [&](int u) { const int T = 3 * (W & 3) + u; const f32x2 v = fold2(acc[u]); const unsigned o = pk2(v[0], v[1]);
                *reinterpret_cast<unsigned*>(qp + (T >> 1) * 512 + (T & 1) * (W < 4 ? 4 : 8)) = __builtin_amdgcn_perm(dppu<0xB1>(o), o, sel); };
            acc[0] = smf(A3, wr[R6][0], acc[0], idx); acc[1] = smf(A3, wr[R6][1], acc[1], idx); acc[0] = smf(A0, wr[7][0], acc[0], idx);
            acc[2] = smf(A3, wr[R6][2], acc[2], idx); acc[1] = smf(A0, wl[1], acc[1], idx); PIN(acc[0]); PIN(acc[1]); PIN(acc[2]); SBAR();
            put(0); SBAR();
            acc[2] = smf(A0, wl[2], acc[2], idx); PIN(acc[2]); SBAR();
            put(1); SBAR(); put(2); SBAR(); AT(4);
        }
        if (s == 0) { if (W == 2 && !early) { kx_fetch(0, true); VMDRAIN(); } LBAR(); }
        if (W == 0 && s + 1 < NSEG_) {
            PIN(ln);
            unsigned kp = ldsb + L_KX + ln * 16, mk = ldsb + L_K + (ln >> 4) * 128 + ((ln + ((ln >> 2) & 4)) & 7) * 16;
            u32x8 ka = cat8(lds16a(kp), lds16a(kp + 1024)), kb = cat8(lds16a(kp + 6144), lds16a(kp + 7168)), kc = cat8(lds16a(kp + 2048), lds16a(kp + 3072)), kd = cat8(lds16a(kp + 8192), lds16a(kp + 9216));
            f32x4 sc[3] = {zero4(), zero4(), zero4()};
            const bool up = (ln & 8) != 0;
            PIN(ka); PIN(kb); PIN(kc); PIN(kd); PIN(sc[0]); PIN(sc[1]); PIN(sc[2]); PIN(kp); PIN(mk);
            __builtin_amdgcn_s_setprio(3);
            LBAR();
            AT(5);
            const u32x4 q0 = lds16a(kp + (L_Q - L_KX)); SBAR(); u32x8 ke = cat8(lds16a(mk), lds16a(mk + 512)); SBAR(); const u32x4 q1 = lds16a(kp + (L_Q - L_KX + 1024)); SBAR(); const u32x4 q2 = lds16a(kp + (L_Q - L_KX + 2048)); SBAR();
            sc[0] = smf(q0, ka, sc[0], idx); sc[1] = smf(q0, kb, sc[1], idx); PIN(sc[0]); PIN(sc[1]); SBAR();
            ka = cat8(lds16a(mk + 1024), lds16a(mk + 1536)); SBAR(); kb = cat8(lds16a(kp + 4096), lds16a(kp + 5120)); SBAR();
            sc[2] = smf(q0, ke, sc[2], idx); PIN(sc[2]); SBAR();
            ke = cat8(lds16a(kp + 10240), lds16a(kp + 11264)); SBAR();
            sc[0] = smf(q1, kc, sc[0], idx); PIN(sc[0]); SBAR();
            kc = cat8(lds16a(mk + 2048), lds16a(mk + 2560)); SBAR();
            sc[1] = smf(q1, kd, sc[1], idx); sc[2] = smf(q1, ka, sc[2], idx); sc[0] = smf(q2, kb, sc[0], idx); sc[1] = smf(q2, ke, sc[1], idx); sc[2] = smf(q2, kc, sc[2], idx); SBAR();
            AT(6);
            const f32x2 s0 = fold2(sc[0]), s1 = fold2(sc[1]), s2 = fold2(sc[2]);
            float ma = __builtin_fmaxf(__builtin_fmaxf(s0[0], s1[0]), s2[0]), mb = __builtin_fmaxf(__builtin_fmaxf(s0[1], s1[1]), s2[1]);
            ROW2("v_max_f32_dpp", ma, mb);
            const float c1 = 0.07216878364870322f * 1.4426950408889634f;
            ma *= -c1; mb *= -c1;
            const float a0 = __builtin_amdgcn_exp2f(__builtin_fmaf(s0[0], c1, ma)), b0 = __builtin_amdgcn_exp2f(__builtin_fmaf(s0[1], c1, mb));
            const float a1 = __builtin_amdgcn_exp2f(__builtin_fmaf(s1[0], c1, ma)), b1 = __builtin_amdgcn_exp2f(__builtin_fmaf(s1[1], c1, mb));
            const float a2 = __builtin_amdgcn_exp2f(__builtin_fmaf(s2[0], c1, ma)), b2 = __builtin_amdgcn_exp2f(__builtin_fmaf(s2[1], c1, mb));
            float za = __builtin_fmaf(a2, 0.5f, a0 + a1), zb = __builtin_fmaf(b2, 0.5f, b0 + b1);
            ROW2("v_add_f32_dpp", za, zb);
            za = __builtin_amdgcn_rcpf(za); zb = __builtin_amdgcn_rcpf(zb);
            const unsigned da = pk2(a0 * za, a1 * za), db = pk2(b0 * zb, b1 * zb);
            const float vm = up ? b2 * zb : a2 * za;
            u32x2 oa = (u32x2){dppu<0xA0>(da), dppu<0xF5>(da)}, ob = (u32x2){dppu<0xA0>(db), dppu<0xF5>(db)}; unsigned om = pk2(dppf<0xA0>(vm), dppf<0xF5>(vm));
            PIN(oa); PIN(ob); PIN(om);
            __builtin_amdgcn_raw_buffer_store_b64(oa, rhx, po, sb, 16);
            __builtin_amdgcn_raw_buffer_store_b64(ob, rhx, po + 64, sb, 16);
            __builtin_amdgcn_raw_buffer_store_b32(om, rhx, pm, sb, 16);
            SBAR(); AT(7);
            tim[1] = rt_now() - (tb + cP);
            tim[4] = (unsigned)s + 1u;
            __builtin_amdgcn_s_setprio(0);
            PIN(ln);
            tt_step(*reinterpret_cast<const unsigned*>(lds + L_TIM + ln * 4), s, s == 0 ? rt_now() - (W == 0 ? TT_E1 : TT_E1 - 35) : 0u, tb, cC1, cP);
        } else {
            if (W == 1) VMDRAIN();
            LBAR();
            AT(5);
            tt_step(*reinterpret_cast<const unsigned*>(lds + L_TIM + ln * 4), s, s == 0 ? rt_now() - (W == 0 ? TT_E1 : TT_E1 - 35) : 0u, tb, cC1, cP);
            if (W == 2 && s + 1 < NSEG_) wait_set(rfl, F_XP + ((s + 1) * NXP_ + xj) * FLP, hop, 0);
            if ((W == 1 || W == 2) && s + 1 < NSEG_) { for (unsigned sp = 0; tim[4] != (unsigned)s + 1u && sp < SPIN_CAP; ++sp) __builtin_amdgcn_s_sleep(2); CBAR(); if (W == 1 && SLATE1) __builtin_amdgcn_s_sleep(SLATE1); if (W == 2 && SLATE2) __builtin_amdgcn_s_sleep(SLATE2); }
            if (W == 1) {
                kdur = min(__builtin_amdgcn_readfirstlane(tim[1]), 4095u); krt = __builtin_amdgcn_readfirstlane(tim[2]) == (unsigned)s + 1u ? 1u : 0u;
#pragma unroll
                for (int m = 0; m < 3; ++m) st16s(lds16(lds + L_K + m * 1024 + ln * 16), rst, ln * 16, ST_KM + ((s & (PR - 1)) * 32 + h) * 3072 + m * 1024);
                {
                    const unsigned sr = sb0 + (unsigned)((s + 2) & 3) * (5u << 14);
                    __builtin_amdgcn_raw_buffer_store_b64((u32x2){~0u, ~0u}, rhx, po, sr, 16); __builtin_amdgcn_raw_buffer_store_b64((u32x2){~0u, ~0u}, rhx, po, sr + 64, 16);
                    __builtin_amdgcn_raw_buffer_store_b32(~0u, rhx, pm, sr, 16);
                }
                VMW(3); if (ln == 0) st16c(rfl, F_KM + (s * 32 + h) * FLP, (u32x4){cP, kdur, krt, tb}); if (ln == 0 && h == 0 && s >= 56) STAMP(370 + s - 56);
            }
            if (W == 2 && s + 1 < NSEG_) dma16cn<12>(rst, ln * 16, ST_KX + (((s + 1) & (KXR - 1)) * 32 + h) * 12288, ldsb + L_KX);
        }
        ATD(0, s == 41 && h == 0);
    }
    if (W == 0) { tim[2] = 0; tim[4] = 0; }
    if (W == 1 && lane_id() == 0) STAMP(512 + blockIdx.x);
    }
    if (W == 0 && tim[0]) set_err((a.ws + WS_HOP));
}

__device__ __forceinline__ void s_part(KA& a, unsigned char* lds, const int h, const int wave) {
    const int lane = lane_id();
    const unsigned ldsb = LDSB();
    u32x8 wr[8][3];
    auto keep = [&](int f, u32x8 v) { unsigned char* p = lds + 32768 + f * 16384 + wave * 2048 + lane * 16; *reinterpret_cast<u32x4*>(p) = lo4(v); *reinterpret_cast<u32x4*>(p + 1024) = hi4(v); };
    load_w<10>(MKR(a.wq, D_ * HE_ * 2), MKR(a.wk, D_ * HE_ * 2), 0u, h * 192, lds, ldsb, wave, lane, [&](int rd, int u, u32x8 v) { if (rd >= 8) keep(3 * rd - 23 + u, v); else if (rd == 7 && u == 2) keep(7, v); else wr[rd][u] = v; });
    keep(0, wr[7][1]);
    if (wave == 7) s_run<7>(a, lds, h, wr); else if (wave == 6) s_run<6>(a, lds, h, wr); else if (wave == 5) s_run<5>(a, lds, h, wr); else if (wave == 4) s_run<4>(a, lds, h, wr);
    else if (wave == 3) s_run<3>(a, lds, h, wr); else if (wave == 2) s_run<2>(a, lds, h, wr); else if (wave == 1) s_run<1>(a, lds, h, wr); else s_run<0>(a, lds, h, wr);
}

template <bool XW> __device__ __forceinline__ void v_run(KA& a, unsigned char* lds, const int h, const int wave) {
    const int lane = lane_id(), tid = wave * 64 + lane;
    const unsigned ldsb = LDSB();
    constexpr int L_MEM = 0, L_V = 10240, L_VX = 13312, L_TIM = 25600, L_E = 26624, L_F = L_E + 1024, L_T = L_F + 2048, L_W = 16384, NO = XW ? 4 : 6, NV = XW ? 2 : 1, KR = XW ? 5 : 4;
    constexpr int NBK = NO - 1;
    u32x8 wo[NO][3], wv[KR][NV];
    const int vt = XW ? 2 * wave : 4 + wave;
    {
        const rsrc_t rv = MKR(a.wv, D_ * HE_ * 2), ro = MKR(a.wo, HE_ * D_ * 2);
        const unsigned l15 = lane & 15, lg = lane >> 4, row = 8 * wave + (lane >> 3), ch = ((lane & 7) ^ (wsw(row) << 1)) * 16;
        const unsigned vo = row * (HE_ * 2) + h * 384 + ch, oo = (h * 192 + row) * (D_ * 2) + ch;
        u32x8 tmp[2][NV];
        load_b<6, 3, 10, L_W, 2>(ldsb, wave,
            [&](int rd, int j, unsigned dst) { if (rd < 5) dma16(ro, oo, (j >> 1) * (64 * D_ * 2) + rd * 256 + (j & 1) * 128, dst); else dma16(rv, vo, ((rd - 5) * 2 + j / 3) * (64 * HE_ * 2) + (j % 3) * 128, dst); },
            [&](int rd, int base) {
                if (rd < 5) {
#pragma unroll
                    for (int i = 0; i < (rd != 3 ? 1 : (XW ? 0 : 2)); ++i) { const int tl = rd != 3 ? wave : 2 * (wave - 4) + i;
#pragma unroll
                        for (int k = 0; k < 3; ++k) { const unsigned char* p = lds + base + (2 * k + (tl >> 2)) * 8192; wo[rd < 3 ? rd : (rd == 3 ? 4 + i : 3)][k] = cat8(ld_frag(p, 4 * lg, 16, tl & 3, l15), ld_frag(p, 32 + 4 * lg, 16, tl & 3, l15)); } }
                } else {
#pragma unroll
                    for (int k = 0; k < 2; ++k)
#pragma unroll
                        for (int u = 0; u < NV; ++u) { const unsigned char* p = lds + base + (3 * k + (vt >> 2)) * 8192; const u32x8 v = cat8(ld_frag(p, 8 * lg, 4, (vt & 3) + u, l15), ld_frag(p, 32 + 8 * lg, 4, (vt & 3) + u, l15));
                            if (2 * (rd - 5) + k < KR) wv[2 * (rd - 5) + k][u] = v; else tmp[k][u] = v; } } },
            [&](int rd) {
                if (rd >= 7) {
#pragma unroll
                    for (int k = 0; k < 2; ++k)
#pragma unroll
                        for (int u = 0; u < NV; ++u) if (2 * (rd - 5) + k >= KR) { unsigned char* p = lds + L_W + (2 * (rd - 5) + k - 4) * 24576 + (vt + u) * 2048 + lane * 16; *reinterpret_cast<u32x4*>(p) = lo4(tmp[k][u]); *reinterpret_cast<u32x4*>(p + 1024) = hi4(tmp[k][u]); } } });
    }
    const rsrc_t rhx = MKR(al2m((a.ws + WS_FR)), FR_USED), rst = MKR((a.ws + WS_ST), ST_BYTES);
    lds_u32* tim = (lds_u32*)(lds + L_TIM);
    if (tid == 0) { tim[0] = 0; tim[2] = 0; }
    const unsigned pol = page_pol(a), sx = ((my_side() ^ pol) & 1u) << 13;
    unsigned uo0, uo1 = 0, l16 = lane * 16;
    { const unsigned long long sd = um_sides(a);
      uo0 = um_base(sd, pol, 8 * (lane >> 4) + (lane < 48 ? 0 : 8) + wave) + h * 256 + (lane & 15) * 16;
      if (!XW) uo1 = um_base(sd, pol, 16 + 2 * wave + (lane >> 5)) + h * 256 + (lane & 15) * 16 + (lane & 16) / 2; }
    auto arm = [&](unsigned so) { const u32x4 on = ones4(); st16s(on, rhx, uo0, so); if (!XW) __builtin_amdgcn_raw_buffer_store_b64((u32x2){on.x, on.y}, rhx, uo1, so, 16); SBAR(); };
    arm(16384);
    const int xj = xp_of(h), xj2 = 3 * (h >> 1) + 2;
    if (XW) uo1 = wave < 2 ? l16 + 16384 + wave * 1024 : M_STAMP + min(lane, 17) * 64;
    PIN(uo0); PIN(uo1); PIN(l16);
    *reinterpret_cast<u32x4*>(lds + L_T + l16) = (u32x4){L_V + ((lane >> 3) & 1) * 256u + (lane >> 4) * 64u + ((lane >> 1) & 1) * 32u + (lane & 1) * 16u + ((lane >> 2) & 1) * 8u,
        (lane & 1) ? 0x03020706u : 0x05040100u, m_idx(lane), F_XP + ((lane & 1) ? xj : xj2) * FLP};
    VMKNOWN();
    ATV;
#define la ((int)l16)
    auto u_so = [&](int t, int i) { return ST_U + (((t & (URING - 1)) * 40 + (i < 3 ? 8 * i + wave : 13 + 2 * wave + i)) * 32 + h) * 1024; };
    u32x4 xf[6][2];
    auto back0 = [&](int q) { xf[q >> 1][q & 1] = lds16a(la + L_VX + (q & 1) * 6144 + (q >> 1) * 1024); SBAR(); };
    auto back = [&](int t, auto&& side) {
        f32x4 acc[2][2]; u32x4 o; unsigned so;
#pragma unroll
        for (int i = 0; i < NBK; ++i) {
#pragma unroll
            for (int j = 0; j < 12; ++j) { const int k = j >> 1, r = j & 1;
                acc[i & 1][r] = dmf(xf[k][r], (k & 1) ? hi4(wo[i + (i > 2)][k >> 1]) : lo4(wo[i + (i > 2)][k >> 1]), k ? acc[i & 1][r] : Z4); PIN(acc[i & 1][r]); SBAR();
                if (i == 0) { side(j); if (j + 4 < 12) back0(j + 4); }
                else { const f32x4 c0 = acc[(i - 1) & 1][0], c1 = acc[(i - 1) & 1][1];
                    if (j == 2) { so = u_so(t, i - 1); asm volatile("" : "+s"(so)); }
                    if (j == 3) { o.x = pk2(c0[0], c1[0]); PIN(o.x); } if (j == 4) { o.y = pk2(c0[2], c1[2]); PIN(o.y); } if (j == 5) { o.z = pk2(c0[1], c1[1]); PIN(o.z); } if (j == 6) { o.w = pk2(c0[3], c1[3]); PIN(o); }
                    if (j == 7) __builtin_amdgcn_raw_buffer_store_b128(o, rst, l16, so, 16);
                    if (j == 9) asm volatile("" :: "v"(o));
                    if (j == 10 && i == NBK - 1) { so = u_so(t, i); asm volatile("" : "+s"(so)); }
                }
                SBAR(); }
        }
        const f32x4 c0 = acc[(NBK - 1) & 1][0], c1 = acc[(NBK - 1) & 1][1];
        st16s((u32x4){pk2(c0[0], c1[0]), pk2(c0[2], c1[2]), pk2(c0[1], c1[1]), pk2(c0[3], c1[3])}, rst, l16, so); };
    auto v_so = [&](int t) { return ST_VX + ((t & (KXR - 1)) * 32 + h) * 12288 + (wave - 4) * 3072; };
#pragma unroll 1
    for (int layer = 0; layer < 2; ++layer) {
    unsigned char* hop = (a.ws + WS_HOP) + layer * HOP_BYTES;
    const rsrc_t rfl = MKR(hop, F_ERR);
    const unsigned sb0 = (unsigned)layer * (20u << 14) + sx;
    if (wave == 0 && l16 == 0) STAMP(140 + h);
    unsigned tb = rt_now(), cC1 = 0, cP = 0, tn = tb, krt = 0, te = 0;
#pragma unroll 1
    for (int s = 0; s < NSEG_; ++s) {
        PIN(l16);
        int w0o = la + L_W + vt * 2048, w1o = w0o + 3 * 24576; unsigned us = (s & 3) << 14;
        PIN(w0o); PIN(w1o); asm volatile("" : "+s"(us));
        const u32x4 lc = lds16a(la + L_T);
        unsigned fw = 1u;
        if (!XW) fw = lds4a(L_F + wave * 256 + (l16 >> 2));
        SBAR();
        auto w_h = [&](int kb, int u) { return lds16a((kb < 7 ? w0o + (kb - 4) * 24576 : w1o + (kb - 7) * 24576) + u * 1024); };
        constexpr int NL = 4;
        u32x4 wl[NL][2];
#pragma unroll
        for (int u = 0; u < 2 * NL; ++u) wl[u >> 1][u & 1] = XW ? w_h(5 + (u >> 2), u & 3) : w_h(4 + (u >> 1), u & 1);
        f32x4 acc[NV];
#pragma unroll
        for (int u = 0; u < NV; ++u) acc[u] = zero4();
        const unsigned sel = lc.y, idx = lc.z;
        unsigned d0 = 0, d1 = 0;
        SBAR();
        if (XW) {
            unsigned s0 = sb0 + (unsigned)(s & 3) * (5u << 14), s1 = s0 + wave * 1024, s2 = s1 + 4096;
            int mw = la + wave * 1024, me = mw + (wave < 2 ? 8192 : L_E - 2048);
            if (wave >= 2) s0 += (s >> 2) * 4;
            const unsigned em = wave < 2 ? ~0u : 0u;
            PIN(mw); PIN(me); asm volatile("" : "+s"(s0), "+s"(s1), "+s"(s2));
            u32x4 m0, m1, m2;
            auto fetch = [&]() { CBAR();
                m0 = __builtin_amdgcn_raw_buffer_load_b128(rhx, l16, s1, 16); SBAR(); m1 = __builtin_amdgcn_raw_buffer_load_b128(rhx, l16, s2, 16); SBAR(); m2 = __builtin_amdgcn_raw_buffer_load_b128(rhx, uo1, s0, 16);
                CBAR(); SBAR(); };
            auto take = [&]() { sts16a(mw, m0); sts16a(mw + 4096, m1); sts16a(me, m2); SBAR();
                return __all(max(max(umax4(m0), umax4(m1)), max(m2.x, max(max(m2.y, m2.z), m2.w) & em)) != 0xffffffffu); };
            AT(0);
            rt_fine<21>(tb + cP + TT_VD);
            AT(6);
            fetch();
            if (__builtin_expect(!take(), 0)) {
                tim[2] = (unsigned)s + 1u;
                for (unsigned sp = 0;; ++sp) {
                    fetch();
                    if (take()) break;
                    if ((sp & 63u) == 63u && (sp > SPIN_CAP || tim[0])) { tim[0] = 1; break; }
                    if (sp > 4) __builtin_amdgcn_s_sleep(RTL);
                }
            }
        } else {
            if (__builtin_expect(s > 0 && !__all(fw != 0u), 0)) { wait_set(rfl, lc.w + min(s + 2, NSEG_ - 1) * (NXP_ * FLP), hop, 0); VMKNOWN(); }
            d0 = v_so(s + 2); d1 = min(s + 3, NSEG_ - 1) * (NXP_ * FLP); asm volatile("" : "+s"(d0), "+s"(d1));
        }
        AT(1);
        if (XW) { LBAR(); __builtin_amdgcn_s_setprio(0); } else { __builtin_amdgcn_s_setprio(YP1); CBAR(); __builtin_amdgcn_s_barrier(); CBAR(); }
        AT(2);
        f32x4 um[NO]; unsigned kdur = 0;
        {
#define VS(u, A, B) do { acc[(u) % NV] = smf(A, B, acc[(u) % NV], idx); PIN(acc[(u) % NV]); SBAR(); } while (0)
#define VL(u) cat8(wl[(u) % NL][0], wl[(u) % NL][1])
#define VA(k) lds16a(la + L_MEM + (k) * 1024); SBAR()
#define VW(u, kb, q) wl[(u) % NL][(q) & 1] = w_h(kb, q); SBAR()
            unsigned tw, tr; int qo;
            if (XW) {
                u32x4 A0 = VA(5); u32x4 A1 = VA(6); u32x4 A2 = VA(0); u32x4 A3 = VA(1);
                VS(0, A0, VL(0)); VS(1, A0, VL(1)); VW(0, 7, 0);
                VS(0, A1, VL(2)); VW(0, 7, 1); A0 = VA(2); VS(1, A1, VL(3)); VW(1, 7, 2);
                VS(0, A2, wv[0][0]); VW(1, 7, 3); A1 = VA(7); VS(1, A2, wv[0][NV - 1]); VW(2, 8, 0);
                VS(0, A3, wv[1][0]); VW(2, 8, 1); A2 = VA(8); VS(1, A3, wv[1][NV - 1]); VW(3, 8, 2);
                VS(0, A0, wv[2][0]); VW(3, 8, 3); A3 = VA(3); VS(1, A0, wv[2][NV - 1]);
                VS(0, A1, VL(0)); A0 = VA(4); VS(1, A1, VL(1)); VW(0, 9, 0);
                VS(0, A2, VL(2)); VW(0, 9, 1); A1 = VA(9); VS(1, A2, VL(3)); VW(1, 9, 2);
                VS(0, A3, wv[3][0]); VW(1, 9, 3); VS(1, A3, wv[3][NV - 1]); um[0] = zero4(); tw = lds4a(la + L_E); tr = tim[2]; SBAR();
                VS(0, A0, wv[KR - 1][0]); um[1] = zero4(); qo = lc.x + (vt >> 1) * 512; PIN(qo); SBAR(); VS(0, A1, VL(0)); um[2] = zero4(); SBAR(); VS(1, A0, wv[KR - 1][NV - 1]); um[3] = zero4(); SBAR(); VS(1, A1, VL(1));
            } else {
                u32x4 A[3];
                constexpr int ord[10] = {0, 4, 5, 6, 7, 1, 2, 3, 8, 9};
#pragma unroll
                for (int k = 0; k < 3; ++k) { A[k] = VA(ord[k]); }
                dma16cn<3>(rst, l16, d0, ldsb + L_VX + (wave - 4) * 3072); dma4c(rfl, lc.w, d1, ldsb + L_F + wave * 256); SBAR();
#pragma unroll
                for (int k = 0; k < 10; ++k) { VS(0, A[k % 3], (ord[k] < 4 ? wv[ord[k] & 3][0] : VL(ord[k]))); if (k >= 2 && k < 6) { VW((k - 2) >> 1, 8 + ((k - 2) >> 1), k & 1); }
                    if (k + 3 < 10) { A[k % 3] = VA(ord[(k + 3) % 10]); }
                    if (k == 0) { qo = lc.x + ((vt >> 1) * 512 + (vt & 1) * 4); PIN(qo); SBAR(); }
                    if (k >= 6 && k < 9) { um[(2 * k - 12) % NO] = zero4(); um[(2 * k - 11) % NO] = zero4(); SBAR(); } }
            }
            unsigned o[NV];
#pragma unroll
            for (int u = 0; u < NV; ++u) { o[u] = fold_pk(acc[u]); o[u] = __builtin_amdgcn_perm(dppu<0xB1>(o[u]), o[u], sel); }
            if (XW) { *(__attribute__((address_space(3))) u32x2*)(uintptr_t)(unsigned)qo = (u32x2){o[0], o[NV - 1]}; SBAR();
                kdur = min(te - tn, 4095u) + (krt == (unsigned)s ? 4096u : 0u);
                tn = tb + cP; krt = __builtin_amdgcn_readfirstlane(tr);
                tt_step(tw, s, tb, tb, cC1, cP); asm volatile("" : "+s"(tb), "+s"(cC1), "+s"(cP));
            } else { *(__attribute__((address_space(3))) unsigned*)(uintptr_t)(unsigned)qo = o[0]; __builtin_amdgcn_s_setprio(2); }
        }
        AT(3);
        VMDRAIN(); LBAR();
        AT(4);
        {
            const u32x4 q0 = lds16a(la + L_V), q1 = lds16a(la + L_V + 1024), q2 = lds16a(la + L_V + 2048); SBAR();
            if (XW && wave == 3 && s > 0) { if (l16 == 0) st16c(rfl, F_UX + ((s + 1) * 32 + h) * FLP, (u32x4){idx, (kdur + l16) & 4095u, (kdur + l16) >> 12, idx}); SBAR(); }
#define VU(u, q, k) do { um[u] = smf(q, wo[u][k], um[u], idx); PIN(um[u]); SBAR(); } while (0)
#define VO(u) min(fold_pk(um[u]), 0xfffffffeu)
            if (!XW) { VU(NO - 2, q0, 0); VU(NO - 1, q0, 0); VU(NO - 2, q1, 1); VU(NO - 1, q1, 1); VU(NO - 2, q2, 2); VU(NO - 1, q2, 2); }
            VU(0, q0, 0); VU(2, q0, 0);
            unsigned e0 = 0, e1 = 0;
            if (!XW) { e0 = VO(NO - 2); PIN(e0); SBAR(); }
            VU(0, q1, 1);
            if (!XW) { e1 = VO(NO - 1); PIN(e1); SBAR(); }
            VU(2, q1, 1);
            if (!XW) { const auto z = __builtin_amdgcn_permlane32_swap(e0, e1, false, false); __builtin_amdgcn_raw_buffer_store_b64((u32x2){z[0], z[1]}, rhx, uo1, us, 16); SBAR(); }
            VU(0, q2, 2); back0(0); VU(2, q2, 2); back0(1); VU(1, q0, 0); back0(2); VU(1, q1, 1); back0(3);
            e0 = VO(0); PIN(e0); SBAR();
            VU(1, q2, 2);
            e1 = VO(2); PIN(e1); SBAR();
            VU(3, q0, 0);
            const auto x02 = __builtin_amdgcn_permlane32_swap(e0, e1, false, false); SBAR();
            VU(3, q1, 1); VU(3, q2, 2);
            e0 = VO(1); PIN(e0); SBAR();
            const auto x13 = __builtin_amdgcn_permlane32_swap(e0, VO(3), false, false);
            const auto y01 = __builtin_amdgcn_permlane16_swap(x02[0], x13[0], false, false), y23 = __builtin_amdgcn_permlane16_swap(x02[1], x13[1], false, false);
            st16s((u32x4){y01[0], y23[0], y01[1], y23[1]}, rhx, uo0, us);
            SBAR();
            AT(5);
            __builtin_amdgcn_s_setprio(XW ? 2 : 0);
        }
        if (__builtin_expect(s == 0, 0)) {
            arm(2u << 14);
#pragma unroll 1
            for (int t = 0; t < 3; ++t) {
                if (!XW) { wait_set(rfl, lds4a(la + L_T + 12) + t * (NXP_ * FLP), hop, t ? 0 : 1); dma16cn<3>(rst, l16, v_so(t), ldsb + L_VX + (wave - 4) * 3072); VMDRAIN(); }
                LBAR();
                back0(0); back0(1); back0(2); back0(3);
                back(t, [](int) {});
                VMDRAIN(); LBAR();
                if (wave == 0 && l16 == 0) st4c(rfl, F_UX + (t * 32 + h) * FLP, 1u);
                if (wave == 0 && l16 == 0 && h == 0) STAMP(10 + t);
            }
        } else back(s + 2, [&](int j) { if (XW && j == 0) { te = rt_now(); asm volatile("" : "+s"(te)); } if (j == 9) arm(us ^ (2u << 14)); });
        AT(7);
        ATD(1, s == 41 && h == 0);
    }
    if (wave == 0 && l16 == 0) { tim[2] = 0; STAMP(512 + blockIdx.x); }
    }
    if (wave == 0 && l16 == 0 && tim[0]) set_err((a.ws + WS_HOP));
#undef la
}

constexpr int RO_UM = 131072, RO_TIM = RO_UM + 3 * 8192, RO_END = NSEG_;
__device__ __forceinline__ void ro_state(KA& a, unsigned char* lds, const int r, const unsigned pol) {
    constexpr int wave = 0;
    const int lane = lane_id();
    const rsrc_t rhx = MKR(al2m((a.ws + WS_FR)), FR_USED);
    lds_u32* tim = (lds_u32*)(lds + RO_TIM + 256);
    const unsigned sx = ((my_side() ^ pol) & 1u) << 13;
    unsigned po = sx + (2 + (r & 1)) * 16384 + lane * 16, uo = um_base(um_sides(a), pol, r >> 1) + lane * 16;
    unsigned ho = (r < 64 ? 0 : 8192) + r * 128 + (lane >> 5) * 64 + (lane & 2) * 16 + (lane & 16) + (lane & 12) + (lane & 1) * 8192;
    unsigned idx = (lane & 1) ? 0xEEEEu : 0x4444u;
    const unsigned pmo = (2 - (r & 1)) * 16384 + (r & 1) * 2048;
    PIN(po); PIN(uo); PIN(ho); PIN(idx);
#pragma unroll 1
    for (int layer = 0; layer < 2; ++layer) {
    const unsigned sb0 = (unsigned)layer * (20u << 14);
    unsigned tb = 0, cC1 = 0, cP = 0; int u3 = 0;
    u32x8 ux[16];
    auto rearm = [&](int t) { const unsigned sr = sb0 + (unsigned)((t + 2) & 3) * (5u << 14);
        if (r % 5 == 0 && lane < 2 && t + 2 < NSEG_) __builtin_amdgcn_raw_buffer_store_b32(~0u, rhx, M_STAMP + (r / 5) * 64 + lane * 8192, sr + ((t + 2) >> 2) * 4, 16);
        __builtin_amdgcn_raw_buffer_store_b32(~0u, rhx, ho, sr, 16); };
#pragma unroll 1
    for (int t = 0; t < RO_END; ++t) {
        const int s = t - 1;
        ATV;
        if (t > 0) {
            unsigned sb = sb0 + (unsigned)(t & 3) * (5u << 14), sp0 = sb0 + (unsigned)(s & 3) * (5u << 14), sp1 = sp0 + 4096, sp2 = sp0 + pmo, su0 = (s & 3) << 14, su1 = su0 + 4096;
            asm volatile("" : "+s"(sb), "+s"(sp0), "+s"(sp1), "+s"(sp2), "+s"(su0), "+s"(su1));
            u32x4 um[8], p[10];
            auto u_half = [&](int z) {
#pragma unroll
                for (int q = 0; q < 4; ++q) { um[4 * z + q] = __builtin_amdgcn_raw_buffer_load_b128(rhx, uo + q * 1024, z ? su1 : su0, 16); SBAR(); } };
            auto u_load = [&]() { CBAR(); u_half(0); u_half(1); CBAR(); };
            auto u_bad = [&]() { unsigned m = 0;
#pragma unroll
                for (int q = 0; q < 8; ++q) m = max(m, umax4(um[q]));
                return m; };
            unsigned tp = tb + cC1;
            {
                auto p_load = [&]() { CBAR();
#pragma unroll
                    for (int q = 0; q < 10; ++q) { p[q] = __builtin_amdgcn_raw_buffer_load_b128(rhx, po + (q & 3) * 1024, q < 4 ? sp0 : (q < 8 ? sp1 : sp2), 16); SBAR(); if (TT_UM < 0 && q >= 8) u_half(q - 8); }
                    CBAR(); };
                unsigned o;
                auto p_use = [&](f32x4 c0, f32x4 c1) {
#pragma unroll
                    for (int q = 0; q < 8; ++q) { c0 = smf(p[q], ux[2 * q], c0, idx); c1 = smf(p[q], ux[2 * q + 1], c1, idx); SBAR(); }
#pragma unroll
                    for (int q = 0; q < 2; ++q) { c0 = smf(p[8 + q], cat8(um[4 * q], um[4 * q + 1]), c0, idx); c1 = smf(p[8 + q], cat8(um[4 * q + 2], um[4 * q + 3]), c1, idx); }
                    SBAR();
                    float v0 = c0[0] + c0[1]; PIN(v0);
                    const float v = v0 + (c1[2] + c1[3]);
                    o = pk2(dppf<0xA0>(v), dppf<0xF5>(v));
                    return !__any(v != v); };
                f32x4 c0 = zero4(), c1 = zero4();
                { const unsigned n = rt_now(); if ((int)(n - tp) > 4) { tp = n; tim[2] = (unsigned)t; } }
                AT(0);
                if (TT_UM < 0) rt_fine<23>(tp); else { rt_fine<23>(tp - TT_UM); u_load(); if (FINE) asm volatile(".rept %0\n\ts_nop 1\n\t.endr" :: "i"(TT_UM < 0 ? 0 : (TT_UM * 23 - 88) / 8) : "memory"); else rt_wait(tp); }
                AT(1); p_load();
                if (__builtin_expect(!p_use(c0, c1), 0)) {
                    tim[2] = (unsigned)t;
                    for (unsigned sp = 0;; ++sp) {
                        if (TT_UM >= 0) u_load();
                        p_load();
                        if (p_use(zero4(), zero4())) break;
                        unsigned m = u_bad();
#pragma unroll
                        for (int q = 0; q < 10; ++q) m = max(m, umax4(p[q]));
                        if (__all(m != 0xffffffffu)) break;
                        if ((sp & 63u) == 63u && (sp > SPIN_CAP || tim[0])) { tim[0] = 1; break; }
                        if (sp > 3 || s == 0) __builtin_amdgcn_s_sleep(R0S);
                    }
                    o = min(o, 0xfffffffeu);
                    tp = rt_now() - TT_RT;
                }
                __builtin_amdgcn_raw_buffer_store_b32(o, rhx, ho, sb, 16);
                SBAR(); AT(2);
                tim[1] = rt_now() - (tb + cC1);
                if (r % 5 == 0 && lane < 2) { __builtin_amdgcn_raw_buffer_store_b32(min(tp, 0xfffffffeu), rhx, M_STAMP + (r / 5) * 64 + lane * 8192, sb + (t >> 2) * 4, 16);
                }
                if (!RLATE) rearm(t);
            }
            LBAR();
            unsigned char* up = lds + RO_UM + u3 * 8192 + lane_id() * 16;
#pragma unroll
            for (int q = 0; q < 8; ++q) *reinterpret_cast<u32x4*>(up + q * 1024) = um[q];
            u3 = u3 == 2 ? 0 : u3 + 1;
            if (RLATE && t < NSEG_) { rt_wait(tb + cP + RLATE); rearm(t); }
        } else LBAR();
        AT(3);
        LBAR();
        AT(4);
        if (t + 1 < NSEG_) {
            const int ln = lane_id();
            const unsigned tpv = *reinterpret_cast<const unsigned*>(lds + RO_TIM + ln * 4);
            const unsigned char* xp = lds + (t & 3) * 32768 + (ln >> 5) * 1024 + (ln & 31) * 16;
#pragma unroll
            for (int q = 0; q < 16; ++q) ux[q] = cat8(lds16(xp + (q >> 1) * 4096 + (q & 1) * 512), lds16(xp + (q >> 1) * 4096 + (q & 1) * 512 + 2048));
            tb = __builtin_amdgcn_readlane(tpv, 0); cC1 = __builtin_amdgcn_readlane(tpv, 1); cP = __builtin_amdgcn_readlane(tpv, 2);
            if (t == 0) tb = rt_now() - cC1;
#pragma unroll
            for (int q = 0; q < 16; ++q) PIN(ux[q]);
        }
        ATD(2, t == 42 && r == 0);
    }
    if (lane == 0) tim[2] = 0;
    LBAR();
    }
    if (lane == 0 && tim[0]) set_err((a.ws + WS_HOP));
}
__device__ __forceinline__ void ro_time(KA& a, unsigned char* lds, const int r, const unsigned pol) {
    const int lane = lane_id();
    const rsrc_t rhx = MKR(al2m((a.ws + WS_FR)), FR_USED);
    lds_u32* tim = (lds_u32*)(lds + RO_TIM + 256);
    const unsigned sx = ((my_side() ^ pol) & 1u) << 13, to = sx + M_STAMP + min(lane, 17) * 64;
#pragma unroll 1
    for (int layer = 0; layer < 2; ++layer) {
    const rsrc_t rfl = MKR((a.ws + WS_HOP) + layer * HOP_BYTES, F_ERR);
    const unsigned sb0 = (unsigned)layer * (20u << 14);
    unsigned tb = 0, cC1 = 0, cP = 0;
#pragma unroll 1
    for (int s = 0; s < RO_END; ++s) {
        LBAR();
        if (s < NSEG_) {
            if (s > 1) rt_wait(tb + cP + TT_TW);
            unsigned tw;
            for (unsigned sp = 0;; ++sp) { CBAR();
                tw = __builtin_amdgcn_raw_buffer_load_b32(rhx, to, sb0 + (unsigned)(s & 3) * (5u << 14) + (s >> 2) * 4, 16);
                if (__all(tw != 0xffffffffu)) break;
                __builtin_amdgcn_s_sleep(6);
                if (sp > SPIN_CAP_W || tim[0]) { tim[0] = 1; break; } }
            tt_step(tw, s, rt_now(), tb, cC1, cP);
            if (lane == 0) *reinterpret_cast<u32x4*>(lds + RO_TIM) = (u32x4){tb, cC1, cP, 0u};
            if (lane == 0 && s > 1) st16c(rfl, F_RD + (s * NR_ + r) * FLP, (u32x4){1u, min((unsigned)tim[1], 4095u), tim[2] == (unsigned)s ? 1u : 0u, 0u});
        }
        LBAR();
    }
    LBAR();
    }
}
__device__ __forceinline__ void ro_master(KA& a) {
    const int lane = lane_id();
    const rsrc_t rhx = MKR(al2m((a.ws + WS_FR)), FR_USED);
    int e1 = TT_E1 * 16, e2 = TT_E2 * 16, x1 = 0, x2 = 0; bool pr2 = true;
#pragma unroll 1
    for (int layer = 0; layer < 2; ++layer) {
    const rsrc_t rfl = MKR((a.ws + WS_HOP) + layer * HOP_BYTES, F_ERR);
    const unsigned sb0 = (unsigned)layer * (20u << 14);
#pragma unroll 1
    for (int s = 0; s < RO_END; ++s) {
        LBAR();
        if (s + 1 < NSEG_) {
            const int q = max(s, 2);
            const u32x4 f = ld16c(rfl, lane < 32 ? F_KM + ((q - 2) * 32 + lane) * FLP : F_UX + (q * 32 + lane - 32) * FLP), g = ld16c(rfl, F_RD + ((q - 1) * NR_ + lane) * FLP), k = ld16c(rfl, F_RD + ((q - 1) * NR_ + 64 + (lane & 15)) * FLP);
            auto wmax = [&](unsigned v) { v = max(v, dppu<0x128>(v)); v = max(v, dppu<0x124>(v)); v = max(v, dppu<0x122>(v)); v = max(v, dppu<0x121>(v));
                return (int)max(max(__builtin_amdgcn_readlane(v, 0), __builtin_amdgcn_readlane(v, 16)), max(__builtin_amdgcn_readlane(v, 32), __builtin_amdgcn_readlane(v, 48))); };
            auto upd = [&](int& e, int m) { if (m > 0) e += (min(m * 16, e + 16 * 12) - e) >> TT_DEC; };
            const bool rs = __any(f.z != 0u), r2 = __any((g.z | k.z) != 0u);
            if (s > 3) {
                if (!rs) upd(e1, wmax(lane < 32 ? f.y : (unsigned)max((int)f.y + TT_UM + TT_UX, 0)));
                if (!r2) upd(e2, wmax(max(g.y, k.y)));
                if (rs && !pr2) x2 += TT_BUMP; else if (r2 && !rs) x1 += TT_BUMP;
                if (!rs && !r2 && (s & TT_DCY) == 0) { x1 -= x1 > 0; x2 -= x2 > 0; }
            }
            pr2 = r2 || s < 3;
            const int xs = max(TT_XS - s * TT_XD, 0);
            const unsigned c1 = (e1 >> 4) + TT_M1 + x1 + xs, c2 = c1 + (e2 >> 4) + TT_M2 + x2 + xs;
            if (lane < 4) __builtin_amdgcn_raw_buffer_store_b32((lane & 2) ? c2 : c1, rhx, M_STAMP + 1024 + (lane & 2) * 32 + (lane & 1) * 8192, sb0 + (unsigned)((s + 1) & 3) * (5u << 14) + ((s + 1) >> 2) * 4, 16);
        }
        LBAR();
    }
    LBAR();
    if (!layer && lane == 0) { const rsrc_t rb = MKR((a.ws + WS_HOP) - 2048, 2048); const u32x2 t = ld8c(rb, 0); st8c(rb, 0, (u32x2){t.x + 1u, t.y}); }
    }
}
template <int TAIL> __device__ __forceinline__ void ro_out(KA& a, unsigned char* lds, const int r, const unsigned uo) {
    constexpr int wave = 2;
    const int lane = lane_id(), l15 = lane & 15, lg = lane >> 4, c = r >> 1, rt = r & 1, cr = (RROT & 2) ? c : 0;
    const rsrc_t rst = MKR((a.ws + WS_ST), ST_BYTES), rx = MKR((a.ws + WS_X), XI_BYTES), ro = MKR(a.out, S_ * D_ * 2);
    const unsigned xo = XI_X + rt * 20480 + (c >> 1) * 1024 + ((c & 1) * 2 + (lg >> 1)) * 256 + l15 * 16 + (lg & 1) * 8;
#pragma unroll 1
    for (int layer = 0; layer < 2; ++layer) {
    unsigned char* hop = (a.ws + WS_HOP) + layer * HOP_BYTES;
    const rsrc_t rfl = MKR(hop, F_ERR);
    unsigned tb = 0, cC1 = 0, cP = 1; int u3 = 0;
    u32x2 o = (u32x2){0u, 0u}, rr; float sq = 0.f; u32x4 p[40], um[8];
    auto put = [&](int s) {
        if (layer) __builtin_amdgcn_raw_buffer_store_b64(o, ro, ((rt * 16 + l15) * D_ + 16 * c + 4 * lg) * 2, s * (32 * D_ * 2), 0);
        else { __builtin_amdgcn_raw_buffer_store_b64(o, rx, xo, s * XI_PITCH, 16); if (lane < 32) __builtin_amdgcn_raw_buffer_store_b32(__float_as_uint(sq), rx, (rt * 40 + c) * 128 + l15 * 8 + lg * 4, s * XI_PITCH, 16); } };
    auto gate = [&](int s, int slp) { wait_set(rfl, F_PX + ((s + QA) * 64 + (lane & 31) * 2 + rt) * FLP, hop, slp); };
    auto req = [&](int s) { const int ln = lane_id();
        rr = __builtin_amdgcn_raw_buffer_load_b64(rx, xo, s * XI_PITCH, 16); SBAR();
#pragma unroll
        for (int q = 0; q < 40; ++q) { p[q] = __builtin_amdgcn_raw_buffer_load_b128(rst, ln * 16 + (q & 3) * 1024, ST_PX + ((s & (PR - 1)) * 2 + rt) * 40960 + (q < 32 ? ((q >> 2) + cr) & 7 : q >> 2) * 4096, 16); SBAR(); }
        CBAR(); };
    auto comp = [&](int s) { const int ln = lane_id();
        const unsigned char* xp = lds + (s & 3) * 32768 + ln * 16; const unsigned char* mp = lds + RO_UM + u3 * 8192 + ln * 16;
        u32x4 uf[8];
#pragma unroll
        for (int q = 0; q < 7; ++q) uf[q] = lds16(xp + (((q >> 2) + cr) & 7) * 4096 + (q & 3) * 1024);
        SBAR();
        f32x4 acc = (f32x4){lo2f(rr.x), hi2f(rr.x), lo2f(rr.y), hi2f(rr.y)};
#pragma unroll
        for (int q = 0; q < 40; ++q) { acc = dmf((TAIL == OLAG && q >= 32) ? um[q & 7] : uf[q & 7], p[q], acc); PIN(acc); SBAR();
            if (q + 7 < (TAIL == OLAG ? 32 : 40)) { uf[(q + 7) & 7] = q + 7 < 32 ? lds16(xp + ((((q + 7) >> 2) + cr) & 7) * 4096 + ((q + 7) & 3) * 1024) : lds16(mp + (q - 25) * 1024); SBAR(); } }
        o = (u32x2){pk2(acc[0], acc[1]), pk2(acc[2], acc[3])};
        sq = row_sums(o); };
    auto fin = [&](int s) { put(s); if (!layer) { VMDRAIN(); if (lane == 0) st16c(rfl, F_OF + (s * NR_ + r) * FLP, (u32x4){cP, 1u, 1u, tb}); } if (lane == 0 && r == 0) STAMP(300 + s - 56); };
    auto times = [&]() { const unsigned tpv = *reinterpret_cast<const unsigned*>(lds + RO_TIM + lane * 4); tb = __builtin_amdgcn_readlane(tpv, 0); cC1 = __builtin_amdgcn_readlane(tpv, 1); cP = __builtin_amdgcn_readlane(tpv, 2); };
    if (TAIL) {
        const int s = NSEG_ - 1 - OLAG + TAIL;
        for (int q = 0; q < 2 * NSEG_; ++q) __builtin_amdgcn_s_barrier();
        CBAR();
        u3 = s % 3;
        if (TAIL == OLAG) {
            const rsrc_t rhx = MKR(al2m((a.ws + WS_FR)), FR_USED);
            times(); rt_wait(tb + cC1);
            for (unsigned sp = 0;; ++sp) { unsigned m = 0; CBAR();
#pragma unroll
                for (int q = 0; q < 8; ++q) um[q] = __builtin_amdgcn_raw_buffer_load_b128(rhx, uo + (q & 3) * 1024, ((s & 3) << 14) + (q >> 2) * 4096, 16);
#pragma unroll
                for (int q = 0; q < 8; ++q) m = max(m, umax4(um[q]));
                if (__all(m != 0xffffffffu)) break;
                if (sp > SPIN_CAP_W) { set_err(hop); break; }
                __builtin_amdgcn_s_sleep(8); }
            rt_wait(tb + TT_TL);
        }
        gate(s, 0); req(s); comp(s); fin(s);
    } else {
#pragma unroll 1
    for (int t = 0; t < NSEG_; ++t) {
        const int s = t - OLAG;
        ATV;
        if (s >= 0) { rt_wait(tb + cC1 + TT_FP); gate(s, 0); }
        LBAR();
        if (s >= 0) {
            if (s > 0) put(s - 1);
            rt_wait(max((int)(tb + cP + TT_BD - rt_now()), 0) + rt_now() + TT_OD);
            AT(0);
            req(s);
            AT(1);
            if (!layer && s > 0) { PIN(rr); if (lane == 0) st16c(rfl, F_OF + ((s - 1) * NR_ + r) * FLP, (u32x4){cP, 1u, 1u, tb + cP}); }
            if (lane == 0 && r == 0 && s > 56) STAMP(300 + s - 57);
        }
        LBAR();
        times();
        if (s >= 0) { comp(s); u3 = u3 == 2 ? 0 : u3 + 1; AT(2); }
        ATD(2, t == 42 && r == 0);
    }
    fin(NSEG_ - 1 - OLAG);
    }
    LBAR();
    }
}
__device__ __forceinline__ void ro_fill(KA& a, unsigned char* lds, const int r) {
    constexpr int wave = 3;
    const int lane = lane_id();
    const unsigned ldsb = LDSB();
    const rsrc_t rst = MKR((a.ws + WS_ST), ST_BYTES);
#pragma unroll 1
    for (int layer = 0; layer < 2; ++layer) {
    unsigned char* hop = (a.ws + WS_HOP) + layer * HOP_BYTES;
    const rsrc_t rfl = MKR(hop, F_ERR);
    unsigned tb = 0, cC1 = 0, cP = 0;
#pragma unroll 1
    for (int t = 0; t < RO_END; ++t) {
        ATV;
        if (t < NSEG_) {
            if (t == 0) wait_set(rfl, F_XP + (r % NXP_) * FLP, hop, 4);
            if (t > 1) rt_wait(tb + cC1 + TT_FP);
            wait_set(rfl, (r < 64 || (t == 0 && lane >= 32)) ? F_UX + (t * 32 + (lane & 31)) * FLP : F_PX + (t * 64 + (lane & 31) * 2 + ((r >> 3) & 1)) * FLP, hop, t > 2 ? 0 : 1);
        }
        LBAR();
        if (t < NSEG_) {
            if (t > 1) rt_wait(tb + cP + TT_BD);
            AT(0);
            const unsigned ub = ST_U + ((t & (URING - 1)) * 40 + (r >> 1)) * 32768, lb = ldsb + (t & 3) * 32768, o = (r & RROT & 1) * 16384;
            dma16cn<16>(rst, lane * 16, ub + o, lb + o); dma16cn<16>(rst, lane * 16, ub + (o ^ 16384), lb + (o ^ 16384));
            AT(1);
            VMDRAIN();
            AT(2);
            if (lane == 0 && r == 0 && t == 0) STAMP(13);
        }
        LBAR();
        if (t < NSEG_) { const unsigned tpv = *reinterpret_cast<const unsigned*>(lds + RO_TIM + lane * 4); tb = __builtin_amdgcn_readlane(tpv, 0); cC1 = __builtin_amdgcn_readlane(tpv, 1); cP = __builtin_amdgcn_readlane(tpv, 2); }
        ATD(2, t == 42 && r == 0);
    }
    LBAR();
    if (lane == 0) STAMP(512 + blockIdx.x);
    }
}
__device__ __forceinline__ void ro_part(KA& a, unsigned char* lds, const int r, const int wave) {
    if (wave == 0) { lds_u32* tim = (lds_u32*)(lds + RO_TIM + 256); tim[0] = 0; tim[1] = 0; tim[2] = 0; }
    if (wave == 0) { __builtin_amdgcn_s_setprio(3); ro_state(a, lds, r, page_pol(a)); }
    else if (wave == 1) ro_time(a, lds, r, page_pol(a));
    else if (wave == 2) ro_out<0>(a, lds, r, 0u);
    else if (wave == 3) { init_wait(a); ro_fill(a, lds, r); }
    else if (wave == 4) ro_out<1>(a, lds, r, 0u);
    else if (wave == 7) ro_out<2>(a, lds, r, 0u);
    else if (wave == 6) { const unsigned pol = page_pol(a); ro_out<3>(a, lds, r, um_base(um_sides(a), pol, r >> 1) + lane_id() * 16); }
    else {
        if (r == 1) { const rsrc_t rfl = MKR(a.ws + WS_HOP, F_ERR); wait_set(rfl, F_QK + lane_id() * FLP, a.ws + WS_HOP, 1); wait_set(rfl, F_QK + (64 + lane_id()) * FLP, a.ws + WS_HOP, 1); if (lane_id() < 8) st4c(rfl, F_IN + lane_id() * FLP, 1u); }
        const rsrc_t rz = MKR((a.ws + WS_HOP) + HOP_BYTES, HOP_BYTES);
        for (unsigned o = r * 1024 + lane_id() * 16; o < HOP_BYTES; o += NR_ * 1024) st16c(rz, o, (u32x4){0u, 0u, 0u, 0u});
        VMDRAIN();
        if (r == 0) ro_master(a); else for (int q = 0; q < 4 * NSEG_ + 2; ++q) __builtin_amdgcn_s_barrier(); }
}

#define QE 6
#define XKB 4
#define XSL 0
#define VMKNOWN5() __builtin_amdgcn_s_waitcnt(0x0F75)
#define PIM 60
#define XNP 3
constexpr int XB = 40960;
struct XWt { u32x4 w[2][20]; f32x4 cs[2]; };
template <int NB, int NR, class G, class H> __device__ __forceinline__ void x_load(XWt& x, unsigned char* lds, const int wave, const int pw, G&& src, H&& more) {
    const unsigned ldsb = LDSB(), lane = lane_id(), l15 = lane & 15, lg = lane >> 4;
    load_b<NB, 4, NR, 0, 0>(ldsb, wave, src, [&](int rd, int base) { if (rd >= 10) more(rd, base); else if (pw >= 0) {
#pragma unroll
        for (int u = 0; u < 2; ++u)
#pragma unroll
            for (int k = 0; k < 2; ++k) x.w[u][(2 * rd + k) % 20] = ld_frag(lds + base + (pw >> 1) * 8192, 32 * k + 8 * lg, 4, (pw & 1) * 2 + u, l15); } }, [](int) {});
    if (pw >= 0) {
        const u32x4 one = (u32x4){0x3F803F80u, 0x3F803F80u, 0x3F803F80u, 0x3F803F80u};
        x.cs[0] = Z4; x.cs[1] = Z4;
#pragma unroll
        for (int k = 0; k < 20; ++k) { x.cs[0] = dmf(x.w[0][k], one, x.cs[0]); x.cs[1] = dmf(x.w[1][k], one, x.cs[1]); }
        PIN(x.cs[0]); PIN(x.cs[1]);
    }
}
__device__ __forceinline__ void c_load(u32x2 (&sv)[4], rsrc_t rx, int t) {
    const int lane = lane_id();
    asm volatile("s_nop 4\n\tbuffer_load_dwordx2 %0, %4, %5, %6 offen sc1\n\tbuffer_load_dwordx2 %1, %4, %5, %6 offen offset:128 sc1\n\tbuffer_load_dwordx2 %2, %4, %5, %6 offen offset:256 sc1\n\tbuffer_load_dwordx2 %3, %4, %5, %6 offen offset:384 sc1"
        : "=&v"(sv[0]), "=&v"(sv[1]), "=&v"(sv[2]), "=&v"(sv[3]) : "v"((lane >> 4) * 512 + (lane & 15) * 8), "s"(rx), "s"(t * XI_PITCH + XI_S8) : "memory");
}
__device__ __forceinline__ void c_got(u32x2 (&sv)[4]) { asm volatile("" : "+v"(sv[0]), "+v"(sv[1]), "+v"(sv[2]), "+v"(sv[3])); }
__device__ __forceinline__ void c_put(const u32x2 (&sv)[4], unsigned char* dst) {
    const f32x2 s = (__builtin_bit_cast(f32x2, sv[0]) + __builtin_bit_cast(f32x2, sv[1])) + (__builtin_bit_cast(f32x2, sv[2]) + __builtin_bit_cast(f32x2, sv[3]));
    const auto a1 = __builtin_amdgcn_permlane16_swap(__float_as_uint(s[0]), __float_as_uint(s[0]), false, false), a2 = __builtin_amdgcn_permlane16_swap(__float_as_uint(s[1]), __float_as_uint(s[1]), false, false);
    float m, i; ln_consts(__uint_as_float(a1[0]) + __uint_as_float(a1[1]), __uint_as_float(a2[0]) + __uint_as_float(a2[1]), m, i);
    const int lane = lane_id();
    *reinterpret_cast<f32x2*>(dst + (lane >> 5) * 128 + (lane & 15) * 8) = (f32x2){-m, i};
}
template <class F> __device__ __forceinline__ void x_proj(const XWt& x, const unsigned char* xb, const unsigned char* cb, u32x4 (&o)[2], F&& mid) {
    const int lane = lane_id();
    unsigned l16 = lane * 16, c8 = (lane & 15) * 8; PIN(l16); PIN(c8);
    const unsigned char* xp = xb + l16;
    f32x4 acc[2][2] = {{Z4, Z4}, {Z4, Z4}};
    u32x4 xf[3][2]; f32x2 ct[2];
#pragma unroll
    for (int k = 0; k < 2; ++k) { xf[k][0] = lds16(xp + k * 1024); SBAR(); xf[k][1] = lds16(xp + 20480 + k * 1024); SBAR(); }
#pragma unroll
    for (int k = 0; k < 20; ++k) {
#pragma unroll
        for (int j = 0; j < 4; ++j) { acc[j >> 1][j & 1] = dmf(x.w[j >> 1][k], xf[k % 3][j & 1], acc[j >> 1][j & 1]); PIN(acc[j >> 1][j & 1]); SBAR();
            if ((j == 1 || j == 2) && k + 2 < 20) { xf[(k + 2) % 3][j - 1] = lds16(xp + (j - 1) * 20480 + (k + 2) * 1024); SBAR(); }
            if (j == 1 && k == 18) { ct[0] = *reinterpret_cast<const f32x2*>(cb + c8); ct[1] = *reinterpret_cast<const f32x2*>(cb + c8 + 128); SBAR(); } }
        mid(k, l16);
    }
#pragma unroll
    for (int rt = 0; rt < 2; ++rt)
#pragma unroll
        for (int q = 0; q < 4; ++q) { const int u = q >> 1, e = 2 * (q & 1);
            o[rt][q] = pk2(__builtin_fmaf(ct[rt][0], x.cs[u][e], acc[u][rt][e]) * ct[rt][1], __builtin_fmaf(ct[rt][0], x.cs[u][e + 1], acc[u][rt][e + 1]) * ct[rt][1]); }
}
__device__ __forceinline__ void xp_part(KA& a, unsigned char* lds, const int j, const int wave) {
    const int lane = lane_id(), tid = wave * 64 + lane;
    const unsigned ldsb = LDSB();
    const int hp = j / 3, kind = j - 3 * hp;
    XWt x;
    {
        const rsrc_t rk = MKR(psel(a.wk, a.wv, kind >> 1), D_ * HE_ * 2), rv = MKR(a.wv, D_ * HE_ * 2);
        const unsigned row = 8 * wave + (lane >> 3), vo = row * (HE_ * 2) + ((lane & 7) ^ (wsw(row) << 1)) * 16 + hp * 768;
        x_load<4, 10>(x, lds, wave, wave, [&](int rd, int q, unsigned dst) { dma16(q < 3 ? rk : rv, vo, rd * (64 * HE_ * 2) + (kind < 2 ? kind * 384 + (q % 3) * 128 : (q >> 1) * 384 + 128 + (q & 1) * 128), dst); }, [](int, int) {});
    }
    const unsigned oo = kind < 2 ? (wave < 6 ? ST_KX + wave * 1024 : ST_VX + (wave - 6) * 1024) + (2 * hp + kind) * 12288 : ST_VX + (2 * hp + (wave >> 2)) * 12288 + (2 + (wave & 3)) * 1024;
    const rsrc_t rst = MKR((a.ws + WS_ST), ST_BYTES), rx = MKR((a.ws + WS_X), XI_BYTES);
    const int wr_ = (wave + (blockIdx.x >> 3) * ROT) & 7;
#pragma unroll 1
    for (int layer = 0; layer < 2; ++layer) {
    unsigned char* hop = (a.ws + WS_HOP) + layer * HOP_BYTES;
    const rsrc_t rfl = MKR(hop, F_ERR);
    auto piece = [&](int t, int b, int k, unsigned l16) { const unsigned o = (wr_ * 5 + k) * 1024; dma16c(rx, l16, (unsigned)t * XI_PITCH + XI_X + o, ldsb + b * XB + o); };
    auto x_dma = [&](int t, int b) {
#pragma unroll
        for (int k = 0; k < 5; ++k) piece(t, b, k, lane * 16); };
    if (tid == 0 && j == 0) STAMP(7);
    if (wave == 2) { if (!layer) init_wait(a); wait_set(rfl, F_AF + (lane & 15) * FLP, hop, XSL); }
    LBAR();
    if (tid == 0 && j == 0) STAMP(8);
    { u32x2 sv[4]; if (wave == 1) c_load(sv, rx, 0);
      x_dma(0, 0);
      if (wave == 1) { VMW(5); c_got(sv); c_put(sv, lds + 3 * XB); } }
    if (wave == 2) wait_set(rfl, F_AF + (16 + (lane & 15)) * FLP, hop, XSL);
    VMKNOWN(); LBAR();
    x_dma(1, 1);
    unsigned tnext = rt_now(); int b = 0;
#pragma unroll 1
    for (int t = 0; t < NSEG_; ++t) {
        ATV;
        AT(0);
        u32x4 fl; u32x2 sv[4];
        const unsigned fo = (lane < 16 && t >= XLD) ? F_KM + ((t - XLD) * 32 + 2 * hp) * FLP : F_AF + (min(t + 2, NSEG_ - 1) * 16 + (lane & 15)) * FLP;
        if (wave == 2) { if (t > XLD) rt_wait(tnext);
            asm volatile("buffer_load_dwordx4 %0, %1, %2, 0 offen sc1" : "=&v"(fl) : "v"(fo), "s"(rfl) : "memory"); }
        if (wave == 1) c_load(sv, rx, min(t + 1, NSEG_ - 1));
        const int bn = b == 0 ? 2 : b - 1;
        u32x4 o[2];
        x_proj(x, lds + b * XB, lds + 3 * XB + (t & 1) * 256, o, [&](int k, unsigned l16) {
            if (k == XKB) { if (wave == 1) VMW(4); else if (wave == 2) VMW(1); else VMDRAIN();
                __builtin_amdgcn_s_barrier(); CBAR();
                if (wave == 0 && t > 0 && l16 == 0) { st4c(rfl, F_XP + ((t - 1) * NXP_ + j) * FLP, 1u); if (j == 0 && t < 4) STAMP(t); }
                AT(2); SBAR(); }
            if (k == XKB + 3) {
                if (wave == 1 || wave == 2) VMDRAIN();
                if (wave == 2) { asm volatile("" : "+v"(fl));
                    if (__builtin_expect(__all(fl.x != 0u), 1)) tnext = __builtin_amdgcn_readfirstlane(fl.w) + XNP * __builtin_amdgcn_readfirstlane(fl.x) + TT_KD; else { wait_timed(rfl, fo, hop, tnext, XNP, TT_KD, t < XLD); VMKNOWN(); } }
                __builtin_amdgcn_s_barrier(); CBAR(); SBAR(); }
            if (k > XKB + 3 && k <= XKB + 13 && ((k - XKB) & 1) == 0 && t + 2 < NSEG_) { piece(t + 2, bn, (k - XKB - 4) >> 1, l16); SBAR(); } });
        const int ln = lane_id();
        st16s(o[0], rst, ln * 16, oo + (t & (KXR - 1)) * (32 * 12288)); st16s(o[1], rst, ln * 16, oo + (t & (KXR - 1)) * (32 * 12288) + 6144);
        AT(1);
        if (wave == 1) { c_got(sv); c_put(sv, lds + 3 * XB + ((t + 1) & 1) * 256); asm volatile("s_waitcnt lgkmcnt(0)" ::: "memory"); }
        ATD(3, t == 48 && j == 0);
        b = b == 2 ? 0 : b + 1;
    }
    VMDRAIN(); LBAR();
    if (tid == 0) st4c(rfl, F_XP + ((NSEG_ - 1) * NXP_ + j) * FLP, 1u);
    if (tid == 0) STAMP(512 + blockIdx.x);
    }
}

template <int AT_> __device__ __forceinline__ void xq_run(KA& a, unsigned char* lds, const int h, const int wave) {
    constexpr bool ATT = AT_ >= 0; constexpr int qt = ATT ? AT_ : 0;
    const int lane = lane_id(), tid = wave * 64 + lane;
    const unsigned ldsb = LDSB();
    constexpr int L_C = 2 * XB, L_Q = L_C + 512, L_VX = L_Q + (QA + 1) * 12288;
    rt_wait(rt_now() + XQD);
    XWt x; u32x4 wo[4][6];
    {
        const rsrc_t rq = MKR(a.wq, D_ * HE_ * 2), ro = MKR(a.wo, HE_ * D_ * 2);
        const unsigned row = 8 * wave + (lane >> 3), ch = ((lane & 7) ^ (wsw(row) << 1)) * 16, vo = row * (HE_ * 2) + ch + h * 384, oo = (h * 192 + row) * (D_ * 2) + ch;
        x_load<3, 12>(x, lds, wave, ATT ? -1 : wave - 2, [&](int rd, int q, unsigned dst) { if (rd < 10) dma16(rq, vo, rd * (64 * HE_ * 2) + q * 128, dst); else dma16(ro, oo, q * (64 * D_ * 2) + (rd - 2) * 128, dst); },
            [&](int rd, int base) { if (rd == 10 + AT_) {
#pragma unroll
                for (int q = 0; q < 24; ++q) wo[q / 6][q % 6] = ld_frag(lds + base + ((q % 6) >> 1) * 8192, (q & 1) * 32 + 4 * (lane >> 4), 16, q / 6, lane & 15); } });
    }
    const rsrc_t rst = MKR((a.ws + WS_ST), ST_BYTES), rx = MKR((a.ws + WS_X), XI_BYTES);
    const int wr_ = (wave - 2 + (blockIdx.x >> 3)) % 6, xj = xp_of(h), xj2 = 3 * (h >> 1) + 2;
    const int l15 = lane & 15, lg = lane >> 4;
    const bool cm[4] = {4 * lg <= l15, 4 * lg + 1 <= l15, 4 * lg + 2 <= l15, 4 * lg + 3 <= l15};
#pragma unroll 1
    for (int layer = 0; layer < 2; ++layer) {
    unsigned char* hop = (a.ws + WS_HOP) + layer * HOP_BYTES;
    const rsrc_t rfl = MKR(hop, F_ERR);
    auto piece = [&](int t, int k, unsigned l16) { const unsigned o = min(wr_ * 7 + k, 39) * 1024; dma16c(rx, l16, (unsigned)t * XI_PITCH + XI_X + o, ldsb + (t & 1) * XB + o); };
    if (wave == 0) { if (!layer) init_wait(a); wait_set(rfl, F_AF + (lane & 31) * FLP, hop, 2); }
    LBAR();
    if (!ATT) {
#pragma unroll
        for (int k = 0; k < 7; ++k) piece(0, k, lane * 16); }
    u32x4 kf[2][6];
    if (AT_ == 0) { u32x2 sv[4]; c_load(sv, rx, 0); VMDRAIN(); c_got(sv); c_put(sv, lds + L_C); }
    VMKNOWN();
    unsigned tnext = rt_now(); int qs = 0;
#pragma unroll 1
    for (int i = 0; i < NSEG_ + QA; ++i) {
        ATV;
        if (i <= NSEG_) LBAR();
        AT(0);
        if (!ATT) {
            if (i < NSEG_) {
                u32x4 o[2];
                x_proj(x, lds + (i & 1) * XB, lds + L_C + (i & 1) * 256, o, [&](int k, unsigned l16) { if ((k & 1) && k < 14 && i + 1 < NSEG_) { piece(i + 1, k >> 1, l16); SBAR(); } });
                unsigned char* qp = lds + L_Q + qs * 12288 + (wave - 2) * 1024 + lane_id() * 16;
                *reinterpret_cast<u32x4*>(qp) = o[0]; *reinterpret_cast<u32x4*>(qp + 6144) = o[1];
                AT(1);
                VMDRAIN();
            }
        } else {
            const int t = i - QA;
            const unsigned fa = F_AF + (min(i + 2, NSEG_ - 1) * 16 + l15) * FLP, fx = F_XP + ((lane & 1) ? xj : xj2) * FLP, ub = ST_U + (((i & (URING - 1)) * 40 + 32 + 4 * qt) * 32 + h) * 1024;
            unsigned l16 = lane_id() * 16; PIN(l16);
            auto v_dma = [&]() { dma16cn<6>(rst, l16, ST_VX + (((i + 1) & (KXR - 1)) * 32 + h) * 12288 + qt * 6144, ldsb + L_VX + ((i + 1) & 1) * 12288 + qt * 6144); };
            auto next = [&]() {
                const unsigned kb = ST_KX + (((t + 1) & (KXR - 1)) * 32 + h) * 12288;
#pragma unroll
                for (int m = 0; m < (qt ? 12 : 6); ++m) asm volatile("buffer_load_dwordx4 %0, %1, %2, %3 offen offset:%4 sc1" : "=&v"(kf[m / 6][m % 6]) : "v"(l16), "s"(rst), "s"(kb + (m >> 2) * 4096), "i"((m & 3) * 1024) : "memory"); };
            auto got = [&]() {
#pragma unroll
                for (int m = 0; m < (qt ? 12 : 6); ++m) PIN(kf[m / 6][m % 6]); };
            u32x4 vf[12]; u32x2 sv[4];
            auto u_two = [&](int u0) {
                f32x4 acc[2][2] = {{Z4, Z4}, {Z4, Z4}};
#pragma unroll
                for (int j = 0; j < 24; ++j) { const int k = j >> 2, u = (j >> 1) & 1, r = j & 1; acc[u][r] = dmf(vf[r * 6 + k], wo[u0 + u][k], acc[u][r]); }
                SBAR();
#pragma unroll
                for (int u = 0; u < 2; ++u) st16s((u32x4){pk2(acc[u][0][0], acc[u][1][0]), pk2(acc[u][0][2], acc[u][1][2]), pk2(acc[u][0][1], acc[u][1][1]), pk2(acc[u][0][3], acc[u][1][3])}, rst, l16, ub + (u0 + u) * 32768);
                SBAR(); };
            if (t < 0) {
                if (!qt) { c_load(sv, rx, i + 1); VMDRAIN(); c_got(sv); c_put(sv, lds + L_C + ((i + 1) & 1) * 256); }
                wait_set(rfl, fx + i * (NXP_ * FLP), hop, 1);
#pragma unroll
                for (int m = 0; m < 12; ++m) vf[m] = __builtin_amdgcn_raw_buffer_load_b128(rst, l16 + (m & 3) * 1024, ST_VX + (i * 32 + h) * 12288 + (m >> 2) * 4096, 16);
                SBAR();
                u_two(0); u_two(2);
                VMDRAIN();
                if (l16 == 0) st4c(rfl, F_PX + (i * 64 + h * 2 + qt) * FLP, 1u);
                wait_set(rfl, (lane < 16 && t == -1) ? fx + (i + 1) * (NXP_ * FLP) : fa, hop, 1); v_dma(); next(); VMDRAIN(); got();
            } else {
                const unsigned char* qp = lds + L_Q + (qs == QA ? 0 : qs + 1) * 12288 + qt * 6144 + l16;
                u32x4 qf[6], km[6], fl;
                const bool uu = i < NSEG_;
#pragma unroll
                for (int m = 0; m < 12; ++m) vf[m] = lds16(lds + L_VX + (i & 1) * 12288 + l16 + m * 1024);
                SBAR();
                if (!qt) c_load(sv, rx, min(i + 1, NSEG_ - 1));
                if (uu && t > QE) { v_dma(); u_two(0); VMW(8); } else { if (uu) u_two(0); VMDRAIN(); }
                got();
                if (!qt) c_got(sv);
                if (l16 == 0 && t <= PIM && t > QE) st4c(rfl, F_PX + ((i - 1) * 64 + h * 2 + qt) * FLP, 1u);
                AT(1);
                const unsigned fo = lane < 16 ? F_KM + (t * 32 + h) * FLP : (lane < 32 ? fx + min(i + (t < QE ? 1 : 2), NSEG_ - 1) * (NXP_ * FLP) : fa);
                const int dl = t + 2 == NSEG_ ? TT_QN : TT_QD;
#pragma unroll
                for (int m = 0; m < 6; ++m) qf[m] = lds16(qp + m * 1024);
                rt_wait(tnext);
                AT(5);
                asm volatile("buffer_load_dwordx4 %0, %1, %2, 0 offen sc1" : "=&v"(fl) : "v"(fo), "s"(rfl) : "memory");
                f32x4 sc[3] = {Z4, Z4, Z4};
#pragma unroll
                for (int m = 0; m < 6; ++m) { sc[0] = dmf(kf[0][m], qf[m], sc[0]); if (qt) sc[1] = dmf(kf[1][m], qf[m], sc[1]); }
                float s2[10]; float mx = -3.0e38f, scl = 0.07216878364870322f;
                auto cvt = [&](int q) {
                    const unsigned r0 = pk2(sc[q >> 1][2 * (q & 1)], sc[q >> 1][2 * (q & 1) + 1]), r1 = pk2(muls(lo2f(r0), scl), muls(hi2f(r0), scl));
                    s2[2 * q] = (q >> 1) != qt || cm[2 * (q & 1)] ? lo2f(r1) : -3.0e38f; s2[2 * q + 1] = (q >> 1) != qt || cm[2 * (q & 1) + 1] ? hi2f(r1) : -3.0e38f;
                    mx = fmaxf(mx, fmaxf(s2[2 * q], s2[2 * q + 1])); PIN(s2[2 * q]); PIN(s2[2 * q + 1]); };
#pragma unroll
                for (int q = 0; q < 4; ++q) { if (qt == 0 && q >= 2) { s2[2 * q] = 0.f; s2[2 * q + 1] = 0.f; } else cvt(q); }
                PIN(mx);
                if (!qt && uu) c_put(sv, lds + L_C + ((i + 1) & 1) * 256);
                asm volatile("s_waitcnt vmcnt(0)" : "+v"(fl) :: "memory");
                if (__all(fl.x != 0u)) tnext = __builtin_amdgcn_readfirstlane(fl.w) + 2u * __builtin_amdgcn_readfirstlane(fl.x) + dl; else wait_timed(rfl, fo, hop, tnext, 2, dl, t + 1 == NSEG_ || t < QE);
                AT(2);
                if (l16 == 0 && h == 0 && qt == 0 && t >= 56) STAMP(360 + t - 56);
                const unsigned mo = (lg * 8 + (((l15 >> 2) * 2 + (l15 & 1) + (lg & 1) * 4) & 7)) * 16;
                if (t <= QE) v_dma();
#pragma unroll
                for (int m = 0; m < 6; ++m) asm volatile("buffer_load_dwordx4 %0, %1, %2, %3 offen offset:%4 sc1" : "=&v"(km[m]) : "v"(mo), "s"(rst), "s"(ST_KM + ((t & (PR - 1)) * 32 + h) * 3072), "i"(m * 512) : "memory");
                if (uu) { u_two(2); VMW(2); } else VMDRAIN();
                asm volatile("" : "+v"(km[0]), "+v"(km[1]), "+v"(km[2]), "+v"(km[3]), "+v"(km[4]), "+v"(km[5]));
#pragma unroll
                for (int m = 0; m < 6; ++m) sc[2] = dmf(km[m], qf[m], sc[2]);
                cvt(4);
                mx = xg_max(mx) * -1.4426950408889634f;
                float sum = 0.f, su1 = 0.f;
#pragma unroll
                for (int q = 0; q < 10; ++q) { if (qt == 0 && q >= 4 && q < 8) continue; s2[q] = __builtin_amdgcn_exp2f(__builtin_fmaf(s2[q], 1.4426950408889634f, mx)); if (q & 1) su1 += s2[q]; else sum += s2[q]; }
                float inv = __builtin_amdgcn_rcpf(xg_sum(sum + su1));
                asm volatile("s_nop 0" : "+v"(inv));
#pragma unroll
                for (int q = 0; q < 10; ++q) { if (qt == 0 && q >= 4 && q < 8) continue; s2[q] = muls(s2[q], inv); }
                const unsigned pb = ST_PX + ((t & (PR - 1)) * 2 + qt) * 40960;
                st16s((u32x4){pk2(s2[0], s2[4]), pk2(s2[2], s2[6]), pk2(s2[1], s2[5]), pk2(s2[3], s2[7])}, rst, l16, pb + h * 1024);
                __builtin_amdgcn_raw_buffer_store_b32(pk2(s2[8], s2[9]), rst, l15 * 16 + (lg & 1) * 8 + (lg >> 1) * 4, pb + 32768 + h * 256, 16);
                AT(3);
                if (t >= PIM || t < QE) { VMDRAIN(); if (l16 == 0) st4c(rfl, F_PX + (i * 64 + h * 2 + qt) * FLP, 1u); }
                next();
                AT(4);
                if (l16 == 0 && h == 0 && qt == 1 && t >= 56) STAMP(340 + t - 56);
            }
        }
        ATD(4, i == 44 && h == 0);
        qs = qs == QA ? 0 : qs + 1;
    }
    VMDRAIN(); LBAR();
    if (tid == 0) STAMP(512 + blockIdx.x);
    }
}
__device__ __forceinline__ void xq_part(KA& a, unsigned char* lds, const int h, const int wave) {
    if (wave < 2) __builtin_amdgcn_s_setprio(1);
    if (wave == 0) xq_run<0>(a, lds, h, wave); else if (wave == 1) xq_run<1>(a, lds, h, wave); else xq_run<-1>(a, lds, h, wave);
}

__device__ __forceinline__ void q_part(KA& a, unsigned char* lds, const int rb, const int g, const int wave) {
    constexpr int STG = 26624, NS = 5, layer = 0;
    const int lane = lane_id(), l15 = lane & 15, lg = lane >> 4, rp = wave & 1, cg = wave >> 1;
    const unsigned ldsb = LDSB();
    const rsrc_t rw = MKR(a.wqkv, D_ * 1920 * 2), rx = MKR(a.x0, S_ * D_ * 2);
    unsigned vo[4];
    vo[0] = (rb * 128 + wave * 16 + (lane >> 2)) * (D_ * 2) + ((lane ^ -lg) & 3) * 16;
#pragma unroll
    for (int u = 0; u < 3; ++u) { const int p = wave + 8 * u, c = p * 16 + (lane >> 2), row = c / 9, e = c - row * 9, sg = e / 3;
        vo[u + 1] = p < 18 ? row * 3840 + sg * 1280 + ((160 * g) & ~63) + (e - sg * 3) * 64 + ((lane & 3) ^ ((row >> 2) & 2)) * 16 : OOB; }
    const unsigned m3 = wave < 2 ? ~0u : 0u, d3 = wave < 2 ? 24576 : NS * STG;
    const int rot = (rb + (g & 1) * 5) % 10;
    auto piece = [&](int st, unsigned sl, int j) { dma16(j ? rw : rx, vo[j], st * (j ? 32 * 3840 : 64), ldsb + wave * 1024 + (j < 3 ? sl + j * 8192 : (sl & m3) + d3)); };
#pragma unroll 1
    for (int st = 0; st < NS - 1; ++st) {
#pragma unroll
        for (int j = 0; j < 4; ++j) piece((st + 2 * rot) % 20, st * STG, j); }
    if (wave == 0 && lane == 0 && rb == 0 && g == 0) STAMP(17);
    f32x4 acc[4][4], gr = Z4, cs[4] = {Z4, Z4, Z4, Z4};
#pragma unroll
    for (int i = 0; i < 16; ++i) acc[i >> 2][i & 3] = Z4;
    u32x4 one = (u32x4){0x3F803F80u, 0x3F803F80u, 0x3F803F80u, 0x3F803F80u}; PIN(one);
    unsigned wp[4]; const unsigned xp = (rp * 4 + cg) * 1024 + l15 * 64 + ((lg ^ -(l15 >> 2)) & 3) * 16;
#pragma unroll
    for (int u = 0; u < 4; ++u) { const int T = min(4 * u + cg, 14); wp[u] = 8192 + (8 * lg + (l15 >> 2)) * 576 + (((T / 5) * 192 + (T % 5 + (g & 1)) * 32) ^ ((lg & 1) * 32)) + (l15 & 3) * 8; }
    u32x4 xf[2][4], wf[2][4];
    auto rw_ = [&](int kk, unsigned sl, int u) { const unsigned char* p = lds + sl + wp[u]; const u32x2 x0 = ldstr(p), x1 = ldstr(p + 4 * 576); wf[kk][u] = (u32x4){x0.x, x0.y, x1.x, x1.y}; SBAR(); };
    auto rx_ = [&](int kk, unsigned sl, int i) { xf[kk][i] = lds16(lds + sl + (xp ^ (i * 1024))); SBAR(); };
    VMW(12);
    LBAR();
    if (wave == 0 && lane == 0 && rb == 0 && g == 0) STAMP(14);
#pragma unroll
    for (int j = 0; j < 8; ++j) { if (j < 4) rx_(0, 0, j); else rw_(0, 0, j - 4); }
    unsigned sn = STG, sf = (NS - 1) * STG;
    int cf = (rot + 2) % 10; const int cb = (rb + 10 - rot) % 10 + (rb < 10 ? 0 : 16);
#pragma unroll 1
    for (int ch = 0; ch < 10; ++ch) {
#pragma unroll
        for (int kk = 0; kk < 2; ++kk) {
            AT(3 * kk);
            VMW(8);
            __builtin_amdgcn_s_barrier(); CBAR();
            AT(3 * kk + 1);
            gr = dmf(xf[kk][0], xf[kk][0], gr); PIN(gr); SBAR();
#pragma unroll
            for (int j = 0; j < 16; ++j) { const int u = j >> 2, i = j & 3;
                if (j == 12 && cg == 3) wf[kk][3] = one;
                acc[i][u] = dmf(wf[kk][u], xf[kk][i], acc[i][u]); PIN(acc[i][u]); SBAR();
                if (!(j & 1)) { if (j < 8) rx_(kk ^ 1, sn, j >> 1); else rw_(kk ^ 1, sn, (j >> 1) - 4); }
                if (i == 1) { piece(2 * cf + kk, sf, u); SBAR(); } }
            if (rp) { cs[0] = dmf(wf[kk][2], one, cs[0]); cs[1] = dmf(wf[kk][3], one, cs[1]); } else { cs[0] = dmf(wf[kk][0], one, cs[0]); cs[1] = dmf(wf[kk][1], one, cs[1]); }
            if (__builtin_expect(ch == cb && kk == rp, 0)) {
#pragma unroll
                for (int u = 0; u < 4; ++u) { const int T = 4 * u + cg; st16s(wf[kk][u], MKR(a.ws + WS_WF_AQKV, D_ * 1920 * 2), T < 15 ? lane * 16 : OOB, (((T / 5) * 40 + 5 * g + T % 5) * 20 + 2 * rb + kk) * 1024); } }
            SBAR();
            sn = sn == (NS - 1) * STG ? 0 : sn + STG; sf = sf == (NS - 1) * STG ? 0 : sf + STG;
        }
        cf = cf == 9 ? 0 : cf + 1;
        if (ch == 7) { vo[0] = vo[1] = vo[2] = vo[3] = OOB; }
        AT(6);
        ATD(6, ch == 5 && rb == 0 && g == 0);
    }
    {
        const rsrc_t rz = MKR(a.ws + WS_HOP + F_AF, HOP_BYTES - F_AF), rh = MKR(al2m(a.ws + WS_FR), FR_FILL); const unsigned w = (rb * 8 + g) * 8 + wave;
#pragma unroll
        for (int j = 0; j < 3; ++j) st16c(rz, (w + j * 1024) * 1024 + lane * 16, (u32x4){0u, 0u, 0u, 0u});
        static_assert(HOP_BYTES - F_AF <= 3u << 20, "flags");
        st16c(rh, (w >> 4) % 20u >= 2u ? w * 1024 + lane * 16 : OOB, (u32x4){~0u, ~0u, ~0u, ~0u});
    }
    if (wave == 0 && lane == 0 && rb == 0 && g == 0) STAMP(15);
    constexpr int KP = 176, L_K = 0, L_V = L_K + 128 * KP, L_Q = L_V + 128 * KP + 64, L_S = NS * STG + 8192, L_CS = L_S + 1024;
    const int row = rp * 64 + cg * 16 + l15;
    if (lg == (l15 >> 2)) *reinterpret_cast<float*>(lds + L_S + row * 8 + 4) = (l15 & 2) ? ((l15 & 1) ? gr[3] : gr[2]) : ((l15 & 1) ? gr[1] : gr[0]);
    if (cg == 3) *reinterpret_cast<float*>(lds + L_S + (row ^ (lg * 16)) * 8) = lg < 2 ? (lg ? acc[1][3][0] : acc[0][3][0]) : (lg == 2 ? acc[2][3][0] : acc[3][3][0]);
    if (l15 < 2) *reinterpret_cast<f32x4*>(lds + L_CS + wave * 128 + l15 * 64 + lg * 16) = l15 ? cs[1] : cs[0];
    VMW(4); LBAR();
    f32x2 ct[4];
    {
        const f32x2 sv = *reinterpret_cast<const f32x2*>(lds + L_S + (row ^ (lg * 16)) * 8);
#pragma unroll
        for (int u = 0; u < 4; ++u) cs[u] = *reinterpret_cast<const f32x4*>(lds + L_CS + cg * 256 + u * 64 + lg * 16);
        float m, iv; ln_consts(sv[0], sv[1], m, iv); m = -m;
        const auto m2 = __builtin_amdgcn_permlane32_swap(__float_as_uint(m), __float_as_uint(m), false, false), i2 = __builtin_amdgcn_permlane32_swap(__float_as_uint(iv), __float_as_uint(iv), false, false);
#pragma unroll
        for (int z = 0; z < 2; ++z) { const auto x = __builtin_amdgcn_permlane16_swap(m2[z], m2[z], false, false), y = __builtin_amdgcn_permlane16_swap(i2[z], i2[z], false, false);
            ct[2 * z] = (f32x2){__uint_as_float(x[0]), __uint_as_float(y[0])}; ct[2 * z + 1] = (f32x2){__uint_as_float(x[1]), __uint_as_float(y[1])}; }
    }
    if (wave == 0 && lane == 0 && rb == 0 && g == 0) STAMP(18);
    auto tile = [&](int u, bool ph, unsigned char* dst, int pitch) {
#pragma unroll
        for (int i = 0; i < 4; ++i) { f32x4 y = acc[i][u] + ct[i][0] * cs[u];
            if (ph) { y = y * ct[i][1] + 1e-6f; const f32x4 z = y * 1.4426950408889634f - 1.4426950408889634e-6f;
#pragma unroll
                for (int r = 0; r < 4; ++r) y[r] = __builtin_amdgcn_exp2f(__builtin_amdgcn_fmed3f(z[r], 0.f, -3.0e38f)) + __builtin_amdgcn_fmed3f(y[r], 1e-6f, 3.0e38f); }
            else y *= ct[i][1];
            *reinterpret_cast<u32x2*>(dst + (i ^ cg) * pitch) = (u32x2){pk2(y[0], y[1]), pk2(y[2], y[3])}; } };
    unsigned char* kp = lds + L_K + (rp * 64 + l15) * KP + 8 * lg + cg * 32;
    if (cg > 0) tile(1, true, kp - 32, 16 * KP);
    if (cg < 2) tile(2, true, kp + 96, 16 * KP); else tile(2, false, kp + (L_V - L_K) - 64, 16 * KP);
    if (cg < 3) tile(3, false, kp + (L_V - L_K) + 64, 16 * KP);
    LBAR();
    if (wave == 0 && lane == 0 && rb == 0 && g == 0) STAMP(19);
    if (wave >= 4) {
        f32x4 d4[4] = {Z4, Z4, Z4, Z4};
#pragma unroll
        for (int ks = 0; ks < 4; ++ks) {
            u32x4 A[2], B[2];
#pragma unroll
            for (int t = 0; t < 2; ++t) { const unsigned char* p = lds + L_K + (32 * ks + 8 * lg + (l15 >> 2)) * KP + 40 * (wave & 3) + 32 * t + 8 * (l15 & 3);
                const u32x2 a0 = ldstr(p), a1 = ldstr(p + 4 * KP), b0 = ldstr(p + (L_V - L_K)), b1 = ldstr(p + (L_V - L_K) + 4 * KP);
                A[t] = (u32x4){a0.x, a0.y, a1.x, a1.y}; B[t] = (u32x4){b0.x, b0.y, b1.x, b1.y}; }
            if (l15 >= 4) B[1] = one;
#pragma unroll
            for (int q = 0; q < 4; ++q) d4[q] = dmf(A[q >> 1], B[q & 1], d4[q]); }
        const rsrc_t rk = MKR(a.ws + WS_KVP, 32 * 16 * 4096);
#pragma unroll
        for (int q = 0; q < 4; ++q) st16c(rk, ((q < 2 || lg == 0) && (!(q & 1) || l15 < 8)) ? ((4 * g + (wave & 3)) * 16 + rb) * 4096 + q * 1024 + lane * 16 : OOB, __builtin_bit_cast(u32x4, d4[q]));
    }
    auto qdst = [&](int T) { const int gi = 4 * T + lg, hd = gi / 5, pt = gi - 5 * hd; return lds + L_Q + rp * 10240 + hd * 640 + (pt < 4 ? 128 + l15 * 32 + pt * 8 : l15 * 8); };
    tile(0, true, qdst(cg), 2560);
    if (cg == 0) tile(1, true, qdst(4), 2560);
    if (wave == 3) boot_wait(a);
    VMDRAIN(); LBAR();
    const rsrc_t rfl = MKR(a.ws + WS_HOP, F_ERR), rq = MKR(a.ws + WS_PQ, S_ * D_ * 2);
    if (wave == 3 && lane == 0) { st4c(rfl, F_QK + (rb * 8 + g) * FLP, 1u); if (rb == 0 && g == 0) STAMP(16); STAMP(1400 + rb * 8 + g); }
#pragma unroll
    for (int j = 0; j < 3; ++j) st16c(rq, (j < 2 || lane < 32) ? (rb * 8 + wave) * 20480 + g * 2560 + j * 1024 + lane * 16 : OOB, lds16(lds + L_Q + wave * 2560 + j * 1024 + lane * 16));
    VMDRAIN(); LBAR();
    if (wave == 3 && lane == 0) st4c(rfl, F_PQ + (rb * 8 + g) * FLP, 1u);
}
__device__ __forceinline__ void kv_part(KA& a, unsigned char* lds, const int i, const int wave) {
    const int lane = lane_id(), l15 = lane & 15, lg = lane >> 4, h = 2 * i + (wave >> 2);
    const rsrc_t rk = MKR(a.ws + WS_KVP, 32 * 16 * 4096);
    if (wave == 7) wait_set(MKR(a.ws + WS_HOP, F_ERR), F_QK + (l15 * 8 + (h >> 2)) * FLP, a.ws + WS_HOP, 0);
    LBAR();
    u32x4 v[4][4];
#pragma unroll
    for (int q = 0; q < 16; ++q) v[q >> 2][q & 3] = __builtin_amdgcn_raw_buffer_load_b128(rk, (((q & 2) == 0 || lg == 0) && (!(q & 1) || l15 < 8)) ? lane * 16 + (q & 3) * 1024 : OOB, (i * 32 + wave * 4 + (q >> 2)) * 4096, 16);
#pragma unroll
    for (int q = 0; q < 4; ++q) *reinterpret_cast<f32x4*>(lds + 32768 + ((wave >> 2) * 16 + q * 4 + (wave & 3)) * 1024 + lane * 16) = (__builtin_bit_cast(f32x4, v[0][q]) + __builtin_bit_cast(f32x4, v[1][q])) + (__builtin_bit_cast(f32x4, v[2][q]) + __builtin_bit_cast(f32x4, v[3][q]));
    LBAR();
    const f32x4* p = reinterpret_cast<const f32x4*>(lds + 32768 + wave * 4096 + lane * 16);
    const f32x4 s = (p[0] + p[64]) + (p[128] + p[192]);
    const int e = 16 * (wave & 1) + l15, d = 8 * (wave & 2) + 4 * lg;
    st8c(MKR(a.ws + WS_KVF, 32768), (d < 24 && e <= 20) ? h * 1024 + e * 48 + d * 2 : OOB, (u32x2){min(pk2(s[0], s[1]), 0xfffffffeu), min(pk2(s[2], s[3]), 0xfffffffeu)});
}
__device__ __forceinline__ void boot_part(KA& a, unsigned char* lds, const int i, const int wave) {
    const int lane = lane_id(), l15 = lane & 15, lg = lane >> 4, w = i * 8 + wave;
    const rsrc_t rb = MKR((a.ws + WS_HOP) - 2048, 2048), rz = MKR((a.ws + WS_HOP), F_AF), rh = MKR(al2m((a.ws + WS_FR)), FR_FILL), rf = MKR((a.ws + WS_WF_AO), D_ * D_ * 2);
    const u32x2 tag = ld8c(rb, 0);
    {
        const unsigned c = (w & 7) * 64 + lane, b = ((w >> 4) & 1) * 8 + (c >> 6), m = c & 15u;
        u32x4 v = (u32x4){~0u, ~0u, ~0u, ~0u};
        if (b < 10u) { const unsigned* mp = reinterpret_cast<const unsigned*>(a.mem0 + (2 * (m >> 2) + (m & 1)) * D_ + b * 64 + ((c >> 4) & 3u) * 16 + (m & 2));
            v = (u32x4){min(mp[0], 0xfffffffeu), min(mp[2], 0xfffffffeu), min(mp[4], 0xfffffffeu), min(mp[6], 0xfffffffeu)}; }
        else if (!(c & 3u)) v.x = c == 196u ? TT_E1 + TT_E2 + 40u : TT_E1 + 20u;
        st16c(rh, (w >> 5) * (20u << 14) + (w & 31) * 1024 + lane * 16, v);
    }
    st16c(rz, w * 1024 + lane * 16, (u32x4){0u, 0u, 0u, 0u});
    st16c(MKR(a.ws + WS_KVF, 65536), w * 1024 + lane * 16, (u32x4){~0u, ~0u, ~0u, ~0u});
    VMDRAIN(); LBAR();
    if (wave == 0 && lane == 0) st8c(rb, 128 + i * 128, (u32x2){tag.x + 1u, ~tag.y});
    if (w == 0 && lane == 0) { const int layer = 0; STAMP(20); }
    u32x4 v[4][4];
#pragma unroll
    for (int q = 0; q < 16; ++q) { const int u = min(w + 64 * (q >> 2), 199), kk = u / 10; v[q >> 2][q & 3] = *reinterpret_cast<const u32x4*>(a.wao + (kk * 32 + (q & 3) * 8 + (lane >> 3)) * D_ + (u - kk * 10) * 64 + (lane & 7) * 8); }
    unsigned char* t = lds + wave * 16384;
#pragma unroll
    for (int q = 0; q < 16; ++q) *reinterpret_cast<u32x4*>(t + q * 1024 + lane * 16) = v[q >> 2][q & 3];
    CBAR();
#pragma unroll
    for (int q = 0; q < 16; ++q) { const int u = w + 64 * (q >> 2), kk = u / 10; const unsigned char* p = t + (q >> 2) * 4096 + (8 * lg + (l15 >> 2)) * 128 + (q & 3) * 32 + (l15 & 3) * 8; const u32x2 x0 = ldstr(p), y0 = ldstr(p + 512);
        if (u < 200) st16c(rf, ((((u - kk * 10) * 4 + (q & 3)) * 20 + kk) * 64 + lane) * 16, (u32x4){x0.x, x0.y, y0.x, y0.y}); }
    boot_wait(a);
    VMDRAIN(); LBAR();
    if (wave == 0 && lane == 0) st4c(rz, F_WO + i * FLP, 1u);
    pol_vote(a, w);
    if (w == 0 && lane == 0) { const int layer = 0; STAMP(21); }
    LBAR();
}

#define ATS 62
#define ATU 0
#define AIM 12
template <int K, bool W3, class F> __device__ __forceinline__ void a_proj(KA& a, unsigned char* lds, const int h, const int wave, F&& pre) {
    constexpr int YB = 51200, L_SK = 3 * YB, L_C = L_SK + 6144 + 64, L_KN = L_C + 256, KP = 48, layer = 0;
    const int lane = lane_id(), l15 = lane & 15, lg = lane >> 4, rt = wave & 1;
    const unsigned ldsb = LDSB();
    unsigned char* hop = (a.ws + WS_HOP);
    const rsrc_t rfl = MKR(hop, F_ERR), rp = MKR((a.ws + WS_PQ), S_ * D_ * 2), rxa = MKR((a.ws + WS_X), XI_BYTES);
    u32x4 wq[2][20]; f32x4 cs[2] = {Z4, Z4}, d4[2][2] = {{Z4, Z4}, {Z4, Z4}};
    if (K) {
        const rsrc_t rw = MKR((a.ws + WS_WF_AQKV), 640 * 1920 * 2);
#pragma unroll
        for (int u = 0; u < 2; ++u) { const int c = 20 * h + (K == 2 ? (1 + u) * 640 + l15 : (u ? min(l15 >> 2, 2) * 640 + 16 + (l15 & 3) : l15));
#pragma unroll
            for (int q = 0; q < 20; ++q) wq[u][q] = __builtin_amdgcn_raw_buffer_load_b128(rw, ((c >> 4) * 20 * 64 + lg * 16 + (c & 15)) * 16, q * 1024, 16); }
#pragma unroll
        for (int q = 0; q < 40; ++q) cs[q & 1] = dmf(wq[q & 1][q >> 1], (u32x4){0x3F803F80u, 0x3F803F80u, 0x3F803F80u, 0x3F803F80u}, cs[q & 1]);
        PIN(cs[0]); PIN(cs[1]); VMKNOWN();
    }
    const unsigned f0 = F_OF + lane * FLP, f1 = F_OF + (64 + l15 + (lane & 16) * 5) * FLP;
    unsigned tnext = rt_now(), per = 0;
    auto kv = [&](int t) {
        u32x4 A[2], B[2];
#pragma unroll
        for (int q = 0; q < 2; ++q) { const unsigned char* p = lds + L_SK + (t & 1) * 64 * KP + (8 * lg + (l15 >> 2)) * KP + 32 * q + 8 * (l15 & 3);
            const u32x2 a0 = ldstr(p), a1 = ldstr(p + 4 * KP), b0 = ldstr(p + 32 * KP), b1 = ldstr(p + 36 * KP);
            A[q] = (u32x4){a0.x, a0.y, a1.x, a1.y}; B[q] = (u32x4){b0.x, b0.y, b1.x, b1.y}; }
        if (l15 >= 4) { A[1] = (u32x4){0u, 0u, 0u, 0u}; B[1] = (u32x4){0x3F803F80u, 0x3F803F80u, 0x3F803F80u, 0x3F803F80u}; }
#pragma unroll
        for (int q = 0; q < 4; ++q) d4[q >> 1][q & 1] = dmf(A[q >> 1], B[q & 1], d4[q >> 1][q & 1]); };
    auto fill = [&](int n, unsigned dst, int g0) {
#pragma unroll
        for (int g = 0; g < 2; ++g) { const unsigned o = min(((g0 + g + (blockIdx.x >> 3) * AROT) & 3) * 13, 37) * 1024; dma16cn<13>(rxa, lane * 16, (unsigned)n * XI_PITCH + o, dst + o); } };
    int nx = 0, kn = 0, yb = 0;
    auto seg = [&](const int t) {
        AT(0);
        if (nx == t) {
            if (kn == t) {
                if (W3) { if (t) kv(t - 1);
                    if (t < TF) rt_wait(tnext);
                    for (unsigned sp = 0;; ++sp) { CBAR(); const u32x4 v = ld16c(rfl, f0 + t * (NR_ * FLP)); const unsigned w = ld4c(rfl, F_OF + (64 + l15 + t * NR_) * FLP);
                        if (__all(v.x != 0u && w != 0u)) { per = __builtin_amdgcn_readfirstlane(v.x); tnext = __builtin_amdgcn_readfirstlane(v.w) + per + (t + 1 < TF - 6 ? TT_AD : TT_AN); break; }
                        if (t < TF) __builtin_amdgcn_s_sleep(40); else __builtin_amdgcn_s_sleep(3);
                        if (sp > SPIN_CAP_W) { set_err(hop); break; } } }
                LBAR(); ++kn;
            } else if (W3 && t) kv(t - 1);
            if (!K && !W3 && wave == 0) { fill(t, ldsb + yb, 0); fill(t, ldsb + yb, 2); VMDRAIN(); }
            ++nx;
            LBAR();
        } else if (W3) kv(t - 1);
        AT(1);
        if (lane == 0 && wave == 4 && h == 0 && t >= 56) STAMP(330 + t - 56);
        const int up = min(kn, t + 3), n = nx + ((nx ^ wave) & 1);
        const unsigned nb = ldsb + (n == t + 1 ? (yb == 2 * YB ? 0 : yb + YB) : (yb == 0 ? 2 * YB : yb - YB));
        u32x4 fa, fb; unsigned fc; int pr = 0; f32x4 acc[2] = {Z4, Z4};
        if (K) {
            const unsigned char* yp = lds + yb + XI_X + rt * 20480 + lane * 16;
            u32x4 yf[4];
#pragma unroll
            for (int q = 0; q < 3; ++q) { yf[q] = lds16(yp + q * 1024); SBAR(); }
#pragma unroll
            for (int q = 0; q < 20; ++q) { acc[0] = dmf(wq[0][q], yf[q & 3], acc[0]); PIN(acc[0]); SBAR(); if (q + 3 < 20) { yf[(q + 3) & 3] = lds16(yp + (q + 3) * 1024); SBAR(); }
                acc[1] = dmf(wq[1][q], yf[q & 3], acc[1]); PIN(acc[1]); SBAR(); }
        } else if (!W3 && wave == 0) {
            const f32x2* sp = reinterpret_cast<const f32x2*>(lds + yb + lg * 2560 + l15 * 8);
            f32x2 sv[20];
#pragma unroll
            for (int q = 0; q < 20; ++q) sv[q] = sp[q * 16];
#pragma unroll
            for (int q = 0; q < 16; ++q) sv[q & 3] += sv[q + 4];
            const u32x2 s4[4] = {__builtin_bit_cast(u32x2, sv[0]), __builtin_bit_cast(u32x2, sv[1]), __builtin_bit_cast(u32x2, sv[2]), __builtin_bit_cast(u32x2, sv[3])};
            c_put(s4, lds + L_C);
        } else if (W3) {
            const int d = (int)(rt_now() - tnext);
            pr = kn >= NSEG_ ? 0 : (kn >= TF || d >= (int)per ? 2 : (d >= 0 ? 1 : 0));
            if (kn + 1 >= NSEG_) pr = min(pr, 1);
            fa = __builtin_amdgcn_raw_buffer_load_b128(rfl, pr ? f0 : OOB, kn * (NR_ * FLP), 16); fb = __builtin_amdgcn_raw_buffer_load_b128(rfl, pr > 1 ? f0 : OOB, (kn + 1) * (NR_ * FLP), 16);
            fc = __builtin_amdgcn_raw_buffer_load_b32(rfl, pr > ((lane >> 4) & 1) ? f1 : OOB, kn * (NR_ * FLP), 16); SBAR();
        } else if (n < up) fill(n, nb, 0);
        AT(2);
        LBAR();
        AT(3);
        if (K) {
            const f32x2 ct = *reinterpret_cast<const f32x2*>(lds + L_C + rt * 128 + l15 * 8);
            float y[8], e[8];
#pragma unroll
            for (int q = 0; q < 8; ++q) y[q] = __builtin_fmaf(ct[0], cs[q >> 2][q & 3], acc[q >> 2][q & 3]) * ct[1];
            SBAR();
#pragma unroll
            for (int q = 0; q < 8 / K; ++q) e[q] = __builtin_amdgcn_fmed3f(y[q], 0.f, -3.0e38f) * 1.4426950408889634f;
            SBAR();
#pragma unroll
            for (int q = 0; q < 8 / K; ++q) e[q] = __builtin_amdgcn_exp2f(e[q]);
            SBAR();
#pragma unroll
            for (int q = 0; q < 8 / K; ++q) e[q] = (e[q] + 1e-6f) + __builtin_amdgcn_fmed3f(y[q], 0.f, 3.0e38f);
#pragma unroll
            for (int q = 4; q < 8; ++q) e[q] = (K == 2 || lg >= 2) ? y[q] : e[q];
            const u32x2 o0 = (u32x2){pk2(e[0], e[1]), pk2(e[2], e[3])}, o1 = (u32x2){pk2(e[4], e[5]), pk2(e[6], e[7])};
            unsigned char* sk = lds + L_SK + (t & 1) * 64 * KP + (rt * 16 + l15) * KP;
            if (K == 2) { *reinterpret_cast<u32x2*>(sk + 8 * lg) = o0; *reinterpret_cast<u32x2*>(sk + 32 * KP + 8 * lg) = o1; }
            else { *reinterpret_cast<u32x2*>(sk + (lg >= 2 ? 32 * KP + 32 : 40 - 8 * lg)) = o1;
                __builtin_amdgcn_raw_buffer_store_b64(o0, rp, 128 + l15 * 32 + lg * 8, (t * 2 + rt) * 20480 + h * 640, 16);
                __builtin_amdgcn_raw_buffer_store_b64(o1, rp, lg ? OOB : l15 * 8, (t * 2 + rt) * 20480 + h * 640, 16);
                if (t == 8) VMDRAIN(); }
        } else if (W3) {
            {
                const unsigned long long z = __ballot(fc == 0u);
                if (__all(fa.x != 0u) && !(z & 0xffffull)) { const bool two = __all(fb.x != 0u) && !z; kn += two ? 2 : 1;
                    per = __builtin_amdgcn_readfirstlane(two ? fb.x : fa.x); tnext = __builtin_amdgcn_readfirstlane(two ? fb.w : fa.w) + per + (kn < TF - 6 ? TT_AD : TT_AN); } }
            *reinterpret_cast<int*>(lds + L_KN + (t & 1) * 4) = kn;
            if (t == 40) wait_set(rfl, F_AQ + (lane & 31) * 4, hop, 4);
        } else if (wave) { if (n < up) fill(n, nb, 2); if (((t + 1) ^ wave) & 1) { } else VMDRAIN(); }
        nx = max(nx, up);
        AT(4);
        LBAR();
        AT(5);
        kn = __builtin_amdgcn_readfirstlane(*reinterpret_cast<const int*>(lds + L_KN + (t & 1) * 4));
        if (W3 && t == 8 && lane == 0) st4c(rfl, F_AQ + h * 4, 1u);
        yb = yb == 2 * YB ? 0 : yb + YB;
        AT(6); AT(7); ATD(5, t == ATS && h == 0);
    };
#pragma unroll 1
    for (int t = 0; t < 50; ++t) seg(t);
    pre();
#pragma unroll 1
    for (int t = 50; t < NSEG_; ++t) seg(t);
    if (W3) {
        kv(NSEG_ - 1);
        const rsrc_t rkv = MKR(a.ws + WS_KVF + 32768, 32768);
#pragma unroll
        for (int y = 0; y < 2; ++y)
#pragma unroll
            for (int x = 0; x < 2; ++x) { const int e = 16 * y + l15, d = 16 * x + 4 * lg;
                const u32x2 o = (u32x2){min(pk2(d4[x][y][0], d4[x][y][1]), 0xfffffffeu), min(pk2(d4[x][y][2], d4[x][y][3]), 0xfffffffeu)};
                st8c(rkv, (d < 24 && e <= 20) ? h * 1024 + e * 48 + d * 2 : OOB, o); }
    }
}
__device__ __forceinline__ void a_part(KA& a, unsigned char* lds, const int ai, const int wave) {
    const int lane = lane_id(), tid = wave * 64 + lane;
    constexpr int L_ST = 20480;
    const int cb = ai & 7, rep = ai >> 3, h = ai;
    const int nt = wave < 5 ? wave : 0;
    u32x4 b[20], tq[4]; u32x2 rres;
    const int ct = 5 * cb + nt;
    const rsrc_t rp = MKR((a.ws + WS_PQ), S_ * D_ * 2), rxa = MKR((a.ws + WS_X), XI_BYTES);
    auto fetch = [&](int u) { const int lane = lane_id(), l15 = lane & 15, lg = lane >> 4;
#pragma unroll
        for (int q = 0; q < 4; ++q) tq[q] = __builtin_amdgcn_raw_buffer_load_b128(rp, (lg < 2 ? 128 + l15 * 32 + lg * 16 : (lg == 2 ? l15 * 8 : OOB)) + q * 640, u * 20480 + wave * 2560, 16); };
    auto x_off = [&]() { const int lane = lane_id(), l15 = lane & 15, lg = lane >> 4; return XI_X + (ct >> 1) * 1024 + ((ct & 1) * 2 + (lg >> 1)) * 256 + l15 * 16 + (lg & 1) * 8; };
    auto b_load = [&]() { const int lane = lane_id();
#pragma unroll
        for (int q = 0; q < 20; ++q) b[q] = __builtin_amdgcn_raw_buffer_load_b128(MKR((a.ws + WS_WF_AO), D_ * D_ * 2), wave < 5 ? lane * 16 + (q & 3) * 1024 : OOB, ct * 20480 + (q >> 2) * 4096, 16); };
    auto outp = [&](const int layer) {
        const int lane = lane_id(), l15 = lane & 15, lg = lane >> 4;
        unsigned char* hop = (a.ws + WS_HOP) + layer * HOP_BYTES;
        const rsrc_t rfl = MKR(hop, F_ERR), rr_ = MKR(psel(a.x0, (a.ws + WS_X), layer), layer ? XI_BYTES : S_ * D_ * 2);
        const unsigned oo = x_off(), ro = layer ? oo : (unsigned)(l15 * D_ + 16 * ct + 4 * lg) * 2;
        auto res = [&](int u) { rres = __builtin_amdgcn_raw_buffer_load_b64(rr_, ro, (u >> 1) * (layer ? XI_PITCH : 40960) + (u & 1) * 20480, 16); };
        if (!layer) { boot_wait(a); wait_set(rfl, F_WO + (lane & 7) * FLP, hop, 1); b_load(); res(rep); if (ai < 16) { VMKNOWN(); kv_part(a, lds, ai, wave); } wait_set(rfl, F_PQ + (lane & 7) * FLP, hop, 1); fetch(rep); }
        unsigned so[5];
#pragma unroll
        for (int q = 0; q < 5; ++q) { const unsigned c = 80 * wave + (q < 4 ? 20 * q + 4 * lg : 20 * lg + 16); so[q] = (c >> 3) * 256 + l15 * 16 + (c & 4) * 2; }
        u32x4 k0[4], k1[4], kz[4];
        {
            const rsrc_t rkv = MKR(a.ws + WS_KVF + layer * 32768, 32768);
            const unsigned ko = (lg < 3 ? lg : 0) * 16;
            for (unsigned sp = 0; sp < SPIN_CAP_W; ++sp) { CBAR();
                unsigned mx = 0u;
#pragma unroll
                for (int q = 0; q < 4; ++q) { k0[q] = __builtin_amdgcn_raw_buffer_load_b128(rkv, ko + l15 * 48 + q * 1024, wave * 4096, 16);
                    k1[q] = __builtin_amdgcn_raw_buffer_load_b128(rkv, (l15 >> 2) == q ? ko + (16 + (l15 & 3)) * 48 + q * 1024 : OOB, wave * 4096, 16); kz[q] = __builtin_amdgcn_raw_buffer_load_b128(rkv, ko + 960 + q * 1024, wave * 4096, 16); }
#pragma unroll
                for (int q = 0; q < 4; ++q) mx = max(mx, max(umax4(k0[q]), max(umax4(k1[q]), umax4(kz[q]))));
                if (__all(mx != 0xffffffffu)) break;
                __builtin_amdgcn_s_sleep(2);
            }
            if (tid == 0 && ai == 0) STAMP(4);
        }
#pragma unroll 1
        for (int u = rep; u < 2 * NSEG_; u += 4) {
            if (!layer && u >= 4 && u < 8) { wait_set(rfl, F_PQ + lane * FLP, hop, 1); wait_set(rfl, F_PQ + (64 + lane) * FLP, hop, 1); }
            if (layer && u >= 8 && u < 12) wait_set(rfl, F_AQ + (lane & 31) * 4, hop, 1);
            if (layer) AT(0);
            {
                f32x4 dz[4], d0[4], d1 = Z4;
#pragma unroll
                for (int q = 0; q < 4; ++q) dz[q] = dmf(kz[q], tq[q], Z4);
#pragma unroll
                for (int q = 0; q < 4; ++q) { d0[q] = dmf(k0[q], tq[q], Z4); d1 = dmf(k1[q], tq[q], d1); }
                SBAR(); fetch(min(u + 4, 2 * NSEG_ - 1)); SBAR();
                float inv[4];
#pragma unroll
                for (int q = 0; q < 4; ++q) inv[q] = __builtin_amdgcn_rcpf(dz[q][0] + 1e-6f);
#pragma unroll
                for (int q = 0; q < 4; ++q) { d0[q] *= inv[q]; *reinterpret_cast<u32x2*>(lds + so[q]) = (u32x2){pk2(d0[q][0], d0[q][1]), pk2(d0[q][2], d0[q][3])}; }
                d1 *= lg < 2 ? (lg == 0 ? inv[0] : inv[1]) : (lg == 2 ? inv[2] : inv[3]);
                *reinterpret_cast<u32x2*>(lds + so[4]) = (u32x2){pk2(d1[0], d1[1]), pk2(d1[2], d1[3])};
            }
            f32x4 acc = (f32x4){lo2f(rres.x), hi2f(rres.x), lo2f(rres.y), hi2f(rres.y)}; PIN(acc);
            if (layer) AT(1);
            LBAR();
            if (layer) AT(2);
            if (layer && u == rep && tid == 448) st4c(rfl, F_AQ + h * 4, 1u);
            if (wave < 5) {
                const unsigned char* ar = lds + lane * 16;
                u32x4 af[8];
#pragma unroll
                for (int q = 0; q < 5; ++q) { af[q] = lds16(ar + 1024 * q); SBAR(); }
#pragma unroll
                for (int q = 0; q < 20; ++q) { acc = dmf(b[q], af[q & 7], acc); PIN(acc); SBAR();
                    if (q + 5 < 20) { af[(q + 5) & 7] = lds16(ar + 1024 * (q + 5)); SBAR(); } }
                const u32x2 o = (u32x2){pk2(acc[0], acc[1]), pk2(acc[2], acc[3])};
                __builtin_amdgcn_raw_buffer_store_b64(o, rxa, oo, (u >> 1) * XI_PITCH + (u & 1) * 20480, 16);
                *reinterpret_cast<float*>(lds + L_ST + wave * 128 + l15 * 8 + (lg & 1) * 4) = row_sums(o);
            }
            if (layer) AT(3);
            LBAR();
            if (layer) AT(4);
            if (wave == 0) {
                if (lane < 32) { const float* sp = reinterpret_cast<const float*>(lds + L_ST + lane * 4); const float v = ((sp[0] + sp[32]) + (sp[64] + sp[96])) + sp[128];
                    __builtin_amdgcn_raw_buffer_store_b32(__float_as_uint(v), rxa, XI_S8 + cb * 128 + lane * 4, (u >> 1) * XI_PITCH + (u & 1) * 1024, 16); }
                if (lane == 0 && u >= AIM + 4) st4c(rfl, F_AF + ((u - 4) * 8 + cb) * FLP, 1u); }
            if (u < AIM || u + 4 >= 2 * NSEG_) { VMKNOWN(); LBAR(); if (tid == 0) st4c(rfl, F_AF + (u * 8 + cb) * FLP, 1u); }
            if (tid == 0 && ai == 0 && u == 0) STAMP(5);
            if (tid == 0 && ai == 0 && u == 124) STAMP(6);
            if (layer) { AT(5); AT(6); AT(7); }
            ATD(5, layer && u == ATU && ai == 0);
            CBAR(); res(min(u + 4, 2 * NSEG_ - 1));
        }
    };
    outp(0);
    VMKNOWN();
    auto pre = [&]() { fetch(rep); rres = __builtin_amdgcn_raw_buffer_load_b64(rxa, x_off(), (rep >> 1) * XI_PITCH + (rep & 1) * 20480, 16); VMKNOWN(); };
    if (wave >= 4) { if (wave >= 6) a_proj<2, false>(a, lds, h, wave, pre); else a_proj<1, false>(a, lds, h, wave, pre); b_load(); outp(1); }
    else { if (wave == 3) a_proj<0, true>(a, lds, h, wave, pre); else a_proj<0, false>(a, lds, h, wave, pre); outp(1); }
}

#define WDL 700
#define WDL2 400
#define WDL3 400
#define XPERM 0x76543210u
__global__ void __launch_bounds__(512, 2) KATTR bmega_kernel(MegaArgs) {
    ATI();
    extern __shared__ __attribute__((aligned(16))) unsigned char lds[];
    const int c = blockIdx.x, x = (XPERM >> ((c & 7) * 4)) & 7, rr = c >> 3, q = x >> 1, wave = __builtin_amdgcn_readfirstlane(threadIdx.x >> 6);
    if (LDSB() != 0u) return;
    const bool ev = (x & 1) == 0;
    if (rr < 8 || (!ev && rr < 20)) rt_wait(rt_now() + (rr < 8 ? (ev ? WDL : WDL2) : WDL3));
    if (rr < 8) { if (ev) s_part(args(), lds, q * 8 + rr, wave); else if (wave < 4) v_run<true>(args(), lds, q * 8 + rr, wave); else v_run<false>(args(), lds, q * 8 + rr, wave); }
    else if (!ev && rr < 20) xp_part(args(), lds, q * 12 + rr - 8, wave);
    else {
        if (ev || rr < 28) q_part(args(), lds, ev ? (rr - 8) >> 1 : (rr + 4) >> 1, 2 * q + (rr & 1), wave); else if (rr >= 30) boot_part(args(), lds, 2 * q + (rr & 1), wave);
        if (rr >= 28) a_part(args(), lds, (ev ? 16 : 0) + (rr - 28) * 4 + q, wave);
        else if (!ev) xq_part(args(), lds, q * 8 + rr - 20, wave);
        else { const int r = q * 20 + rr - 8;
            if (wave == 0 && !(r & 1) && lane_id() == 0) st4c(MKR(args().ws + WS_HOP, F_ERR), F_SD + (r >> 1) * 4, 1u + my_side());
            ro_part(args(), lds, r, wave); }
    }
}

extern "C" void kernel_launch(void* const* d_in, const int* in_sizes, int n_in, void* d_out, int out_size, void* d_ws, size_t ws_size, hipStream_t stream) {
    (void)hipFuncSetAttribute((const void*)bmega_kernel, hipFuncAttributeMaxDynamicSharedMemorySize, MG_LDS);
    MegaArgs ma; ma.wq = (const bf16_t*)d_in[5]; ma.wk = (const bf16_t*)d_in[6]; ma.wv = (const bf16_t*)d_in[7]; ma.wo = (const bf16_t*)d_in[8];
    ma.wqkv = (const bf16_t*)d_in[1]; ma.wao = (const bf16_t*)d_in[2]; ma.mem0 = (const bf16_t*)d_in[4];
    ma.x0 = (const bf16_t*)d_in[0]; ma.out = (bf16_t*)d_out; ma.ws = (unsigned char*)d_ws;
    bmega_kernel<<<256, 512, MG_LDS, stream>>>(ma);
}
```

```cpp
#include <hip/hip_runtime.h>
#include <hip/hip_bf16.h>
#include <stdint.h>
#include <type_traits>

typedef __attribute__((ext_vector_type(8))) short bf16x8;
typedef __attribute__((ext_vector_type(4))) float f32x4;
typedef __attribute__((ext_vector_type(4))) unsigned u32x4;
typedef __attribute__((ext_vector_type(2))) unsigned u32x2;
typedef uint16_t bf16_t;

typedef __attribute__((ext_vector_type(2))) __bf16 bf16x2_t;
typedef __attribute__((ext_vector_type(2))) float f32x2;
__device__ __forceinline__ unsigned pk2(float lo, float hi) { f32x2 v = {lo, hi}; return __builtin_bit_cast(unsigned, __builtin_convertvector(v, bf16x2_t)); }
__device__ __forceinline__ float rbf(float f) { return __uint_as_float(pk2(f, f) << 16); }
__device__ __forceinline__ float lo2f(unsigned p) { return __uint_as_float(p << 16); }
__device__ __forceinline__ float hi2f(unsigned p) { return __uint_as_float(p & 0xffff0000u); }

constexpr int S_ = 2048, D_ = 640, H_ = 32, E_ = 192, NSEG_ = 64;
constexpr int HE_ = H_ * E_;

constexpr int NR_ = 80;
constexpr int NXP_ = 48;
constexpr size_t al(size_t x) { return (x + 255) & ~(size_t)255; }
constexpr size_t WS_WF_AQKV = 0;
constexpr size_t WS_WF_AO   = WS_WF_AQKV + al((size_t)640 * 1920 * 2);
constexpr size_t WS_PQ      = WS_WF_AO + al((size_t)640 * 640 * 2);
constexpr unsigned XI_X = 10240, XI_S8 = XI_X + 40960, XI_PITCH = XI_S8 + 2048, XI_BYTES = NSEG_ * XI_PITCH;
constexpr size_t WS_X       = WS_PQ + al((size_t)S_ * D_ * 2);
constexpr size_t WS_KVP     = WS_X + al(XI_BYTES);
constexpr size_t WS_KVF     = WS_KVP + al((size_t)32 * 16 * 4096);
constexpr size_t WS_ST      = WS_KVF + al((size_t)32 * 1024 * 2);
#define KXR 32
#define PR 32
#define URING 32
constexpr unsigned ST_KX = 0;
constexpr unsigned ST_VX = ST_KX + KXR * 32u * 12288;
constexpr unsigned ST_KM = ST_VX + KXR * 32u * 12288;
constexpr unsigned ST_PX = ST_KM + PR * 32u * 3072;
constexpr unsigned ST_U  = ST_PX + PR * 2u * 40960;
constexpr unsigned ST_BYTES = ST_U + (unsigned)URING * 40 * 32768;
constexpr size_t WS_FR      = WS_ST + al(ST_BYTES);
constexpr unsigned FR_FILL = 40u << 14, FR_UM = FR_FILL, FR_USED = FR_UM + (160u << 14);
__device__ __forceinline__ unsigned char* al2m(unsigned char* p) { return (unsigned char*)(((uintptr_t)p + (((uintptr_t)1 << 21) - 1)) & ~(((uintptr_t)1 << 21) - 1)); }
constexpr unsigned M_STAMP = 16384 + 2048;
constexpr unsigned FLP = 128;
constexpr unsigned F_POL = 0, F_AQ = F_POL + 256, F_SD = F_AQ + 256, F_WO = F_SD + 256, F_IN = F_WO + 8 * FLP, F_QK = F_IN + 8 * FLP, F_PQ = F_QK + 128 * FLP, F_AF = 36864, F_XP = F_AF + 64 * 16 * FLP, F_PX = F_XP + 64 * NXP_ * FLP, F_KM = F_PX + 70 * 64 * FLP, F_UX = F_KM + 64 * 32 * FLP,
    F_OF = F_UX + 66 * 32 * FLP, F_RD = F_OF + 64 * NR_ * FLP, F_ERR = F_RD + 64 * NR_ * FLP, HOP_BYTES = F_ERR + 256;
constexpr size_t WS_BOOT    = WS_FR + ((size_t)6 << 20);
constexpr size_t WS_HOP     = WS_BOOT + 2048;
constexpr size_t WS_DBG     = WS_HOP + al(2 * HOP_BYTES);
constexpr size_t WS_END     = WS_DBG + al((size_t)2 << 20);
static_assert(WS_END <= (size_t)256 * 1024 * 1024, "ws too big");
static_assert(FR_USED <= (4u << 20), "hand-off pages");

__device__ __forceinline__ float phi1(float x) { return (__builtin_amdgcn_exp2f(fminf(x, 0.f) * 1.4426950408889634f) + 1e-6f) + fmaxf(x, 0.f); }
__device__ __forceinline__ unsigned phi2(float x0, float x1) { return pk2(phi1(x0), phi1(x1)); }
constexpr unsigned OOB = 0x40000000u;
#define CBAR() asm volatile("" ::: "memory")
#define VMDRAIN() asm volatile("s_waitcnt vmcnt(0)" ::: "memory")
#define PIN(x) asm volatile("" : "+v"(x))
#define VMKNOWN() __builtin_amdgcn_s_waitcnt(0x0F70)
#define LBAR() do { asm volatile("s_waitcnt lgkmcnt(0)" ::: "memory"); __builtin_amdgcn_s_barrier(); asm volatile("" ::: "memory"); } while (0)
typedef __amdgpu_buffer_rsrc_t rsrc_t;
__device__ __forceinline__ u32x4 ld16c(rsrc_t r, unsigned off) { return __builtin_amdgcn_raw_buffer_load_b128(r, off, 0, 16); }
__device__ __forceinline__ u32x2 ld8c(rsrc_t r, unsigned off) { return __builtin_amdgcn_raw_buffer_load_b64(r, off, 0, 16); }
__device__ __forceinline__ unsigned ld4c(rsrc_t r, unsigned off) { return __builtin_amdgcn_raw_buffer_load_b32(r, off, 0, 16); }
__device__ __forceinline__ void st16c(rsrc_t r, unsigned off, u32x4 v) { __builtin_amdgcn_raw_buffer_store_b128(v, r, off, 0, 16); }
__device__ __forceinline__ void st8c(rsrc_t r, unsigned off, u32x2 v) { __builtin_amdgcn_raw_buffer_store_b64(v, r, off, 0, 16); }
__device__ __forceinline__ void st4c(rsrc_t r, unsigned off, unsigned v) { __builtin_amdgcn_raw_buffer_store_b32(v, r, off, 0, 16); }
template <int CTRL> __device__ __forceinline__ unsigned dppu(unsigned x) { return (unsigned)__builtin_amdgcn_mov_dpp((int)x, CTRL, 0xf, 0xf, true); }
template <int CTRL> __device__ __forceinline__ float dppf(float x) { return __uint_as_float(dppu<CTRL>(__float_as_uint(x))); }
__device__ __forceinline__ float xg_max(float x) {
    auto s = __builtin_amdgcn_permlane16_swap(__float_as_uint(x), __float_as_uint(x), false, false); x = fmaxf(__uint_as_float(s[0]), __uint_as_float(s[1]));
    auto t = __builtin_amdgcn_permlane32_swap(__float_as_uint(x), __float_as_uint(x), false, false); return fmaxf(__uint_as_float(t[0]), __uint_as_float(t[1])); }
__device__ __forceinline__ float xg_sum(float x) {
    auto s = __builtin_amdgcn_permlane16_swap(__float_as_uint(x), __float_as_uint(x), false, false); x = __uint_as_float(s[0]) + __uint_as_float(s[1]);
    auto t = __builtin_amdgcn_permlane32_swap(__float_as_uint(x), __float_as_uint(x), false, false); return __uint_as_float(t[0]) + __uint_as_float(t[1]); }

typedef __attribute__((ext_vector_type(8))) unsigned u32x8;
typedef __attribute__((ext_vector_type(8))) __bf16 bfv8;
typedef __attribute__((ext_vector_type(16))) __bf16 bfv16;
typedef short v4i16_t __attribute__((ext_vector_type(4)));
typedef __attribute__((address_space(3))) volatile unsigned lds_u32;
#define SBAR() __builtin_amdgcn_sched_barrier(0)
__device__ __forceinline__ int lane_id() { int l; asm volatile("v_mbcnt_lo_u32_b32 %0, -1, 0\n\tv_mbcnt_hi_u32_b32 %0, -1, %0" : "=v"(l)); return l; }
#define Z4 ((f32x4){0.f, 0.f, 0.f, 0.f})
__device__ __forceinline__ f32x4 smf(u32x4 A, u32x8 B, f32x4 c, unsigned idx) { return __builtin_amdgcn_smfmac_f32_16x16x64_bf16(__builtin_bit_cast(bfv8, A), __builtin_bit_cast(bfv16, B), c, (int)idx, 0, 0); }
__device__ __forceinline__ f32x4 dmf(u32x4 A, u32x4 B, f32x4 c) { return __builtin_amdgcn_mfma_f32_16x16x32_bf16(__builtin_bit_cast(bfv8, A), __builtin_bit_cast(bfv8, B), c, 0, 0, 0); }
__device__ __forceinline__ u32x4 lo4(u32x8 v) { return (u32x4){v[0], v[1], v[2], v[3]}; }
__device__ __forceinline__ u32x4 hi4(u32x8 v) { return (u32x4){v[4], v[5], v[6], v[7]}; }
__device__ __forceinline__ u32x8 cat8(u32x4 a, u32x4 b) { return (u32x8){a.x, a.y, a.z, a.w, b.x, b.y, b.z, b.w}; }
__device__ __forceinline__ u32x4 lds16(const unsigned char* p) { return *reinterpret_cast<const u32x4*>(p); }
__device__ __forceinline__ u32x4 lds16a(int ad) { return *(const __attribute__((address_space(3))) u32x4*)(uintptr_t)(unsigned)ad; }
__device__ __forceinline__ unsigned lds4a(int ad) { return *(const __attribute__((address_space(3))) unsigned*)(uintptr_t)(unsigned)ad; }
__device__ __forceinline__ f32x2 lds8a(int ad) { return *(const __attribute__((address_space(3))) f32x2*)(uintptr_t)(unsigned)ad; }
__device__ __forceinline__ void sts8a(int ad, f32x2 v) { *(__attribute__((address_space(3))) f32x2*)(uintptr_t)(unsigned)ad = v; }
__device__ __forceinline__ void sts4a(int ad, unsigned v) { *(__attribute__((address_space(3))) unsigned*)(uintptr_t)(unsigned)ad = v; }
__device__ __forceinline__ void sts16a(int ad, u32x4 v) { *(__attribute__((address_space(3))) u32x4*)(uintptr_t)(unsigned)ad = v; }
__device__ __forceinline__ u32x2 ldstr(const unsigned char* p) { return __builtin_bit_cast(u32x2, __builtin_amdgcn_ds_read_tr16_b64_v4i16((__attribute__((address_space(3))) v4i16_t*)p)); }
__device__ __forceinline__ unsigned umax4(u32x4 v) { return max(max(v.x, v.y), max(v.z, v.w)); }
__device__ __forceinline__ void dma16(rsrc_t r, unsigned voff, unsigned soff, unsigned ldsd) {
    unsigned keep;
    asm volatile("s_mov_b32 %0, m0\n\ts_mov_b32 m0, %1\n\ts_nop 2\n\tbuffer_load_dwordx4 %2, %3, %4 offen lds\n\ts_mov_b32 m0, %0" : "=&s"(keep) : "s"(ldsd), "v"(voff), "s"(r), "s"(soff) : "memory");
}
__device__ __forceinline__ void dma16c(rsrc_t r, unsigned voff, unsigned soff, unsigned ldsd) {
    unsigned keep;
    asm volatile("s_mov_b32 %0, m0\n\ts_mov_b32 m0, %1\n\ts_nop 2\n\tbuffer_load_dwordx4 %2, %3, %4 offen sc1 lds\n\ts_mov_b32 m0, %0" : "=&s"(keep) : "s"(ldsd), "v"(voff), "s"(r), "s"(soff) : "memory");
}
__device__ __forceinline__ void dma4c(rsrc_t r, unsigned voff, unsigned soff, unsigned ldsd) {
    unsigned keep;
    asm volatile("s_mov_b32 %0, m0\n\ts_mov_b32 m0, %1\n\ts_nop 2\n\tbuffer_load_dword %2, %3, %4 offen sc1 lds\n\ts_mov_b32 m0, %0" : "=&s"(keep) : "s"(ldsd), "v"(voff), "s"(r), "s"(soff) : "memory");
}
template <int N> __device__ __forceinline__ void dma16cn(rsrc_t r, unsigned voff, unsigned soff, unsigned ldsd) {
    unsigned keep;
    if (N >= 4) asm volatile("s_mov_b32 %0, m0\n\ts_mov_b32 m0, %1\n\ts_nop 2\n\tbuffer_load_dwordx4 %2, %3, %4 offen sc1 lds\n\tbuffer_load_dwordx4 %2, %3, %4 offen offset:1024 sc1 lds\n\t"
        "buffer_load_dwordx4 %2, %3, %4 offen offset:2048 sc1 lds\n\tbuffer_load_dwordx4 %2, %3, %4 offen offset:3072 sc1 lds\n\ts_mov_b32 m0, %0" : "=&s"(keep) : "s"(ldsd), "v"(voff), "s"(r), "s"(soff) : "memory");
    else if (N == 3) asm volatile("s_mov_b32 %0, m0\n\ts_mov_b32 m0, %1\n\ts_nop 2\n\tbuffer_load_dwordx4 %2, %3, %4 offen sc1 lds\n\tbuffer_load_dwordx4 %2, %3, %4 offen offset:1024 sc1 lds\n\t"
        "buffer_load_dwordx4 %2, %3, %4 offen offset:2048 sc1 lds\n\ts_mov_b32 m0, %0" : "=&s"(keep) : "s"(ldsd), "v"(voff), "s"(r), "s"(soff) : "memory");
    else if (N >= 2) asm volatile("s_mov_b32 %0, m0\n\ts_mov_b32 m0, %1\n\ts_nop 2\n\tbuffer_load_dwordx4 %2, %3, %4 offen sc1 lds\n\tbuffer_load_dwordx4 %2, %3, %4 offen offset:1024 sc1 lds\n\ts_mov_b32 m0, %0"
        : "=&s"(keep) : "s"(ldsd), "v"(voff), "s"(r), "s"(soff) : "memory");
    else dma16c(r, voff, soff, ldsd);
    constexpr int D = N >= 4 ? 4 : N;
    if constexpr (N > D) dma16cn<N - D>(r, voff, soff + D * 1024, ldsd + D * 1024);
}
__device__ __forceinline__ void st16s(u32x4 v, rsrc_t r, unsigned voff, unsigned soff) { __builtin_amdgcn_raw_buffer_store_b128(v, r, voff, soff, 16); asm volatile("s_nop 1" :: "v"(v) : "memory"); }
#define VMW(n) asm volatile("s_waitcnt vmcnt(%0)" :: "i"(n) : "memory")

struct MegaArgs { const bf16_t* wq; const bf16_t* wk; const bf16_t* wv; const bf16_t* wo; const bf16_t* wqkv; const bf16_t* wao; const bf16_t* mem0; const bf16_t* x0; bf16_t* out; unsigned char* ws; };
typedef const __attribute__((address_space(4))) MegaArgs KA;
__device__ __forceinline__ KA& args() { KA* p = (KA*)__builtin_amdgcn_kernarg_segment_ptr(); asm volatile("" : "+s"(p)); return *p; }
#define STAMP(idx) do { } while (0)
#define KATTR
#define ATI() do { } while (0)
#define AT(n) do { } while (0)
#define ATV do { } while (0)
#define ATD(role, on) do { } while (0)
#define SPINB 17
constexpr unsigned SPIN_CAP = 2u << SPINB;
constexpr unsigned SPIN_CAP_W = 1u << SPINB;
constexpr int MG_LDS = 163840;

#define PARAM(n, v) constexpr int n = v
#define TT_M1  12
#define TT_M2  13
#define TT_V   100
#define TT_V1 TT_V
#define TT_DEC 2
#define TT_BUMP 2
#define TT_DCY 3
#define TT_XS 5
#define TT_XD 1
#define TT_RT 34
#define TT_UM -2
#define TT_TW 8
#define TT_MD 60
#define TT_BD 12
#define TT_KD 120
#define TT_QD 20
#define TT_QN -40
#define TT_AD 250
#define XQD 0
#define SPRIO 2
#define PFD 6
#define DPS 5
#define PF0 2
#define KIM 6
#define XLD 9
#define NEAR 60
#define ROT 1
#define TF 60
#define RTS 12
#define RTL 8
#define R0S 4
#define VPRIO 0
#define XPD 4
#define TT_FP 13
#define TT_OD 30
#define TT_VD 2
#define YP1 0
#define QA 3
#define OIM 63
#define TT_UX 0
#define RLATE 0
#define PLATE 0
#define SLATE1 0
#define SLATE2 0
#define TT_TL 250
#define TT_AN 110
#define RROT 0
#define AROT 0
#define RFD 7
#define SFD 6
#define STK 0
#define VFD 6
#define VTK 2
#define VH 0
#define OSL 6
#define YP2 0
#define YA2 1
#define YD 0
#define YNB 6
#define TT_UP 2
#define TT_DN 3
#define OLAG 3
constexpr int TT_E1 = 132, TT_E2 = 48;
__device__ __forceinline__ unsigned rt_now() { return (unsigned)__builtin_amdgcn_s_memrealtime(); }
__device__ __forceinline__ void rt_wait(unsigned t) {
    unsigned d;
    asm volatile("s_branch 2f\n"
        "1:\n\ts_cmp_lt_u32 %0, 12\n\ts_cbranch_scc1 2f\n\ts_sleep 2\n\ts_cmp_lt_u32 %0, 30\n\ts_cbranch_scc1 2f\n\ts_sleep 5\n"
        "2:\n\ts_memrealtime vcc\n\ts_waitcnt lgkmcnt(0)\n\ts_sub_u32 %0, %1, vcc_lo\n\ts_cmp_lt_u32 %0, 20000\n\ts_cbranch_scc1 1b" : "=&s"(d) : "s"(t - 1u) : "scc", "vcc", "memory");
}
#define FINE 1
#define FC 100
template <int K> __device__ __forceinline__ void rt_fine(unsigned t) {
    if (!FINE) { rt_wait(t); return; }
    asm volatile("s_branch 2f\n"
        "1:\n\ts_cmp_lt_u32 s70, 12\n\ts_cbranch_scc1 2f\n\ts_sleep 2\n\ts_cmp_lt_u32 s70, 30\n\ts_cbranch_scc1 2f\n\ts_sleep 5\n"
        "2:\n\ts_memrealtime vcc\n\ts_waitcnt lgkmcnt(0)\n\ts_sub_u32 s70, %0, vcc_lo\n\ts_cmp_lt_u32 s70, 20000\n\ts_cbranch_scc1 1b\n\t"
        "s_cmp_lt_u32 s70, -8\n\ts_cbranch_scc1 5f\n\t"
        "s_memrealtime s[70:71]\n\ts_nop 4\n\ts_memrealtime s[72:73]\n\ts_nop 4\n\ts_memrealtime s[74:75]\n\ts_nop 4\n\ts_memrealtime s[76:77]\n\ts_nop 4\n\ts_memrealtime vcc\n\ts_waitcnt lgkmcnt(0)\n\t"
        "s_add_u32 s72, s72, s74\n\ts_add_u32 s76, s76, vcc_lo\n\ts_add_u32 s72, s72, s76\n\ts_lshl_b32 s71, s70, 2\n\ts_sub_u32 s72, s72, s71\n\ts_mul_i32 s72, s72, 6\n\t"
        "s_sub_u32 s70, %0, s70\n\ts_mul_i32 s70, s70, %1\n\ts_sub_i32 s70, s70, s72\n\ts_add_i32 s70, s70, %2\n\ts_max_i32 s70, s70, 0\n\ts_min_i32 s70, s70, 255\n\ts_lshr_b32 s70, s70, 3\n\ts_lshl_b32 s70, s70, 2\n\t"
        "s_getpc_b64 vcc\n3:\n\ts_sub_u32 s70, 4f-3b, s70\n\ts_add_u32 vcc_lo, vcc_lo, s70\n\ts_addc_u32 vcc_hi, vcc_hi, 0\n\ts_setpc_b64 vcc\n"
        "5:\n\ts_add_u32 s70, s70, 18\n\ts_cmp_gt_u32 s70, 18\n\ts_cbranch_scc1 4f\n\ts_memrealtime vcc\n\ts_waitcnt lgkmcnt(0)\n\ts_sub_u32 s70, %0, vcc_lo\n\ts_branch 5b\n"
        "\t.rept 32\n\ts_nop 1\n\t.endr\n4:"
        :: "s"(t - 19u), "i"(K), "i"(15 * K - FC) : "scc", "vcc", "s70", "s71", "s72", "s73", "s74", "s75", "s76", "s77", "memory");
}
template <int N> __device__ __forceinline__ void nops() { if (N > 0) asm volatile(".rept %0\n\ts_nop 1\n\t.endr" :: "i"(N > 0 ? N : 0) : "memory"); }
__device__ __forceinline__ const void* psel(const void* p0, const void* p1, int l) { return (const void*)((uintptr_t)p0 + (((uintptr_t)p1 - (uintptr_t)p0) & (uintptr_t)(-(long)l))); }
#define MKR(p, n) __builtin_amdgcn_make_buffer_rsrc((void*)(p), 0, (int)(n), 0x00020000)
__device__ __forceinline__ unsigned my_side() { return (__builtin_amdgcn_s_getreg((3 << 11) | 20) & 15u) >= 4u ? 1u : 0u; }
__device__ __forceinline__ void tt_step(unsigned tw, int s, unsigned t_poll, unsigned& tb, unsigned& cC1, unsigned& cP) {
    const unsigned tgt0 = tb + cP, pc2 = cP - cC1;
    int d = (int)(tw + pc2 - tgt0);
    d = max(d, (int)dppu<0x128>((unsigned)d)); d = max(d, (int)dppu<0x124>((unsigned)d)); d = max(d, (int)dppu<0x122>((unsigned)d)); d = max(d, (int)dppu<0x121>((unsigned)d));
    d = __builtin_amdgcn_readlane(d, 0);
    tb = s == 0 ? t_poll : ((s == 1 || d > 0) ? tgt0 + (unsigned)d - pc2 + (s == 1 ? TT_V1 : TT_V) : tgt0);
    cC1 = __builtin_amdgcn_readlane(tw, 16); cP = __builtin_amdgcn_readlane(tw, 17);
}
__device__ __forceinline__ void set_err(unsigned char* hop) { unsigned z, v; asm volatile("v_mov_b32 %0, 0\n\tv_mov_b32 %1, 1\n\ts_nop 4\n\tglobal_store_dword %0, %1, %2\n\ts_nop 1" : "=&v"(z), "=&v"(v) : "s"(hop + F_ERR) : "memory"); }
__device__ __forceinline__ void wait_set(rsrc_t rfl, unsigned off, unsigned char* hop, int slp) {
    for (unsigned sp = 0;; ++sp) { CBAR();
        const unsigned v = ld4c(rfl, off);
        if (__all(v != 0u)) break;
        for (int q = 0; q < slp; ++q) __builtin_amdgcn_s_sleep(8);
        if (sp > SPIN_CAP_W) { set_err(hop); break; } }
}
__device__ __forceinline__ void boot_wait(KA& a) {
    const rsrc_t r = MKR((a.ws + WS_HOP) - 2048, 2048); const unsigned off = 128 + (lane_id() & 7) * 128;
    for (unsigned sp = 0; sp < SPIN_CAP_W; ++sp) { CBAR();
        const u32x2 t = ld8c(r, 0), v = ld8c(r, off);
        if (__all(v.x == t.x + 1u && v.y == ~t.y)) break;
        __builtin_amdgcn_s_sleep(16); }
}
__device__ __forceinline__ void init_wait(KA& a) { boot_wait(a); wait_set(MKR(a.ws + WS_HOP, F_ERR), F_IN + (blockIdx.x & 7) * FLP, a.ws + WS_HOP, 3); }
__device__ __forceinline__ void pol_vote(KA& a, const int v) {
    const rsrc_t r = MKR(al2m((a.ws + WS_FR)), 1 << 22);
    const unsigned lane = lane_id(), b0 = (48u + v + (v & 1) * 128u) << 14;
    if (lane < 16) st4c(r, b0 + (lane & 1) * 8192 + (lane >> 1) * 896, 0u);
    VMDRAIN();
    unsigned best[2] = {~0u, ~0u}, acc = 0;
#pragma unroll 1
    for (int q = 0; q < 12; ++q) {
        const unsigned long long t0 = __builtin_amdgcn_s_memtime();
        unsigned x = ld4c(r, b0 + ((q ^ (v >> 1)) & 1) * 8192 + (q >> 1) * 896 + acc);
        asm volatile("s_waitcnt vmcnt(0)" : "+v"(x) :: "memory");
        const unsigned dt = (unsigned)(__builtin_amdgcn_s_memtime() - t0);
        acc += x; if ((q ^ (v >> 1)) & 1) best[1] = min(best[1], dt); else best[0] = min(best[0], dt);
    }
    const int df = max(-300, min(300, (int)best[0] - (int)best[1]));
    if (lane == 0) st4c(MKR((a.ws + WS_HOP), F_ERR), F_POL + v * 4, (unsigned)((my_side() ? -df : df) | 1));
}
__device__ __forceinline__ unsigned page_pol(KA& a) {
    const rsrc_t rfl = MKR((a.ws + WS_HOP), F_ERR); const unsigned off = F_POL + lane_id() * 4;
    init_wait(a); wait_set(rfl, off, (a.ws + WS_HOP), 2);
    const unsigned long long b = __ballot((int)ld4c(rfl, off) > 0);
    return (__popcll(b & 0x5555555555555555ull) > 16 ? 1u : 0u) | (__popcll(b & 0xAAAAAAAAAAAAAAAAull) > 16 ? 2u : 0u);
}
__device__ __forceinline__ unsigned wait_timed(rsrc_t rfl, unsigned off, unsigned char* hop, unsigned& tnext, int nper, int dly, bool fast) {
    if (!fast && (int)(tnext - rt_now()) < 4500) rt_wait(tnext);
    for (unsigned sp = 0;; ++sp) { CBAR();
        const u32x4 v = ld16c(rfl, off);
        if (__all(v.x != 0u)) { tnext = __builtin_amdgcn_readfirstlane(v.w) + (unsigned)nper * __builtin_amdgcn_readfirstlane(v.x) + (unsigned)dly; return __builtin_amdgcn_readfirstlane(v.x); }
        if (fast) __builtin_amdgcn_s_sleep(2); else __builtin_amdgcn_s_sleep(40);
        if (sp > SPIN_CAP_W) { set_err(hop); return 0u; } }
}
__device__ __forceinline__ unsigned blk_of(int j, unsigned nr) { return (j < 8 ? j & 3 : 4) + 5u * (((j < 8 ? j >> 2 : j) & 1u) ^ nr); }
__device__ __forceinline__ unsigned blk_off(int j, unsigned nr) { const unsigned b = blk_of(j, nr); return b * 1024 + (b < 8 ? 0 : 8192); }
template <int NRND, class F> __device__ __forceinline__ void load_w(rsrc_t r0, rsrc_t r1, unsigned nr, unsigned col0, unsigned char* lds, unsigned ldsb, int wave, int lane, F&& put) {
    unsigned vo[3];
#pragma unroll
    for (int i = 0; i < 3; ++i) { const int c = 64 * i + lane, row = c / 24, ch = c - row * 24; vo[i] = (unsigned)((8 * wave + row) * HE_ + col0 + ch * 8) * 2; }
    auto issue = [&](int rd) {
#pragma unroll
        for (int i = 0; i < 3; ++i) { dma16(r0, vo[i], blk_of(rd, nr) * (64 * HE_ * 2), ldsb + (rd % 3) * 49152 + wave * 3072 + i * 1024);
            dma16(r1, vo[i], blk_of(rd, nr) * (64 * HE_ * 2), ldsb + (rd % 3) * 49152 + 24576 + wave * 3072 + i * 1024); }
    };
    issue(0); issue(1); issue(2);
    const unsigned char* rp = lds + (wave >> 2) * 24576 + (8 * (lane >> 4) + ((lane & 15) >> 2)) * 384 + (wave & 3) * 96 + (lane & 3) * 8;
#pragma unroll
    for (int rd = 0; rd < NRND; ++rd) {
        if (rd + 2 < NRND) VMW(12); else if (rd + 1 < NRND) VMW(6); else VMW(0);
        LBAR();
#pragma unroll
        for (int u = 0; u < 3; ++u) { const unsigned char* p = rp + (rd % 3) * 49152 + u * 32;
            const u32x2 a0 = ldstr(p), a1 = ldstr(p + 4 * 384), b0 = ldstr(p + 32 * 384), b1 = ldstr(p + 36 * 384);
            put(rd, u, (u32x8){a0.x, a0.y, a1.x, a1.y, b0.x, b0.y, b1.x, b1.y}); }
        LBAR();
        if (rd + 3 < NRND) issue(rd + 3);
    }
}

__device__ __forceinline__ unsigned wsw(unsigned row) { return ((row >> 1) & 1u) | ((((row >> 2) ^ (row >> 3)) & 1u) << 1); }
template <int NB, int RB, int NRND, int LB, int R0, class G, class F, class H> __device__ __forceinline__ void load_b(unsigned ldsb, int wave, G&& src, F&& take, H&& back) {
    auto issue = [&](int rd) {
#pragma unroll
        for (int j = 0; j < NB; ++j) src(rd, j, ldsb + LB + ((rd + R0) % RB) * (NB * 8192) + j * 8192 + wave * 1024); };
#pragma unroll
    for (int rd = 0; rd < RB; ++rd) issue(rd);
#pragma unroll
    for (int rd = 0; rd < NRND; ++rd) {
        VMW((NRND - 1 - rd < RB - 1 ? NRND - 1 - rd : RB - 1) * NB);
        LBAR();
        take(rd, LB + ((rd + R0) % RB) * (NB * 8192));
        LBAR();
        back(rd);
        if (rd + RB < NRND) issue(rd + RB);
    }
}
__device__ __forceinline__ u32x4 ld_frag(const unsigned char* blk, unsigned r0, unsigned dr, unsigned win, unsigned l15) {
    const unsigned ra = r0 + (l15 >> 2), rb = ra + dr;
    const u32x2 x = ldstr(blk + ra * 128 + ((win ^ wsw(ra)) << 5) + (l15 & 3) * 8), y = ldstr(blk + rb * 128 + ((win ^ wsw(rb)) << 5) + (l15 & 3) * 8);
    return (u32x4){x.x, x.y, y.x, y.y};
}
__device__ __forceinline__ void ln_consts(float s1, float s2, float& mb, float& inv) {
    const float mean = s1 * (1.0f / D_), var = fmaxf(__builtin_fmaf(-mean, mean, s2 * (1.0f / D_)), 0.f);
    mb = rbf(mean); inv = __builtin_amdgcn_rcpf(rbf(__builtin_amdgcn_sqrtf(rbf(rbf(var) + 1e-5f))));
}
__device__ __forceinline__ float row_sums(u32x2 o) {
    const float y0 = lo2f(o.x), y1 = hi2f(o.x), y2 = lo2f(o.y), y3 = hi2f(o.y);
    const auto e = __builtin_amdgcn_permlane16_swap(__float_as_uint((y0 + y1) + (y2 + y3)), __float_as_uint(__builtin_fmaf(y0, y0, y1 * y1) + __builtin_fmaf(y2, y2, y3 * y3)), false, false);
    const float x = __uint_as_float(e[0]) + __uint_as_float(e[1]);
    const auto f = __builtin_amdgcn_permlane32_swap(__float_as_uint(x), __float_as_uint(x), false, false);
    return __uint_as_float(f[0]) + __uint_as_float(f[1]);
}
__device__ __forceinline__ unsigned m_idx(int ln) { return (ln & 2) ? 0xEEEEu : 0x4444u; }
__device__ __forceinline__ f32x4 zero4() { f32x2 a, b; asm volatile("v_mov_b64 %0, 0\n\tv_mov_b64 %1, 0\n\ts_nop 0" : "=v"(a), "=v"(b)); return (f32x4){a[0], a[1], b[0], b[1]}; }
__device__ __forceinline__ u32x4 ones4() { u32x2 a, b; asm volatile("v_mov_b64 %0, -1\n\tv_mov_b64 %1, -1" : "=v"(a), "=v"(b)); return (u32x4){a[0], a[1], b[0], b[1]}; }
__device__ __forceinline__ f32x2 fold2(f32x4 v) { return (f32x2){v[0], v[1]} + (f32x2){v[2], v[3]}; }
__device__ __forceinline__ unsigned fold_pk(f32x4 v) { float x = v[0] + v[2]; PIN(x); return pk2(x, v[1] + v[3]); }
__device__ __forceinline__ float muls(float x, float y) { float r; asm("v_mul_f32 %0, %1, %2" : "=v"(r) : "v"(x), "v"(y)); return r; }
#define LDSB() __builtin_amdgcn_readfirstlane((unsigned)(uintptr_t)(__attribute__((address_space(3))) unsigned char*)lds)
__device__ __forceinline__ int xp_of(int h) { return 3 * (h >> 1) + (h & 1); }
__device__ __forceinline__ unsigned um_base(unsigned long long sd, unsigned pol, unsigned c) {
    const unsigned s = (unsigned)(sd >> c) & 1u; const unsigned long long same = (s ? sd : ~sd) & ((1ull << c) - 1ull);
    const unsigned o = FR_UM + ((unsigned)__popcll(same) << 16);
    return o + (((s ^ (pol >> (o >> 21))) & 1u) << 13);
}
__device__ __forceinline__ unsigned long long um_sides(KA& a) {
    const rsrc_t rfl = MKR((a.ws + WS_HOP), F_ERR); const unsigned l = lane_id(), off = F_SD + (l < 40 ? l : 0) * 4;
    wait_set(rfl, off, (a.ws + WS_HOP), 2);
    return __ballot(ld4c(rfl, off) == 2u);
}

#define ROW2(op, a, b) asm volatile("s_nop 1\n\t" op " %0, %0, %0 row_ror:8 row_mask:0xf bank_mask:0xf\n\t" op " %1, %1, %1 row_ror:8 row_mask:0xf bank_mask:0xf\n\ts_nop 0\n\t" \
    op " %0, %0, %0 row_ror:4 row_mask:0xf bank_mask:0xf\n\t" op " %1, %1, %1 row_ror:4 row_mask:0xf bank_mask:0xf\n\ts_nop 0\n\t" \
    op " %0, %0, %0 row_ror:2 row_mask:0xf bank_mask:0xf\n\t" op " %1, %1, %1 row_ror:2 row_mask:0xf bank_mask:0xf\n\ts_nop 0\n\t" \
    op " %0, %0, %0 row_ror:1 row_mask:0xf bank_mask:0xf\n\t" op " %1, %1, %1 row_ror:1 row_mask:0xf bank_mask:0xf" : "+v"(a), "+v"(b))
template <int W> __device__ __forceinline__ void s_run(KA& a, unsigned char* lds, const int h, const u32x8 (&wr)[8][3]) {
    constexpr int wave = W; const int lane = lane_id();
    const unsigned ldsb = LDSB();
    const rsrc_t rhx = MKR(al2m((a.ws + WS_FR)), FR_USED);
    constexpr int L_MEM = 0, L_Q = 10240, L_K = 13312, L_KX = 16384, L_TIM = 28672, L_W = 32768;
    lds_u32* tim = (lds_u32*)(lds + L_TIM + 256);
    if (W == 0) { tim[0] = 0; tim[1] = 0; tim[2] = 0; tim[4] = 0; }
    const unsigned pol = page_pol(a), sx = ((my_side() ^ pol) & 1u) << 13;
    unsigned po = (2 + (lane >> 5)) * 16384 + h * 256 + ((lane >> 4) & 1) * 128 + (lane & 8) * 4 + (lane & 2) * 8 + (lane & 4) * 2 + (lane & 1) * 8192;
    unsigned pm = 4 * 16384 + (lane >> 5) * 2048 + (h >> 4) * 1024 + ((h >> 1) & 3) * 256 + ((lane >> 3) & 3) * 64 + (h & 8) * 4 + (lane & 4) * 4 + (h & 1) * 8 + (lane & 2) * 2 + (lane & 1) * 8192;
    const int xj = xp_of(h);
    unsigned idx = m_idx(lane);
    PIN(idx); if (W < 2) { PIN(po); PIN(pm); }
#pragma unroll 1
    for (int layer = 0; layer < 2; ++layer) {
    unsigned char* hop = (a.ws + WS_HOP) + layer * HOP_BYTES;
    const rsrc_t rfl = MKR(hop, F_ERR), rst = MKR((a.ws + WS_ST), ST_BYTES);
    const unsigned sb0 = (unsigned)layer * (20u << 14);
    auto kx_fetch = [&](int t, bool wait) {
        int ln = lane_id();
        if (wait) wait_set(rfl, F_XP + (t * NXP_ + xj) * FLP, hop, 0);
        dma16cn<12>(rst, ln * 16, ST_KX + ((t & (KXR - 1)) * 32 + h) * 12288, ldsb + L_KX);
    };
    if (W == 3 && lane_id() == 0) STAMP(100 + h);
    bool early = false;
    if (W == 2) { early = __all(ld4c(rfl, F_XP + xj * FLP) != 0u); if (early) kx_fetch(0, false); }
    VMKNOWN();
    unsigned tb = 0, cC1 = 0, cP = 0, kdur = 0, krt = 0;
#pragma unroll 1
    for (int s = 0; s < NSEG_; ++s) {
        int ln = lane_id();
        ATV;
        const unsigned sb = sb0 + (unsigned)(s & 3) * (5u << 14);
        unsigned qo = (W < 4 ? L_Q + ((ln >> 3) & 1) * 256 + (ln >> 4) * 64 + ((ln >> 1) & 1) * 32 + (ln & 1) * 16 + ((ln >> 2) & 1) * 8
                             : L_K + ((ln & 15) >> 2) * 128 + (((ln >> 4) * 2 + (ln & 1) + (ln & 4)) & 7) * 16 + (ln & 2) * 2);
        unsigned sel = (ln & 1) ? 0x03020706u : 0x05040100u, w2o = L_W + 65536 + wave * 2048 + ln * 16;
        PIN(qo); PIN(sel); PIN(w2o);
        unsigned char* qp = lds + qo;
        u32x8 wl[3];
        const unsigned char* wp = lds + L_W + wave * 2048 + ln * 16;
        auto w_lds = [&](int f) { const unsigned char* p = f < 4 ? wp + f * 16384 : lds + w2o + (f - 4) * 16384; return cat8(lds16(p), lds16(p + 1024)); };
#pragma unroll
        for (int u = 0; u < 3; ++u) wl[u] = w_lds(1 + u);
        f32x4 acc[3] = {zero4(), zero4(), zero4()};
        unsigned tp = s == 0 ? rt_now() : tb + cP;
#define G3(A, Wt) do { acc[0] = smf(A, Wt[0], acc[0], idx); acc[1] = smf(A, Wt[1], acc[1], idx); acc[2] = smf(A, Wt[2], acc[2], idx); PIN(acc[0]); PIN(acc[1]); PIN(acc[2]); SBAR(); } while (0)
#define S1(u, A, B) do { acc[u] = smf(A, B, acc[u], idx); PIN(acc[u]); SBAR(); } while (0)
        u32x4 m0, n0, n1; unsigned nw = 0;
        unsigned char* mp = lds + L_MEM + ln * 16;
        constexpr int B0 = W == 4 ? 8 : (W == 5 ? 1 : W), B1 = W == 3 ? 9 : 2 * W - 8; constexpr bool ER = W != 1 && W < 6, LT = W == 2 || W == 3 || W >= 6;
        unsigned mo = sx + ln * 16, to = sx + M_STAMP + min(ln, 17) * 64 + (s >> 2) * 4, s0 = sb + blk_off(B0, h >> 4), s1 = sb + blk_off(B1, h >> 4), s2 = sb + blk_off(B1 + 1, h >> 4);
        auto late = [&]() { CBAR();
            if (W == 2) nw = __builtin_amdgcn_raw_buffer_load_b32(rhx, to, sb, 16); else n0 = __builtin_amdgcn_raw_buffer_load_b128(rhx, mo, s1, 16);
            if (W >= 6) n1 = __builtin_amdgcn_raw_buffer_load_b128(rhx, mo, s2, 16);
            CBAR(); SBAR(); };
        auto stuck = [&](unsigned sp) { if ((sp & 63u) == 63u && (sp > SPIN_CAP || tim[0])) { tim[0] = 1; return true; } if (sp > RTS) __builtin_amdgcn_s_sleep(RTL); return false; };
        if (W != 1) {
            PIN(wl[0]); PIN(wl[1]); PIN(wl[2]); PIN(mo); if (W == 2) PIN(to); if (ER) asm volatile("" : "+s"(s0)); if (LT && W != 2) asm volatile("" : "+s"(s1)); if (W >= 6) asm volatile("" : "+s"(s2));
            rt_fine<23>(ER ? tp : tp + SFD);
            if (ER) { CBAR(); m0 = __builtin_amdgcn_raw_buffer_load_b128(rhx, mo, s0, 16); CBAR(); SBAR(); if (LT) nops<(SFD * 23 - 12) / 8>(); }
            if (LT) late();
            if (ER) { *reinterpret_cast<u32x4*>(mp + B0 * 1024) = m0;
                if (__builtin_expect(!__all(umax4(m0) != 0xffffffffu), 0)) {
                    tim[2] = (unsigned)s + 1u;
                    for (unsigned sp = 0;; ++sp) { CBAR(); m0 = __builtin_amdgcn_raw_buffer_load_b128(rhx, mo, s0, 16); if (__all(umax4(m0) != 0xffffffffu) || stuck(sp)) break; }
                    *reinterpret_cast<u32x4*>(mp + B0 * 1024) = m0; } }
        }
        if (W == 0) __builtin_amdgcn_s_setprio(SPRIO);
        AT(2);
        LBAR();
        AT(3);
        {
            constexpr int F = (ER && W != 4) ? B0 : 0, R1 = F == 0 ? 1 : 0, R2 = F <= 1 ? 2 : 1, R3 = F <= 2 ? 3 : 2, R6 = 6;
            if (!ER) { m0 = lds16(mp); SBAR(); }
            u32x4 A0 = lds16(mp + (W == 4 ? 0 : 8) * 1024); SBAR(); u32x4 A1 = lds16(mp + R1 * 1024); SBAR(); u32x4 A2 = lds16(mp + R2 * 1024); SBAR(); u32x4 A3 = lds16(mp + R3 * 1024); SBAR();
            auto take = [&]() { if (LT) {
                auto put_ = [&]() { if (W == 2) *reinterpret_cast<unsigned*>(lds + L_TIM + ln * 4) = nw; else *reinterpret_cast<u32x4*>(mp + B1 * 1024) = n0; if (W >= 6) *reinterpret_cast<u32x4*>(mp + B1 * 1024 + 1024) = n1;
                    return __all((W == 2 ? nw : max(umax4(n0), W >= 6 ? umax4(n1) : 0u)) != 0xffffffffu); };
                if (__builtin_expect(!put_(), 0)) { tim[2] = (unsigned)s + 1u; for (unsigned sp = 0;; ++sp) { late(); if (put_() || stuck(sp)) break; } }
                SBAR(); } };
            if (W != 4) G3(m0, wr[F]);
            if (STK == 0) take();
            { const u32x4 a8 = W == 4 ? m0 : A0;
              S1(0, a8, wl[0]); wl[0] = w_lds(4); SBAR(); S1(1, a8, wl[1]); wl[1] = w_lds(5); SBAR(); S1(2, a8, wl[2]); wl[2] = w_lds(6); SBAR(); }
            if (W == 4) G3(A0, wr[F]);
            if (STK == 1) take();
            G3(A1, wr[R1]);
            if (STK == 2) take();
            G3(A2, wr[R2]);
            if (LT) asm volatile("s_waitcnt lgkmcnt(0)" ::: "memory");
            CBAR(); __builtin_amdgcn_s_barrier(); CBAR(); SBAR();
            m0 = lds16(mp + 9 * 1024); SBAR(); A1 = lds16(mp + 4 * 1024); SBAR();
            S1(0, A3, wr[R3][0]); A2 = lds16(mp + 5 * 1024); SBAR(); S1(1, A3, wr[R3][1]); A0 = lds16(mp + 7 * 1024); SBAR(); S1(2, A3, wr[R3][2]); A3 = lds16(mp + 6 * 1024); SBAR();
            S1(1, m0, wl[1]); wl[1] = w_lds(0); SBAR(); S1(2, m0, wl[2]); wl[2] = w_lds(7); SBAR(); S1(0, m0, wl[0]);
            G3(A1, wr[4]); G3(A2, wr[5]);
            auto put = [&](int u) { const int T = 3 * (W & 3) + u; const unsigned o = fold_pk(acc[u]);
                *reinterpret_cast<unsigned*>(qp + (T >> 1) * 512 + (T & 1) * (W < 4 ? 4 : 8)) = __builtin_amdgcn_perm(dppu<0xB1>(o), o, sel); };
            acc[0] = smf(A3, wr[R6][0], acc[0], idx); acc[1] = smf(A3, wr[R6][1], acc[1], idx); acc[0] = smf(A0, wr[7][0], acc[0], idx);
            acc[2] = smf(A3, wr[R6][2], acc[2], idx); acc[1] = smf(A0, wl[1], acc[1], idx); PIN(acc[0]); PIN(acc[1]); PIN(acc[2]); SBAR();
            put(0); SBAR();
            acc[2] = smf(A0, wl[2], acc[2], idx); PIN(acc[2]); SBAR();
            put(1); SBAR(); put(2); SBAR(); AT(4);
        }
        if (s == 0) { if (W == 2 && !early) { kx_fetch(0, true); VMDRAIN(); } LBAR(); }
        if (W == 0 && s + 1 < NSEG_) {
            PIN(ln);
            unsigned kp = ldsb + L_KX + ln * 16, mk = ldsb + L_K + (ln >> 4) * 128 + ((ln + ((ln >> 2) & 4)) & 7) * 16;
            u32x8 ka = cat8(lds16a(kp), lds16a(kp + 1024)), kb = cat8(lds16a(kp + 6144), lds16a(kp + 7168)), kc = cat8(lds16a(kp + 2048), lds16a(kp + 3072)), kd = cat8(lds16a(kp + 8192), lds16a(kp + 9216));
            f32x4 sc[3] = {zero4(), zero4(), zero4()};
            const bool up = (ln & 8) != 0;
            PIN(ka); PIN(kb); PIN(kc); PIN(kd); PIN(sc[0]); PIN(sc[1]); PIN(sc[2]); PIN(kp); PIN(mk);
            __builtin_amdgcn_s_setprio(3);
            LBAR();
            AT(5);
            const u32x4 q0 = lds16a(kp + (L_Q - L_KX)); SBAR(); u32x8 ke = cat8(lds16a(mk), lds16a(mk + 512)); SBAR(); const u32x4 q1 = lds16a(kp + (L_Q - L_KX + 1024)); SBAR(); const u32x4 q2 = lds16a(kp + (L_Q - L_KX + 2048)); SBAR();
            sc[0] = smf(q0, ka, sc[0], idx); sc[1] = smf(q0, kb, sc[1], idx); PIN(sc[0]); PIN(sc[1]); SBAR();
            ka = cat8(lds16a(mk + 1024), lds16a(mk + 1536)); SBAR(); kb = cat8(lds16a(kp + 4096), lds16a(kp + 5120)); SBAR();
            sc[2] = smf(q0, ke, sc[2], idx); PIN(sc[2]); SBAR();
            ke = cat8(lds16a(kp + 10240), lds16a(kp + 11264)); SBAR();
            sc[0] = smf(q1, kc, sc[0], idx); PIN(sc[0]); SBAR();
            kc = cat8(lds16a(mk + 2048), lds16a(mk + 2560)); SBAR();
            sc[1] = smf(q1, kd, sc[1], idx); sc[2] = smf(q1, ka, sc[2], idx); sc[0] = smf(q2, kb, sc[0], idx); sc[1] = smf(q2, ke, sc[1], idx); sc[2] = smf(q2, kc, sc[2], idx); SBAR();
            AT(6);
            const f32x2 s0 = fold2(sc[0]), s1 = fold2(sc[1]), s2 = fold2(sc[2]);
            float ma = __builtin_fmaxf(__builtin_fmaxf(s0[0], s1[0]), s2[0]), mb = __builtin_fmaxf(__builtin_fmaxf(s0[1], s1[1]), s2[1]);
            ROW2("v_max_f32_dpp", ma, mb);
            const float c1 = 0.07216878364870322f * 1.4426950408889634f;
            ma *= -c1; mb *= -c1;
            const float a0 = __builtin_amdgcn_exp2f(__builtin_fmaf(s0[0], c1, ma)), b0 = __builtin_amdgcn_exp2f(__builtin_fmaf(s0[1], c1, mb));
            const float a1 = __builtin_amdgcn_exp2f(__builtin_fmaf(s1[0], c1, ma)), b1 = __builtin_amdgcn_exp2f(__builtin_fmaf(s1[1], c1, mb));
            const float a2 = __builtin_amdgcn_exp2f(__builtin_fmaf(s2[0], c1, ma)), b2 = __builtin_amdgcn_exp2f(__builtin_fmaf(s2[1], c1, mb));
            float za = __builtin_fmaf(a2, 0.5f, a0 + a1), zb = __builtin_fmaf(b2, 0.5f, b0 + b1);
            ROW2("v_add_f32_dpp", za, zb);
            za = __builtin_amdgcn_rcpf(za); zb = __builtin_amdgcn_rcpf(zb);
            const unsigned da = pk2(a0 * za, a1 * za), db = pk2(b0 * zb, b1 * zb);
            const float vm = up ? b2 * zb : a2 * za;
            u32x2 oa = (u32x2){dppu<0xA0>(da), dppu<0xF5>(da)}, ob = (u32x2){dppu<0xA0>(db), dppu<0xF5>(db)}; unsigned om = pk2(dppf<0xA0>(vm), dppf<0xF5>(vm));
            PIN(oa); PIN(ob); PIN(om);
            __builtin_amdgcn_raw_buffer_store_b64(oa, rhx, po, sb, 16);
            __builtin_amdgcn_raw_buffer_store_b64(ob, rhx, po + 64, sb, 16);
            __builtin_amdgcn_raw_buffer_store_b32(om, rhx, pm, sb, 16);
            SBAR(); AT(7);
            tim[1] = rt_now() - (tb + cP);
            tim[4] = (unsigned)s + 1u;
            __builtin_amdgcn_s_setprio(0);
            PIN(ln);
            tt_step(*reinterpret_cast<const unsigned*>(lds + L_TIM + ln * 4), s, s == 0 ? rt_now() - (W == 0 ? TT_E1 : TT_E1 - 35) : 0u, tb, cC1, cP);
        } else {
            if (W == 1) VMDRAIN();
            LBAR();
            AT(5);
            tt_step(*reinterpret_cast<const unsigned*>(lds + L_TIM + ln * 4), s, s == 0 ? rt_now() - (W == 0 ? TT_E1 : TT_E1 - 35) : 0u, tb, cC1, cP);
            if (W == 2 && s + 1 < NSEG_) wait_set(rfl, F_XP + ((s + 1) * NXP_ + xj) * FLP, hop, 0);
            if ((W == 1 || W == 2) && s + 1 < NSEG_) { for (unsigned sp = 0; tim[4] != (unsigned)s + 1u && sp < SPIN_CAP; ++sp) __builtin_amdgcn_s_sleep(2); CBAR(); if (W == 1 && SLATE1) __builtin_amdgcn_s_sleep(SLATE1); if (W == 2 && SLATE2) __builtin_amdgcn_s_sleep(SLATE2); }
            if (W == 1) {
                kdur = min(__builtin_amdgcn_readfirstlane(tim[1]), 4095u); krt = __builtin_amdgcn_readfirstlane(tim[2]) == (unsigned)s + 1u ? 1u : 0u;
#pragma unroll
                for (int m = 0; m < 3; ++m) st16s(lds16(lds + L_K + m * 1024 + ln * 16), rst, ln * 16, ST_KM + ((s & (PR - 1)) * 32 + h) * 3072 + m * 1024);
                {
                    const unsigned sr = sb0 + (unsigned)((s + 2) & 3) * (5u << 14);
                    __builtin_amdgcn_raw_buffer_store_b64((u32x2){~0u, ~0u}, rhx, po, sr, 16); __builtin_amdgcn_raw_buffer_store_b64((u32x2){~0u, ~0u}, rhx, po, sr + 64, 16);
                    __builtin_amdgcn_raw_buffer_store_b32(~0u, rhx, pm, sr, 16);
                }
                VMW(3); if (ln == 0) st16c(rfl, F_KM + (s * 32 + h) * FLP, (u32x4){cP, kdur, krt, tb}); if (ln == 0 && h == 0 && s >= 56) STAMP(370 + s - 56);
            }
            if (W == 2 && s + 1 < NSEG_) dma16cn<12>(rst, ln * 16, ST_KX + (((s + 1) & (KXR - 1)) * 32 + h) * 12288, ldsb + L_KX);
        }
        ATD(0, s == 41 && h == 0);
    }
    if (W == 0) { tim[2] = 0; tim[4] = 0; }
    if (W == 1 && lane_id() == 0) STAMP(512 + blockIdx.x);
    }
    if (W == 0 && tim[0]) set_err((a.ws + WS_HOP));
}

__device__ __forceinline__ void s_part(KA& a, unsigned char* lds, const int h, const int wave) {
    const int lane = lane_id();
    const unsigned ldsb = LDSB();
    u32x8 wr[8][3];
    auto keep = [&](int f, u32x8 v) { unsigned char* p = lds + 32768 + f * 16384 + wave * 2048 + lane * 16; *reinterpret_cast<u32x4*>(p) = lo4(v); *reinterpret_cast<u32x4*>(p + 1024) = hi4(v); };
    load_w<10>(MKR(a.wq, D_ * HE_ * 2), MKR(a.wk, D_ * HE_ * 2), h >> 4, h * 192, lds, ldsb, wave, lane, [&](int rd, int u, u32x8 v) { if (rd >= 8) keep(3 * rd - 23 + u, v); else if (rd == 7 && u == 2) keep(7, v); else wr[rd][u] = v; });
    keep(0, wr[7][1]);
    if (wave == 7) s_run<7>(a, lds, h, wr); else if (wave == 6) s_run<6>(a, lds, h, wr); else if (wave == 5) s_run<5>(a, lds, h, wr); else if (wave == 4) s_run<4>(a, lds, h, wr);
    else if (wave == 3) s_run<3>(a, lds, h, wr); else if (wave == 2) s_run<2>(a, lds, h, wr); else if (wave == 1) s_run<1>(a, lds, h, wr); else s_run<0>(a, lds, h, wr);
}

#define LBAR_NONE() do { } while (0)
__device__ __forceinline__ unsigned vblk(int j, unsigned nr) { const unsigned v = (unsigned)(0x9873265410ull >> (4 * j)) & 15u; return nr ? (v < 5 ? v + 5 : v - 5) : v; }
template <bool XW> __device__ __forceinline__ void v_run(KA& a, unsigned char* lds, const int h, const int wave) {
    const int lane = lane_id(), tid = wave * 64 + lane;
    const unsigned ldsb = LDSB();
    constexpr int L_MEM = 0, L_V = 10240, L_VX = 13312, L_TIM = 25600, L_E = 26624, L_F = L_E + 2048, L_T = L_F + 1024, L_U = L_T + 1024, L_W = 16384, NO = XW ? 4 : 6, NV = XW ? 2 : 1, KR = XW ? 5 : 4;
    constexpr int NBK = XW ? 2 : YNB;
    u32x8 wo[NO][3], wv[KR][NV];
    const int vt = XW ? 2 * wave : 4 + wave;
    {
        const rsrc_t rv = MKR(a.wv, D_ * HE_ * 2), ro = MKR(a.wo, HE_ * D_ * 2);
        const unsigned l15 = lane & 15, lg = lane >> 4, row = 8 * wave + (lane >> 3), ch = ((lane & 7) ^ (wsw(row) << 1)) * 16;
        const unsigned vo = row * (HE_ * 2) + h * 384 + ch, oo = (h * 192 + row) * (D_ * 2) + ch;
        u32x8 tmp[2][NV];
        load_b<6, 3, 10, L_W, 2>(ldsb, wave,
            [&](int rd, int j, unsigned dst) { if (rd < 5) dma16(ro, oo, (j >> 1) * (64 * D_ * 2) + rd * 256 + (j & 1) * 128, dst); else dma16(rv, vo, vblk((rd - 5) * 2 + j / 3, h >> 4) * (64 * HE_ * 2) + (j % 3) * 128, dst); },
            [&](int rd, int base) {
                if (rd < 5) {
#pragma unroll
                    for (int i = 0; i < (rd < 2 ? 1 : (XW == (rd == 4) ? 2 : 0)); ++i) { const int tl = rd < 2 ? wave : 2 * (wave & 3) + i;
#pragma unroll
                        for (int k = 0; k < 3; ++k) { const unsigned char* p = lds + base + (2 * k + (tl >> 2)) * 8192; wo[rd < 2 ? rd : (rd == 3 ? 4 + i : 2 + i) % NO][k] = cat8(ld_frag(p, 4 * lg, 16, tl & 3, l15), ld_frag(p, 32 + 4 * lg, 16, tl & 3, l15)); } }
                } else {
#pragma unroll
                    for (int k = 0; k < 2; ++k)
#pragma unroll
                        for (int u = 0; u < NV; ++u) { const unsigned char* p = lds + base + (3 * k + (vt >> 2)) * 8192; const u32x8 v = cat8(ld_frag(p, 8 * lg, 4, (vt & 3) + u, l15), ld_frag(p, 32 + 8 * lg, 4, (vt & 3) + u, l15));
                            if (2 * (rd - 5) + k < KR) wv[2 * (rd - 5) + k][u] = v; else tmp[k][u] = v; } } },
            [&](int rd) {
                if (rd >= 7) {
#pragma unroll
                    for (int k = 0; k < 2; ++k)
#pragma unroll
                        for (int u = 0; u < NV; ++u) if (2 * (rd - 5) + k >= KR) { unsigned char* p = lds + L_W + (2 * (rd - 5) + k - 4) * 24576 + (vt + u) * 2048 + lane * 16; *reinterpret_cast<u32x4*>(p) = lo4(tmp[k][u]); *reinterpret_cast<u32x4*>(p + 1024) = hi4(tmp[k][u]); } } });
    }
    const rsrc_t rhx = MKR(al2m((a.ws + WS_FR)), FR_USED), rst = MKR((a.ws + WS_ST), ST_BYTES);
    lds_u32* tim = (lds_u32*)(lds + L_TIM);
    if (tid == 0) { tim[0] = 0; tim[2] = 0; }
    const unsigned pol = page_pol(a), sx = ((my_side() ^ pol) & 1u) << 13;
    unsigned uo0, uo1 = 0, l16 = lane * 16;
    { const unsigned long long sd = um_sides(a);
      uo0 = um_base(sd, pol, lane < 32 ? 8 * (lane >> 4) + wave : (XW ? 30 : 6) + 2 * wave + (lane >> 4)) + h * 256 + (lane & 15) * 16;
      if (!XW) uo1 = um_base(sd, pol, 16 + 2 * wave + (lane >> 5)) + h * 256 + (lane & 15) * 16 + (lane & 16) / 2; }
    auto arm = [&](unsigned so) { const u32x4 on = ones4(); st16s(on, rhx, uo0, so); if (!XW) __builtin_amdgcn_raw_buffer_store_b64((u32x2){on.x, on.y}, rhx, uo1, so, 16); SBAR(); };
    arm(16384);
    if (XW) sts4a(L_U + wave * 256 + lane * 4, uo0);
    const int xj = xp_of(h), xj2 = 3 * (h >> 1) + 2;
    if (XW) uo1 = wave < 2 ? l16 : M_STAMP + min(lane, 17) * 64;
    const int j0 = (0x6510 >> (4 * (wave & 3))) & 15, j1 = (0x9842 >> (4 * (wave & 3))) & 15, j2 = wave ? 7 : 3;
    auto voff = [&](int j) { const unsigned b = vblk(j, h >> 4); return b * 1024 + (b < 8 ? 0 : 8192); };
    const unsigned o0 = voff(j0), o1 = voff(j1), o2 = wave < 2 ? voff(j2) : 0u;
    PIN(uo0); PIN(uo1); PIN(l16);
    *reinterpret_cast<u32x4*>(lds + L_T + l16) = (u32x4){L_V + ((lane >> 3) & 1) * 256u + (lane >> 4) * 64u + ((lane >> 1) & 1) * 32u + (lane & 1) * 16u + ((lane >> 2) & 1) * 8u,
        (lane & 1) ? 0x03020706u : 0x05040100u, m_idx(lane), F_XP + ((lane & 1) ? xj : xj2) * FLP};
    VMKNOWN();
    ATV;
#define la ((int)l16)
    auto u_so = [&](int t, int i) { return ST_U + (((t & (URING - 1)) * 40 + (i < 2 ? 8 * i + wave : (i < 4 ? 6 : 12) + 2 * wave + i)) * 32 + h) * 1024; };
    u32x4 xf[6][2];
    auto back0 = [&](int q) { xf[q >> 1][q & 1] = lds16a(la + L_VX + (q & 1) * 6144 + (q >> 1) * 1024); SBAR(); };
    auto back = [&](int t, auto&& side) {
        f32x4 acc[2][2]; u32x4 o; unsigned so;
#pragma unroll
        for (int i = 0; i < NBK; ++i) {
#pragma unroll
            for (int j = 0; j < 12; ++j) { const int k = j >> 1, r = j & 1;
                acc[i & 1][r] = dmf(xf[k][r], (k & 1) ? hi4(wo[i][k >> 1]) : lo4(wo[i][k >> 1]), k ? acc[i & 1][r] : Z4); PIN(acc[i & 1][r]); SBAR();
                if (i == 0) { side(j); if (j + 4 < 12) back0(j + 4); }
                else { const f32x4 c0 = acc[(i - 1) & 1][0], c1 = acc[(i - 1) & 1][1];
                    if (j == 2) { so = u_so(t, i - 1); asm volatile("" : "+s"(so)); }
                    if (j == 3) { o.x = pk2(c0[0], c1[0]); PIN(o.x); } if (j == 4) { o.y = pk2(c0[2], c1[2]); PIN(o.y); } if (j == 5) { o.z = pk2(c0[1], c1[1]); PIN(o.z); } if (j == 6) { o.w = pk2(c0[3], c1[3]); PIN(o); }
                    if (j == 7) __builtin_amdgcn_raw_buffer_store_b128(o, rst, l16, so, 16);
                    if (j == 9) asm volatile("" :: "v"(o));
                    if (j == 10 && i == NBK - 1) { so = u_so(t, i); asm volatile("" : "+s"(so)); }
                }
                SBAR(); }
        }
        const f32x4 c0 = acc[(NBK - 1) & 1][0], c1 = acc[(NBK - 1) & 1][1];
        st16s((u32x4){pk2(c0[0], c1[0]), pk2(c0[2], c1[2]), pk2(c0[1], c1[1]), pk2(c0[3], c1[3])}, rst, l16, so); };
    auto v_so = [&](int t) { return ST_VX + ((t & (KXR - 1)) * 32 + h) * 12288 + (wave & 3) * 3072; };
#pragma unroll 1
    for (int layer = 0; layer < 2; ++layer) {
    unsigned char* hop = (a.ws + WS_HOP) + layer * HOP_BYTES;
    const rsrc_t rfl = MKR(hop, F_ERR);
    const unsigned sb0 = (unsigned)layer * (20u << 14) + sx;
    if (wave == 0 && l16 == 0) STAMP(140 + h);
    unsigned tb = rt_now(), cC1 = 0, cP = 0;
#pragma unroll 1
    for (int s = 0; s < NSEG_; ++s) {
        PIN(l16);
        int w0o = la + L_W + vt * 2048, w1o = w0o + 3 * 24576; unsigned us = (s & 3) << 14;
        PIN(w0o); PIN(w1o); asm volatile("" : "+s"(us));
        const u32x4 lc = lds16a(la + L_T);
        unsigned fw = 1u, d0 = 0, d1 = 0;
        if (!XW) fw = lds4a(L_F + (l16 >> 2));
        SBAR();
        auto w_h = [&](int kb, int u) { return lds16a((kb < 7 ? w0o + (kb - 4) * 24576 : w1o + (kb - 7) * 24576) + u * 1024); };
        constexpr int NL = 4;
        u32x4 wl[NL][2];
#pragma unroll
        for (int u = 0; u < 2 * NL; ++u) wl[u >> 1][u & 1] = XW ? w_h(5 + (u >> 2), u & 3) : w_h(4 + (u >> 1), u & 1);
        f32x4 acc[NV];
#pragma unroll
        for (int u = 0; u < NV; ++u) acc[u] = zero4();
        const unsigned sel = lc.y, idx = lc.z;
        SBAR();
        u32x4 m1, m2; unsigned s1 = 0, s2 = 0; int w1 = 0, w2 = 0;
        auto late = [&]() { CBAR(); m1 = __builtin_amdgcn_raw_buffer_load_b128(rhx, l16, s1, 16); SBAR(); m2 = __builtin_amdgcn_raw_buffer_load_b128(rhx, uo1, s2, 16); CBAR(); SBAR(); };
        auto stuck = [&](unsigned sp) { if ((sp & 63u) == 63u && (sp > SPIN_CAP || tim[0])) { tim[0] = 1; return true; } if (sp > 8) __builtin_amdgcn_s_sleep(RTL); return false; };
        if (XW) {
            const unsigned sb = sb0 + (unsigned)(s & 3) * (5u << 14);
            unsigned s0 = sb + o0; s1 = sb + o1; s2 = sb + (wave < 2 ? o2 : (s >> 2) * 4);
            int w0 = la + j0 * 1024; w1 = la + j1 * 1024; w2 = la + (wave < 2 ? j2 * 1024 : L_E - 2048 + wave * 1024);
            PIN(w0); PIN(w1); PIN(w2); asm volatile("" : "+s"(s0), "+s"(s1), "+s"(s2));
            AT(0);
            rt_fine<21>(tb + cP + TT_VD);
            AT(6);
            CBAR(); u32x4 m0 = __builtin_amdgcn_raw_buffer_load_b128(rhx, l16, s0, 16); CBAR(); SBAR();
            nops<(VFD * 21 - 12) / 8>();
            late();
            sts16a(w0, m0);
            if (__builtin_expect(!__all(umax4(m0) != 0xffffffffu), 0)) {
                tim[2] = (unsigned)s + 1u;
                for (unsigned sp = 0;; ++sp) { CBAR(); m0 = __builtin_amdgcn_raw_buffer_load_b128(rhx, l16, s0, 16); if (__all(umax4(m0) != 0xffffffffu) || stuck(sp)) break; }
                sts16a(w0, m0); }
        } else {
            if (__builtin_expect(s > 0 && !__all(fw != 0u), 0)) { wait_set(rfl, lc.w + min(s + 2, NSEG_ - 1) * (NXP_ * FLP), hop, 0); VMKNOWN(); }
            d0 = v_so(s + 2); d1 = min(s + 3, NSEG_ - 1) * (NXP_ * FLP); asm volatile("" : "+s"(d0), "+s"(d1));
        }
        AT(1);
        if (XW) { LBAR(); __builtin_amdgcn_s_setprio(0); } else { __builtin_amdgcn_s_setprio(YP1); CBAR(); __builtin_amdgcn_s_barrier(); CBAR(); }
        AT(2);
        f32x4 um[NO];
        {
#define VS(u, A, B) do { acc[(u) % NV] = smf(A, B, acc[(u) % NV], idx); PIN(acc[(u) % NV]); SBAR(); } while (0)
#define VL(u) cat8(wl[(u) % NL][0], wl[(u) % NL][1])
#define VA(k) lds16a(la + L_MEM + (k) * 1024); SBAR()
#define VW(u, kb, q) wl[(u) % NL][(q) & 1] = w_h(kb, q); SBAR()
            int qo;
            if (XW) {
                u32x4 A0 = VA(5); u32x4 A1 = VA(6); u32x4 A2 = VA(0); u32x4 A3 = VA(1);
                auto take = [&]() { const unsigned em = wave < 2 ? ~0u : 0u;
                    auto put_ = [&]() { sts16a(w1, m1); sts16a(w2, m2); SBAR(); return __all(max(umax4(m1), max(m2.x, max(max(m2.y, m2.z), m2.w) & em)) != 0xffffffffu); };
                    if (__builtin_expect(!put_(), 0)) { tim[2] = (unsigned)s + 1u; for (unsigned sp = 0;; ++sp) { late(); if (put_() || stuck(sp)) break; } }
                    SBAR(); };
                VS(0, A0, VL(0)); VS(1, A0, VL(1)); VW(0, 7, 0);
                if (VTK == 0) take();
                VS(0, A1, VL(2)); VW(0, 7, 1); VS(1, A1, VL(3)); VW(1, 7, 2);
                if (VTK == 1) take();
                VS(0, A2, wv[0][0]); VW(1, 7, 3); VS(1, A2, wv[0][NV - 1]);
                if (VTK == 2) take();
                LBAR(); SBAR();
                A0 = VA(2); A1 = VA(7);
                VS(0, A3, wv[1][0]); A2 = VA(8); VW(2, 8, 0); VS(1, A3, wv[1][NV - 1]); VW(2, 8, 1); VW(3, 8, 2);
                VS(0, A0, wv[2][0]); VW(3, 8, 3); A3 = VA(3); VS(1, A0, wv[2][NV - 1]);
                VS(0, A1, VL(0)); A0 = VA(4); VS(1, A1, VL(1)); VW(0, 9, 0);
                VS(0, A2, VL(2)); VW(0, 9, 1); A1 = VA(9); VS(1, A2, VL(3)); VW(1, 9, 2);
                VS(0, A3, wv[3][0]); VW(1, 9, 3); VS(1, A3, wv[3][NV - 1]); um[0] = zero4(); SBAR();
                VS(0, A0, wv[KR - 1][0]); um[1] = zero4(); qo = lc.x + (vt >> 1) * 512; PIN(qo); SBAR(); VS(0, A1, VL(0)); um[2] = zero4(); SBAR(); VS(1, A0, wv[KR - 1][NV - 1]); um[3] = zero4(); SBAR(); VS(1, A1, VL(1));
            } else {
                u32x4 A0 = VA(5); u32x4 A1 = VA(6); u32x4 A2 = VA(0); u32x4 A3 = VA(1);
                dma16cn<3>(rst, l16, d0, L_VX + (wave & 3) * 3072); SBAR();
                VS(0, A0, VL(1)); VS(0, A1, VL(2)); VW(1, 8, 0); VW(1, 8, 1); VS(0, A2, wv[0][0]); VW(2, 9, 0); VW(2, 9, 1);
                CBAR(); __builtin_amdgcn_s_barrier(); CBAR(); SBAR();
                if (YP2) __builtin_amdgcn_s_setprio(YP2);
                A0 = VA(4); A1 = VA(7); A2 = VA(2);
                VS(0, A3, wv[1][0]); A3 = VA(3); qo = lc.x + ((vt >> 1) * 512 + (vt & 1) * 4); PIN(qo); SBAR();
                VS(0, A0, VL(0)); A0 = VA(8); VS(0, A1, VL(3)); A1 = VA(9);
                VS(0, A2, wv[2][0]); um[0] = zero4(); um[1] = zero4(); SBAR(); VS(0, A3, wv[3][0]); um[2] = zero4(); um[3] = zero4(); SBAR();
                VS(0, A0, VL(1)); um[NO - 2] = zero4(); um[NO - 1] = zero4(); SBAR(); VS(0, A1, VL(2));
            }
            unsigned o[NV];
#pragma unroll
            for (int u = 0; u < NV; ++u) { o[u] = fold_pk(acc[u]); o[u] = __builtin_amdgcn_perm(dppu<0xB1>(o[u]), o[u], sel); }
            if (XW) { *(__attribute__((address_space(3))) u32x2*)(uintptr_t)(unsigned)qo = (u32x2){o[0], o[NV - 1]}; SBAR();
            } else { *(__attribute__((address_space(3))) unsigned*)(uintptr_t)(unsigned)qo = o[0]; __builtin_amdgcn_s_setprio(2); }
        }
        AT(3);
        VMDRAIN(); LBAR();
        AT(4);
        {
            const u32x4 q0 = lds16a(la + L_V), q1 = lds16a(la + L_V + 1024), q2 = lds16a(la + L_V + 2048); SBAR();
#define VU(u, q, k) do { um[u] = smf(q, wo[u][k], um[u], idx); PIN(um[u]); SBAR(); } while (0)
#define VO(u) min(fold_pk(um[u]), 0xfffffffeu)
            if (!XW) { VU(NO - 2, q0, 0); VU(NO - 1, q0, 0); VU(NO - 2, q1, 1); VU(NO - 1, q1, 1); VU(NO - 2, q2, 2); VU(NO - 1, q2, 2); }
            VU(0, q0, 0); VU(2, q0, 0);
            unsigned e0 = 0, e1 = 0;
            if (!XW) { e0 = VO(NO - 2); PIN(e0); SBAR(); }
            VU(0, q1, 1);
            if (!XW) { e1 = VO(NO - 1); PIN(e1); SBAR(); }
            VU(2, q1, 1);
            if (!XW) { const auto z = __builtin_amdgcn_permlane32_swap(e0, e1, false, false); __builtin_amdgcn_raw_buffer_store_b64((u32x2){z[0], z[1]}, rhx, uo1, us, 16); SBAR(); }
            VU(0, q2, 2); back0(0); VU(2, q2, 2); back0(1); VU(1, q0, 0); back0(2); VU(1, q1, 1); back0(3);
            e0 = VO(0); PIN(e0); SBAR();
            VU(1, q2, 2);
            e1 = VO(2); PIN(e1); SBAR();
            VU(3, q0, 0);
            const auto x02 = __builtin_amdgcn_permlane32_swap(e0, e1, false, false); SBAR();
            VU(3, q1, 1); VU(3, q2, 2);
            e0 = VO(1); PIN(e0); SBAR();
            const auto x13 = __builtin_amdgcn_permlane32_swap(e0, VO(3), false, false);
            const auto y01 = __builtin_amdgcn_permlane16_swap(x02[0], x13[0], false, false), y23 = __builtin_amdgcn_permlane16_swap(x02[1], x13[1], false, false);
            st16s((u32x4){y01[0], y23[0], y01[1], y23[1]}, rhx, uo0, us);
            SBAR();
            AT(5);
            __builtin_amdgcn_s_setprio(XW ? 2 : 0);
        }
        if (__builtin_expect(s == 0, 0)) {
            arm(2u << 14);
            if (!XW) sts4a(L_F + (l16 >> 2), 0u);
#pragma unroll 1
            for (int t = 0; t < 3; ++t) {
                if (XW) { wait_set(rfl, lds4a(la + L_T + 12) + t * (NXP_ * FLP), hop, t ? 0 : 1); dma16cn<3>(rst, l16, v_so(t), L_VX + wave * 3072); VMDRAIN(); }
                LBAR();
                back0(0); back0(1); back0(2); back0(3);
                back(t, [](int) {});
                VMDRAIN(); LBAR();
                if (wave == 0 && l16 == 0) st4c(rfl, F_UX + (t * 32 + h) * FLP, 65536u);
                if (wave == 0 && l16 == 0 && h == 0) STAMP(10 + t);
            }
        } else {
            unsigned tw, tr, te; int d;
            back(s + 2, [&](int j) {
                if (XW) {
                    if (j == 0) { tw = lds4a(la + L_E); tr = tim[2]; te = rt_now(); asm volatile("" : "+s"(te)); }
                    if (j == 2) { d = (int)(tw - tb - cC1); PIN(d); }
                    if (j == 3) { d = max(d, (int)dppu<0x128>((unsigned)d)); PIN(d); } if (j == 4) { d = max(d, (int)dppu<0x124>((unsigned)d)); PIN(d); }
                    if (j == 5) { d = max(d, (int)dppu<0x122>((unsigned)d)); PIN(d); } if (j == 6) { d = max(d, (int)dppu<0x121>((unsigned)d)); PIN(d); }
                    if (j == 7) tim[4 + (s & 1)] = 65536u + min(te - (tb + cP), 4095u) + (tr == (unsigned)s + 1u ? 4096u : 0u);
                    if (j == 8) { const int e = __builtin_amdgcn_readlane(d, 0); tb = (s == 1 || e > 0) ? tb + cC1 + (unsigned)e + (s == 1 ? TT_V1 : TT_V) : tb + cP; cC1 = __builtin_amdgcn_readlane(tw, 16); cP = __builtin_amdgcn_readlane(tw, 17); asm volatile("" : "+s"(tb), "+s"(cC1), "+s"(cP)); }
                }
                if (!XW && j == 0) { tw = tim[4 + ((s + 1) & 1)]; tr = lds4a(L_U + (wave & 3) * 256 + (l16 >> 2)); }
                if (!XW && j == 2) __builtin_amdgcn_raw_buffer_store_b32(tw | 65536u, rfl, wave == 7 ? l16 << 20 : OOB, F_UX + ((s + 1) * 32 + h) * FLP, 16);
                if (!XW && j == 8) { st16s(ones4(), rhx, tr, us ^ (2u << 14)); SBAR(); }
                if (!XW && j == 10) arm(us ^ (2u << 14));
                if (!XW && j == 5) dma4c(rfl, lds4a(la + L_T + 12), d1, L_F);
                });
            if (!XW) VMW(4);
        }
        AT(7);
        ATD(1, s == 41 && h == 0);
    }
    if (wave == 0 && l16 == 0) { tim[2] = 0; STAMP(512 + blockIdx.x); }
    }
    if (wave == 0 && l16 == 0 && tim[0]) set_err((a.ws + WS_HOP));
#undef la
}

constexpr int RO_UM = 131072, RO_TIM = RO_UM + 3 * 8192, RO_END = NSEG_;
__device__ __forceinline__ void ro_state(KA& a, unsigned char* lds, const int r, const unsigned pol) {
    constexpr int wave = 0;
    const int lane = lane_id();
    const rsrc_t rhx = MKR(al2m((a.ws + WS_FR)), FR_USED);
    lds_u32* tim = (lds_u32*)(lds + RO_TIM + 256);
    const unsigned sx = ((my_side() ^ pol) & 1u) << 13;
    unsigned po = sx + (2 + (r & 1)) * 16384 + lane * 16, uo = um_base(um_sides(a), pol, r >> 1) + lane * 16;
    unsigned ho = (r < 64 ? 0 : 8192) + r * 128 + (lane >> 5) * 64 + (lane & 2) * 16 + (lane & 16) + (lane & 12) + (lane & 1) * 8192;
    unsigned idx = (lane & 1) ? 0xEEEEu : 0x4444u;
    const unsigned pmo = (2 - (r & 1)) * 16384 + (r & 1) * 2048;
    const unsigned nr = r < 40 ? 0u : 1u;
    unsigned so = (r % 5 == 0 && lane < 2) ? M_STAMP + (r / 5) * 64 + lane * 8192 : OOB;
    PIN(po); PIN(uo); PIN(ho); PIN(idx); PIN(so);
#pragma unroll 1
    for (int layer = 0; layer < 2; ++layer) {
    const unsigned sb0 = (unsigned)layer * (20u << 14);
    unsigned tb = 0, cC1 = 0, cP = 0; int u3 = 0;
    u32x8 ux[16];
    auto rearm = [&](int t) { const unsigned sr = sb0 + (unsigned)((t + 2) & 3) * (5u << 14);
        if (t + 2 < NSEG_) __builtin_amdgcn_raw_buffer_store_b32(~0u, rhx, so, sr + ((t + 2) >> 2) * 4, 16);
        __builtin_amdgcn_raw_buffer_store_b32(~0u, rhx, ho, sr, 16); };
#pragma unroll 1
    for (int t = 0; t < RO_END; ++t) {
        const int s = t - 1;
        ATV;
        if (t > 0) {
            const unsigned sp_ = sb0 + (unsigned)(s & 3) * (5u << 14);
            unsigned sb = sb0 + (unsigned)(t & 3) * (5u << 14), ss = sb + (t >> 2) * 4, sp0 = sp_ + nr * 4096, sp1 = sp0 ^ 4096, sm0 = sp_ + pmo + nr * 1024, sm1 = sm0 ^ 1024, su0 = ((s & 3) << 14) + nr * 4096, su1 = su0 ^ 4096;
            asm volatile("" : "+s"(sb), "+s"(ss), "+s"(sp0), "+s"(sp1), "+s"(sm0), "+s"(sm1), "+s"(su0), "+s"(su1));
            u32x4 um[8], p[10];
            auto u_bad = [&]() { unsigned m = 0;
#pragma unroll
                for (int q = 0; q < 8; ++q) m = max(m, umax4(um[q]));
                return m; };
            unsigned tp = tb + cC1;
            {
                auto p_half = [&](int z) { CBAR();
#pragma unroll
                    for (int q = 0; q < 4; ++q) { p[4 * z + q] = __builtin_amdgcn_raw_buffer_load_b128(rhx, po + q * 1024, z ? sp1 : sp0, 16); SBAR(); }
                    p[8 + z] = __builtin_amdgcn_raw_buffer_load_b128(rhx, po, z ? sm1 : sm0, 16); SBAR();
#pragma unroll
                    for (int q = 0; q < 4; ++q) { um[4 * z + q] = __builtin_amdgcn_raw_buffer_load_b128(rhx, uo + q * 1024, z ? su1 : su0, 16); SBAR(); }
                    CBAR(); };
                unsigned o;
                auto p_use = [&](f32x4 c0, f32x4 c1) {
#pragma unroll
                    for (int z = 0; z < 2; ++z) {
#pragma unroll
                        for (int q = 4 * z; q < 4 * z + 4; ++q) { c0 = smf(p[q], ux[2 * q], c0, idx); c1 = smf(p[q], ux[2 * q + 1], c1, idx); SBAR(); }
                        c0 = smf(p[8 + z], cat8(um[4 * z], um[4 * z + 1]), c0, idx); SBAR(); c1 = smf(p[8 + z], cat8(um[4 * z + 2], um[4 * z + 3]), c1, idx); SBAR(); }
                    SBAR();
                    float v0 = c0[0] + c0[1]; PIN(v0);
                    const float v = v0 + (c1[2] + c1[3]);
                    o = pk2(dppf<0xA0>(v), dppf<0xF5>(v));
                    return !__any(v != v); };
                f32x4 c0 = zero4(), c1 = zero4();
                { const unsigned n = rt_now(); if ((int)(n - tp) > 4) { tp = n; tim[2] = (unsigned)t; } }
                unsigned tv = min(tp, 0xfffffffeu); PIN(tv);
                AT(0);
                rt_fine<23>(tp);
                AT(1); p_half(0); nops<(RFD * 23 - 100) / 8>(); p_half(1);
                if (__builtin_expect(!p_use(c0, c1), 0)) {
                    tim[2] = (unsigned)t;
                    for (unsigned sp = 0;; ++sp) {
                        p_half(0); p_half(1);
                        if (p_use(zero4(), zero4())) break;
                        unsigned m = u_bad();
#pragma unroll
                        for (int q = 0; q < 10; ++q) m = max(m, umax4(p[q]));
                        if (__all(m != 0xffffffffu)) break;
                        if ((sp & 63u) == 63u && (sp > SPIN_CAP || tim[0])) { tim[0] = 1; break; }
                        if (sp > 3 || s == 0) __builtin_amdgcn_s_sleep(R0S);
                    }
                    o = min(o, 0xfffffffeu);
                    tv = min(rt_now() - TT_RT, 0xfffffffeu);
                }
                __builtin_amdgcn_raw_buffer_store_b32(o, rhx, ho, sb, 16); SBAR();
                __builtin_amdgcn_raw_buffer_store_b32(tv, rhx, so, ss, 16);
                SBAR(); AT(2);
                tim[1] = rt_now() - (tb + cC1);
                if (!RLATE) rearm(t);
            }
            LBAR();
            unsigned char* up = lds + RO_UM + u3 * 8192 + lane_id() * 16;
#pragma unroll
            for (int q = 0; q < 8; ++q) *reinterpret_cast<u32x4*>(up + ((q >> 2) ^ nr) * 4096 + (q & 3) * 1024) = um[q];
            u3 = u3 == 2 ? 0 : u3 + 1;
            if (RLATE && t < NSEG_) { rt_wait(tb + cP + RLATE); rearm(t); }
        } else LBAR();
        AT(3);
        LBAR();
        AT(4);
        if (t + 1 < NSEG_) {
            const int ln = lane_id();
            const unsigned tpv = *reinterpret_cast<const unsigned*>(lds + RO_TIM + ln * 4);
            const unsigned char* xp = lds + (t & 3) * 32768 + (ln >> 5) * 1024 + (ln & 31) * 16;
#pragma unroll
            for (int q = 0; q < 16; ++q) { const unsigned char* p = xp + ((q >> 3) ^ nr) * 16384 + ((q >> 1) & 3) * 4096 + (q & 1) * 512; ux[q] = cat8(lds16(p), lds16(p + 2048)); }
            tb = __builtin_amdgcn_readlane(tpv, 0); cC1 = __builtin_amdgcn_readlane(tpv, 1); cP = __builtin_amdgcn_readlane(tpv, 2);
            if (t == 0) tb = rt_now() - cC1;
#pragma unroll
            for (int q = 0; q < 16; ++q) PIN(ux[q]);
        }
        ATD(2, t == 42 && r == 0);
    }
    if (lane == 0) tim[2] = 0;
    LBAR();
    }
    if (lane == 0 && tim[0]) set_err((a.ws + WS_HOP));
}
__device__ __forceinline__ void ro_time(KA& a, unsigned char* lds, const int r, const unsigned pol) {
    const int lane = lane_id();
    const rsrc_t rhx = MKR(al2m((a.ws + WS_FR)), FR_USED);
    lds_u32* tim = (lds_u32*)(lds + RO_TIM + 256);
    const unsigned sx = ((my_side() ^ pol) & 1u) << 13, to = sx + M_STAMP + min(lane, 17) * 64;
#pragma unroll 1
    for (int layer = 0; layer < 2; ++layer) {
    const rsrc_t rfl = MKR((a.ws + WS_HOP) + layer * HOP_BYTES, F_ERR);
    const unsigned sb0 = (unsigned)layer * (20u << 14);
    unsigned tb = 0, cC1 = 0, cP = 0;
#pragma unroll 1
    for (int s = 0; s < RO_END; ++s) {
        LBAR();
        if (s < NSEG_) {
            if (s > 1) rt_wait(tb + cP + TT_TW);
            unsigned tw;
            for (unsigned sp = 0;; ++sp) { CBAR();
                tw = __builtin_amdgcn_raw_buffer_load_b32(rhx, to, sb0 + (unsigned)(s & 3) * (5u << 14) + (s >> 2) * 4, 16);
                if (__all(tw != 0xffffffffu)) break;
                __builtin_amdgcn_s_sleep(6);
                if (sp > SPIN_CAP_W || tim[0]) { tim[0] = 1; break; } }
            tt_step(tw, s, rt_now(), tb, cC1, cP);
            if (lane == 0) *reinterpret_cast<u32x4*>(lds + RO_TIM) = (u32x4){tb, cC1, cP, 0u};
            if (lane == 0 && s > 1) st16c(rfl, F_RD + (s * NR_ + r) * FLP, (u32x4){1u, min((unsigned)tim[1], 4095u), tim[2] == (unsigned)s ? 1u : 0u, 0u});
        }
        LBAR();
    }
    LBAR();
    }
}
__device__ __forceinline__ void ro_master(KA& a) {
    const int lane = lane_id();
    const rsrc_t rhx = MKR(al2m((a.ws + WS_FR)), FR_USED);
    int e1 = TT_E1 * 16, e2 = TT_E2 * 16, x1 = 0, x2 = 0; bool pr2 = true;
#pragma unroll 1
    for (int layer = 0; layer < 2; ++layer) {
    const rsrc_t rfl = MKR((a.ws + WS_HOP) + layer * HOP_BYTES, F_ERR);
    const unsigned sb0 = (unsigned)layer * (20u << 14);
#pragma unroll 1
    for (int s = 0; s < RO_END; ++s) {
        LBAR();
        const int xs = max(TT_XS - s * TT_XD, 0);
        const unsigned c1 = (e1 >> 4) + TT_M1 + x1 + xs, c2 = c1 + (e2 >> 4) + TT_M2 + x2 + xs;
        const int q = max(s, 2);
        u32x4 f = ld16c(rfl, lane < 32 ? F_KM + ((q - 2) * 32 + lane) * FLP : F_UX + (q * 32 + lane - 32) * FLP), g = ld16c(rfl, F_RD + ((q - 1) * NR_ + lane) * FLP), k = ld16c(rfl, F_RD + ((q - 1) * NR_ + 64 + (lane & 15)) * FLP);
        if (lane < 4 && s + 1 < NSEG_) __builtin_amdgcn_raw_buffer_store_b32((lane & 2) ? c2 : c1, rhx, M_STAMP + 1024 + (lane & 2) * 32 + (lane & 1) * 8192, sb0 + (unsigned)((s + 1) & 3) * (5u << 14) + ((s + 1) >> 2) * 4, 16);
        SBAR();
        LBAR();
        SBAR();
        if (lane >= 32) { f.y = f.x & 4095u; f.z = (f.x >> 12) & 1u; }
        auto wmax = [&](unsigned v) { v = max(v, dppu<0x128>(v)); v = max(v, dppu<0x124>(v)); v = max(v, dppu<0x122>(v)); v = max(v, dppu<0x121>(v));
            return (int)max(max(__builtin_amdgcn_readlane(v, 0), __builtin_amdgcn_readlane(v, 16)), max(__builtin_amdgcn_readlane(v, 32), __builtin_amdgcn_readlane(v, 48))); };
        auto upd = [&](int& e, int m) { if (m > 0) { const int d = min(m * 16, e + 16 * 12) - e; e += d >> (d > 0 ? TT_UP : (s < 12 && !layer ? TT_DEC : TT_DN)); } };
        const bool rs = __any(f.z != 0u), r2 = __any((g.z | k.z) != 0u);
        if (s > 3) {
            if (!rs) upd(e1, wmax(lane < 32 ? f.y : (unsigned)max((int)f.y + TT_UM + TT_UX, 0)));
            if (!r2) upd(e2, wmax(max(g.y, k.y)));
            if (rs && !pr2) x2 += TT_BUMP; else if (r2 && !rs) x1 += TT_BUMP;
            if (!rs && !r2 && (s & TT_DCY) == 0) { x1 -= x1 > 0; x2 -= x2 > 0; }
        }
        pr2 = r2 || s < 3;
    }
    LBAR();
    if (!layer && lane == 0) { const rsrc_t rb = MKR((a.ws + WS_HOP) - 2048, 2048); const u32x2 t = ld8c(rb, 0); st8c(rb, 0, (u32x2){t.x + 1u, t.y}); }
    }
}
template <int TAIL> __device__ __forceinline__ void ro_out(KA& a, unsigned char* lds, const int r, const unsigned uo) {
    constexpr int wave = 2;
    const int lane = lane_id(), l15 = lane & 15, lg = lane >> 4, c = r >> 1, rt = r & 1, cr = (RROT & 2) ? c : 0;
    const rsrc_t rst = MKR((a.ws + WS_ST), ST_BYTES), rx = MKR((a.ws + WS_X), XI_BYTES), ro = MKR(a.out, S_ * D_ * 2);
    const unsigned xo = XI_X + rt * 20480 + (c >> 1) * 1024 + ((c & 1) * 2 + (lg >> 1)) * 256 + l15 * 16 + (lg & 1) * 8;
#pragma unroll 1
    for (int layer = 0; layer < 2; ++layer) {
    unsigned char* hop = (a.ws + WS_HOP) + layer * HOP_BYTES;
    const rsrc_t rfl = MKR(hop, F_ERR);
    unsigned tb = 0, cC1 = 0, cP = 1; int u3 = 0;
    u32x2 o = (u32x2){0u, 0u}, rr; float sq = 0.f; u32x4 p[40], um[8];
    auto put = [&](int s) {
        if (layer) __builtin_amdgcn_raw_buffer_store_b64(o, ro, ((rt * 16 + l15) * D_ + 16 * c + 4 * lg) * 2, s * (32 * D_ * 2), 0);
        else { __builtin_amdgcn_raw_buffer_store_b64(o, rx, xo, s * XI_PITCH, 16); if (lane < 32) __builtin_amdgcn_raw_buffer_store_b32(__float_as_uint(sq), rx, (rt * 40 + c) * 128 + l15 * 8 + lg * 4, s * XI_PITCH, 16); } };
    auto gate = [&](int s, int slp) { wait_set(rfl, F_PX + ((s + QA) * 64 + (lane & 31) * 2 + rt) * FLP, hop, slp); };
    auto req = [&](int s) { const int ln = lane_id();
        rr = __builtin_amdgcn_raw_buffer_load_b64(rx, xo, s * XI_PITCH, 16); SBAR();
#pragma unroll
        for (int q = 0; q < 40; ++q) { p[q] = __builtin_amdgcn_raw_buffer_load_b128(rst, ln * 16 + (q & 3) * 1024, ST_PX + ((s & (PR - 1)) * 2 + rt) * 40960 + (q < 32 ? ((q >> 2) + cr) & 7 : q >> 2) * 4096, 16); SBAR(); }
        CBAR(); };
    auto comp = [&](int s) { const int ln = lane_id();
        const unsigned char* xp = lds + (s & 3) * 32768 + ln * 16; const unsigned char* mp = lds + RO_UM + u3 * 8192 + ln * 16;
        u32x4 uf[8];
#pragma unroll
        for (int q = 0; q < 7; ++q) uf[q] = lds16(xp + (((q >> 2) + cr) & 7) * 4096 + (q & 3) * 1024);
        SBAR();
        f32x4 acc = (f32x4){lo2f(rr.x), hi2f(rr.x), lo2f(rr.y), hi2f(rr.y)};
#pragma unroll
        for (int q = 0; q < 40; ++q) { acc = dmf((TAIL == OLAG && q >= 32) ? um[q & 7] : uf[q & 7], p[q], acc); PIN(acc); SBAR();
            if (q + 7 < (TAIL == OLAG ? 32 : 40)) { uf[(q + 7) & 7] = q + 7 < 32 ? lds16(xp + ((((q + 7) >> 2) + cr) & 7) * 4096 + ((q + 7) & 3) * 1024) : lds16(mp + (q - 25) * 1024); SBAR(); } }
        o = (u32x2){pk2(acc[0], acc[1]), pk2(acc[2], acc[3])};
        sq = row_sums(o); };
    auto fin = [&](int s) { put(s); if (!layer) { VMDRAIN(); if (lane == 0) st16c(rfl, F_OF + (s * NR_ + r) * FLP, (u32x4){cP, 1u, 1u, tb}); } if (lane == 0 && r == 0) STAMP(300 + s - 56); };
    auto times = [&]() { const unsigned tpv = *reinterpret_cast<const unsigned*>(lds + RO_TIM + lane * 4); tb = __builtin_amdgcn_readlane(tpv, 0); cC1 = __builtin_amdgcn_readlane(tpv, 1); cP = __builtin_amdgcn_readlane(tpv, 2); };
    if (TAIL) {
        const int s = NSEG_ - 1 - OLAG + TAIL;
        for (int q = 0; q < 2 * NSEG_; ++q) __builtin_amdgcn_s_barrier();
        CBAR();
        u3 = s % 3;
        if (TAIL == OLAG) {
            const rsrc_t rhx = MKR(al2m((a.ws + WS_FR)), FR_USED);
            times(); rt_wait(tb + cC1);
            for (unsigned sp = 0;; ++sp) { unsigned m = 0; CBAR();
#pragma unroll
                for (int q = 0; q < 8; ++q) um[q] = __builtin_amdgcn_raw_buffer_load_b128(rhx, uo + (q & 3) * 1024, ((s & 3) << 14) + (q >> 2) * 4096, 16);
#pragma unroll
                for (int q = 0; q < 8; ++q) m = max(m, umax4(um[q]));
                if (__all(m != 0xffffffffu)) break;
                if (sp > SPIN_CAP_W) { set_err(hop); break; }
                __builtin_amdgcn_s_sleep(8); }
            rt_wait(tb + TT_TL);
        }
        gate(s, 0); req(s); comp(s); fin(s);
    } else {
#pragma unroll 1
    for (int t = 0; t < NSEG_; ++t) {
        const int s = t - OLAG;
        ATV;
        LBAR();
        if (s >= 0) {
            const unsigned to_ = max((int)(tb + cP + TT_BD - rt_now()), 0) + rt_now() + TT_OD;
            gate(s, 0);
            if (s > 0) put(s - 1);
            rt_wait(to_);
            AT(0);
            req(s);
            AT(1);
            if (!layer && s > 0) { PIN(rr); if (lane == 0) st16c(rfl, F_OF + ((s - 1) * NR_ + r) * FLP, (u32x4){cP, 1u, 1u, tb + cP}); }
            if (lane == 0 && r == 0 && s > 56) STAMP(300 + s - 57);
        }
        LBAR();
        times();
        if (s >= 0) { __builtin_amdgcn_s_sleep(OSL); comp(s); u3 = u3 == 2 ? 0 : u3 + 1; AT(2); }
        ATD(2, t == 42 && r == 0);
    }
    fin(NSEG_ - 1 - OLAG);
    }
    LBAR();
    }
}
__device__ __forceinline__ void ro_fill(KA& a, unsigned char* lds, const int r) {
    constexpr int wave = 3;
    const int lane = lane_id();
    const unsigned ldsb = LDSB();
    const rsrc_t rst = MKR((a.ws + WS_ST), ST_BYTES);
#pragma unroll 1
    for (int layer = 0; layer < 2; ++layer) {
    unsigned char* hop = (a.ws + WS_HOP) + layer * HOP_BYTES;
    const rsrc_t rfl = MKR(hop, F_ERR);
    unsigned tb = 0, cC1 = 0, cP = 0;
#pragma unroll 1
    for (int t = 0; t < RO_END; ++t) {
        ATV;
        if (t < NSEG_) {
            if (t == 0) wait_set(rfl, F_XP + (r % NXP_) * FLP, hop, 4);
            if (t > 1) rt_wait(tb + cC1 + TT_FP);
            wait_set(rfl, (r < 64 || (t == 0 && lane >= 32)) ? F_UX + (t * 32 + (lane & 31)) * FLP : F_PX + (t * 64 + (lane & 31) * 2 + ((r >> 3) & 1)) * FLP, hop, t > 2 ? 0 : 1);
        }
        LBAR();
        if (t < NSEG_) {
            if (t > 1) rt_wait(tb + cP + TT_BD);
            AT(0);
            const unsigned ub = ST_U + ((t & (URING - 1)) * 40 + (r >> 1)) * 32768, lb = ldsb + (t & 3) * 32768, o = (r & RROT & 1) * 16384;
            dma16cn<16>(rst, lane * 16, ub + o, lb + o); dma16cn<16>(rst, lane * 16, ub + (o ^ 16384), lb + (o ^ 16384));
            AT(1);
            VMDRAIN();
            AT(2);
            if (lane == 0 && r == 0 && t == 0) STAMP(13);
        }
        LBAR();
        if (t < NSEG_) { const unsigned tpv = *reinterpret_cast<const unsigned*>(lds + RO_TIM + lane * 4); tb = __builtin_amdgcn_readlane(tpv, 0); cC1 = __builtin_amdgcn_readlane(tpv, 1); cP = __builtin_amdgcn_readlane(tpv, 2); }
        ATD(2, t == 42 && r == 0);
    }
    LBAR();
    if (lane == 0) STAMP(512 + blockIdx.x);
    }
}
__device__ __forceinline__ void ro_part(KA& a, unsigned char* lds, const int r, const int wave) {
    if (wave == 0) { lds_u32* tim = (lds_u32*)(lds + RO_TIM + 256); tim[0] = 0; tim[1] = 0; tim[2] = 0; }
    if (wave == 0) { __builtin_amdgcn_s_setprio(3); ro_state(a, lds, r, page_pol(a)); }
    else if (wave == 1) ro_time(a, lds, r, page_pol(a));
    else if (wave == 2) ro_out<0>(a, lds, r, 0u);
    else if (wave == 3) { init_wait(a); ro_fill(a, lds, r); }
    else if (wave == 4) ro_out<1>(a, lds, r, 0u);
    else if (wave == 7) ro_out<2>(a, lds, r, 0u);
    else if (wave == 6) { const unsigned pol = page_pol(a); ro_out<3>(a, lds, r, um_base(um_sides(a), pol, r >> 1) + lane_id() * 16); }
    else {
        if (r == 1) { const rsrc_t rfl = MKR(a.ws + WS_HOP, F_ERR); wait_set(rfl, F_QK + lane_id() * FLP, a.ws + WS_HOP, 1); wait_set(rfl, F_QK + (64 + lane_id()) * FLP, a.ws + WS_HOP, 1); if (lane_id() < 8) st4c(rfl, F_IN + lane_id() * FLP, 1u); }
        const rsrc_t rz = MKR((a.ws + WS_HOP) + HOP_BYTES, HOP_BYTES);
        for (unsigned o = r * 1024 + lane_id() * 16; o < HOP_BYTES; o += NR_ * 1024) st16c(rz, o, (u32x4){0u, 0u, 0u, 0u});
        VMDRAIN();
        if (r == 0) ro_master(a); else for (int q = 0; q < 4 * NSEG_ + 2; ++q) __builtin_amdgcn_s_barrier(); }
}

#define QE 6
#define XKB 4
#define XSL 0
#define VMKNOWN5() __builtin_amdgcn_s_waitcnt(0x0F75)
#define PIM 60
#define XNP 3
constexpr int XB = 40960;
struct XWt { u32x4 w[2][20]; f32x4 cs[2]; };
template <int NB, int NR, class G, class H> __device__ __forceinline__ void x_load(XWt& x, unsigned char* lds, const int wave, const int pw, G&& src, H&& more) {
    const unsigned ldsb = LDSB(), lane = lane_id(), l15 = lane & 15, lg = lane >> 4;
    load_b<NB, 4, NR, 0, 0>(ldsb, wave, src, [&](int rd, int base) { if (rd >= 10) more(rd, base); else if (pw >= 0) {
#pragma unroll
        for (int u = 0; u < 2; ++u)
#pragma unroll
            for (int k = 0; k < 2; ++k) x.w[u][(2 * rd + k) % 20] = ld_frag(lds + base + (pw >> 1) * 8192, 32 * k + 8 * lg, 4, (pw & 1) * 2 + u, l15); } }, [](int) {});
    if (pw >= 0) {
        const u32x4 one = (u32x4){0x3F803F80u, 0x3F803F80u, 0x3F803F80u, 0x3F803F80u};
        x.cs[0] = Z4; x.cs[1] = Z4;
#pragma unroll
        for (int k = 0; k < 20; ++k) { x.cs[0] = dmf(x.w[0][k], one, x.cs[0]); x.cs[1] = dmf(x.w[1][k], one, x.cs[1]); }
        PIN(x.cs[0]); PIN(x.cs[1]);
    }
}
__device__ __forceinline__ void c_load(u32x2 (&sv)[4], rsrc_t rx, int t) {
    const int lane = lane_id();
    asm volatile("s_nop 4\n\tbuffer_load_dwordx2 %0, %4, %5, %6 offen sc1\n\tbuffer_load_dwordx2 %1, %4, %5, %6 offen offset:128 sc1\n\tbuffer_load_dwordx2 %2, %4, %5, %6 offen offset:256 sc1\n\tbuffer_load_dwordx2 %3, %4, %5, %6 offen offset:384 sc1"
        : "=&v"(sv[0]), "=&v"(sv[1]), "=&v"(sv[2]), "=&v"(sv[3]) : "v"((lane >> 4) * 512 + (lane & 15) * 8), "s"(rx), "s"(t * XI_PITCH + XI_S8) : "memory");
}
__device__ __forceinline__ void c_got(u32x2 (&sv)[4]) { asm volatile("" : "+v"(sv[0]), "+v"(sv[1]), "+v"(sv[2]), "+v"(sv[3])); }
__device__ __forceinline__ void c_put(const u32x2 (&sv)[4], unsigned char* dst) {
    const f32x2 s = (__builtin_bit_cast(f32x2, sv[0]) + __builtin_bit_cast(f32x2, sv[1])) + (__builtin_bit_cast(f32x2, sv[2]) + __builtin_bit_cast(f32x2, sv[3]));
    const auto a1 = __builtin_amdgcn_permlane16_swap(__float_as_uint(s[0]), __float_as_uint(s[0]), false, false), a2 = __builtin_amdgcn_permlane16_swap(__float_as_uint(s[1]), __float_as_uint(s[1]), false, false);
    float m, i; ln_consts(__uint_as_float(a1[0]) + __uint_as_float(a1[1]), __uint_as_float(a2[0]) + __uint_as_float(a2[1]), m, i);
    const int lane = lane_id();
    *reinterpret_cast<f32x2*>(dst + (lane >> 5) * 128 + (lane & 15) * 8) = (f32x2){-m, i};
}
template <class F> __device__ __forceinline__ void x_proj(const XWt& x, const unsigned char* xb, const unsigned char* cb, u32x4 (&o)[2], F&& mid) {
    const int lane = lane_id();
    unsigned l16 = lane * 16, c8 = (lane & 15) * 8; PIN(l16); PIN(c8);
    const unsigned char* xp = xb + l16;
    f32x4 acc[2][2] = {{Z4, Z4}, {Z4, Z4}};
    u32x4 xf[3][2]; f32x2 ct[2];
#pragma unroll
    for (int k = 0; k < 2; ++k) { xf[k][0] = lds16(xp + k * 1024); SBAR(); xf[k][1] = lds16(xp + 20480 + k * 1024); SBAR(); }
#pragma unroll
    for (int k = 0; k < 20; ++k) {
#pragma unroll
        for (int j = 0; j < 4; ++j) { acc[j >> 1][j & 1] = dmf(x.w[j >> 1][k], xf[k % 3][j & 1], acc[j >> 1][j & 1]); PIN(acc[j >> 1][j & 1]); SBAR();
            if ((j == 1 || j == 2) && k + 2 < 20) { xf[(k + 2) % 3][j - 1] = lds16(xp + (j - 1) * 20480 + (k + 2) * 1024); SBAR(); }
            if (j == 1 && k == 18) { ct[0] = *reinterpret_cast<const f32x2*>(cb + c8); ct[1] = *reinterpret_cast<const f32x2*>(cb + c8 + 128); SBAR(); } }
        mid(k, l16);
    }
#pragma unroll
    for (int rt = 0; rt < 2; ++rt)
#pragma unroll
        for (int q = 0; q < 4; ++q) { const int u = q >> 1, e = 2 * (q & 1);
            o[rt][q] = pk2(__builtin_fmaf(ct[rt][0], x.cs[u][e], acc[u][rt][e]) * ct[rt][1], __builtin_fmaf(ct[rt][0], x.cs[u][e + 1], acc[u][rt][e + 1]) * ct[rt][1]); }
}
__device__ __forceinline__ void xp_part(KA& a, unsigned char* lds, const int j, const int wave) {
    const int lane = lane_id(), tid = wave * 64 + lane;
    const unsigned ldsb = LDSB();
    const int hp = j / 3, kind = j - 3 * hp;
    XWt x;
    {
        const rsrc_t rk = MKR(psel(a.wk, a.wv, kind >> 1), D_ * HE_ * 2), rv = MKR(a.wv, D_ * HE_ * 2);
        const unsigned row = 8 * wave + (lane >> 3), vo = row * (HE_ * 2) + ((lane & 7) ^ (wsw(row) << 1)) * 16 + hp * 768;
        x_load<4, 10>(x, lds, wave, wave, [&](int rd, int q, unsigned dst) { dma16(q < 3 ? rk : rv, vo, rd * (64 * HE_ * 2) + (kind < 2 ? kind * 384 + (q % 3) * 128 : (q >> 1) * 384 + 128 + (q & 1) * 128), dst); }, [](int, int) {});
    }
    const unsigned oo = kind < 2 ? (wave < 6 ? ST_KX + wave * 1024 : ST_VX + (wave - 6) * 1024) + (2 * hp + kind) * 12288 : ST_VX + (2 * hp + (wave >> 2)) * 12288 + (2 + (wave & 3)) * 1024;
    const rsrc_t rst = MKR((a.ws + WS_ST), ST_BYTES), rx = MKR((a.ws + WS_X), XI_BYTES);
    const int wr_ = (wave + (blockIdx.x >> 3) * ROT) & 7;
#pragma unroll 1
    for (int layer = 0; layer < 2; ++layer) {
    unsigned char* hop = (a.ws + WS_HOP) + layer * HOP_BYTES;
    const rsrc_t rfl = MKR(hop, F_ERR);
    auto piece = [&](int t, int b, int k, unsigned l16) { const unsigned o = (wr_ * 5 + k) * 1024; dma16c(rx, l16, (unsigned)t * XI_PITCH + XI_X + o, ldsb + b * XB + o); };
    auto x_dma = [&](int t, int b) {
#pragma unroll
        for (int k = 0; k < 5; ++k) piece(t, b, k, lane * 16); };
    if (tid == 0 && j == 0) STAMP(7);
    if (wave == 2) { if (!layer) init_wait(a); wait_set(rfl, F_AF + (lane & 15) * FLP, hop, XSL); }
    LBAR();
    if (tid == 0 && j == 0) STAMP(8);
    { u32x2 sv[4]; if (wave == 1) c_load(sv, rx, 0);
      x_dma(0, 0);
      if (wave == 1) { VMW(5); c_got(sv); c_put(sv, lds + 3 * XB); } }
    if (wave == 2) wait_set(rfl, F_AF + (16 + (lane & 15)) * FLP, hop, XSL);
    VMKNOWN(); LBAR();
    x_dma(1, 1);
    unsigned tnext = rt_now(); int b = 0;
#pragma unroll 1
    for (int t = 0; t < NSEG_; ++t) {
        ATV;
        AT(0);
        u32x4 fl; u32x2 sv[4];
        const unsigned fo = (lane < 16 && t >= XLD) ? F_KM + ((t - XLD) * 32 + 2 * hp) * FLP : F_AF + (min(t + 2, NSEG_ - 1) * 16 + (lane & 15)) * FLP;
        if (wave == 2) { if (t > XLD) rt_wait(tnext);
            asm volatile("buffer_load_dwordx4 %0, %1, %2, 0 offen sc1" : "=&v"(fl) : "v"(fo), "s"(rfl) : "memory"); }
        if (wave == 1) c_load(sv, rx, min(t + 1, NSEG_ - 1));
        const int bn = b == 0 ? 2 : b - 1;
        u32x4 o[2];
        x_proj(x, lds + b * XB, lds + 3 * XB + (t & 1) * 256, o, [&](int k, unsigned l16) {
            if (k == XKB) { if (wave == 1) VMW(4); else if (wave == 2) VMW(1); else VMDRAIN();
                __builtin_amdgcn_s_barrier(); CBAR();
                if (wave == 0 && t > 0 && l16 == 0) { st4c(rfl, F_XP + ((t - 1) * NXP_ + j) * FLP, 1u); if (j == 0 && t < 4) STAMP(t); }
                AT(2); SBAR(); }
            if (k == XKB + 3) {
                if (wave == 1 || wave == 2) VMDRAIN();
                if (wave == 2) { asm volatile("" : "+v"(fl));
                    if (__builtin_expect(__all(fl.x != 0u), 1)) tnext = __builtin_amdgcn_readfirstlane(fl.w) + XNP * __builtin_amdgcn_readfirstlane(fl.x) + TT_KD; else { wait_timed(rfl, fo, hop, tnext, XNP, TT_KD, t < XLD); VMKNOWN(); } }
                __builtin_amdgcn_s_barrier(); CBAR(); SBAR(); }
            if (k > XKB + 3 && k <= XKB + 13 && ((k - XKB) & 1) == 0 && t + 2 < NSEG_) { piece(t + 2, bn, (k - XKB - 4) >> 1, l16); SBAR(); } });
        const int ln = lane_id();
        st16s(o[0], rst, ln * 16, oo + (t & (KXR - 1)) * (32 * 12288)); st16s(o[1], rst, ln * 16, oo + (t & (KXR - 1)) * (32 * 12288) + 6144);
        AT(1);
        if (wave == 1) { c_got(sv); c_put(sv, lds + 3 * XB + ((t + 1) & 1) * 256); asm volatile("s_waitcnt lgkmcnt(0)" ::: "memory"); }
        ATD(3, t == 48 && j == 0);
        b = b == 2 ? 0 : b + 1;
    }
    VMDRAIN(); LBAR();
    if (tid == 0) st4c(rfl, F_XP + ((NSEG_ - 1) * NXP_ + j) * FLP, 1u);
    if (tid == 0) STAMP(512 + blockIdx.x);
    }
}

template <int AT_> __device__ __forceinline__ void xq_run(KA& a, unsigned char* lds, const int h, const int wave) {
    constexpr bool ATT = AT_ >= 0; constexpr int qt = ATT ? AT_ : 0;
    const int lane = lane_id(), tid = wave * 64 + lane;
    const unsigned ldsb = LDSB();
    constexpr int L_C = 2 * XB, L_Q = L_C + 512, L_VX = L_Q + (QA + 1) * 12288;
    rt_wait(rt_now() + XQD);
    XWt x; u32x4 wo[4][6];
    {
        const rsrc_t rq = MKR(a.wq, D_ * HE_ * 2), ro = MKR(a.wo, HE_ * D_ * 2);
        const unsigned row = 8 * wave + (lane >> 3), ch = ((lane & 7) ^ (wsw(row) << 1)) * 16, vo = row * (HE_ * 2) + ch + h * 384, oo = (h * 192 + row) * (D_ * 2) + ch;
        x_load<3, 12>(x, lds, wave, ATT ? -1 : wave - 2, [&](int rd, int q, unsigned dst) { if (rd < 10) dma16(rq, vo, rd * (64 * HE_ * 2) + q * 128, dst); else dma16(ro, oo, q * (64 * D_ * 2) + (rd - 2) * 128, dst); },
            [&](int rd, int base) { if (rd == 10 + AT_) {
#pragma unroll
                for (int q = 0; q < 24; ++q) wo[q / 6][q % 6] = ld_frag(lds + base + ((q % 6) >> 1) * 8192, (q & 1) * 32 + 4 * (lane >> 4), 16, q / 6, lane & 15); } });
    }
    const rsrc_t rst = MKR((a.ws + WS_ST), ST_BYTES), rx = MKR((a.ws + WS_X), XI_BYTES);
    const int wr_ = (wave - 2 + (blockIdx.x >> 3)) % 6, xj = xp_of(h), xj2 = 3 * (h >> 1) + 2;
    const int l15 = lane & 15, lg = lane >> 4;
    const bool cm[4] = {4 * lg <= l15, 4 * lg + 1 <= l15, 4 * lg + 2 <= l15, 4 * lg + 3 <= l15};
#pragma unroll 1
    for (int layer = 0; layer < 2; ++layer) {
    unsigned char* hop = (a.ws + WS_HOP) + layer * HOP_BYTES;
    const rsrc_t rfl = MKR(hop, F_ERR);
    auto piece = [&](int t, int k, unsigned l16) { const unsigned o = min(wr_ * 7 + k, 39) * 1024; dma16c(rx, l16, (unsigned)t * XI_PITCH + XI_X + o, ldsb + (t & 1) * XB + o); };
    if (wave == 0) { if (!layer) init_wait(a); wait_set(rfl, F_AF + (lane & 31) * FLP, hop, 2); }
    LBAR();
    if (!ATT) {
#pragma unroll
        for (int k = 0; k < 7; ++k) piece(0, k, lane * 16); }
    u32x4 kf[2][6];
    if (AT_ == 0) { u32x2 sv[4]; c_load(sv, rx, 0); VMDRAIN(); c_got(sv); c_put(sv, lds + L_C); }
    VMKNOWN();
    unsigned tnext = rt_now(); int qs = 0;
#pragma unroll 1
    for (int i = 0; i < NSEG_ + QA; ++i) {
        ATV;
        if (i <= NSEG_) LBAR();
        AT(0);
        if (!ATT) {
            if (i < NSEG_) {
                u32x4 o[2];
                x_proj(x, lds + (i & 1) * XB, lds + L_C + (i & 1) * 256, o, [&](int k, unsigned l16) { if ((k & 1) && k < 14 && i + 1 < NSEG_) { piece(i + 1, k >> 1, l16); SBAR(); } });
                unsigned char* qp = lds + L_Q + qs * 12288 + (wave - 2) * 1024 + lane_id() * 16;
                *reinterpret_cast<u32x4*>(qp) = o[0]; *reinterpret_cast<u32x4*>(qp + 6144) = o[1];
                AT(1);
                VMDRAIN();
            }
        } else {
            const int t = i - QA;
            const unsigned fa = F_AF + (min(i + 2, NSEG_ - 1) * 16 + l15) * FLP, fx = F_XP + ((lane & 1) ? xj : xj2) * FLP, ub = ST_U + (((i & (URING - 1)) * 40 + 32 + 4 * qt) * 32 + h) * 1024;
            unsigned l16 = lane_id() * 16; PIN(l16);
            auto v_dma = [&]() { dma16cn<6>(rst, l16, ST_VX + (((i + 1) & (KXR - 1)) * 32 + h) * 12288 + qt * 6144, ldsb + L_VX + ((i + 1) & 1) * 12288 + qt * 6144); };
            auto next = [&]() {
                const unsigned kb = ST_KX + (((t + 1) & (KXR - 1)) * 32 + h) * 12288;
#pragma unroll
                for (int m = 0; m < (qt ? 12 : 6); ++m) asm volatile("buffer_load_dwordx4 %0, %1, %2, %3 offen offset:%4 sc1" : "=&v"(kf[m / 6][m % 6]) : "v"(l16), "s"(rst), "s"(kb + (m >> 2) * 4096), "i"((m & 3) * 1024) : "memory"); };
            auto got = [&]() {
#pragma unroll
                for (int m = 0; m < (qt ? 12 : 6); ++m) PIN(kf[m / 6][m % 6]); };
            u32x4 vf[12]; u32x2 sv[4];
            auto u_two = [&](int u0) {
                f32x4 acc[2][2] = {{Z4, Z4}, {Z4, Z4}};
#pragma unroll
                for (int j = 0; j < 24; ++j) { const int k = j >> 2, u = (j >> 1) & 1, r = j & 1; acc[u][r] = dmf(vf[r * 6 + k], wo[u0 + u][k], acc[u][r]); }
                SBAR();
#pragma unroll
                for (int u = 0; u < 2; ++u) st16s((u32x4){pk2(acc[u][0][0], acc[u][1][0]), pk2(acc[u][0][2], acc[u][1][2]), pk2(acc[u][0][1], acc[u][1][1]), pk2(acc[u][0][3], acc[u][1][3])}, rst, l16, ub + (u0 + u) * 32768);
                SBAR(); };
            if (t < 0) {
                if (!qt) { c_load(sv, rx, i + 1); VMDRAIN(); c_got(sv); c_put(sv, lds + L_C + ((i + 1) & 1) * 256); }
                wait_set(rfl, fx + i * (NXP_ * FLP), hop, 1);
#pragma unroll
                for (int m = 0; m < 12; ++m) vf[m] = __builtin_amdgcn_raw_buffer_load_b128(rst, l16 + (m & 3) * 1024, ST_VX + (i * 32 + h) * 12288 + (m >> 2) * 4096, 16);
                SBAR();
                u_two(0); u_two(2);
                VMDRAIN();
                if (l16 == 0) st4c(rfl, F_PX + (i * 64 + h * 2 + qt) * FLP, 1u);
                wait_set(rfl, (lane < 16 && t == -1) ? fx + (i + 1) * (NXP_ * FLP) : fa, hop, 1); v_dma(); next(); VMDRAIN(); got();
            } else {
                const unsigned char* qp = lds + L_Q + (qs == QA ? 0 : qs + 1) * 12288 + qt * 6144 + l16;
                u32x4 qf[6], km[6], fl;
                const bool uu = i < NSEG_;
#pragma unroll
                for (int m = 0; m < 12; ++m) vf[m] = lds16(lds + L_VX + (i & 1) * 12288 + l16 + m * 1024);
                SBAR();
                if (!qt) c_load(sv, rx, min(i + 1, NSEG_ - 1));
                if (uu && t > QE) { v_dma(); u_two(0); VMW(8); } else { if (uu) u_two(0); VMDRAIN(); }
                got();
                if (!qt) c_got(sv);
                if (l16 == 0 && t <= PIM && t > QE) st4c(rfl, F_PX + ((i - 1) * 64 + h * 2 + qt) * FLP, 1u);
                AT(1);
                const unsigned fo = lane < 16 ? F_KM + (t * 32 + h) * FLP : (lane < 32 ? fx + min(i + (t < QE ? 1 : 2), NSEG_ - 1) * (NXP_ * FLP) : fa);
                const int dl = t + 2 == NSEG_ ? TT_QN : TT_QD;
#pragma unroll
                for (int m = 0; m < 6; ++m) qf[m] = lds16(qp + m * 1024);
                rt_wait(tnext);
                AT(5);
                asm volatile("buffer_load_dwordx4 %0, %1, %2, 0 offen sc1" : "=&v"(fl) : "v"(fo), "s"(rfl) : "memory");
                f32x4 sc[3] = {Z4, Z4, Z4};
#pragma unroll
                for (int m = 0; m < 6; ++m) { sc[0] = dmf(kf[0][m], qf[m], sc[0]); if (qt) sc[1] = dmf(kf[1][m], qf[m], sc[1]); }
                float s2[10]; float mx = -3.0e38f, scl = 0.07216878364870322f;
                auto cvt = [&](int q) {
                    const unsigned r0 = pk2(sc[q >> 1][2 * (q & 1)], sc[q >> 1][2 * (q & 1) + 1]), r1 = pk2(muls(lo2f(r0), scl), muls(hi2f(r0), scl));
                    s2[2 * q] = (q >> 1) != qt || cm[2 * (q & 1)] ? lo2f(r1) : -3.0e38f; s2[2 * q + 1] = (q >> 1) != qt || cm[2 * (q & 1) + 1] ? hi2f(r1) : -3.0e38f;
                    mx = fmaxf(mx, fmaxf(s2[2 * q], s2[2 * q + 1])); PIN(s2[2 * q]); PIN(s2[2 * q + 1]); };
#pragma unroll
                for (int q = 0; q < 4; ++q) { if (qt == 0 && q >= 2) { s2[2 * q] = 0.f; s2[2 * q + 1] = 0.f; } else cvt(q); }
                PIN(mx);
                if (!qt && uu) c_put(sv, lds + L_C + ((i + 1) & 1) * 256);
                asm volatile("s_waitcnt vmcnt(0)" : "+v"(fl) :: "memory");
                if (__all(fl.x != 0u)) tnext = __builtin_amdgcn_readfirstlane(fl.w) + 2u * __builtin_amdgcn_readfirstlane(fl.x) + dl; else wait_timed(rfl, fo, hop, tnext, 2, dl, t + 1 == NSEG_ || t < QE);
                AT(2);
                if (l16 == 0 && h == 0 && qt == 0 && t >= 56) STAMP(360 + t - 56);
                const unsigned mo = (lg * 8 + (((l15 >> 2) * 2 + (l15 & 1) + (lg & 1) * 4) & 7)) * 16;
                if (t <= QE) v_dma();
#pragma unroll
                for (int m = 0; m < 6; ++m) asm volatile("buffer_load_dwordx4 %0, %1, %2, %3 offen offset:%4 sc1" : "=&v"(km[m]) : "v"(mo), "s"(rst), "s"(ST_KM + ((t & (PR - 1)) * 32 + h) * 3072), "i"(m * 512) : "memory");
                if (uu) { u_two(2); VMW(2); } else VMDRAIN();
                asm volatile("" : "+v"(km[0]), "+v"(km[1]), "+v"(km[2]), "+v"(km[3]), "+v"(km[4]), "+v"(km[5]));
#pragma unroll
                for (int m = 0; m < 6; ++m) sc[2] = dmf(km[m], qf[m], sc[2]);
                cvt(4);
                mx = xg_max(mx) * -1.4426950408889634f;
                float sum = 0.f, su1 = 0.f;
#pragma unroll
                for (int q = 0; q < 10; ++q) { if (qt == 0 && q >= 4 && q < 8) continue; s2[q] = __builtin_amdgcn_exp2f(__builtin_fmaf(s2[q], 1.4426950408889634f, mx)); if (q & 1) su1 += s2[q]; else sum += s2[q]; }
                float inv = __builtin_amdgcn_rcpf(xg_sum(sum + su1));
                asm volatile("s_nop 0" : "+v"(inv));
#pragma unroll
                for (int q = 0; q < 10; ++q) { if (qt == 0 && q >= 4 && q < 8) continue; s2[q] = muls(s2[q], inv); }
                const unsigned pb = ST_PX + ((t & (PR - 1)) * 2 + qt) * 40960;
                st16s((u32x4){pk2(s2[0], s2[4]), pk2(s2[2], s2[6]), pk2(s2[1], s2[5]), pk2(s2[3], s2[7])}, rst, l16, pb + h * 1024);
                __builtin_amdgcn_raw_buffer_store_b32(pk2(s2[8], s2[9]), rst, l15 * 16 + (lg & 1) * 8 + (lg >> 1) * 4, pb + 32768 + h * 256, 16);
                AT(3);
                if (t >= PIM || t < QE) { VMDRAIN(); if (l16 == 0) st4c(rfl, F_PX + (i * 64 + h * 2 + qt) * FLP, 1u); }
                next();
                AT(4);
                if (l16 == 0 && h == 0 && qt == 1 && t >= 56) STAMP(340 + t - 56);
            }
        }
        ATD(4, i == 44 && h == 0);
        qs = qs == QA ? 0 : qs + 1;
    }
    VMDRAIN(); LBAR();
    if (tid == 0) STAMP(512 + blockIdx.x);
    }
}
__device__ __forceinline__ void xq_part(KA& a, unsigned char* lds, const int h, const int wave) {
    if (wave < 2) __builtin_amdgcn_s_setprio(1);
    if (wave == 0) xq_run<0>(a, lds, h, wave); else if (wave == 1) xq_run<1>(a, lds, h, wave); else xq_run<-1>(a, lds, h, wave);
}

__device__ __forceinline__ void q_part(KA& a, unsigned char* lds, const int rb, const int g, const int wave) {
    constexpr int STG = 26624, NS = 5, layer = 0;
    const int lane = lane_id(), l15 = lane & 15, lg = lane >> 4, rp = wave & 1, cg = wave >> 1;
    const unsigned ldsb = LDSB();
    const rsrc_t rw = MKR(a.wqkv, D_ * 1920 * 2), rx = MKR(a.x0, S_ * D_ * 2);
    unsigned vo[4];
    vo[0] = (rb * 128 + wave * 16 + (lane >> 2)) * (D_ * 2) + ((lane ^ -lg) & 3) * 16;
#pragma unroll
    for (int u = 0; u < 3; ++u) { const int p = wave + 8 * u, c = p * 16 + (lane >> 2), row = c / 9, e = c - row * 9, sg = e / 3;
        vo[u + 1] = p < 18 ? row * 3840 + sg * 1280 + ((160 * g) & ~63) + (e - sg * 3) * 64 + ((lane & 3) ^ ((row >> 2) & 2)) * 16 : OOB; }
    const unsigned m3 = wave < 2 ? ~0u : 0u, d3 = wave < 2 ? 24576 : NS * STG;
    const int rot = (rb + (g & 1) * 5) % 10;
    auto piece = [&](int st, unsigned sl, int j) { dma16(j ? rw : rx, vo[j], st * (j ? 32 * 3840 : 64), ldsb + wave * 1024 + (j < 3 ? sl + j * 8192 : (sl & m3) + d3)); };
#pragma unroll 1
    for (int st = 0; st < NS - 1; ++st) {
#pragma unroll
        for (int j = 0; j < 4; ++j) piece((st + 2 * rot) % 20, st * STG, j); }
    if (wave == 0 && lane == 0 && rb == 0 && g == 0) STAMP(17);
    f32x4 acc[4][4], gr = Z4, cs[4] = {Z4, Z4, Z4, Z4};
#pragma unroll
    for (int i = 0; i < 16; ++i) acc[i >> 2][i & 3] = Z4;
    u32x4 one = (u32x4){0x3F803F80u, 0x3F803F80u, 0x3F803F80u, 0x3F803F80u}; PIN(one);
    unsigned wp[4]; const unsigned xp = (rp * 4 + cg) * 1024 + l15 * 64 + ((lg ^ -(l15 >> 2)) & 3) * 16;
#pragma unroll
    for (int u = 0; u < 4; ++u) { const int T = min(4 * u + cg, 14); wp[u] = 8192 + (8 * lg + (l15 >> 2)) * 576 + (((T / 5) * 192 + (T % 5 + (g & 1)) * 32) ^ ((lg & 1) * 32)) + (l15 & 3) * 8; }
    u32x4 xf[2][4], wf[2][4];
    auto rw_ = [&](int kk, unsigned sl, int u) { const unsigned char* p = lds + sl + wp[u]; const u32x2 x0 = ldstr(p), x1 = ldstr(p + 4 * 576); wf[kk][u] = (u32x4){x0.x, x0.y, x1.x, x1.y}; SBAR(); };
    auto rx_ = [&](int kk, unsigned sl, int i) { xf[kk][i] = lds16(lds + sl + (xp ^ (i * 1024))); SBAR(); };
    VMW(12);
    LBAR();
    if (wave == 0 && lane == 0 && rb == 0 && g == 0) STAMP(14);
#pragma unroll
    for (int j = 0; j < 8; ++j) { if (j < 4) rx_(0, 0, j); else rw_(0, 0, j - 4); }
    unsigned sn = STG, sf = (NS - 1) * STG;
    int cf = (rot + 2) % 10; const int cb = (rb + 10 - rot) % 10 + (rb < 10 ? 0 : 16);
#pragma unroll 1
    for (int ch = 0; ch < 10; ++ch) {
#pragma unroll
        for (int kk = 0; kk < 2; ++kk) {
            AT(3 * kk);
            VMW(8);
            __builtin_amdgcn_s_barrier(); CBAR();
            AT(3 * kk + 1);
            gr = dmf(xf[kk][0], xf[kk][0], gr); PIN(gr); SBAR();
#pragma unroll
            for (int j = 0; j < 16; ++j) { const int u = j >> 2, i = j & 3;
                if (j == 12 && cg == 3) wf[kk][3] = one;
                acc[i][u] = dmf(wf[kk][u], xf[kk][i], acc[i][u]); PIN(acc[i][u]); SBAR();
                if (!(j & 1)) { if (j < 8) rx_(kk ^ 1, sn, j >> 1); else rw_(kk ^ 1, sn, (j >> 1) - 4); }
                if (i == 1) { piece(2 * cf + kk, sf, u); SBAR(); } }
            if (rp) { cs[0] = dmf(wf[kk][2], one, cs[0]); cs[1] = dmf(wf[kk][3], one, cs[1]); } else { cs[0] = dmf(wf[kk][0], one, cs[0]); cs[1] = dmf(wf[kk][1], one, cs[1]); }
            if (__builtin_expect(ch == cb && kk == rp, 0)) {
#pragma unroll
                for (int u = 0; u < 4; ++u) { const int T = 4 * u + cg; st16s(wf[kk][u], MKR(a.ws + WS_WF_AQKV, D_ * 1920 * 2), T < 15 ? lane * 16 : OOB, (((T / 5) * 40 + 5 * g + T % 5) * 20 + 2 * rb + kk) * 1024); } }
            SBAR();
            sn = sn == (NS - 1) * STG ? 0 : sn + STG; sf = sf == (NS - 1) * STG ? 0 : sf + STG;
        }
        cf = cf == 9 ? 0 : cf + 1;
        if (ch == 7) { vo[0] = vo[1] = vo[2] = vo[3] = OOB; }
        AT(6);
        ATD(6, ch == 5 && rb == 0 && g == 0);
    }
    {
        const rsrc_t rz = MKR(a.ws + WS_HOP + F_AF, HOP_BYTES - F_AF), rh = MKR(al2m(a.ws + WS_FR), FR_FILL); const unsigned w = (rb * 8 + g) * 8 + wave;
#pragma unroll
        for (int j = 0; j < 3; ++j) st16c(rz, (w + j * 1024) * 1024 + lane * 16, (u32x4){0u, 0u, 0u, 0u});
        static_assert(HOP_BYTES - F_AF <= 3u << 20, "flags");
        st16c(rh, (w >> 4) % 20u >= 2u ? w * 1024 + lane * 16 : OOB, (u32x4){~0u, ~0u, ~0u, ~0u});
    }
    if (wave == 0 && lane == 0 && rb == 0 && g == 0) STAMP(15);
    constexpr int KP = 176, L_K = 0, L_V = L_K + 128 * KP, L_Q = L_V + 128 * KP + 64, L_S = NS * STG + 8192, L_CS = L_S + 1024;
    const int row = rp * 64 + cg * 16 + l15;
    if (lg == (l15 >> 2)) *reinterpret_cast<float*>(lds + L_S + row * 8 + 4) = (l15 & 2) ? ((l15 & 1) ? gr[3] : gr[2]) : ((l15 & 1) ? gr[1] : gr[0]);
    if (cg == 3) *reinterpret_cast<float*>(lds + L_S + (row ^ (lg * 16)) * 8) = lg < 2 ? (lg ? acc[1][3][0] : acc[0][3][0]) : (lg == 2 ? acc[2][3][0] : acc[3][3][0]);
    if (l15 < 2) *reinterpret_cast<f32x4*>(lds + L_CS + wave * 128 + l15 * 64 + lg * 16) = l15 ? cs[1] : cs[0];
    VMW(4); LBAR();
    f32x2 ct[4];
    {
        const f32x2 sv = *reinterpret_cast<const f32x2*>(lds + L_S + (row ^ (lg * 16)) * 8);
#pragma unroll
        for (int u = 0; u < 4; ++u) cs[u] = *reinterpret_cast<const f32x4*>(lds + L_CS + cg * 256 + u * 64 + lg * 16);
        float m, iv; ln_consts(sv[0], sv[1], m, iv); m = -m;
        const auto m2 = __builtin_amdgcn_permlane32_swap(__float_as_uint(m), __float_as_uint(m), false, false), i2 = __builtin_amdgcn_permlane32_swap(__float_as_uint(iv), __float_as_uint(iv), false, false);
#pragma unroll
        for (int z = 0; z < 2; ++z) { const auto x = __builtin_amdgcn_permlane16_swap(m2[z], m2[z], false, false), y = __builtin_amdgcn_permlane16_swap(i2[z], i2[z], false, false);
            ct[2 * z] = (f32x2){__uint_as_float(x[0]), __uint_as_float(y[0])}; ct[2 * z + 1] = (f32x2){__uint_as_float(x[1]), __uint_as_float(y[1])}; }
    }
    if (wave == 0 && lane == 0 && rb == 0 && g == 0) STAMP(18);
    auto tile = [&](int u, bool ph, unsigned char* dst, int pitch) {
#pragma unroll
        for (int i = 0; i < 4; ++i) { f32x4 y = acc[i][u] + ct[i][0] * cs[u];
            if (ph) { y = y * ct[i][1] + 1e-6f; const f32x4 z = y * 1.4426950408889634f - 1.4426950408889634e-6f;
#pragma unroll
                for (int r = 0; r < 4; ++r) y[r] = __builtin_amdgcn_exp2f(__builtin_amdgcn_fmed3f(z[r], 0.f, -3.0e38f)) + __builtin_amdgcn_fmed3f(y[r], 1e-6f, 3.0e38f); }
            else y *= ct[i][1];
            *reinterpret_cast<u32x2*>(dst + (i ^ cg) * pitch) = (u32x2){pk2(y[0], y[1]), pk2(y[2], y[3])}; } };
    unsigned char* kp = lds + L_K + (rp * 64 + l15) * KP + 8 * lg + cg * 32;
    if (cg > 0) tile(1, true, kp - 32, 16 * KP);
    if (cg < 2) tile(2, true, kp + 96, 16 * KP); else tile(2, false, kp + (L_V - L_K) - 64, 16 * KP);
    if (cg < 3) tile(3, false, kp + (L_V - L_K) + 64, 16 * KP);
    LBAR();
    if (wave == 0 && lane == 0 && rb == 0 && g == 0) STAMP(19);
    if (wave >= 4) {
        f32x4 d4[4] = {Z4, Z4, Z4, Z4};
#pragma unroll
        for (int ks = 0; ks < 4; ++ks) {
            u32x4 A[2], B[2];
#pragma unroll
            for (int t = 0; t < 2; ++t) { const unsigned char* p = lds + L_K + (32 * ks + 8 * lg + (l15 >> 2)) * KP + 40 * (wave & 3) + 32 * t + 8 * (l15 & 3);
                const u32x2 a0 = ldstr(p), a1 = ldstr(p + 4 * KP), b0 = ldstr(p + (L_V - L_K)), b1 = ldstr(p + (L_V - L_K) + 4 * KP);
                A[t] = (u32x4){a0.x, a0.y, a1.x, a1.y}; B[t] = (u32x4){b0.x, b0.y, b1.x, b1.y}; }
            if (l15 >= 4) B[1] = one;
#pragma unroll
            for (int q = 0; q < 4; ++q) d4[q] = dmf(A[q >> 1], B[q & 1], d4[q]); }
        const rsrc_t rk = MKR(a.ws + WS_KVP, 32 * 16 * 4096);
#pragma unroll
        for (int q = 0; q < 4; ++q) st16c(rk, ((q < 2 || lg == 0) && (!(q & 1) || l15 < 8)) ? ((4 * g + (wave & 3)) * 16 + rb) * 4096 + q * 1024 + lane * 16 : OOB, __builtin_bit_cast(u32x4, d4[q]));
    }
    auto qdst = [&](int T) { const int gi = 4 * T + lg, hd = gi / 5, pt = gi - 5 * hd; return lds + L_Q + rp * 10240 + hd * 640 + (pt < 4 ? 128 + l15 * 32 + pt * 8 : l15 * 8); };
    tile(0, true, qdst(cg), 2560);
    if (cg == 0) tile(1, true, qdst(4), 2560);
    if (wave == 3) boot_wait(a);
    VMDRAIN(); LBAR();
    const rsrc_t rfl = MKR(a.ws + WS_HOP, F_ERR), rq = MKR(a.ws + WS_PQ, S_ * D_ * 2);
    if (wave == 3 && lane == 0) { st4c(rfl, F_QK + (rb * 8 + g) * FLP, 1u); if (rb == 0 && g == 0) STAMP(16); STAMP(1400 + rb * 8 + g); }
#pragma unroll
    for (int j = 0; j < 3; ++j) st16c(rq, (j < 2 || lane < 32) ? (rb * 8 + wave) * 20480 + g * 2560 + j * 1024 + lane * 16 : OOB, lds16(lds + L_Q + wave * 2560 + j * 1024 + lane * 16));
    VMDRAIN(); LBAR();
    if (wave == 3 && lane == 0) st4c(rfl, F_PQ + (rb * 8 + g) * FLP, 1u);
}
__device__ __forceinline__ void kv_part(KA& a, unsigned char* lds, const int i, const int wave) {
    const int lane = lane_id(), l15 = lane & 15, lg = lane >> 4, h = 2 * i + (wave >> 2);
    const rsrc_t rk = MKR(a.ws + WS_KVP, 32 * 16 * 4096);
    if (wave == 7) wait_set(MKR(a.ws + WS_HOP, F_ERR), F_QK + (l15 * 8 + (h >> 2)) * FLP, a.ws + WS_HOP, 0);
    LBAR();
    u32x4 v[4][4];
#pragma unroll
    for (int q = 0; q < 16; ++q) v[q >> 2][q & 3] = __builtin_amdgcn_raw_buffer_load_b128(rk, (((q & 2) == 0 || lg == 0) && (!(q & 1) || l15 < 8)) ? lane * 16 + (q & 3) * 1024 : OOB, (i * 32 + wave * 4 + (q >> 2)) * 4096, 16);
#pragma unroll
    for (int q = 0; q < 4; ++q) *reinterpret_cast<f32x4*>(lds + 32768 + ((wave >> 2) * 16 + q * 4 + (wave & 3)) * 1024 + lane * 16) = (__builtin_bit_cast(f32x4, v[0][q]) + __builtin_bit_cast(f32x4, v[1][q])) + (__builtin_bit_cast(f32x4, v[2][q]) + __builtin_bit_cast(f32x4, v[3][q]));
    LBAR();
    const f32x4* p = reinterpret_cast<const f32x4*>(lds + 32768 + wave * 4096 + lane * 16);
    const f32x4 s = (p[0] + p[64]) + (p[128] + p[192]);
    const int e = 16 * (wave & 1) + l15, d = 8 * (wave & 2) + 4 * lg;
    st8c(MKR(a.ws + WS_KVF, 32768), (d < 24 && e <= 20) ? h * 1024 + e * 48 + d * 2 : OOB, (u32x2){min(pk2(s[0], s[1]), 0xfffffffeu), min(pk2(s[2], s[3]), 0xfffffffeu)});
}
__device__ __forceinline__ void boot_part(KA& a, unsigned char* lds, const int i, const int wave) {
    const int lane = lane_id(), l15 = lane & 15, lg = lane >> 4, w = i * 8 + wave;
    const rsrc_t rb = MKR((a.ws + WS_HOP) - 2048, 2048), rz = MKR((a.ws + WS_HOP), F_AF), rh = MKR(al2m((a.ws + WS_FR)), FR_FILL), rf = MKR((a.ws + WS_WF_AO), D_ * D_ * 2);
    const u32x2 tag = ld8c(rb, 0);
    {
        const unsigned c = (w & 7) * 64 + lane, b = ((w >> 4) & 1) * 8 + (c >> 6), m = c & 15u;
        u32x4 v = (u32x4){~0u, ~0u, ~0u, ~0u};
        if (b < 10u) { const unsigned* mp = reinterpret_cast<const unsigned*>(a.mem0 + (2 * (m >> 2) + (m & 1)) * D_ + b * 64 + ((c >> 4) & 3u) * 16 + (m & 2));
            v = (u32x4){min(mp[0], 0xfffffffeu), min(mp[2], 0xfffffffeu), min(mp[4], 0xfffffffeu), min(mp[6], 0xfffffffeu)}; }
        else if (!(c & 3u)) v.x = c == 196u ? TT_E1 + TT_E2 + 40u : TT_E1 + 20u;
        st16c(rh, (w >> 5) * (20u << 14) + (w & 31) * 1024 + lane * 16, v);
    }
    st16c(rz, w * 1024 + lane * 16, (u32x4){0u, 0u, 0u, 0u});
    st16c(MKR(a.ws + WS_KVF, 65536), w * 1024 + lane * 16, (u32x4){~0u, ~0u, ~0u, ~0u});
    VMDRAIN(); LBAR();
    if (wave == 0 && lane == 0) st8c(rb, 128 + i * 128, (u32x2){tag.x + 1u, ~tag.y});
    if (w == 0 && lane == 0) { const int layer = 0; STAMP(20); }
    u32x4 v[4][4];
#pragma unroll
    for (int q = 0; q < 16; ++q) { const int u = min(w + 64 * (q >> 2), 199), kk = u / 10; v[q >> 2][q & 3] = *reinterpret_cast<const u32x4*>(a.wao + (kk * 32 + (q & 3) * 8 + (lane >> 3)) * D_ + (u - kk * 10) * 64 + (lane & 7) * 8); }
    unsigned char* t = lds + wave * 16384;
#pragma unroll
    for (int q = 0; q < 16; ++q) *reinterpret_cast<u32x4*>(t + q * 1024 + lane * 16) = v[q >> 2][q & 3];
    CBAR();
#pragma unroll
    for (int q = 0; q < 16; ++q) { const int u = w + 64 * (q >> 2), kk = u / 10; const unsigned char* p = t + (q >> 2) * 4096 + (8 * lg + (l15 >> 2)) * 128 + (q & 3) * 32 + (l15 & 3) * 8; const u32x2 x0 = ldstr(p), y0 = ldstr(p + 512);
        if (u < 200) st16c(rf, ((((u - kk * 10) * 4 + (q & 3)) * 20 + kk) * 64 + lane) * 16, (u32x4){x0.x, x0.y, y0.x, y0.y}); }
    boot_wait(a);
    VMDRAIN(); LBAR();
    if (wave == 0 && lane == 0) st4c(rz, F_WO + i * FLP, 1u);
    pol_vote(a, w);
    if (w == 0 && lane == 0) { const int layer = 0; STAMP(21); }
    LBAR();
}

#define ATS 62
#define ATU 0
#define AIM 12
template <int K, bool W3, class F> __device__ __forceinline__ void a_proj(KA& a, unsigned char* lds, const int h, const int wave, F&& pre) {
    constexpr int YB = 51200, L_SK = 3 * YB, L_C = L_SK + 6144 + 64, L_KN = L_C + 256, KP = 48, layer = 0;
    const int lane = lane_id(), l15 = lane & 15, lg = lane >> 4, rt = wave & 1;
    const unsigned ldsb = LDSB();
    unsigned char* hop = (a.ws + WS_HOP);
    const rsrc_t rfl = MKR(hop, F_ERR), rp = MKR((a.ws + WS_PQ), S_ * D_ * 2), rxa = MKR((a.ws + WS_X), XI_BYTES);
    u32x4 wq[2][20]; f32x4 cs[2] = {Z4, Z4}, d4[2][2] = {{Z4, Z4}, {Z4, Z4}};
    if (K) {
        const rsrc_t rw = MKR((a.ws + WS_WF_AQKV), 640 * 1920 * 2);
#pragma unroll
        for (int u = 0; u < 2; ++u) { const int c = 20 * h + (K == 2 ? (1 + u) * 640 + l15 : (u ? min(l15 >> 2, 2) * 640 + 16 + (l15 & 3) : l15));
#pragma unroll
            for (int q = 0; q < 20; ++q) wq[u][q] = __builtin_amdgcn_raw_buffer_load_b128(rw, ((c >> 4) * 20 * 64 + lg * 16 + (c & 15)) * 16, q * 1024, 16); }
#pragma unroll
        for (int q = 0; q < 40; ++q) cs[q & 1] = dmf(wq[q & 1][q >> 1], (u32x4){0x3F803F80u, 0x3F803F80u, 0x3F803F80u, 0x3F803F80u}, cs[q & 1]);
        PIN(cs[0]); PIN(cs[1]); VMKNOWN();
    }
    const unsigned f0 = F_OF + lane * FLP, f1 = F_OF + (64 + l15 + (lane & 16) * 5) * FLP;
    unsigned tnext = rt_now(), per = 0;
    auto kv = [&](int t) {
        u32x4 A[2], B[2];
#pragma unroll
        for (int q = 0; q < 2; ++q) { const unsigned char* p = lds + L_SK + (t & 1) * 64 * KP + (8 * lg + (l15 >> 2)) * KP + 32 * q + 8 * (l15 & 3);
            const u32x2 a0 = ldstr(p), a1 = ldstr(p + 4 * KP), b0 = ldstr(p + 32 * KP), b1 = ldstr(p + 36 * KP);
            A[q] = (u32x4){a0.x, a0.y, a1.x, a1.y}; B[q] = (u32x4){b0.x, b0.y, b1.x, b1.y}; }
        if (l15 >= 4) { A[1] = (u32x4){0u, 0u, 0u, 0u}; B[1] = (u32x4){0x3F803F80u, 0x3F803F80u, 0x3F803F80u, 0x3F803F80u}; }
#pragma unroll
        for (int q = 0; q < 4; ++q) d4[q >> 1][q & 1] = dmf(A[q >> 1], B[q & 1], d4[q >> 1][q & 1]); };
    auto fill = [&](int n, unsigned dst, int g0) {
#pragma unroll
        for (int g = 0; g < 2; ++g) { const unsigned o = min(((g0 + g + (blockIdx.x >> 3) * AROT) & 3) * 13, 37) * 1024; dma16cn<13>(rxa, lane * 16, (unsigned)n * XI_PITCH + o, dst + o); } };
    int nx = 0, kn = 0, yb = 0;
    auto seg = [&](const int t) {
        AT(0);
        if (nx == t) {
            if (kn == t) {
                if (W3) { if (t) kv(t - 1);
                    if (t < TF) rt_wait(tnext);
                    for (unsigned sp = 0;; ++sp) { CBAR(); const u32x4 v = ld16c(rfl, f0 + t * (NR_ * FLP)); const unsigned w = ld4c(rfl, F_OF + (64 + l15 + t * NR_) * FLP);
                        if (__all(v.x != 0u && w != 0u)) { per = __builtin_amdgcn_readfirstlane(v.x); tnext = __builtin_amdgcn_readfirstlane(v.w) + per + (t + 1 < TF - 6 ? TT_AD : TT_AN); break; }
                        if (t < TF) __builtin_amdgcn_s_sleep(40); else __builtin_amdgcn_s_sleep(3);
                        if (sp > SPIN_CAP_W) { set_err(hop); break; } } }
                LBAR(); ++kn;
            } else if (W3 && t) kv(t - 1);
            if (!K && !W3 && wave == 0) { fill(t, ldsb + yb, 0); fill(t, ldsb + yb, 2); VMDRAIN(); }
            ++nx;
            LBAR();
        } else if (W3) kv(t - 1);
        AT(1);
        if (lane == 0 && wave == 4 && h == 0 && t >= 56) STAMP(330 + t - 56);
        const int up = min(kn, t + 3), n = nx + ((nx ^ wave) & 1);
        const unsigned nb = ldsb + (n == t + 1 ? (yb == 2 * YB ? 0 : yb + YB) : (yb == 0 ? 2 * YB : yb - YB));
        u32x4 fa, fb; unsigned fc; int pr = 0; f32x4 acc[2] = {Z4, Z4};
        if (K) {
            const unsigned char* yp = lds + yb + XI_X + rt * 20480 + lane * 16;
            u32x4 yf[4];
#pragma unroll
            for (int q = 0; q < 3; ++q) { yf[q] = lds16(yp + q * 1024); SBAR(); }
#pragma unroll
            for (int q = 0; q < 20; ++q) { acc[0] = dmf(wq[0][q], yf[q & 3], acc[0]); PIN(acc[0]); SBAR(); if (q + 3 < 20) { yf[(q + 3) & 3] = lds16(yp + (q + 3) * 1024); SBAR(); }
                acc[1] = dmf(wq[1][q], yf[q & 3], acc[1]); PIN(acc[1]); SBAR(); }
        } else if (!W3 && wave == 0) {
            const f32x2* sp = reinterpret_cast<const f32x2*>(lds + yb + lg * 2560 + l15 * 8);
            f32x2 sv[20];
#pragma unroll
            for (int q = 0; q < 20; ++q) sv[q] = sp[q * 16];
#pragma unroll
            for (int q = 0; q < 16; ++q) sv[q & 3] += sv[q + 4];
            const u32x2 s4[4] = {__builtin_bit_cast(u32x2, sv[0]), __builtin_bit_cast(u32x2, sv[1]), __builtin_bit_cast(u32x2, sv[2]), __builtin_bit_cast(u32x2, sv[3])};
            c_put(s4, lds + L_C);
        } else if (W3) {
            const int d = (int)(rt_now() - tnext);
            pr = kn >= NSEG_ ? 0 : (kn >= TF || d >= (int)per ? 2 : (d >= 0 ? 1 : 0));
            if (kn + 1 >= NSEG_) pr = min(pr, 1);
            fa = __builtin_amdgcn_raw_buffer_load_b128(rfl, pr ? f0 : OOB, kn * (NR_ * FLP), 16); fb = __builtin_amdgcn_raw_buffer_load_b128(rfl, pr > 1 ? f0 : OOB, (kn + 1) * (NR_ * FLP), 16);
            fc = __builtin_amdgcn_raw_buffer_load_b32(rfl, pr > ((lane >> 4) & 1) ? f1 : OOB, kn * (NR_ * FLP), 16); SBAR();
        } else if (n < up) fill(n, nb, 0);
        AT(2);
        LBAR();
        AT(3);
        if (K) {
            const f32x2 ct = *reinterpret_cast<const f32x2*>(lds + L_C + rt * 128 + l15 * 8);
            float y[8], e[8];
#pragma unroll
            for (int q = 0; q < 8; ++q) y[q] = __builtin_fmaf(ct[0], cs[q >> 2][q & 3], acc[q >> 2][q & 3]) * ct[1];
            SBAR();
#pragma unroll
            for (int q = 0; q < 8 / K; ++q) e[q] = __builtin_amdgcn_fmed3f(y[q], 0.f, -3.0e38f) * 1.4426950408889634f;
            SBAR();
#pragma unroll
            for (int q = 0; q < 8 / K; ++q) e[q] = __builtin_amdgcn_exp2f(e[q]);
            SBAR();
#pragma unroll
            for (int q = 0; q < 8 / K; ++q) e[q] = (e[q] + 1e-6f) + __builtin_amdgcn_fmed3f(y[q], 0.f, 3.0e38f);
#pragma unroll
            for (int q = 4; q < 8; ++q) e[q] = (K == 2 || lg >= 2) ? y[q] : e[q];
            const u32x2 o0 = (u32x2){pk2(e[0], e[1]), pk2(e[2], e[3])}, o1 = (u32x2){pk2(e[4], e[5]), pk2(e[6], e[7])};
            unsigned char* sk = lds + L_SK + (t & 1) * 64 * KP + (rt * 16 + l15) * KP;
            if (K == 2) { *reinterpret_cast<u32x2*>(sk + 8 * lg) = o0; *reinterpret_cast<u32x2*>(sk + 32 * KP + 8 * lg) = o1; }
            else { *reinterpret_cast<u32x2*>(sk + (lg >= 2 ? 32 * KP + 32 : 40 - 8 * lg)) = o1;
                __builtin_amdgcn_raw_buffer_store_b64(o0, rp, 128 + l15 * 32 + lg * 8, (t * 2 + rt) * 20480 + h * 640, 16);
                __builtin_amdgcn_raw_buffer_store_b64(o1, rp, lg ? OOB : l15 * 8, (t * 2 + rt) * 20480 + h * 640, 16);
                if (t == 8) VMDRAIN(); }
        } else if (W3) {
            {
                const unsigned long long z = __ballot(fc == 0u);
                if (__all(fa.x != 0u) && !(z & 0xffffull)) { const bool two = __all(fb.x != 0u) && !z; kn += two ? 2 : 1;
                    per = __builtin_amdgcn_readfirstlane(two ? fb.x : fa.x); tnext = __builtin_amdgcn_readfirstlane(two ? fb.w : fa.w) + per + (kn < TF - 6 ? TT_AD : TT_AN); } }
            *reinterpret_cast<int*>(lds + L_KN + (t & 1) * 4) = kn;
            if (t == 40) wait_set(rfl, F_AQ + (lane & 31) * 4, hop, 4);
        } else if (wave) { if (n < up) fill(n, nb, 2); if (((t + 1) ^ wave) & 1) { } else VMDRAIN(); }
        nx = max(nx, up);
        AT(4);
        LBAR();
        AT(5);
        kn = __builtin_amdgcn_readfirstlane(*reinterpret_cast<const int*>(lds + L_KN + (t & 1) * 4));
        if (W3 && t == 8 && lane == 0) st4c(rfl, F_AQ + h * 4, 1u);
        yb = yb == 2 * YB ? 0 : yb + YB;
        AT(6); AT(7); ATD(5, t == ATS && h == 0);
    };
#pragma unroll 1
    for (int t = 0; t < 50; ++t) seg(t);
    pre();
#pragma unroll 1
    for (int t = 50; t < NSEG_; ++t) seg(t);
    if (W3) {
        kv(NSEG_ - 1);
        const rsrc_t rkv = MKR(a.ws + WS_KVF + 32768, 32768);
#pragma unroll
        for (int y = 0; y < 2; ++y)
#pragma unroll
            for (int x = 0; x < 2; ++x) { const int e = 16 * y + l15, d = 16 * x + 4 * lg;
                const u32x2 o = (u32x2){min(pk2(d4[x][y][0], d4[x][y][1]), 0xfffffffeu), min(pk2(d4[x][y][2], d4[x][y][3]), 0xfffffffeu)};
                st8c(rkv, (d < 24 && e <= 20) ? h * 1024 + e * 48 + d * 2 : OOB, o); }
    }
}
__device__ __forceinline__ void a_part(KA& a, unsigned char* lds, const int ai, const int wave) {
    const int lane = lane_id(), tid = wave * 64 + lane;
    constexpr int L_ST = 20480;
    const int cb = ai & 7, rep = ai >> 3, h = ai;
    const int nt = wave < 5 ? wave : 0;
    u32x4 b[20], tq[4]; u32x2 rres;
    const int ct = 5 * cb + nt;
    const rsrc_t rp = MKR((a.ws + WS_PQ), S_ * D_ * 2), rxa = MKR((a.ws + WS_X), XI_BYTES);
    auto fetch = [&](int u) { const int lane = lane_id(), l15 = lane & 15, lg = lane >> 4;
#pragma unroll
        for (int q = 0; q < 4; ++q) tq[q] = __builtin_amdgcn_raw_buffer_load_b128(rp, (lg < 2 ? 128 + l15 * 32 + lg * 16 : (lg == 2 ? l15 * 8 : OOB)) + q * 640, u * 20480 + wave * 2560, 16); };
    auto x_off = [&]() { const int lane = lane_id(), l15 = lane & 15, lg = lane >> 4; return XI_X + (ct >> 1) * 1024 + ((ct & 1) * 2 + (lg >> 1)) * 256 + l15 * 16 + (lg & 1) * 8; };
    auto b_load = [&]() { const int lane = lane_id();
#pragma unroll
        for (int q = 0; q < 20; ++q) b[q] = __builtin_amdgcn_raw_buffer_load_b128(MKR((a.ws + WS_WF_AO), D_ * D_ * 2), wave < 5 ? lane * 16 + (q & 3) * 1024 : OOB, ct * 20480 + (q >> 2) * 4096, 16); };
    auto outp = [&](const int layer) {
        const int lane = lane_id(), l15 = lane & 15, lg = lane >> 4;
        unsigned char* hop = (a.ws + WS_HOP) + layer * HOP_BYTES;
        const rsrc_t rfl = MKR(hop, F_ERR), rr_ = MKR(psel(a.x0, (a.ws + WS_X), layer), layer ? XI_BYTES : S_ * D_ * 2);
        const unsigned oo = x_off(), ro = layer ? oo : (unsigned)(l15 * D_ + 16 * ct + 4 * lg) * 2;
        auto res = [&](int u) { rres = __builtin_amdgcn_raw_buffer_load_b64(rr_, ro, (u >> 1) * (layer ? XI_PITCH : 40960) + (u & 1) * 20480, 16); };
        if (!layer) { boot_wait(a); wait_set(rfl, F_WO + (lane & 7) * FLP, hop, 1); b_load(); res(rep); if (ai < 16) { VMKNOWN(); kv_part(a, lds, ai, wave); } wait_set(rfl, F_PQ + (lane & 7) * FLP, hop, 1); fetch(rep); }
        unsigned so[5];
#pragma unroll
        for (int q = 0; q < 5; ++q) { const unsigned c = 80 * wave + (q < 4 ? 20 * q + 4 * lg : 20 * lg + 16); so[q] = (c >> 3) * 256 + l15 * 16 + (c & 4) * 2; }
        u32x4 k0[4], k1[4], kz[4];
        {
            const rsrc_t rkv = MKR(a.ws + WS_KVF + layer * 32768, 32768);
            const unsigned ko = (lg < 3 ? lg : 0) * 16;
            for (unsigned sp = 0; sp < SPIN_CAP_W; ++sp) { CBAR();
                unsigned mx = 0u;
#pragma unroll
                for (int q = 0; q < 4; ++q) { k0[q] = __builtin_amdgcn_raw_buffer_load_b128(rkv, ko + l15 * 48 + q * 1024, wave * 4096, 16);
                    k1[q] = __builtin_amdgcn_raw_buffer_load_b128(rkv, (l15 >> 2) == q ? ko + (16 + (l15 & 3)) * 48 + q * 1024 : OOB, wave * 4096, 16); kz[q] = __builtin_amdgcn_raw_buffer_load_b128(rkv, ko + 960 + q * 1024, wave * 4096, 16); }
#pragma unroll
                for (int q = 0; q < 4; ++q) mx = max(mx, max(umax4(k0[q]), max(umax4(k1[q]), umax4(kz[q]))));
                if (__all(mx != 0xffffffffu)) break;
                __builtin_amdgcn_s_sleep(2);
            }
            if (tid == 0 && ai == 0) STAMP(4);
        }
#pragma unroll 1
        for (int u = rep; u < 2 * NSEG_; u += 4) {
            if (!layer && u >= 4 && u < 8) { wait_set(rfl, F_PQ + lane * FLP, hop, 1); wait_set(rfl, F_PQ + (64 + lane) * FLP, hop, 1); }
            if (layer && u >= 8 && u < 12) wait_set(rfl, F_AQ + (lane & 31) * 4, hop, 1);
            if (layer) AT(0);
            {
                f32x4 dz[4], d0[4], d1 = Z4;
#pragma unroll
                for (int q = 0; q < 4; ++q) dz[q] = dmf(kz[q], tq[q], Z4);
#pragma unroll
                for (int q = 0; q < 4; ++q) { d0[q] = dmf(k0[q], tq[q], Z4); d1 = dmf(k1[q], tq[q], d1); }
                SBAR(); fetch(min(u + 4, 2 * NSEG_ - 1)); SBAR();
                float inv[4];
#pragma unroll
                for (int q = 0; q < 4; ++q) inv[q] = __builtin_amdgcn_rcpf(dz[q][0] + 1e-6f);
#pragma unroll
                for (int q = 0; q < 4; ++q) { d0[q] *= inv[q]; *reinterpret_cast<u32x2*>(lds + so[q]) = (u32x2){pk2(d0[q][0], d0[q][1]), pk2(d0[q][2], d0[q][3])}; }
                d1 *= lg < 2 ? (lg == 0 ? inv[0] : inv[1]) : (lg == 2 ? inv[2] : inv[3]);
                *reinterpret_cast<u32x2*>(lds + so[4]) = (u32x2){pk2(d1[0], d1[1]), pk2(d1[2], d1[3])};
            }
            f32x4 acc = (f32x4){lo2f(rres.x), hi2f(rres.x), lo2f(rres.y), hi2f(rres.y)}; PIN(acc);
            if (layer) AT(1);
            LBAR();
            if (layer) AT(2);
            if (layer && u == rep && tid == 448) st4c(rfl, F_AQ + h * 4, 1u);
            if (wave < 5) {
                const unsigned char* ar = lds + lane * 16;
                u32x4 af[8];
#pragma unroll
                for (int q = 0; q < 5; ++q) { af[q] = lds16(ar + 1024 * q); SBAR(); }
#pragma unroll
                for (int q = 0; q < 20; ++q) { acc = dmf(b[q], af[q & 7], acc); PIN(acc); SBAR();
                    if (q + 5 < 20) { af[(q + 5) & 7] = lds16(ar + 1024 * (q + 5)); SBAR(); } }
                const u32x2 o = (u32x2){pk2(acc[0], acc[1]), pk2(acc[2], acc[3])};
                __builtin_amdgcn_raw_buffer_store_b64(o, rxa, oo, (u >> 1) * XI_PITCH + (u & 1) * 20480, 16);
                *reinterpret_cast<float*>(lds + L_ST + wave * 128 + l15 * 8 + (lg & 1) * 4) = row_sums(o);
            }
            if (layer) AT(3);
            LBAR();
            if (layer) AT(4);
            if (wave == 0) {
                if (lane < 32) { const float* sp = reinterpret_cast<const float*>(lds + L_ST + lane * 4); const float v = ((sp[0] + sp[32]) + (sp[64] + sp[96])) + sp[128];
                    __builtin_amdgcn_raw_buffer_store_b32(__float_as_uint(v), rxa, XI_S8 + cb * 128 + lane * 4, (u >> 1) * XI_PITCH + (u & 1) * 1024, 16); }
                if (lane == 0 && u >= AIM + 4) st4c(rfl, F_AF + ((u - 4) * 8 + cb) * FLP, 1u); }
            if (u < AIM || u + 4 >= 2 * NSEG_) { VMKNOWN(); LBAR(); if (tid == 0) st4c(rfl, F_AF + (u * 8 + cb) * FLP, 1u); }
            if (tid == 0 && ai == 0 && u == 0) STAMP(5);
            if (tid == 0 && ai == 0 && u == 124) STAMP(6);
            if (layer) { AT(5); AT(6); AT(7); }
            ATD(5, layer && u == ATU && ai == 0);
            CBAR(); res(min(u + 4, 2 * NSEG_ - 1));
        }
    };
    outp(0);
    VMKNOWN();
    auto pre = [&]() { fetch(rep); rres = __builtin_amdgcn_raw_buffer_load_b64(rxa, x_off(), (rep >> 1) * XI_PITCH + (rep & 1) * 20480, 16); VMKNOWN(); };
    if (wave >= 4) { if (wave >= 6) a_proj<2, false>(a, lds, h, wave, pre); else a_proj<1, false>(a, lds, h, wave, pre); b_load(); outp(1); }
    else { if (wave == 3) a_proj<0, true>(a, lds, h, wave, pre); else a_proj<0, false>(a, lds, h, wave, pre); outp(1); }
}

#define MAPV 1
#define WDL 700
#define WDL2 400
#define WDL3 400
#define XPERM 0x76543210u
__global__ void __launch_bounds__(512, 2) KATTR bmega_kernel(MegaArgs) {
    ATI();
    extern __shared__ __attribute__((aligned(16))) unsigned char lds[];
    const int c = blockIdx.x, x = (XPERM >> ((c & 7) * 4)) & 7, rr = c >> 3, q = x >> 1, wave = __builtin_amdgcn_readfirstlane(threadIdx.x >> 6);
    if (LDSB() != 0u) return;
    int role, id, qt = -1;
    if (MAPV == 1) { const bool ev = (x & 1) == 0;
        if (rr < 8) { role = ev ? 0 : 1; id = q * 8 + rr; } else if (rr >= 28) { role = 5; id = (ev ? 16 : 0) + (rr - 28) * 4 + q; } else if (ev) { role = 2; id = q * 20 + rr - 8; } else if (rr < 20) { role = 3; id = q * 12 + rr - 8; } else { role = 4; id = q * 8 + rr - 20; }
        if (rr >= 8 && (ev || role == 4)) qt = (ev ? (rr - 8) >> 1 : (rr + 4) >> 1) * 8 + 2 * q + (rr & 1); }
    else if (MAPV == 4) { const bool ev = (x & 1) == 0;
        if (ev) { if (rr < 16) { role = rr >> 3; id = q * 8 + (rr & 7); } else { role = 2; id = q * 20 + rr - 16; qt = ((rr - 16) >> 1) * 8 + 2 * q + (rr & 1); } }
        else { if (rr < 12) { role = 3; id = q * 12 + rr; } else if (rr < 20) { role = 4; id = q * 8 + rr - 12; } else if (rr < 24) { role = 2; id = q * 20 + rr - 4; } else { role = 5; id = (rr < 28 ? 16 : 0) + (rr & 3) * 4 + q; }
            if (rr >= 12 && rr < 28) qt = ((rr + 4) >> 1) * 8 + 2 * q + (rr & 1); } }
    else if (MAPV == 5) { const bool ev = (x & 1) == 0;
        if (ev) { if (rr < 8) { role = 0; id = q * 8 + rr; } else if (rr < 28) { role = 2; id = q * 20 + rr - 8; qt = ((rr - 8) >> 1) * 8 + 2 * q + (rr & 1); } else { role = 3; id = q * 12 + rr - 20; } }
        else { role = rr < 8 ? 1 : (rr >> 3) + 2; id = role == 5 ? (rr < 28 ? 16 : 0) + (rr & 3) * 4 + q : (role == 3 ? q * 12 : q * 8) + (rr & 7); if (rr >= 16 && rr < 28) qt = ((rr + 4) >> 1) * 8 + 2 * q + (rr & 1); } }
    else if (x < 4) { if (rr < 8) { role = 0; id = x * 8 + rr; } else if (rr < 28) { role = 2; id = x * 20 + rr - 8; qt = ((rr - 8) >> 1) * 8 + 2 * x + (rr & 1); } else { role = 3; id = 32 + x * 4 + rr - 28; } }
    else if (MAPV == 2) { const int y = x - 4; role = rr < 8 ? 1 : (rr >> 3) + 2; id = role == 5 ? (rr < 28 ? 16 : 0) + (rr & 3) * 4 + y : y * 8 + (rr & 7); if (rr >= 16 && rr < 28) qt = ((rr + 4) >> 1) * 8 + 2 * y + (rr & 1); }
    else { const int y = (x - 4) >> 1; if (x & 1) { role = rr < 16 ? 3 : 4; id = y * 16 + (rr & 15); if (rr >= 16) qt = (10 + ((rr - 16) >> 2)) * 8 + 4 * y + (rr & 3); }
        else { role = rr < 16 ? 1 : 5; id = rr < 16 ? y * 16 + rr : (rr < 24 ? 16 : 0) + (rr & 7) * 2 + y; if (rr >= 16 && rr < 24) qt = (14 + ((rr - 16) >> 2)) * 8 + 4 * y + (rr & 3); } }
    if (role == 0 || role == 1 || role == 3) rt_wait(rt_now() + (role == 0 ? WDL : (role == 1 ? WDL2 : WDL3)));
    if (role == 0) s_part(args(), lds, id, wave);
    else if (role == 1) { if (wave < 4) v_run<true>(args(), lds, id, wave); else v_run<false>(args(), lds, id, wave); }
    else if (role == 3) xp_part(args(), lds, id, wave);
    else {
        if (qt >= 0) q_part(args(), lds, qt >> 3, qt & 7, wave); else if (id >= 8) boot_part(args(), lds, id - 8, wave);
        if (role == 5) a_part(args(), lds, id, wave);
        else if (role == 4) xq_part(args(), lds, id, wave);
        else {
            if (wave == 0 && !(id & 1) && lane_id() == 0) st4c(MKR(args().ws + WS_HOP, F_ERR), F_SD + (id >> 1) * 4, 1u + my_side());
            ro_part(args(), lds, id, wave); }
    }
}

extern "C" void kernel_launch(void* const* d_in, const int* in_sizes, int n_in, void* d_out, int out_size, void* d_ws, size_t ws_size, hipStream_t stream) {
    (void)hipFuncSetAttribute((const void*)bmega_kernel, hipFuncAttributeMaxDynamicSharedMemorySize, MG_LDS);
    MegaArgs ma; ma.wq = (const bf16_t*)d_in[5]; ma.wk = (const bf16_t*)d_in[6]; ma.wv = (const bf16_t*)d_in[7]; ma.wo = (const bf16_t*)d_in[8];
    ma.wqkv = (const bf16_t*)d_in[1]; ma.wao = (const bf16_t*)d_in[2]; ma.mem0 = (const bf16_t*)d_in[4];
    ma.x0 = (const bf16_t*)d_in[0]; ma.out = (bf16_t*)d_out; ma.ws = (unsigned char*)d_ws;
    bmega_kernel<<<256, 512, MG_LDS, stream>>>(ma);
}
```

```cpp
#include <hip/hip_runtime.h>
#include <hip/hip_bf16.h>
#include <stdint.h>
#include <type_traits>

typedef __attribute__((ext_vector_type(8))) short bf16x8;
typedef __attribute__((ext_vector_type(4))) float f32x4;
typedef __attribute__((ext_vector_type(4))) unsigned u32x4;
typedef __attribute__((ext_vector_type(2))) unsigned u32x2;
typedef uint16_t bf16_t;

typedef __attribute__((ext_vector_type(2))) __bf16 bf16x2_t;
typedef __attribute__((ext_vector_type(2))) float f32x2;
__device__ __forceinline__ unsigned pk2(float lo, float hi) { f32x2 v = {lo, hi}; return __builtin_bit_cast(unsigned, __builtin_convertvector(v, bf16x2_t)); }
__device__ __forceinline__ float rbf(float f) { return __uint_as_float(pk2(f, f) << 16); }
__device__ __forceinline__ float lo2f(unsigned p) { return __uint_as_float(p << 16); }
__device__ __forceinline__ float hi2f(unsigned p) { return __uint_as_float(p & 0xffff0000u); }

constexpr int S_ = 2048, D_ = 640, H_ = 32, E_ = 192, NSEG_ = 64;
constexpr int HE_ = H_ * E_;

constexpr int NR_ = 80;
constexpr int NXP_ = 48;
constexpr size_t al(size_t x) { return (x + 255) & ~(size_t)255; }
constexpr size_t WS_WF_AQKV = 0;
constexpr size_t WS_WF_AO   = WS_WF_AQKV + al((size_t)640 * 1920 * 2);
constexpr size_t WS_PQ      = WS_WF_AO + al((size_t)640 * 640 * 2);
constexpr unsigned XI_X = 10240, XI_S8 = XI_X + 40960, XI_PITCH = XI_S8 + 2048, XI_BYTES = NSEG_ * XI_PITCH;
constexpr size_t WS_X       = WS_PQ + al((size_t)S_ * D_ * 2);
constexpr size_t WS_KVP     = WS_X + al(XI_BYTES);
constexpr size_t WS_KVF     = WS_KVP + al((size_t)32 * 16 * 4096);
constexpr size_t WS_ST      = WS_KVF + al((size_t)32 * 1024 * 2);
#define KXR 32
#define PR 32
#define URING 32
constexpr unsigned ST_KX = 0;
constexpr unsigned ST_VX = ST_KX + KXR * 32u * 12288;
constexpr unsigned ST_KM = ST_VX + KXR * 32u * 12288;
constexpr unsigned ST_PX = ST_KM + PR * 32u * 3072;
constexpr unsigned ST_U  = ST_PX + PR * 2u * 40960;
constexpr unsigned ST_BYTES = ST_U + (unsigned)URING * 40 * 32768;
constexpr size_t WS_FR      = WS_ST + al(ST_BYTES);
constexpr unsigned FR_FILL = 40u << 14, FR_UM = FR_FILL, FR_USED = FR_UM + (160u << 14);
__device__ __forceinline__ unsigned char* al2m(unsigned char* p) { return (unsigned char*)(((uintptr_t)p + (((uintptr_t)1 << 21) - 1)) & ~(((uintptr_t)1 << 21) - 1)); }
constexpr unsigned M_STAMP = 16384 + 2048;
constexpr unsigned FLP = 128;
constexpr unsigned F_POL = 0, F_AQ = F_POL + 256, F_SD = F_AQ + 256, F_WO = F_SD + 256, F_IN = F_WO + 8 * FLP, F_QK = F_IN + 8 * FLP, F_PQ = F_QK + 128 * FLP, F_AF = 36864, F_XP = F_AF + 64 * 16 * FLP, F_PX = F_XP + 64 * NXP_ * FLP, F_KM = F_PX + 70 * 64 * FLP, F_UX = F_KM + 64 * 32 * FLP,
    F_UW = F_UX + 66 * 32 * FLP, F_OF = F_UW + 64 * 8 * FLP, F_RD = F_OF + 64 * NR_ * FLP, F_ERR = F_RD + 64 * NR_ * FLP, HOP_BYTES = F_ERR + 256;
constexpr size_t WS_BOOT    = WS_FR + ((size_t)6 << 20);
constexpr size_t WS_HOP     = WS_BOOT + 2048;
constexpr size_t WS_DBG     = WS_HOP + al(2 * HOP_BYTES);
constexpr size_t WS_END     = WS_DBG + al((size_t)2 << 20);
static_assert(WS_END <= (size_t)256 * 1024 * 1024, "ws too big");
static_assert(FR_USED <= (4u << 20), "hand-off pages");

__device__ __forceinline__ float phi1(float x) { return (__builtin_amdgcn_exp2f(fminf(x, 0.f) * 1.4426950408889634f) + 1e-6f) + fmaxf(x, 0.f); }
__device__ __forceinline__ unsigned phi2(float x0, float x1) { return pk2(phi1(x0), phi1(x1)); }
constexpr unsigned OOB = 0x40000000u;
#define CBAR() asm volatile("" ::: "memory")
#define VMDRAIN() asm volatile("s_waitcnt vmcnt(0)" ::: "memory")
#define PIN(x) asm volatile("" : "+v"(x))
#define VMKNOWN() __builtin_amdgcn_s_waitcnt(0x0F70)
#define LBAR() do { asm volatile("s_waitcnt lgkmcnt(0)" ::: "memory"); __builtin_amdgcn_s_barrier(); asm volatile("" ::: "memory"); } while (0)
typedef __amdgpu_buffer_rsrc_t rsrc_t;
__device__ __forceinline__ u32x4 ld16c(rsrc_t r, unsigned off) { return __builtin_amdgcn_raw_buffer_load_b128(r, off, 0, 16); }
__device__ __forceinline__ u32x2 ld8c(rsrc_t r, unsigned off) { return __builtin_amdgcn_raw_buffer_load_b64(r, off, 0, 16); }
__device__ __forceinline__ unsigned ld4c(rsrc_t r, unsigned off) { return __builtin_amdgcn_raw_buffer_load_b32(r, off, 0, 16); }
__device__ __forceinline__ void st16c(rsrc_t r, unsigned off, u32x4 v) { __builtin_amdgcn_raw_buffer_store_b128(v, r, off, 0, 16); }
__device__ __forceinline__ void st8c(rsrc_t r, unsigned off, u32x2 v) { __builtin_amdgcn_raw_buffer_store_b64(v, r, off, 0, 16); }
__device__ __forceinline__ void st4c(rsrc_t r, unsigned off, unsigned v) { __builtin_amdgcn_raw_buffer_store_b32(v, r, off, 0, 16); }
template <int CTRL> __device__ __forceinline__ unsigned dppu(unsigned x) { return (unsigned)__builtin_amdgcn_mov_dpp((int)x, CTRL, 0xf, 0xf, true); }
template <int CTRL> __device__ __forceinline__ float dppf(float x) { return __uint_as_float(dppu<CTRL>(__float_as_uint(x))); }
__device__ __forceinline__ float xg_max(float x) {
    auto s = __builtin_amdgcn_permlane16_swap(__float_as_uint(x), __float_as_uint(x), false, false); x = fmaxf(__uint_as_float(s[0]), __uint_as_float(s[1]));
    auto t = __builtin_amdgcn_permlane32_swap(__float_as_uint(x), __float_as_uint(x), false, false); return fmaxf(__uint_as_float(t[0]), __uint_as_float(t[1])); }
__device__ __forceinline__ float xg_sum(float x) {
    auto s = __builtin_amdgcn_permlane16_swap(__float_as_uint(x), __float_as_uint(x), false, false); x = __uint_as_float(s[0]) + __uint_as_float(s[1]);
    auto t = __builtin_amdgcn_permlane32_swap(__float_as_uint(x), __float_as_uint(x), false, false); return __uint_as_float(t[0]) + __uint_as_float(t[1]); }

typedef __attribute__((ext_vector_type(8))) unsigned u32x8;
typedef __attribute__((ext_vector_type(8))) __bf16 bfv8;
typedef __attribute__((ext_vector_type(16))) __bf16 bfv16;
typedef short v4i16_t __attribute__((ext_vector_type(4)));
typedef __attribute__((address_space(3))) volatile unsigned lds_u32;
#define SBAR() __builtin_amdgcn_sched_barrier(0)
__device__ __forceinline__ int lane_id() { int l; asm volatile("v_mbcnt_lo_u32_b32 %0, -1, 0\n\tv_mbcnt_hi_u32_b32 %0, -1, %0" : "=v"(l)); return l; }
#define Z4 ((f32x4){0.f, 0.f, 0.f, 0.f})
__device__ __forceinline__ f32x4 smf(u32x4 A, u32x8 B, f32x4 c, unsigned idx) { return __builtin_amdgcn_smfmac_f32_16x16x64_bf16(__builtin_bit_cast(bfv8, A), __builtin_bit_cast(bfv16, B), c, (int)idx, 0, 0); }
__device__ __forceinline__ f32x4 dmf(u32x4 A, u32x4 B, f32x4 c) { return __builtin_amdgcn_mfma_f32_16x16x32_bf16(__builtin_bit_cast(bfv8, A), __builtin_bit_cast(bfv8, B), c, 0, 0, 0); }
__device__ __forceinline__ u32x4 lo4(u32x8 v) { return (u32x4){v[0], v[1], v[2], v[3]}; }
__device__ __forceinline__ u32x4 hi4(u32x8 v) { return (u32x4){v[4], v[5], v[6], v[7]}; }
__device__ __forceinline__ u32x8 cat8(u32x4 a, u32x4 b) { return (u32x8){a.x, a.y, a.z, a.w, b.x, b.y, b.z, b.w}; }
__device__ __forceinline__ u32x4 lds16(const unsigned char* p) { return *reinterpret_cast<const u32x4*>(p); }
__device__ __forceinline__ u32x4 lds16a(int ad) { return *(const __attribute__((address_space(3))) u32x4*)(uintptr_t)(unsigned)ad; }
__device__ __forceinline__ unsigned lds4a(int ad) { return *(const __attribute__((address_space(3))) unsigned*)(uintptr_t)(unsigned)ad; }
__device__ __forceinline__ f32x2 lds8a(int ad) { return *(const __attribute__((address_space(3))) f32x2*)(uintptr_t)(unsigned)ad; }
__device__ __forceinline__ void sts8a(int ad, f32x2 v) { *(__attribute__((address_space(3))) f32x2*)(uintptr_t)(unsigned)ad = v; }
__device__ __forceinline__ void sts4a(int ad, unsigned v) { *(__attribute__((address_space(3))) unsigned*)(uintptr_t)(unsigned)ad = v; }
__device__ __forceinline__ void sts16a(int ad, u32x4 v) { *(__attribute__((address_space(3))) u32x4*)(uintptr_t)(unsigned)ad = v; }
__device__ __forceinline__ u32x2 ldstr(const unsigned char* p) { return __builtin_bit_cast(u32x2, __builtin_amdgcn_ds_read_tr16_b64_v4i16((__attribute__((address_space(3))) v4i16_t*)p)); }
__device__ __forceinline__ unsigned umax4(u32x4 v) { return max(max(v.x, v.y), max(v.z, v.w)); }
__device__ __forceinline__ void dma16(rsrc_t r, unsigned voff, unsigned soff, unsigned ldsd) {
    unsigned keep;
    asm volatile("s_mov_b32 %0, m0\n\ts_mov_b32 m0, %1\n\ts_nop 2\n\tbuffer_load_dwordx4 %2, %3, %4 offen lds\n\ts_mov_b32 m0, %0" : "=&s"(keep) : "s"(ldsd), "v"(voff), "s"(r), "s"(soff) : "memory");
}
__device__ __forceinline__ void dma16c(rsrc_t r, unsigned voff, unsigned soff, unsigned ldsd) {
    unsigned keep;
    asm volatile("s_mov_b32 %0, m0\n\ts_mov_b32 m0, %1\n\ts_nop 2\n\tbuffer_load_dwordx4 %2, %3, %4 offen sc1 lds\n\ts_mov_b32 m0, %0" : "=&s"(keep) : "s"(ldsd), "v"(voff), "s"(r), "s"(soff) : "memory");
}
__device__ __forceinline__ void dma4c(rsrc_t r, unsigned voff, unsigned soff, unsigned ldsd) {
    unsigned keep;
    asm volatile("s_mov_b32 %0, m0\n\ts_mov_b32 m0, %1\n\ts_nop 2\n\tbuffer_load_dword %2, %3, %4 offen sc1 lds\n\ts_mov_b32 m0, %0" : "=&s"(keep) : "s"(ldsd), "v"(voff), "s"(r), "s"(soff) : "memory");
}
template <int N> __device__ __forceinline__ void dma16cn(rsrc_t r, unsigned voff, unsigned soff, unsigned ldsd) {
    unsigned keep;
    if (N >= 4) asm volatile("s_mov_b32 %0, m0\n\ts_mov_b32 m0, %1\n\ts_nop 2\n\tbuffer_load_dwordx4 %2, %3, %4 offen sc1 lds\n\tbuffer_load_dwordx4 %2, %3, %4 offen offset:1024 sc1 lds\n\t"
        "buffer_load_dwordx4 %2, %3, %4 offen offset:2048 sc1 lds\n\tbuffer_load_dwordx4 %2, %3, %4 offen offset:3072 sc1 lds\n\ts_mov_b32 m0, %0" : "=&s"(keep) : "s"(ldsd), "v"(voff), "s"(r), "s"(soff) : "memory");
    else if (N == 3) asm volatile("s_mov_b32 %0, m0\n\ts_mov_b32 m0, %1\n\ts_nop 2\n\tbuffer_load_dwordx4 %2, %3, %4 offen sc1 lds\n\tbuffer_load_dwordx4 %2, %3, %4 offen offset:1024 sc1 lds\n\t"
        "buffer_load_dwordx4 %2, %3, %4 offen offset:2048 sc1 lds\n\ts_mov_b32 m0, %0" : "=&s"(keep) : "s"(ldsd), "v"(voff), "s"(r), "s"(soff) : "memory");
    else if (N >= 2) asm volatile("s_mov_b32 %0, m0\n\ts_mov_b32 m0, %1\n\ts_nop 2\n\tbuffer_load_dwordx4 %2, %3, %4 offen sc1 lds\n\tbuffer_load_dwordx4 %2, %3, %4 offen offset:1024 sc1 lds\n\ts_mov_b32 m0, %0"
        : "=&s"(keep) : "s"(ldsd), "v"(voff), "s"(r), "s"(soff) : "memory");
    else dma16c(r, voff, soff, ldsd);
    constexpr int D = N >= 4 ? 4 : N;
    if constexpr (N > D) dma16cn<N - D>(r, voff, soff + D * 1024, ldsd + D * 1024);
}
__device__ __forceinline__ void st16s(u32x4 v, rsrc_t r, unsigned voff, unsigned soff) { __builtin_amdgcn_raw_buffer_store_b128(v, r, voff, soff, 16); asm volatile("s_nop 1" :: "v"(v) : "memory"); }
#define VMW(n) asm volatile("s_waitcnt vmcnt(%0)" :: "i"(n) : "memory")

struct MegaArgs { const bf16_t* wq; const bf16_t* wk; const bf16_t* wv; const bf16_t* wo; const bf16_t* wqkv; const bf16_t* wao; const bf16_t* mem0; const bf16_t* x0; bf16_t* out; unsigned char* ws; };
typedef const __attribute__((address_space(4))) MegaArgs KA;
__device__ __forceinline__ KA& args() { KA* p = (KA*)__builtin_amdgcn_kernarg_segment_ptr(); asm volatile("" : "+s"(p)); return *p; }
#define STAMP(idx) do { } while (0)
#define KATTR
#define ATI() do { } while (0)
#define AT(n) do { } while (0)
#define ATV do { } while (0)
#define ATD(role, on) do { } while (0)
#define SPINB 17
constexpr unsigned SPIN_CAP = 2u << SPINB;
constexpr unsigned SPIN_CAP_W = 1u << SPINB;
constexpr int MG_LDS = 163840;

#define PARAM(n, v) constexpr int n = v
#define TT_M1  14
#define TT_M2  13
#define TT_V   100
#define TT_V1 TT_V
#define TT_DEC 2
#define TT_BUMP 2
#define TT_DCY 3
#define TT_XS 5
#define TT_XD 1
#define TT_RT 34
#define TT_UM -2
#define TT_TW 8
#define TT_MD 60
#define TT_BD 12
#define TT_KD 95
#define TT_QD 5
#define TT_QN -40
#define TT_AD 250
#define XQD 0
#define SPRIO 2
#define PFD 6
#define DPS 5
#define PF0 2
#define KIM 6
#define XLD 9
#define NEAR 60
#define ROT 1
#define TF 60
#define RTS 12
#define RTL 8
#define R0S 4
#define VPRIO 0
#define XPD 4
#define TT_FP 13
#define TT_OD 30
#define TT_VD 3
#define YP1 0
#define QA 3
#define OIM 63
#define TT_UX 0
#define RLATE 0
#define PLATE 0
#define SLATE1 0
#define SLATE2 0
#define TT_TL 250
#define TT_AN 130
#define RROT 0
#define AROT 0
#define RFD 9
#define SFD 8
#define STK 0
#define VFD 7
#define VTK 2
#define VH 0
#define OSL 0
#define YP2 1
#define YA2 1
#define YD 0
#define YNB 6
#define TT_UP 2
#define TT_DN 3
#define XP2 2
#define YP3 0
#define TT_X1 14
#define VNH 0
#define ASL 10
#define OLAG 3
constexpr int TT_E1 = 132, TT_E2 = 48;
__device__ __forceinline__ unsigned rt_now() { return (unsigned)__builtin_amdgcn_s_memrealtime(); }
__device__ __forceinline__ void rt_wait(unsigned t) {
    unsigned d;
    asm volatile("s_branch 2f\n"
        "1:\n\ts_cmp_lt_u32 %0, 12\n\ts_cbranch_scc1 2f\n\ts_sleep 2\n\ts_cmp_lt_u32 %0, 30\n\ts_cbranch_scc1 2f\n\ts_sleep 5\n"
        "2:\n\ts_memrealtime vcc\n\ts_waitcnt lgkmcnt(0)\n\ts_sub_u32 %0, %1, vcc_lo\n\ts_cmp_lt_u32 %0, 20000\n\ts_cbranch_scc1 1b" : "=&s"(d) : "s"(t - 1u) : "scc", "vcc", "memory");
}
#define FINE 1
#define FC 100
template <int K> __device__ __forceinline__ void rt_fine(unsigned t) {
    if (!FINE) { rt_wait(t); return; }
    asm volatile("s_branch 2f\n"
        "1:\n\ts_cmp_lt_u32 s70, 12\n\ts_cbranch_scc1 2f\n\ts_sleep 2\n\ts_cmp_lt_u32 s70, 30\n\ts_cbranch_scc1 2f\n\ts_sleep 5\n"
        "2:\n\ts_memrealtime vcc\n\ts_waitcnt lgkmcnt(0)\n\ts_sub_u32 s70, %0, vcc_lo\n\ts_cmp_lt_u32 s70, 20000\n\ts_cbranch_scc1 1b\n\t"
        "s_cmp_lt_u32 s70, -8\n\ts_cbranch_scc1 5f\n\t"
        "s_memrealtime s[70:71]\n\ts_nop 4\n\ts_memrealtime s[72:73]\n\ts_nop 4\n\ts_memrealtime s[74:75]\n\ts_nop 4\n\ts_memrealtime s[76:77]\n\ts_nop 4\n\ts_memrealtime vcc\n\ts_waitcnt lgkmcnt(0)\n\t"
        "s_add_u32 s72, s72, s74\n\ts_add_u32 s76, s76, vcc_lo\n\ts_add_u32 s72, s72, s76\n\ts_lshl_b32 s71, s70, 2\n\ts_sub_u32 s72, s72, s71\n\ts_mul_i32 s72, s72, 6\n\t"
        "s_sub_u32 s70, %0, s70\n\ts_mul_i32 s70, s70, %1\n\ts_sub_i32 s70, s70, s72\n\ts_add_i32 s70, s70, %2\n\ts_max_i32 s70, s70, 0\n\ts_min_i32 s70, s70, 255\n\ts_lshr_b32 s70, s70, 3\n\ts_lshl_b32 s70, s70, 2\n\t"
        "s_getpc_b64 vcc\n3:\n\ts_sub_u32 s70, 4f-3b, s70\n\ts_add_u32 vcc_lo, vcc_lo, s70\n\ts_addc_u32 vcc_hi, vcc_hi, 0\n\ts_setpc_b64 vcc\n"
        "5:\n\ts_add_u32 s70, s70, 18\n\ts_cmp_gt_u32 s70, 18\n\ts_cbranch_scc1 4f\n\ts_memrealtime vcc\n\ts_waitcnt lgkmcnt(0)\n\ts_sub_u32 s70, %0, vcc_lo\n\ts_branch 5b\n"
        "\t.rept 32\n\ts_nop 1\n\t.endr\n4:"
        :: "s"(t - 19u), "i"(K), "i"(15 * K - FC) : "scc", "vcc", "s70", "s71", "s72", "s73", "s74", "s75", "s76", "s77", "memory");
}
template <int N> __device__ __forceinline__ void nops() { if (N > 0) asm volatile(".rept %0\n\ts_nop 1\n\t.endr" :: "i"(N > 0 ? N : 0) : "memory"); }
__device__ __forceinline__ const void* psel(const void* p0, const void* p1, int l) { return (const void*)((uintptr_t)p0 + (((uintptr_t)p1 - (uintptr_t)p0) & (uintptr_t)(-(long)l))); }
#define MKR(p, n) __builtin_amdgcn_make_buffer_rsrc((void*)(p), 0, (int)(n), 0x00020000)
__device__ __forceinline__ unsigned my_side() { return (__builtin_amdgcn_s_getreg((3 << 11) | 20) & 15u) >= 4u ? 1u : 0u; }
__device__ __forceinline__ void tt_step(unsigned tw, int s, unsigned t_poll, unsigned& tb, unsigned& cC1, unsigned& cP) {
    const unsigned tgt0 = tb + cP, pc2 = cP - cC1;
    int d = (int)(tw + pc2 - tgt0);
    d = max(d, (int)dppu<0x128>((unsigned)d)); d = max(d, (int)dppu<0x124>((unsigned)d)); d = max(d, (int)dppu<0x122>((unsigned)d)); d = max(d, (int)dppu<0x121>((unsigned)d));
    d = __builtin_amdgcn_readlane(d, 0);
    tb = s == 0 ? t_poll : ((s == 1 || d > 0) ? tgt0 + (unsigned)d - pc2 + (s == 1 ? TT_V1 : TT_V) : tgt0);
    cC1 = __builtin_amdgcn_readlane(tw, 16); cP = __builtin_amdgcn_readlane(tw, 17);
}
__device__ __forceinline__ void set_err(unsigned char* hop) { unsigned z, v; asm volatile("v_mov_b32 %0, 0\n\tv_mov_b32 %1, 1\n\ts_nop 4\n\tglobal_store_dword %0, %1, %2\n\ts_nop 1" : "=&v"(z), "=&v"(v) : "s"(hop + F_ERR) : "memory"); }
__device__ __forceinline__ void wait_set(rsrc_t rfl, unsigned off, unsigned char* hop, int slp) {
    for (unsigned sp = 0;; ++sp) { CBAR();
        const unsigned v = ld4c(rfl, off);
        if (__all(v != 0u)) break;
        for (int q = 0; q < slp; ++q) __builtin_amdgcn_s_sleep(8);
        if (sp > SPIN_CAP_W) { set_err(hop); break; } }
}
__device__ __forceinline__ void boot_wait(KA& a) {
    const rsrc_t r = MKR((a.ws + WS_HOP) - 2048, 2048); const unsigned off = 128 + (lane_id() & 7) * 128;
    for (unsigned sp = 0; sp < SPIN_CAP_W; ++sp) { CBAR();
        const u32x2 t = ld8c(r, 0), v = ld8c(r, off);
        if (__all(v.x == t.x + 1u && v.y == ~t.y)) break;
        __builtin_amdgcn_s_sleep(16); }
}
__device__ __forceinline__ void init_wait(KA& a) { boot_wait(a); wait_set(MKR(a.ws + WS_HOP, F_ERR), F_IN + (blockIdx.x & 7) * FLP, a.ws + WS_HOP, 3); }
__device__ __forceinline__ void pol_vote(KA& a, const int v) {
    const rsrc_t r = MKR(al2m((a.ws + WS_FR)), 1 << 22);
    const unsigned lane = lane_id(), b0 = (48u + v + (v & 1) * 128u) << 14;
    if (lane < 16) st4c(r, b0 + (lane & 1) * 8192 + (lane >> 1) * 896, 0u);
    VMDRAIN();
    unsigned best[2] = {~0u, ~0u}, acc = 0;
#pragma unroll 1
    for (int q = 0; q < 12; ++q) {
        const unsigned long long t0 = __builtin_amdgcn_s_memtime();
        unsigned x = ld4c(r, b0 + ((q ^ (v >> 1)) & 1) * 8192 + (q >> 1) * 896 + acc);
        asm volatile("s_waitcnt vmcnt(0)" : "+v"(x) :: "memory");
        const unsigned dt = (unsigned)(__builtin_amdgcn_s_memtime() - t0);
        acc += x; if ((q ^ (v >> 1)) & 1) best[1] = min(best[1], dt); else best[0] = min(best[0], dt);
    }
    const int df = max(-300, min(300, (int)best[0] - (int)best[1]));
    if (lane == 0) st4c(MKR((a.ws + WS_HOP), F_ERR), F_POL + v * 4, (unsigned)((my_side() ? -df : df) | 1));
}
__device__ __forceinline__ unsigned page_pol(KA& a) {
    const rsrc_t rfl = MKR((a.ws + WS_HOP), F_ERR); const unsigned off = F_POL + lane_id() * 4;
    init_wait(a); wait_set(rfl, off, (a.ws + WS_HOP), 2);
    const unsigned long long b = __ballot((int)ld4c(rfl, off) > 0);
    return (__popcll(b & 0x5555555555555555ull) > 16 ? 1u : 0u) | (__popcll(b & 0xAAAAAAAAAAAAAAAAull) > 16 ? 2u : 0u);
}
__device__ __forceinline__ unsigned wait_timed(rsrc_t rfl, unsigned off, unsigned char* hop, unsigned& tnext, int nper, int dly, bool fast) {
    if (!fast && (int)(tnext - rt_now()) < 4500) rt_wait(tnext);
    for (unsigned sp = 0;; ++sp) { CBAR();
        const u32x4 v = ld16c(rfl, off);
        if (__all(v.x != 0u)) { tnext = __builtin_amdgcn_readfirstlane(v.w) + (unsigned)nper * __builtin_amdgcn_readfirstlane(v.x) + (unsigned)dly; return __builtin_amdgcn_readfirstlane(v.x); }
        if (fast) __builtin_amdgcn_s_sleep(2); else __builtin_amdgcn_s_sleep(40);
        if (sp > SPIN_CAP_W) { set_err(hop); return 0u; } }
}
__device__ __forceinline__ unsigned blk_of(int j, unsigned nr) { return (j < 8 ? j & 3 : 4) + 5u * (((j < 8 ? j >> 2 : j) & 1u) ^ nr); }
__device__ __forceinline__ unsigned blk_off(int j, unsigned nr) { const unsigned b = blk_of(j, nr); return b * 1024 + (b < 8 ? 0 : 8192); }
template <int NRND, class F> __device__ __forceinline__ void load_w(rsrc_t r0, rsrc_t r1, unsigned nr, unsigned col0, unsigned char* lds, unsigned ldsb, int wave, int lane, F&& put) {
    unsigned vo[3];
#pragma unroll
    for (int i = 0; i < 3; ++i) { const int c = 64 * i + lane, row = c / 24, ch = c - row * 24; vo[i] = (unsigned)((8 * wave + row) * HE_ + col0 + ch * 8) * 2; }
    auto issue = [&](int rd) {
#pragma unroll
        for (int i = 0; i < 3; ++i) { dma16(r0, vo[i], blk_of(rd, nr) * (64 * HE_ * 2), ldsb + (rd % 3) * 49152 + wave * 3072 + i * 1024);
            dma16(r1, vo[i], blk_of(rd, nr) * (64 * HE_ * 2), ldsb + (rd % 3) * 49152 + 24576 + wave * 3072 + i * 1024); }
    };
    issue(0); issue(1); issue(2);
    const unsigned char* rp = lds + (wave >> 2) * 24576 + (8 * (lane >> 4) + ((lane & 15) >> 2)) * 384 + (wave & 3) * 96 + (lane & 3) * 8;
#pragma unroll
    for (int rd = 0; rd < NRND; ++rd) {
        if (rd + 2 < NRND) VMW(12); else if (rd + 1 < NRND) VMW(6); else VMW(0);
        LBAR();
#pragma unroll
        for (int u = 0; u < 3; ++u) { const unsigned char* p = rp + (rd % 3) * 49152 + u * 32;
            const u32x2 a0 = ldstr(p), a1 = ldstr(p + 4 * 384), b0 = ldstr(p + 32 * 384), b1 = ldstr(p + 36 * 384);
            put(rd, u, (u32x8){a0.x, a0.y, a1.x, a1.y, b0.x, b0.y, b1.x, b1.y}); }
        LBAR();
        if (rd + 3 < NRND) issue(rd + 3);
    }
}

__device__ __forceinline__ unsigned wsw(unsigned row) { return ((row >> 1) & 1u) | ((((row >> 2) ^ (row >> 3)) & 1u) << 1); }
template <int NB, int RB, int NRND, int LB, int R0, class G, class F, class H> __device__ __forceinline__ void load_b(unsigned ldsb, int wave, G&& src, F&& take, H&& back) {
    auto issue = [&](int rd) {
#pragma unroll
        for (int j = 0; j < NB; ++j) src(rd, j, ldsb + LB + ((rd + R0) % RB) * (NB * 8192) + j * 8192 + wave * 1024); };
#pragma unroll
    for (int rd = 0; rd < RB; ++rd) issue(rd);
#pragma unroll
    for (int rd = 0; rd < NRND; ++rd) {
        VMW((NRND - 1 - rd < RB - 1 ? NRND - 1 - rd : RB - 1) * NB);
        LBAR();
        take(rd, LB + ((rd + R0) % RB) * (NB * 8192));
        LBAR();
        back(rd);
        if (rd + RB < NRND) issue(rd + RB);
    }
}
__device__ __forceinline__ u32x4 ld_frag(const unsigned char* blk, unsigned r0, unsigned dr, unsigned win, unsigned l15) {
    const unsigned ra = r0 + (l15 >> 2), rb = ra + dr;
    const u32x2 x = ldstr(blk + ra * 128 + ((win ^ wsw(ra)) << 5) + (l15 & 3) * 8), y = ldstr(blk + rb * 128 + ((win ^ wsw(rb)) << 5) + (l15 & 3) * 8);
    return (u32x4){x.x, x.y, y.x, y.y};
}
__device__ __forceinline__ void ln_consts(float s1, float s2, float& mb, float& inv) {
    const float mean = s1 * (1.0f / D_), var = fmaxf(__builtin_fmaf(-mean, mean, s2 * (1.0f / D_)), 0.f);
    mb = rbf(mean); inv = __builtin_amdgcn_rcpf(rbf(__builtin_amdgcn_sqrtf(rbf(rbf(var) + 1e-5f))));
}
__device__ __forceinline__ float row_sums(u32x2 o) {
    const float y0 = lo2f(o.x), y1 = hi2f(o.x), y2 = lo2f(o.y), y3 = hi2f(o.y);
    const auto e = __builtin_amdgcn_permlane16_swap(__float_as_uint((y0 + y1) + (y2 + y3)), __float_as_uint(__builtin_fmaf(y0, y0, y1 * y1) + __builtin_fmaf(y2, y2, y3 * y3)), false, false);
    const float x = __uint_as_float(e[0]) + __uint_as_float(e[1]);
    const auto f = __builtin_amdgcn_permlane32_swap(__float_as_uint(x), __float_as_uint(x), false, false);
    return __uint_as_float(f[0]) + __uint_as_float(f[1]);
}
__device__ __forceinline__ unsigned m_idx(int ln) { return (ln & 2) ? 0xEEEEu : 0x4444u; }
__device__ __forceinline__ f32x4 zero4() { f32x2 a, b; asm volatile("v_mov_b64 %0, 0\n\tv_mov_b64 %1, 0\n\ts_nop 0" : "=v"(a), "=v"(b)); return (f32x4){a[0], a[1], b[0], b[1]}; }
__device__ __forceinline__ u32x4 ones4() { u32x2 a, b; asm volatile("v_mov_b64 %0, -1\n\tv_mov_b64 %1, -1" : "=v"(a), "=v"(b)); return (u32x4){a[0], a[1], b[0], b[1]}; }
__device__ __forceinline__ f32x2 fold2(f32x4 v) { return (f32x2){v[0], v[1]} + (f32x2){v[2], v[3]}; }
__device__ __forceinline__ unsigned fold_pk(f32x4 v) { float x = v[0] + v[2]; PIN(x); return pk2(x, v[1] + v[3]); }
__device__ __forceinline__ float adds(float x, float y) { float r; asm("v_add_f32 %0, %1, %2" : "=v"(r) : "v"(x), "v"(y)); return r; }
__device__ __forceinline__ float muls(float x, float y) { float r; asm("v_mul_f32 %0, %1, %2" : "=v"(r) : "v"(x), "v"(y)); return r; }
#define LDSB() __builtin_amdgcn_readfirstlane((unsigned)(uintptr_t)(__attribute__((address_space(3))) unsigned char*)lds)
__device__ __forceinline__ int xp_of(int h) { return 3 * (h >> 1) + (h & 1); }
__device__ __forceinline__ unsigned um_base(unsigned long long sd, unsigned pol, unsigned c) {
    const unsigned s = (unsigned)(sd >> c) & 1u; const unsigned long long same = (s ? sd : ~sd) & ((1ull << c) - 1ull);
    const unsigned o = FR_UM + ((unsigned)__popcll(same) << 16);
    return o + (((s ^ (pol >> (o >> 21))) & 1u) << 13);
}
__device__ __forceinline__ unsigned long long um_sides(KA& a) {
    const rsrc_t rfl = MKR((a.ws + WS_HOP), F_ERR); const unsigned l = lane_id(), off = F_SD + (l < 40 ? l : 0) * 4;
    wait_set(rfl, off, (a.ws + WS_HOP), 2);
    return __ballot(ld4c(rfl, off) == 2u);
}

#define ROW2(op, a, b) asm volatile("s_nop 1\n\t" op " %0, %0, %0 row_ror:8 row_mask:0xf bank_mask:0xf\n\t" op " %1, %1, %1 row_ror:8 row_mask:0xf bank_mask:0xf\n\ts_nop 0\n\t" \
    op " %0, %0, %0 row_ror:4 row_mask:0xf bank_mask:0xf\n\t" op " %1, %1, %1 row_ror:4 row_mask:0xf bank_mask:0xf\n\ts_nop 0\n\t" \
    op " %0, %0, %0 row_ror:2 row_mask:0xf bank_mask:0xf\n\t" op " %1, %1, %1 row_ror:2 row_mask:0xf bank_mask:0xf\n\ts_nop 0\n\t" \
    op " %0, %0, %0 row_ror:1 row_mask:0xf bank_mask:0xf\n\t" op " %1, %1, %1 row_ror:1 row_mask:0xf bank_mask:0xf" : "+v"(a), "+v"(b))
template <int W> __device__ __forceinline__ void s_run(KA& a, unsigned char* lds, const int h, const u32x8 (&wr)[8][3]) {
    constexpr int wave = W; const int lane = lane_id();
    const unsigned ldsb = LDSB();
    const rsrc_t rhx = MKR(al2m((a.ws + WS_FR)), FR_USED);
    constexpr int L_MEM = 0, L_Q = 10240, L_K = 13312, L_KX = 16384, L_TIM = 28672, L_W = 32768;
    lds_u32* tim = (lds_u32*)(lds + L_TIM + 256);
    if (W == 0) { tim[0] = 0; tim[1] = 0; tim[2] = 0; tim[4] = 0; }
    const unsigned pol = page_pol(a), sx = ((my_side() ^ pol) & 1u) << 13;
    unsigned po = (2 + (lane >> 5)) * 16384 + h * 256 + ((lane >> 4) & 1) * 128 + (lane & 8) * 4 + (lane & 2) * 8 + (lane & 4) * 2 + (lane & 1) * 8192;
    unsigned pm = 4 * 16384 + (lane >> 5) * 2048 + (h >> 4) * 1024 + ((h >> 1) & 3) * 256 + ((lane >> 3) & 3) * 64 + (h & 8) * 4 + (lane & 4) * 4 + (h & 1) * 8 + (lane & 2) * 2 + (lane & 1) * 8192;
    const int xj = xp_of(h);
    unsigned idx = m_idx(lane);
    PIN(idx); if (W < 2) { PIN(po); PIN(pm); }
#pragma unroll 1
    for (int layer = 0; layer < 2; ++layer) {
    unsigned char* hop = (a.ws + WS_HOP) + layer * HOP_BYTES;
    const rsrc_t rfl = MKR(hop, F_ERR), rst = MKR((a.ws + WS_ST), ST_BYTES);
    const unsigned sb0 = (unsigned)layer * (20u << 14);
    auto kx_fetch = [&](int t, bool wait) {
        int ln = lane_id();
        if (wait) wait_set(rfl, F_XP + (t * NXP_ + xj) * FLP, hop, 0);
        dma16cn<12>(rst, ln * 16, ST_KX + ((t & (KXR - 1)) * 32 + h) * 12288, ldsb + L_KX);
    };
    if (W == 3 && lane_id() == 0) STAMP(100 + h);
    bool early = false;
    if (W == 2) { early = __all(ld4c(rfl, F_XP + xj * FLP) != 0u); if (early) kx_fetch(0, false); }
    VMKNOWN();
    unsigned tb = 0, cC1 = 0, cP = 0, kdur = 0, krt = 0;
#pragma unroll 1
    for (int s = 0; s < NSEG_; ++s) {
        int ln = lane_id();
        ATV;
        const unsigned sb = sb0 + (unsigned)(s & 3) * (5u << 14);
        unsigned qo = (W < 4 ? L_Q + ((ln >> 3) & 1) * 256 + (ln >> 4) * 64 + ((ln >> 1) & 1) * 32 + (ln & 1) * 16 + ((ln >> 2) & 1) * 8
                             : L_K + ((ln & 15) >> 2) * 128 + (((ln >> 4) * 2 + (ln & 1) + (ln & 4)) & 7) * 16 + (ln & 2) * 2);
        unsigned sel = (ln & 1) ? 0x03020706u : 0x05040100u, w2o = L_W + 65536 + wave * 2048 + ln * 16;
        PIN(qo); PIN(sel); PIN(w2o);
        unsigned char* qp = lds + qo;
        u32x8 wl[3];
        const unsigned char* wp = lds + L_W + wave * 2048 + ln * 16;
        auto w_lds = [&](int f) { const unsigned char* p = f < 4 ? wp + f * 16384 : lds + w2o + (f - 4) * 16384; return cat8(lds16(p), lds16(p + 1024)); };
#pragma unroll
        for (int u = 0; u < 3; ++u) wl[u] = w_lds(1 + u);
        f32x4 acc[3] = {zero4(), zero4(), zero4()};
        unsigned tp = s == 0 ? rt_now() : tb + cP;
#define G3(A, Wt) do { acc[0] = smf(A, Wt[0], acc[0], idx); acc[1] = smf(A, Wt[1], acc[1], idx); acc[2] = smf(A, Wt[2], acc[2], idx); PIN(acc[0]); PIN(acc[1]); PIN(acc[2]); SBAR(); } while (0)
#define S1(u, A, B) do { acc[u] = smf(A, B, acc[u], idx); PIN(acc[u]); SBAR(); } while (0)
        u32x4 m0, n0, n1; unsigned nw = 0;
        unsigned char* mp = lds + L_MEM + ln * 16;
        constexpr int B0 = W == 4 ? 8 : (W == 5 ? 1 : W), B1 = W == 3 ? 9 : 2 * W - 8; constexpr bool ER = W != 1 && W < 6, LT = W == 2 || W == 3 || W >= 6;
        unsigned mo = sx + ln * 16, to = sx + M_STAMP + min(ln, 17) * 64 + (s >> 2) * 4, s0 = sb + blk_off(B0, h >> 4), s1 = sb + blk_off(B1, h >> 4), s2 = sb + blk_off(B1 + 1, h >> 4);
        auto late = [&]() { CBAR();
            if (W == 2) nw = __builtin_amdgcn_raw_buffer_load_b32(rhx, to, sb, 16); else n0 = __builtin_amdgcn_raw_buffer_load_b128(rhx, mo, s1, 16);
            if (W >= 6) n1 = __builtin_amdgcn_raw_buffer_load_b128(rhx, mo, s2, 16);
            CBAR(); SBAR(); };
        auto stuck = [&](unsigned sp) { if ((sp & 63u) == 63u && (sp > SPIN_CAP || tim[0])) { tim[0] = 1; return true; } if (sp > RTS) __builtin_amdgcn_s_sleep(RTL); return false; };
        if (W != 1) {
            PIN(wl[0]); PIN(wl[1]); PIN(wl[2]); PIN(mo); if (W == 2) PIN(to); if (ER) asm volatile("" : "+s"(s0)); if (LT && W != 2) asm volatile("" : "+s"(s1)); if (W >= 6) asm volatile("" : "+s"(s2));
            rt_fine<23>(ER ? tp : tp + SFD);
            if (ER) { CBAR(); m0 = __builtin_amdgcn_raw_buffer_load_b128(rhx, mo, s0, 16); CBAR(); SBAR(); if (LT) nops<(SFD * 23 - 12) / 8>(); }
            if (LT) late();
            if (ER) { *reinterpret_cast<u32x4*>(mp + B0 * 1024) = m0;
                if (__builtin_expect(!__all(umax4(m0) != 0xffffffffu), 0)) {
                    tim[2] = (unsigned)s + 1u;
                    for (unsigned sp = 0;; ++sp) { CBAR(); m0 = __builtin_amdgcn_raw_buffer_load_b128(rhx, mo, s0, 16); if (__all(umax4(m0) != 0xffffffffu) || stuck(sp)) break; }
                    *reinterpret_cast<u32x4*>(mp + B0 * 1024) = m0; } }
        }
        if (W == 0) __builtin_amdgcn_s_setprio(SPRIO);
        AT(2);
        LBAR();
        AT(3);
        {
            constexpr int F = (ER && W != 4) ? B0 : 0, R1 = F == 0 ? 1 : 0, R2 = F <= 1 ? 2 : 1, R3 = F <= 2 ? 3 : 2, R6 = 6;
            if (!ER) { m0 = lds16(mp); SBAR(); }
            u32x4 A0 = lds16(mp + (W == 4 ? 0 : 8) * 1024); SBAR(); u32x4 A1 = lds16(mp + R1 * 1024); SBAR(); u32x4 A2 = lds16(mp + R2 * 1024); SBAR(); u32x4 A3 = lds16(mp + R3 * 1024); SBAR();
            auto take = [&]() { if (LT) {
                auto put_ = [&]() { if (W == 2) *reinterpret_cast<unsigned*>(lds + L_TIM + ln * 4) = nw; else *reinterpret_cast<u32x4*>(mp + B1 * 1024) = n0; if (W >= 6) *reinterpret_cast<u32x4*>(mp + B1 * 1024 + 1024) = n1;
                    return __all((W == 2 ? nw : max(umax4(n0), W >= 6 ? umax4(n1) : 0u)) != 0xffffffffu); };
                if (__builtin_expect(!put_(), 0)) { tim[2] = (unsigned)s + 1u; for (unsigned sp = 0;; ++sp) { late(); if (put_() || stuck(sp)) break; } }
                SBAR(); } };
            if (W != 4) G3(m0, wr[F]);
            if (STK == 0) take();
            { const u32x4 a8 = W == 4 ? m0 : A0;
              S1(0, a8, wl[0]); wl[0] = w_lds(4); SBAR(); S1(1, a8, wl[1]); wl[1] = w_lds(5); SBAR(); S1(2, a8, wl[2]); wl[2] = w_lds(6); SBAR(); }
            if (W == 4) G3(A0, wr[F]);
            if (STK == 1) take();
            G3(A1, wr[R1]);
            if (STK == 2) take();
            G3(A2, wr[R2]);
            if (LT) asm volatile("s_waitcnt lgkmcnt(0)" ::: "memory");
            CBAR(); __builtin_amdgcn_s_barrier(); CBAR(); SBAR();
            m0 = lds16(mp + 9 * 1024); SBAR(); A1 = lds16(mp + 4 * 1024); SBAR();
            S1(0, A3, wr[R3][0]); A2 = lds16(mp + 5 * 1024); SBAR(); S1(1, A3, wr[R3][1]); A0 = lds16(mp + 7 * 1024); SBAR(); S1(2, A3, wr[R3][2]); A3 = lds16(mp + 6 * 1024); SBAR();
            S1(1, m0, wl[1]); wl[1] = w_lds(0); SBAR(); S1(2, m0, wl[2]); wl[2] = w_lds(7); SBAR(); S1(0, m0, wl[0]);
            G3(A1, wr[4]); G3(A2, wr[5]);
            auto put = [&](int u) { const int T = 3 * (W & 3) + u; const unsigned o = fold_pk(acc[u]);
                *reinterpret_cast<unsigned*>(qp + (T >> 1) * 512 + (T & 1) * (W < 4 ? 4 : 8)) = __builtin_amdgcn_perm(dppu<0xB1>(o), o, sel); };
            acc[0] = smf(A3, wr[R6][0], acc[0], idx); acc[1] = smf(A3, wr[R6][1], acc[1], idx); acc[0] = smf(A0, wr[7][0], acc[0], idx);
            acc[2] = smf(A3, wr[R6][2], acc[2], idx); acc[1] = smf(A0, wl[1], acc[1], idx); PIN(acc[0]); PIN(acc[1]); PIN(acc[2]); SBAR();
            put(0); SBAR();
            acc[2] = smf(A0, wl[2], acc[2], idx); PIN(acc[2]); SBAR();
            put(1); SBAR(); put(2); SBAR(); AT(4);
        }
        if (s == 0) { if (W == 2 && !early) { kx_fetch(0, true); VMDRAIN(); } LBAR(); }
        if (W == 0 && s + 1 < NSEG_) {
            PIN(ln);
            unsigned kp = ldsb + L_KX + ln * 16, mk = ldsb + L_K + (ln >> 4) * 128 + ((ln + ((ln >> 2) & 4)) & 7) * 16;
            u32x8 ka = cat8(lds16a(kp), lds16a(kp + 1024)), kb = cat8(lds16a(kp + 6144), lds16a(kp + 7168)), kc = cat8(lds16a(kp + 2048), lds16a(kp + 3072)), kd = cat8(lds16a(kp + 8192), lds16a(kp + 9216));
            f32x4 sc[3] = {zero4(), zero4(), zero4()};
            const bool up = (ln & 8) != 0;
            PIN(ka); PIN(kb); PIN(kc); PIN(kd); PIN(sc[0]); PIN(sc[1]); PIN(sc[2]); PIN(kp); PIN(mk);
            __builtin_amdgcn_s_setprio(3);
            LBAR();
            AT(5);
            const u32x4 q0 = lds16a(kp + (L_Q - L_KX)); SBAR(); u32x8 ke = cat8(lds16a(mk), lds16a(mk + 512)); SBAR(); const u32x4 q1 = lds16a(kp + (L_Q - L_KX + 1024)); SBAR(); const u32x4 q2 = lds16a(kp + (L_Q - L_KX + 2048)); SBAR();
            sc[0] = smf(q0, ka, sc[0], idx); sc[1] = smf(q0, kb, sc[1], idx); PIN(sc[0]); PIN(sc[1]); SBAR();
            ka = cat8(lds16a(mk + 1024), lds16a(mk + 1536)); SBAR(); kb = cat8(lds16a(kp + 4096), lds16a(kp + 5120)); SBAR();
            sc[2] = smf(q0, ke, sc[2], idx); PIN(sc[2]); SBAR();
            ke = cat8(lds16a(kp + 10240), lds16a(kp + 11264)); SBAR();
            sc[0] = smf(q1, kc, sc[0], idx); PIN(sc[0]); SBAR();
            kc = cat8(lds16a(mk + 2048), lds16a(mk + 2560)); SBAR();
            sc[1] = smf(q1, kd, sc[1], idx); sc[2] = smf(q1, ka, sc[2], idx); sc[0] = smf(q2, kb, sc[0], idx); sc[1] = smf(q2, ke, sc[1], idx); sc[2] = smf(q2, kc, sc[2], idx); SBAR();
            AT(6);
            const f32x2 s0 = fold2(sc[0]), s1 = fold2(sc[1]), s2 = fold2(sc[2]);
            float ma = __builtin_fmaxf(__builtin_fmaxf(s0[0], s1[0]), s2[0]), mb = __builtin_fmaxf(__builtin_fmaxf(s0[1], s1[1]), s2[1]);
            ROW2("v_max_f32_dpp", ma, mb);
            const float c1 = 0.07216878364870322f * 1.4426950408889634f;
            ma *= -c1; mb *= -c1;
            const float a0 = __builtin_amdgcn_exp2f(__builtin_fmaf(s0[0], c1, ma)), b0 = __builtin_amdgcn_exp2f(__builtin_fmaf(s0[1], c1, mb));
            const float a1 = __builtin_amdgcn_exp2f(__builtin_fmaf(s1[0], c1, ma)), b1 = __builtin_amdgcn_exp2f(__builtin_fmaf(s1[1], c1, mb));
            const float a2 = __builtin_amdgcn_exp2f(__builtin_fmaf(s2[0], c1, ma)), b2 = __builtin_amdgcn_exp2f(__builtin_fmaf(s2[1], c1, mb));
            float za = __builtin_fmaf(a2, 0.5f, a0 + a1), zb = __builtin_fmaf(b2, 0.5f, b0 + b1);
            ROW2("v_add_f32_dpp", za, zb);
            za = __builtin_amdgcn_rcpf(za); zb = __builtin_amdgcn_rcpf(zb);
            const unsigned da = pk2(a0 * za, a1 * za), db = pk2(b0 * zb, b1 * zb);
            const float vm = up ? b2 * zb : a2 * za;
            u32x2 oa = (u32x2){dppu<0xA0>(da), dppu<0xF5>(da)}, ob = (u32x2){dppu<0xA0>(db), dppu<0xF5>(db)}; unsigned om = pk2(dppf<0xA0>(vm), dppf<0xF5>(vm));
            PIN(oa); PIN(ob); PIN(om);
            __builtin_amdgcn_raw_buffer_store_b64(oa, rhx, po, sb, 16);
            __builtin_amdgcn_raw_buffer_store_b64(ob, rhx, po + 64, sb, 16);
            __builtin_amdgcn_raw_buffer_store_b32(om, rhx, pm, sb, 16);
            SBAR(); AT(7);
            tim[1] = rt_now() - (tb + cP);
            tim[4] = (unsigned)s + 1u;
            __builtin_amdgcn_s_setprio(0);
            PIN(ln);
            tt_step(*reinterpret_cast<const unsigned*>(lds + L_TIM + ln * 4), s, s == 0 ? rt_now() - (W == 0 ? TT_E1 : TT_E1 - 35) : 0u, tb, cC1, cP);
        } else {
            if (W == 1) VMDRAIN();
            LBAR();
            AT(5);
            tt_step(*reinterpret_cast<const unsigned*>(lds + L_TIM + ln * 4), s, s == 0 ? rt_now() - (W == 0 ? TT_E1 : TT_E1 - 35) : 0u, tb, cC1, cP);
            if (W == 2 && s + 1 < NSEG_) wait_set(rfl, F_XP + ((s + 1) * NXP_ + xj) * FLP, hop, 0);
            if ((W == 1 || W == 2) && s + 1 < NSEG_) { for (unsigned sp = 0; tim[4] != (unsigned)s + 1u && sp < SPIN_CAP; ++sp) __builtin_amdgcn_s_sleep(2); CBAR(); if (W == 1 && SLATE1) __builtin_amdgcn_s_sleep(SLATE1); if (W == 2 && SLATE2) __builtin_amdgcn_s_sleep(SLATE2); }
            if (W == 1) {
                kdur = min(__builtin_amdgcn_readfirstlane(tim[1]), 4095u); krt = __builtin_amdgcn_readfirstlane(tim[2]) == (unsigned)s + 1u ? 1u : 0u;
#pragma unroll
                for (int m = 0; m < 3; ++m) st16s(lds16(lds + L_K + m * 1024 + ln * 16), rst, ln * 16, ST_KM + ((s & (PR - 1)) * 32 + h) * 3072 + m * 1024);
                {
                    const unsigned sr = sb0 + (unsigned)((s + 2) & 3) * (5u << 14);
                    __builtin_amdgcn_raw_buffer_store_b64((u32x2){~0u, ~0u}, rhx, po, sr, 16); __builtin_amdgcn_raw_buffer_store_b64((u32x2){~0u, ~0u}, rhx, po, sr + 64, 16);
                    __builtin_amdgcn_raw_buffer_store_b32(~0u, rhx, pm, sr, 16);
                }
                VMW(3); if (ln == 0) st16c(rfl, F_KM + (s * 32 + h) * FLP, (u32x4){cP, kdur, krt, tb}); if (ln == 0 && h == 0 && s >= 56) STAMP(370 + s - 56);
            }
            if (W == 2 && s + 1 < NSEG_) dma16cn<12>(rst, ln * 16, ST_KX + (((s + 1) & (KXR - 1)) * 32 + h) * 12288, ldsb + L_KX);
        }
        ATD(0, s == 41 && h == 0);
    }
    if (W == 0) { tim[2] = 0; tim[4] = 0; }
    if (W == 1 && lane_id() == 0) STAMP(512 + blockIdx.x);
    }
    if (W == 0 && tim[0]) set_err((a.ws + WS_HOP));
}

__device__ __forceinline__ void s_part(KA& a, unsigned char* lds, const int h, const int wave) {
    const int lane = lane_id();
    const unsigned ldsb = LDSB();
    u32x8 wr[8][3];
    auto keep = [&](int f, u32x8 v) { unsigned char* p = lds + 32768 + f * 16384 + wave * 2048 + lane * 16; *reinterpret_cast<u32x4*>(p) = lo4(v); *reinterpret_cast<u32x4*>(p + 1024) = hi4(v); };
    load_w<10>(MKR(a.wq, D_ * HE_ * 2), MKR(a.wk, D_ * HE_ * 2), h >> 4, h * 192, lds, ldsb, wave, lane, [&](int rd, int u, u32x8 v) { if (rd >= 8) keep(3 * rd - 23 + u, v); else if (rd == 7 && u == 2) keep(7, v); else wr[rd][u] = v; });
    keep(0, wr[7][1]);
    if (wave == 7) s_run<7>(a, lds, h, wr); else if (wave == 6) s_run<6>(a, lds, h, wr); else if (wave == 5) s_run<5>(a, lds, h, wr); else if (wave == 4) s_run<4>(a, lds, h, wr);
    else if (wave == 3) s_run<3>(a, lds, h, wr); else if (wave == 2) s_run<2>(a, lds, h, wr); else if (wave == 1) s_run<1>(a, lds, h, wr); else s_run<0>(a, lds, h, wr);
}

#define WLD 4
#define TT_WD 45
#define ATH 0
#define LBAR_NONE() do { } while (0)
template <int N, class W, class O, class F> __device__ __forceinline__ void u_tiles(u32x4 (&xf)[6][2], rsrc_t rst, unsigned l16, W&& w, O&& off, F&& side) {
    f32x4 acc[2][2]; u32x4 o; unsigned so;
#pragma unroll
    for (int i = 0; i < N; ++i) {
#pragma unroll
        for (int j = 0; j < 12; ++j) { const int k = j >> 1, r = j & 1;
            acc[i & 1][r] = dmf(xf[k][r], w(i, k), k ? acc[i & 1][r] : Z4); PIN(acc[i & 1][r]); SBAR();
            side(i, j);
            if (i > 0) { const f32x4 c0 = acc[(i - 1) & 1][0], c1 = acc[(i - 1) & 1][1];
                if (j == 2) { so = off(i - 1); asm volatile("" : "+s"(so)); }
                if (j == 3) { o.x = pk2(c0[0], c1[0]); PIN(o.x); } if (j == 4) { o.y = pk2(c0[2], c1[2]); PIN(o.y); } if (j == 5) { o.z = pk2(c0[1], c1[1]); PIN(o.z); } if (j == 6) { o.w = pk2(c0[3], c1[3]); PIN(o); }
                if (j == 7) __builtin_amdgcn_raw_buffer_store_b128(o, rst, l16, so, 16);
                if (j == 9) asm volatile("" :: "v"(o)); }
            SBAR(); }
    }
    const f32x4 c0 = acc[(N - 1) & 1][0], c1 = acc[(N - 1) & 1][1];
    st16s((u32x4){pk2(c0[0], c1[0]), pk2(c0[2], c1[2]), pk2(c0[1], c1[1]), pk2(c0[3], c1[3])}, rst, l16, off(N - 1));
}
__device__ __forceinline__ unsigned vblk(int j, unsigned nr) { const unsigned v = (unsigned)(0x9873265410ull >> (4 * j)) & 15u; return nr ? (v < 5 ? v + 5 : v - 5) : v; }
template <bool XW> __device__ __forceinline__ void v_run(KA& a, unsigned char* lds, const int h, const int wave) {
    const int lane = lane_id(), tid = wave * 64 + lane;
    const unsigned ldsb = LDSB();
    constexpr int L_MEM = 0, L_V = 10240, L_VX = 13312, L_TIM = 25600, L_E = 26624, L_F = L_E + 2048, L_T = L_F + 1024, L_U = L_T + 1024, L_W = 16384, NO = XW ? 4 : 6, NV = XW ? 2 : 1, KR = XW ? 8 : 4;
    constexpr int NBK = XW ? 2 : 6;
    u32x8 wo[NBK][3], wv[KR][NV];
    const int lw0 = XW ? 65536 + wave * 8192 : wave * 8192, lw1 = XW ? 114688 + wave * 12288 : 81920 + wave * 4096;
    const int vt = XW ? 2 * wave : 4 + wave;
    {
        const rsrc_t rv = MKR(a.wv, D_ * HE_ * 2), ro = MKR(a.wo, HE_ * D_ * 2);
        const unsigned l15 = lane & 15, lg = lane >> 4, row = 8 * wave + (lane >> 3), ch = ((lane & 7) ^ (wsw(row) << 1)) * 16;
        const unsigned vo = row * (HE_ * 2) + h * 384 + ch, oo = (h * 192 + row) * (D_ * 2) + ch;
        u32x8 tmp[6];
        load_b<6, 3, 10, L_W, 2>(ldsb, wave,
            [&](int rd, int j, unsigned dst) { if (rd < 4 || rd == 9) dma16(ro, oo, (j >> 1) * (64 * D_ * 2) + (rd % 5) * 256 + (j & 1) * 128, dst); else dma16(rv, vo, vblk((rd - 4) * 2 + j / 3, h >> 4) * (64 * HE_ * 2) + (j % 3) * 128, dst); },
            [&](int rd, int base) {
                if (rd < 4 || rd == 9) {
#pragma unroll
                    for (int i = 0; i < (rd < 2 ? 1 : (XW == (rd == 9) ? 2 : 0)); ++i) { const int tl = rd < 2 ? wave : (rd == 2 ? (wave & 3) + 4 * i : 2 * (wave & 3) + i);
#pragma unroll
                        for (int k = 0; k < 3; ++k) { const unsigned char* p = lds + base + (2 * k + (tl >> 2)) * 8192; const u32x8 v = cat8(ld_frag(p, 4 * lg, 16, tl & 3, l15), ld_frag(p, 32 + 4 * lg, 16, tl & 3, l15));
                            if (rd == 9) tmp[3 * i + k] = v; else wo[(rd < 2 ? rd : 2 * rd - 2 + i) % NBK][k] = v; } }
                } else {
#pragma unroll
                    for (int k = 0; k < 2; ++k)
#pragma unroll
                        for (int u = 0; u < NV; ++u) { const unsigned char* p = lds + base + (3 * k + (vt >> 2)) * 8192; const u32x8 v = cat8(ld_frag(p, 8 * lg, 4, (vt & 3) + u, l15), ld_frag(p, 32 + 8 * lg, 4, (vt & 3) + u, l15));
                            const int j = 2 * (rd - 4) + k; if (j < KR) wv[j % KR][u] = v; else tmp[XW ? 2 * k + u : j & 3] = v; } } },
            [&](int rd) {
                const int n = XW ? (rd == 8 ? 4 : (rd == 9 ? 6 : 0)) : (rd == 7 ? 4 : (rd == 8 ? 2 : 0));
#pragma unroll
                for (int q = 0; q < n; ++q) { unsigned char* p = lds + (n == 4 ? lw0 : lw1) + q * 2048 + lane * 16; *reinterpret_cast<u32x4*>(p) = lo4(tmp[q]); *reinterpret_cast<u32x4*>(p + 1024) = hi4(tmp[q]); } });
    }
    const rsrc_t rhx = MKR(al2m((a.ws + WS_FR)), FR_USED), rst = MKR((a.ws + WS_ST), ST_BYTES);
    lds_u32* tim = (lds_u32*)(lds + L_TIM);
    if (tid == 0) { tim[0] = 0; tim[2] = 0; }
    const unsigned pol = page_pol(a), sx = ((my_side() ^ pol) & 1u) << 13;
    unsigned uo0, uo1 = 0, l16 = lane * 16;
    { const unsigned long long sd = um_sides(a);
      uo0 = um_base(sd, pol, lane < 32 ? 8 * (lane >> 4) + wave : (XW ? 30 + 2 * wave + (lane >> 4) : 4 + wave + 4 * (lane >> 4))) + h * 256 + (lane & 15) * 16;
      if (!XW) uo1 = um_base(sd, pol, 16 + 2 * wave + (lane >> 5)) + h * 256 + (lane & 15) * 16 + (lane & 16) / 2; }
    auto arm = [&](unsigned so) { const u32x4 on = ones4(); st16s(on, rhx, uo0, so); if (!XW) __builtin_amdgcn_raw_buffer_store_b64((u32x2){on.x, on.y}, rhx, uo1, so, 16); SBAR(); };
    arm(16384); arm(32768); arm(49152);
    const int xj = xp_of(h), xj2 = 3 * (h >> 1) + 2;
    if (XW) uo1 = wave < 2 ? l16 : M_STAMP + min(lane, 17) * 64;
    const int j0 = (0x6510 >> (4 * (wave & 3))) & 15, j1 = (0x9842 >> (4 * (wave & 3))) & 15, j2 = wave ? 7 : 3;
    auto voff = [&](int j) { const unsigned b = vblk(j, h >> 4); return b * 1024 + (b < 8 ? 0 : 8192); };
    const unsigned o0 = voff(j0), o1 = voff(j1), o2 = wave < 2 ? voff(j2) : 0u;
    PIN(uo0); PIN(uo1); PIN(l16);
    *reinterpret_cast<u32x4*>(lds + L_T + l16) = (u32x4){L_V + ((lane >> 3) & 1) * 256u + (lane >> 4) * 64u + ((lane >> 1) & 1) * 32u + (lane & 1) * 16u + ((lane >> 2) & 1) * 8u,
        (lane & 1) ? 0x03020706u : 0x05040100u, m_idx(lane), F_XP + ((lane & 1) ? xj : xj2) * FLP};
    VMKNOWN();
    ATV;
#define la ((int)l16)
    auto u_so = [&](int t, int i) { return ST_U + (((t & (URING - 1)) * 40 + (i < 2 ? 8 * i + wave : (i < 4 ? 4 + wave + 4 * i : 12 + 2 * wave + i))) * 32 + h) * 1024; };
    u32x4 xf[6][2];
    auto back0 = [&](int q) { xf[q >> 1][q & 1] = lds16a(la + L_VX + (q & 1) * 6144 + (q >> 1) * 1024); SBAR(); };
    auto back = [&](auto N, int t, auto&& side) { u_tiles<decltype(N)::value>(xf, rst, l16, [&](int i, int k) { return (k & 1) ? hi4(wo[i][k >> 1]) : lo4(wo[i][k >> 1]); }, [&](int i) { return u_so(t, i); }, [&](int i, int j) { if (i == 0) { side(j); if (j + 4 < 12) back0(j + 4); } }); };
    auto v_so = [&](int t) { return ST_VX + ((t & (KXR - 1)) * 32 + h) * 12288 + (wave & 3) * 3072; };
#pragma unroll 1
    for (int layer = 0; layer < 2; ++layer) {
    unsigned char* hop = (a.ws + WS_HOP) + layer * HOP_BYTES;
    const rsrc_t rfl = MKR(hop, F_ERR);
    const unsigned sb0 = (unsigned)layer * (20u << 14) + sx;
    if (wave == 0 && l16 == 0) STAMP(140 + h);
    unsigned tb = rt_now(), cC1 = 0, cP = 0;
#pragma unroll 1
    for (int s = 0; s < NSEG_; ++s) {
        PIN(l16);
        int w0o = la + lw0, w1o = la + lw1; unsigned us = (s & 3) << 14;
        PIN(w0o); PIN(w1o); asm volatile("" : "+s"(us));
        const u32x4 lc = lds16a(la + L_T);
        unsigned fw = 1u, d0 = 0, d1 = 0;
        if (!XW) fw = lds4a(L_F + (l16 >> 2));
        SBAR();
        constexpr int NL = 4;
        u32x4 wl[NL][2];
#pragma unroll
        for (int u = 0; u < 2 * NL; ++u) wl[u >> 1][u & 1] = lds16a(w0o + u * 1024);
        f32x4 acc[NV];
#pragma unroll
        for (int u = 0; u < NV; ++u) acc[u] = zero4();
        const unsigned sel = lc.y, idx = lc.z;
        SBAR();
        u32x4 m1, m2; unsigned s1 = 0, s2 = 0; int w1 = 0, w2 = 0;
        auto late = [&]() { CBAR(); m1 = __builtin_amdgcn_raw_buffer_load_b128(rhx, l16, s1, 16); SBAR(); m2 = __builtin_amdgcn_raw_buffer_load_b128(rhx, uo1, s2, 16); CBAR(); SBAR(); };
        auto stuck = [&](unsigned sp) { if ((sp & 63u) == 63u && (sp > SPIN_CAP || tim[0])) { tim[0] = sp; return true; } if (sp > 8) __builtin_amdgcn_s_sleep(RTL); return false; };
        if (XW) {
            const unsigned sb = sb0 + (unsigned)(s & 3) * (5u << 14);
            unsigned s0 = sb + o0; s1 = sb + o1; s2 = sb + (wave < 2 ? o2 : (s >> 2) * 4);
            int w0 = la + j0 * 1024; w1 = la + j1 * 1024; w2 = la + (wave < 2 ? j2 * 1024 : L_E - 2048 + wave * 1024);
            PIN(w0); PIN(w1); PIN(w2); asm volatile("" : "+s"(s0), "+s"(s1), "+s"(s2));
            AT(0);
            rt_fine<21>(tb + cP + TT_VD);
            AT(6);
            CBAR(); u32x4 m0 = __builtin_amdgcn_raw_buffer_load_b128(rhx, l16, s0, 16); CBAR(); SBAR();
            nops<(VFD * 21 - 12) / 8>();
            late();
            sts16a(w0, m0);
            if (__builtin_expect(!__all(umax4(m0) != 0xffffffffu), 0)) {
                tim[2] = (unsigned)s + 1u;
                for (unsigned sp = 0;; ++sp) { CBAR(); m0 = __builtin_amdgcn_raw_buffer_load_b128(rhx, l16, s0, 16); if (__all(umax4(m0) != 0xffffffffu) || stuck(sp)) break; }
                sts16a(w0, m0); }
        } else {
            if (__builtin_expect(s > 0 && !__all(fw != 0u), 0)) { wait_set(rfl, lc.w + min(s + 2, NSEG_ - 1) * (NXP_ * FLP), hop, 0); VMKNOWN(); }
            d0 = v_so(s + 2); d1 = min(s + 3, NSEG_ - 1) * (NXP_ * FLP); asm volatile("" : "+s"(d0), "+s"(d1));
        }
        AT(1);
        if (XW) { LBAR(); __builtin_amdgcn_s_setprio(0); } else { __builtin_amdgcn_s_setprio(YP1); CBAR(); __builtin_amdgcn_s_barrier(); CBAR(); }
        AT(2);
        f32x4 um[NO];
        {
#define VS(u, A, B) do { acc[(u) % NV] = smf(A, B, acc[(u) % NV], idx); PIN(acc[(u) % NV]); SBAR(); } while (0)
#define VL(u) cat8(wl[(u) % NL][0], wl[(u) % NL][1])
#define VA(k) lds16a(la + L_MEM + (k) * 1024); SBAR()
#define VW(u, f, q) wl[u][q] = lds16a(w1o + (f) * 2048 + (q) * 1024); SBAR()
            int qo;
            if (XW) {
                u32x4 A0 = VA(5); u32x4 A1 = VA(6); u32x4 A2 = VA(0);
                auto take = [&]() { const unsigned em = wave < 2 ? ~0u : 0u;
                    auto put_ = [&]() { sts16a(w1, m1); sts16a(w2, m2); SBAR(); return __all(max(umax4(m1), max(m2.x, max(max(m2.y, m2.z), m2.w) & em)) != 0xffffffffu); };
                    if (__builtin_expect(!put_(), 0)) { tim[2] = (unsigned)s + 1u; for (unsigned sp = 0;; ++sp) { late(); if (put_() || stuck(sp)) break; } }
                    SBAR(); };
#define V2(A, j) VS(0, A, wv[(j) % KR][0]); VS(1, A, wv[(j) % KR][NV - 1])
                V2(A0, 5);
                take();
                u32x4 A3 = VA(1);
                V2(A1, 6);
                LBAR(); SBAR();
                A0 = VA(8); A1 = VA(9);
                V2(A2, 0); A2 = VA(2); V2(A3, 1); A3 = VA(3);
                VS(0, A0, VL(0)); VS(1, A0, VL(1)); A0 = VA(4); VS(0, A1, VL(2)); VS(1, A1, VL(3)); A1 = VA(7);
                VS(0, A2, wv[2][0]); VW(0, 0, 0); VS(1, A2, wv[2][NV - 1]); VW(0, 0, 1);
                VS(0, A3, wv[3][0]); VW(1, 1, 0); um[0] = zero4(); SBAR(); VS(1, A3, wv[3][NV - 1]); VW(1, 1, 1);
                VS(0, A0, wv[4 % KR][0]); VW(2, 2, 0); um[1] = zero4(); qo = lc.x + (vt >> 1) * 512; PIN(qo); SBAR(); VS(0, A1, wv[7 % KR][0]); VW(2, 2, 1);
                VS(1, A0, wv[4 % KR][NV - 1]); VS(1, A1, wv[7 % KR][NV - 1]);
            } else {
                u32x4 A0 = VA(5); u32x4 A1 = VA(6); u32x4 A2 = VA(0); u32x4 A3 = VA(1);
                dma16cn<3>(rst, l16, d0, L_VX + (wave & 3) * 3072); SBAR();
                VS(0, A0, VL(1)); VS(0, A1, VL(2));
                CBAR(); __builtin_amdgcn_s_barrier(); CBAR(); SBAR();
                if (YP2) __builtin_amdgcn_s_setprio(YP2);
                A0 = VA(4); A1 = VA(7);
                VS(0, A2, wv[0][0]); A2 = VA(2);
                VS(0, A3, wv[1][0]); A3 = VA(3); qo = lc.x + ((vt >> 1) * 512 + (vt & 1) * 4); PIN(qo); SBAR();
                VW(1, 0, 0); VW(1, 0, 1); VS(0, A0, VL(0)); A0 = VA(8); VW(2, 1, 0); VS(0, A1, VL(3)); A1 = VA(9); VW(2, 1, 1);
                VS(0, A2, wv[2][0]); um[0] = zero4(); um[1] = zero4(); SBAR(); VS(0, A3, wv[3][0]); um[2] = zero4(); um[3] = zero4(); SBAR();
                VS(0, A0, VL(1)); um[NO - 2] = zero4(); um[NO - 1] = zero4(); SBAR(); VS(0, A1, VL(2));
            }
            unsigned o[NV];
#pragma unroll
            for (int u = 0; u < NV; ++u) { o[u] = fold_pk(acc[u]); o[u] = __builtin_amdgcn_perm(dppu<0xB1>(o[u]), o[u], sel); }
            if (XW) { *(__attribute__((address_space(3))) u32x2*)(uintptr_t)(unsigned)qo = (u32x2){o[0], o[NV - 1]}; SBAR(); um[2] = zero4(); um[3] = zero4();
            } else { *(__attribute__((address_space(3))) unsigned*)(uintptr_t)(unsigned)qo = o[0]; __builtin_amdgcn_s_setprio(2); }
        }
        AT(3);
        VMDRAIN(); LBAR();
        AT(4);
        {
            const u32x4 q0 = lds16a(la + L_V), q1 = lds16a(la + L_V + 1024), q2 = lds16a(la + L_V + 2048); SBAR();
#define VU(u, q, k) do { um[u] = smf(q, (XW && (u) >= 2) ? VL(k) : wo[(u) % NBK][k], um[u], idx); PIN(um[u]); SBAR(); } while (0)
#define VO(u) min(fold_pk(um[u]), 0xfffffffeu)
            if (!XW) { VU(NO - 2, q0, 0); VU(NO - 1, q0, 0); VU(NO - 2, q1, 1); VU(NO - 1, q1, 1); VU(NO - 2, q2, 2); VU(NO - 1, q2, 2); }
            VU(0, q0, 0); VU(2, q0, 0);
            unsigned e0 = 0, e1 = 0;
            if (!XW) { e0 = VO(NO - 2); PIN(e0); SBAR(); }
            VU(0, q1, 1);
            if (XW) { VW(0, 3, 0); VW(0, 3, 1); }
            if (!XW) { e1 = VO(NO - 1); PIN(e1); SBAR(); }
            VU(2, q1, 1);
            if (!XW) { const auto z = __builtin_amdgcn_permlane32_swap(e0, e1, false, false); __builtin_amdgcn_raw_buffer_store_b64((u32x2){z[0], z[1]}, rhx, uo1, us, 16); SBAR(); }
            VU(0, q2, 2); if (XW) { VW(1, 4, 0); VW(1, 4, 1); } else back0(2); VU(2, q2, 2); if (!XW) back0(3); VU(1, q0, 0); if (XW) { VW(2, 5, 0); VW(2, 5, 1); } back0(0); VU(1, q1, 1); back0(1);
            e0 = VO(0); PIN(e0); SBAR();
            VU(1, q2, 2);
            e1 = VO(2); PIN(e1); SBAR();
            VU(3, q0, 0);
            const auto x02 = __builtin_amdgcn_permlane32_swap(e0, e1, false, false); SBAR();
            VU(3, q1, 1); if (XW) { back0(2); back0(3); } VU(3, q2, 2);
            e0 = VO(1); PIN(e0); SBAR();
            const auto x13 = __builtin_amdgcn_permlane32_swap(e0, VO(3), false, false);
            const auto y01 = __builtin_amdgcn_permlane16_swap(x02[0], x13[0], false, false), y23 = __builtin_amdgcn_permlane16_swap(x02[1], x13[1], false, false);
            st16s((u32x4){y01[0], y23[0], y01[1], y23[1]}, rhx, uo0, us);
            SBAR();
            AT(5);
            __builtin_amdgcn_s_setprio(XW ? 2 : 0);
        }
        if (__builtin_expect(s == 0, 0)) {
            if (!XW) sts4a(L_F + (l16 >> 2), 0u);
#pragma unroll 1
            for (int t = 0; t < 3; ++t) {
                if (XW) { wait_set(rfl, lds4a(la + L_T + 12) + t * (NXP_ * FLP), hop, t ? 0 : 1); dma16cn<3>(rst, l16, v_so(t), L_VX + wave * 3072); VMDRAIN(); }
                LBAR();
                back0(0); back0(1); back0(2); back0(3);
                back(std::integral_constant<int, NBK>{}, t, [](int) {});
                VMDRAIN(); LBAR();
                if (wave == 0 && l16 == 0) st4c(rfl, F_UX + (t * 32 + h) * FLP, (unsigned)(t + 1) << 16);
                if (wave == 0 && l16 == 0 && h == 0) STAMP(10 + t);
            }
        } else {
            unsigned tw, tr, te; int d;
            back(std::integral_constant<int, XW ? 2 : 3>{}, s + 2, [&](int j) {
                if (XW) {
                    if (j == 0) { tw = lds4a(la + L_E); tr = tim[2]; te = rt_now(); asm volatile("" : "+s"(te)); }
                    if (j == 2) { d = (int)(tw - tb - cC1); PIN(d); }
                    if (j == 3) { d = max(d, (int)dppu<0x128>((unsigned)d)); PIN(d); } if (j == 4) { d = max(d, (int)dppu<0x124>((unsigned)d)); PIN(d); }
                    if (j == 5) { d = max(d, (int)dppu<0x122>((unsigned)d)); PIN(d); } if (j == 6) { d = max(d, (int)dppu<0x121>((unsigned)d)); PIN(d); }
                    if (j == 7) tim[4 + (s & 1)] = min(te - (tb + cP), 4095u) + (__builtin_amdgcn_readfirstlane(tr) == (unsigned)s + 1u ? 4096u : 0u);
                    if (j == 8) { const int e = __builtin_amdgcn_readlane(d, 0); tb = (s == 1 || e > 0) ? tb + cC1 + (unsigned)e + (s == 1 ? TT_V1 : TT_V) : tb + cP; cC1 = __builtin_amdgcn_readlane(tw, 16); cP = __builtin_amdgcn_readlane(tw, 17); asm volatile("" : "+s"(tb), "+s"(cC1), "+s"(cP)); }
                }
                if (!XW && j == 0) tw = tim[4 + ((s + 1) & 1)];
                if (!XW && j == 2) __builtin_amdgcn_raw_buffer_store_b32(tw | 65536u, rfl, wave == 7 ? l16 << 20 : OOB, F_UX + ((s + 1) * 32 + h) * FLP, 16);
                if (!XW && j == 5) dma4c(rfl, lds4a(la + L_T + 12), d1, L_F);
                });
            if (!XW) VMW(3);
        }
        AT(7);
        ATD(1, s == 41 && h == ATH);
    }
    if (wave == 0 && l16 == 0) { tim[2] = 0; STAMP(512 + blockIdx.x); }
    }
    if (wave == 0 && l16 == 0 && tim[0]) set_err((a.ws + WS_HOP));
#undef la
}

__device__ __forceinline__ void w_part(KA& a, unsigned char* lds, const int w, const int wave) {
    const int lane = lane_id(), hw = wave >> 1, t0 = (wave & 1) * 6;
    const unsigned ldsb = LDSB();
    u32x4 wo[6][6];
    {
        const rsrc_t ro = MKR(a.wo, HE_ * D_ * 2);
        const unsigned row = 8 * wave + (lane >> 3), oo = ((4 * w + (row >> 5)) * 192 + (row & 31)) * (D_ * 2) + 640 + ((lane & 7) ^ (wsw(row) << 1)) * 16;
        load_b<6, 3, 6, 0, 0>(ldsb, wave, [&](int rd, int j, unsigned dst) { dma16(ro, oo, (rd * 32 + (j / 3) * 384) * (D_ * 2) + (j % 3) * 128, dst); },
            [&](int rd, int base) {
#pragma unroll
                for (int i = 0; i < 6; ++i) wo[i][rd] = ld_frag(lds + base + ((hw >> 1) * 3 + ((t0 + i) >> 2)) * 8192, (hw & 1) * 32 + 4 * (lane >> 4), 16, (t0 + i) & 3, lane & 15); }, [](int) {});
    }
    const rsrc_t rst = MKR(a.ws + WS_ST, ST_BYTES);
    if (wave == 0) init_wait(a);
#pragma unroll 1
    for (int layer = 0; layer < 2; ++layer) {
    unsigned char* hop = a.ws + WS_HOP + layer * HOP_BYTES;
    const rsrc_t rfl = MKR(hop, F_ERR);
    unsigned l16 = lane_id() * 16; PIN(l16);
    const unsigned fx = F_XP + (6 * w + (lane & 7) % 6) * FLP;
    auto v_so = [&](int t) { return ST_VX + ((t & (KXR - 1)) * 32 + 4 * w) * 12288 + wave * 6144; };
    if (wave == 0) { wait_set(rfl, fx + NXP_ * FLP, hop, 8); wait_set(rfl, fx + 3 * (NXP_ * FLP), hop, 1); }
    LBAR();
    dma16cn<6>(rst, l16, v_so(3), ldsb + 49152 + wave * 6144);
    VMKNOWN(); LBAR();
    unsigned tnext = rt_now();
#pragma unroll 1
    for (int t = 3; t < NSEG_; ++t) {
        u32x4 fl, xf[6][2];
        const unsigned fo = (lane < 8 && t >= WLD) ? F_KM + ((t - WLD) * 32 + 4 * w) * FLP : fx + min(t + 1, NSEG_ - 1) * (NXP_ * FLP);
        if (wave == 0) asm volatile("buffer_load_dwordx4 %0, %1, %2, 0 offen sc1" : "=&v"(fl) : "v"(fo), "s"(rfl) : "memory");
        const int xp = ldsb + (t & 1) * 49152 + hw * 12288 + l16;
        auto rd = [&](int q) { xf[q >> 1][q & 1] = lds16a(xp + (q & 1) * 6144 + (q >> 1) * 1024); SBAR(); };
        rd(0); rd(1); rd(2); rd(3);
        u_tiles<6>(xf, rst, l16, [&](int i, int k) { return wo[i][k]; }, [&](int i) { return ST_U + (((t & (URING - 1)) * 40 + 20 + t0 + i) * 32 + 4 * w + hw) * 1024; }, [&](int i, int j) {
            if (i == 0 && j + 4 < 12) rd(j + 4);
            if (i == 1 && j == 0) { if (wave == 0) VMW(1); else VMDRAIN(); __builtin_amdgcn_s_barrier(); CBAR(); if (wave == 1 && t > 3 && l16 == 0) st4c(rfl, F_UW + ((t - 1) * 8 + w) * FLP, 1u); }
            if (i == 2 && j == 0) {
                if (wave == 0) { asm volatile("s_waitcnt vmcnt(1)" : "+v"(fl) :: "memory");
                    if (__builtin_expect(__all(fl.x != 0u), 1)) tnext = __builtin_amdgcn_readfirstlane(fl.w) + 2u * __builtin_amdgcn_readfirstlane(fl.x) + TT_WD; else { wait_timed(rfl, fo, hop, tnext, 2, TT_WD, t < WLD); VMKNOWN(); } }
                __builtin_amdgcn_s_barrier(); CBAR(); }
            if ((i == 2 || i == 3) && (j == 1 || j == 8 || j == 11)) { const int p = (i - 2) * 3 + (j > 1) + (j > 8); dma16c(rst, l16, v_so(t + 1) + p * 1024, ldsb + ((t + 1) & 1) * 49152 + wave * 6144 + p * 1024); } });
        VMW(3);
        if (wave == 0 && t >= WLD) rt_wait(tnext);
        LBAR();
    }
    VMDRAIN(); LBAR();
    if (wave == 1 && l16 == 0) st4c(rfl, F_UW + ((NSEG_ - 1) * 8 + w) * FLP, 1u);
    }
}

constexpr int RO_UM = 131072, RO_TIM = RO_UM + 3 * 8192, RO_END = NSEG_;
__device__ __forceinline__ void ro_state(KA& a, unsigned char* lds, const int r, const unsigned pol) {
    constexpr int wave = 0;
    const int lane = lane_id();
    const rsrc_t rhx = MKR(al2m((a.ws + WS_FR)), FR_USED), rst = MKR(a.ws + WS_ST, ST_BYTES);
    lds_u32* tim = (lds_u32*)(lds + RO_TIM + 256);
    const unsigned sx = ((my_side() ^ pol) & 1u) << 13;
    unsigned po = sx + (2 + (r & 1)) * 16384 + lane * 16, uo = um_base(um_sides(a), pol, r >> 1) + lane * 16;
    unsigned ho = (r < 64 ? 0 : 8192) + r * 128 + (lane >> 5) * 64 + (lane & 2) * 16 + (lane & 16) + (lane & 12) + (lane & 1) * 8192;
    unsigned idx = (lane & 1) ? 0xEEEEu : 0x4444u;
    const unsigned pmo = (2 - (r & 1)) * 16384 + (r & 1) * 2048;
    const unsigned nr = r < 40 ? 0u : 1u;
    unsigned so = (r % 5 == 0 && lane < 2) ? M_STAMP + (r / 5) * 64 + lane * 8192 : OOB;
    PIN(po); PIN(uo); PIN(ho); PIN(idx); PIN(so);
#pragma unroll 1
    for (int layer = 0; layer < 2; ++layer) {
    const unsigned sb0 = (unsigned)layer * (20u << 14);
    unsigned tb = 0, cC1 = 0, cP = 0; int u3 = 0;
    u32x8 ux[16];
#pragma unroll 1
    for (int t = 0; t < RO_END; ++t) {
        const int s = t - 1;
        ATV;
        if (t > 0) {
            const unsigned sp_ = sb0 + (unsigned)(s & 3) * (5u << 14);
            unsigned sb = sb0 + (unsigned)(t & 3) * (5u << 14), ss = sb + (t >> 2) * 4, sp0 = sp_ + nr * 4096, sp1 = sp0 ^ 4096, sm0 = sp_ + pmo + nr * 1024, sm1 = sm0 ^ 1024, su0 = ((s & 3) << 14) + nr * 4096, su1 = su0 ^ 4096;
            asm volatile("" : "+s"(sb), "+s"(ss), "+s"(sp0), "+s"(sp1), "+s"(sm0), "+s"(sm1), "+s"(su0), "+s"(su1));
            u32x4 um[8], p[10];
            auto u_bad = [&]() { unsigned m = 0;
#pragma unroll
                for (int q = 0; q < 8; ++q) m = max(m, umax4(um[q]));
                return m; };
            unsigned tp = tb + cC1;
            {
                auto p_half = [&](int z) { CBAR();
#pragma unroll
                    for (int q = 0; q < 4; ++q) { p[4 * z + q] = __builtin_amdgcn_raw_buffer_load_b128(rhx, po + q * 1024, z ? sp1 : sp0, 16); SBAR(); }
                    p[8 + z] = __builtin_amdgcn_raw_buffer_load_b128(rhx, po, z ? sm1 : sm0, 16); SBAR();
#pragma unroll
                    for (int q = 0; q < 4; ++q) { um[4 * z + q] = __builtin_amdgcn_raw_buffer_load_b128(rhx, uo + q * 1024, z ? su1 : su0, 16); SBAR(); }
                    CBAR(); };
                unsigned o;
                auto p_use = [&](f32x4 c0, f32x4 c1) {
#pragma unroll
                    for (int z = 0; z < 2; ++z) {
#pragma unroll
                        for (int q = 4 * z; q < 4 * z + 4; ++q) { c0 = smf(p[q], ux[2 * q], c0, idx); c1 = smf(p[q], ux[2 * q + 1], c1, idx); SBAR(); }
                        c0 = smf(p[8 + z], cat8(um[4 * z], um[4 * z + 1]), c0, idx); SBAR(); c1 = smf(p[8 + z], cat8(um[4 * z + 2], um[4 * z + 3]), c1, idx); SBAR(); }
                    SBAR();
                    float v0 = c0[0] + c0[1]; PIN(v0);
                    const float v = v0 + (c1[2] + c1[3]);
                    o = pk2(dppf<0xA0>(v), dppf<0xF5>(v));
                    return !__any(v != v); };
                f32x4 c0 = zero4(), c1 = zero4();
                { const unsigned n = rt_now(); if ((int)(n - tp) > 4) { tp = n; tim[2] = (unsigned)t; } }
                unsigned tv = min(tp, 0xfffffffeu); PIN(tv);
                AT(0);
                rt_fine<23>(tp);
                AT(1); p_half(0); nops<(RFD * 23 - 100) / 8>(); p_half(1);
                if (__builtin_expect(!p_use(c0, c1), 0)) {
                    tim[2] = (unsigned)t;
                    for (unsigned sp = 0;; ++sp) {
                        p_half(0); p_half(1);
                        if (p_use(zero4(), zero4())) break;
                        unsigned m = u_bad();
#pragma unroll
                        for (int q = 0; q < 10; ++q) m = max(m, umax4(p[q]));
                        if (__all(m != 0xffffffffu)) break;
                        if ((sp & 63u) == 63u && (sp > SPIN_CAP || tim[0])) { tim[0] = 1; break; }
                        if (sp > 3 || s == 0) __builtin_amdgcn_s_sleep(R0S);
                    }
                    o = min(o, 0xfffffffeu);
                    tv = min(rt_now() - TT_RT, 0xfffffffeu);
                }
                __builtin_amdgcn_raw_buffer_store_b32(o, rhx, ho, sb, 16); SBAR();
                __builtin_amdgcn_raw_buffer_store_b32(tv, rhx, so, ss, 16);
                SBAR(); AT(2);
                tim[1] = rt_now() - (tb + cC1);
            }
            LBAR();
            unsigned char* up = lds + RO_UM + u3 * 8192 + lane_id() * 16;
#pragma unroll
            for (int q = 0; q < 8; ++q) *reinterpret_cast<u32x4*>(up + ((q >> 2) ^ nr) * 4096 + (q & 3) * 1024) = um[q];
            u3 = u3 == 2 ? 0 : u3 + 1;
        } else LBAR();
        {
            const int ln = lane_id();
            if (t > 1) rt_wait(tb + cP + TT_BD);
            AT(3);
            const unsigned ub = ST_U + ((t & (URING - 1)) * 40 + (r >> 1)) * 32768, lb = (t & 3) * 32768, o = nr * 16384;
            dma16cn<16>(rst, ln * 16, ub + o, lb + o); dma16cn<16>(rst, ln * 16, ub + (o ^ 16384), lb + (o ^ 16384));
            const unsigned char* xp = lds + lb + (ln >> 5) * 1024 + (ln & 31) * 16;
#pragma unroll
            for (int q = 0; q < 16; ++q) { if (!(q & 1)) VMW(28 - 2 * q); const unsigned char* p = xp + ((q >> 3) ^ nr) * 16384 + ((q >> 1) & 3) * 4096 + (q & 1) * 512; ux[q] = cat8(lds16(p), lds16(p + 2048)); }
            AT(4);
            if (ln == 0 && r == 0 && t == 0) STAMP(13);
            LBAR();
            AT(5);
            const unsigned tpv = *reinterpret_cast<const unsigned*>(lds + RO_TIM + ln * 4);
            tb = __builtin_amdgcn_readlane(tpv, 0); cC1 = __builtin_amdgcn_readlane(tpv, 1); cP = __builtin_amdgcn_readlane(tpv, 2);
            if (t == 0) tb = rt_now() - cC1;
#pragma unroll
            for (int q = 0; q < 16; ++q) PIN(ux[q]);
        }
        ATD(2, t == 42 && r == 0);
    }
    if (lane == 0) tim[2] = 0;
    LBAR();
    }
    if (lane == 0 && tim[0]) set_err((a.ws + WS_HOP));
}
__device__ __forceinline__ void ro_time(KA& a, unsigned char* lds, const int r, const unsigned pol) {
    const int lane = lane_id();
    const rsrc_t rhx = MKR(al2m((a.ws + WS_FR)), FR_USED);
    lds_u32* tim = (lds_u32*)(lds + RO_TIM + 256);
    const unsigned sx = ((my_side() ^ pol) & 1u) << 13, to = sx + M_STAMP + min(lane, 17) * 64;
#pragma unroll 1
    for (int layer = 0; layer < 2; ++layer) {
    const rsrc_t rfl = MKR((a.ws + WS_HOP) + layer * HOP_BYTES, F_ERR);
    const unsigned sb0 = (unsigned)layer * (20u << 14);
    unsigned tb = 0, cC1 = 0, cP = 0;
#pragma unroll 1
    for (int s = 0; s < RO_END; ++s) {
        LBAR();
        if (s < NSEG_) {
            if (s > 1) rt_wait(tb + cP + TT_TW);
            unsigned tw;
            for (unsigned sp = 0;; ++sp) { CBAR();
                tw = __builtin_amdgcn_raw_buffer_load_b32(rhx, to, sb0 + (unsigned)(s & 3) * (5u << 14) + (s >> 2) * 4, 16);
                if (__all(tw != 0xffffffffu)) break;
                __builtin_amdgcn_s_sleep(6);
                if (sp > SPIN_CAP_W || tim[0]) { tim[0] = 1; break; } }
            tt_step(tw, s, rt_now(), tb, cC1, cP);
            if (lane == 0) *reinterpret_cast<u32x4*>(lds + RO_TIM) = (u32x4){tb, cC1, cP, 0u};
            if (lane == 0 && s > 1) st16c(rfl, F_RD + (s * NR_ + r) * FLP, (u32x4){1u, min((unsigned)tim[1], 4095u), tim[2] == (unsigned)s ? 1u : 0u, 0u});
        }
        LBAR();
    }
    LBAR();
    }
}
__device__ __forceinline__ void ro_master(KA& a) {
    const int lane = lane_id();
    const rsrc_t rhx = MKR(al2m((a.ws + WS_FR)), FR_USED);
    int e1 = TT_E1 * 16, e2 = TT_E2 * 16, x1 = 0, x2 = 0; bool pr2 = true;
#pragma unroll 1
    for (int layer = 0; layer < 2; ++layer) {
    const rsrc_t rfl = MKR((a.ws + WS_HOP) + layer * HOP_BYTES, F_ERR);
    const unsigned sb0 = (unsigned)layer * (20u << 14);
#pragma unroll 1
    for (int s = 0; s < RO_END; ++s) {
        LBAR();
        const int xs = max(TT_XS - s * TT_XD, 0);
        const unsigned c1 = max(e1 >> 4, TT_E1 + TT_X1 - 2 * s) + TT_M1 + x1, c2 = c1 + (e2 >> 4) + TT_M2 + x2 + xs;
        const int q = max(s, 2);
        u32x4 f = ld16c(rfl, lane < 32 ? F_KM + ((q - 2) * 32 + lane) * FLP : F_UX + (q * 32 + lane - 32) * FLP), g = ld16c(rfl, F_RD + ((q - 1) * NR_ + lane) * FLP), k = ld16c(rfl, F_RD + ((q - 1) * NR_ + 64 + (lane & 15)) * FLP);
        if (lane < 4 && s + 1 < NSEG_) __builtin_amdgcn_raw_buffer_store_b32((lane & 2) ? c2 : c1, rhx, M_STAMP + 1024 + (lane & 2) * 32 + (lane & 1) * 8192, sb0 + (unsigned)((s + 1) & 3) * (5u << 14) + ((s + 1) >> 2) * 4, 16);
        SBAR();
        LBAR();
        SBAR();
        if (lane >= 32) { f.y = f.x & 4095u; f.z = (f.x >> 12) & 1u; }
        auto wmax = [&](unsigned v) { v = max(v, dppu<0x128>(v)); v = max(v, dppu<0x124>(v)); v = max(v, dppu<0x122>(v)); v = max(v, dppu<0x121>(v));
            return (int)max(max(__builtin_amdgcn_readlane(v, 0), __builtin_amdgcn_readlane(v, 16)), max(__builtin_amdgcn_readlane(v, 32), __builtin_amdgcn_readlane(v, 48))); };
        auto upd = [&](int& e, int m) { if (m > 0) { const int d = min(m * 16, e + 16 * 12) - e; e += d >> (d > 0 ? TT_UP : (s < 12 && !layer ? TT_DEC : TT_DN)); } };
        const bool rs = __any(f.z != 0u), r2 = __any((g.z | k.z) != 0u);
        if (s > 3) {
            if (!rs) upd(e1, wmax(lane < 32 ? f.y : (unsigned)max((int)f.y + TT_UM + TT_UX, 0)));
            if (!r2) upd(e2, wmax(max(g.y, k.y)));
            if (rs && !pr2) x2 += TT_BUMP; else if (r2 && !rs) x1 += TT_BUMP;
            if (!rs && !r2 && (s & TT_DCY) == 0) { x1 -= x1 > 0; x2 -= x2 > 0; }
        }
        pr2 = r2 || s < 3;
    }
    LBAR();
    if (!layer && lane == 0) { const rsrc_t rb = MKR((a.ws + WS_HOP) - 2048, 2048); const u32x2 t = ld8c(rb, 0); st8c(rb, 0, (u32x2){t.x + 1u, t.y}); }
    }
}
template <int TAIL> __device__ __forceinline__ void ro_out(KA& a, unsigned char* lds, const int r, const unsigned uo) {
    constexpr int wave = 2;
    const int lane = lane_id(), l15 = lane & 15, lg = lane >> 4, c = r >> 1, rt = r & 1, cr = (RROT & 2) ? c : 0;
    const rsrc_t rst = MKR((a.ws + WS_ST), ST_BYTES), rx = MKR((a.ws + WS_X), XI_BYTES), ro = MKR(a.out, S_ * D_ * 2);
    const unsigned xo = XI_X + rt * 20480 + (c >> 1) * 1024 + ((c & 1) * 2 + (lg >> 1)) * 256 + l15 * 16 + (lg & 1) * 8;
#pragma unroll 1
    for (int layer = 0; layer < 2; ++layer) {
    unsigned char* hop = (a.ws + WS_HOP) + layer * HOP_BYTES;
    const rsrc_t rfl = MKR(hop, F_ERR);
    unsigned tb = 0, cC1 = 0, cP = 1; int u3 = 0;
    u32x2 o = (u32x2){0u, 0u}, rr; float sq = 0.f; u32x4 p[40], um[8];
    auto put = [&](int s) {
        if (layer) __builtin_amdgcn_raw_buffer_store_b64(o, ro, ((rt * 16 + l15) * D_ + 16 * c + 4 * lg) * 2, s * (32 * D_ * 2), 0);
        else { __builtin_amdgcn_raw_buffer_store_b64(o, rx, xo, s * XI_PITCH, 16); if (lane < 32) __builtin_amdgcn_raw_buffer_store_b32(__float_as_uint(sq), rx, (rt * 40 + c) * 128 + l15 * 8 + lg * 4, s * XI_PITCH, 16); } };
    auto gate = [&](int s, int slp) { wait_set(rfl, F_PX + ((s + QA) * 64 + (lane & 31) * 2 + rt) * FLP, hop, slp); };
    auto req = [&](int s) { const int ln = lane_id();
        rr = __builtin_amdgcn_raw_buffer_load_b64(rx, xo, s * XI_PITCH, 16); SBAR();
#pragma unroll
        for (int q = 0; q < 40; ++q) { p[q] = __builtin_amdgcn_raw_buffer_load_b128(rst, ln * 16 + (q & 3) * 1024, ST_PX + ((s & (PR - 1)) * 2 + rt) * 40960 + (q < 32 ? ((q >> 2) + cr) & 7 : q >> 2) * 4096, 16); SBAR(); }
        CBAR(); };
    auto comp = [&](int s) { const int ln = lane_id();
        const unsigned char* xp = lds + (s & 3) * 32768 + ln * 16; const unsigned char* mp = lds + RO_UM + u3 * 8192 + ln * 16;
        u32x4 uf[8];
#pragma unroll
        for (int q = 0; q < 7; ++q) uf[q] = lds16(xp + (((q >> 2) + cr) & 7) * 4096 + (q & 3) * 1024);
        SBAR();
        f32x4 acc = (f32x4){lo2f(rr.x), hi2f(rr.x), lo2f(rr.y), hi2f(rr.y)};
#pragma unroll
        for (int q = 0; q < 40; ++q) { acc = dmf((TAIL == OLAG && q >= 32) ? um[q & 7] : uf[q & 7], p[q], acc); PIN(acc); SBAR();
            if (q + 7 < (TAIL == OLAG ? 32 : 40)) { uf[(q + 7) & 7] = q + 7 < 32 ? lds16(xp + ((((q + 7) >> 2) + cr) & 7) * 4096 + ((q + 7) & 3) * 1024) : lds16(mp + (q - 25) * 1024); SBAR(); } }
        o = (u32x2){pk2(acc[0], acc[1]), pk2(acc[2], acc[3])};
        sq = row_sums(o); };
    auto fin = [&](int s) { put(s); if (!layer) { VMDRAIN(); if (lane == 0) st16c(rfl, F_OF + (s * NR_ + r) * FLP, (u32x4){cP, 1u, 1u, tb}); } if (lane == 0 && r == 0) STAMP(300 + s - 56); };
    auto times = [&]() { const unsigned tpv = *reinterpret_cast<const unsigned*>(lds + RO_TIM + lane * 4); tb = __builtin_amdgcn_readlane(tpv, 0); cC1 = __builtin_amdgcn_readlane(tpv, 1); cP = __builtin_amdgcn_readlane(tpv, 2); };
    if (TAIL) {
        const int s = NSEG_ - 1 - OLAG + TAIL;
        for (int t = 0; t < NSEG_; ++t) {
            __builtin_amdgcn_s_barrier();
            if (TAIL == OLAG) __builtin_amdgcn_s_sleep(ASL);
            if (TAIL == OLAG && t > 0) { const unsigned sr = (unsigned)layer * (20u << 14) + (unsigned)((t + 2) & 3) * (5u << 14);
                __builtin_amdgcn_raw_buffer_store_b32(~0u, MKR(al2m(a.ws + WS_FR), FR_USED), (r < 64 ? 0 : 8192) + r * 128 + (lane >> 5) * 8192 + (lane & 31) * 4, sr, 16);
                __builtin_amdgcn_raw_buffer_store_b32(~0u, MKR(al2m(a.ws + WS_FR), FR_USED), (r % 5 == 0 && lane < 2 && t + 2 < NSEG_) ? M_STAMP + (r / 5) * 64 + lane * 8192 : OOB, sr + ((t + 2) >> 2) * 4, 16); }
            if (TAIL == OLAG && (t >= 2 || layer)) {
#pragma unroll
                for (int q = 0; q < 4; ++q) st16s((u32x4){~0u, ~0u, ~0u, ~0u}, MKR(al2m(a.ws + WS_FR), FR_USED), uo + q * 1024, (((t + 2) & 3) << 14) + rt * 4096); }
            if (TAIL == OLAG) VMDRAIN();
            __builtin_amdgcn_s_barrier(); }
        CBAR();
        u3 = s % 3;
        if (TAIL == OLAG) {
            const rsrc_t rhx = MKR(al2m((a.ws + WS_FR)), FR_USED);
            times(); rt_wait(tb + cC1);
            for (unsigned sp = 0;; ++sp) { unsigned m = 0; CBAR();
#pragma unroll
                for (int q = 0; q < 8; ++q) um[q] = __builtin_amdgcn_raw_buffer_load_b128(rhx, uo + (q & 3) * 1024, ((s & 3) << 14) + (q >> 2) * 4096, 16);
#pragma unroll
                for (int q = 0; q < 8; ++q) m = max(m, umax4(um[q]));
                if (__all(m != 0xffffffffu)) break;
                if (sp > SPIN_CAP_W) { set_err(hop); break; }
                __builtin_amdgcn_s_sleep(8); }
            rt_wait(tb + TT_TL);
        }
        gate(s, 0); req(s); comp(s); fin(s);
    } else {
#pragma unroll 1
    for (int t = 0; t < NSEG_; ++t) {
        const int s = t - OLAG;
        ATV;
        LBAR();
        if (s >= 0) {
            const unsigned to_ = max((int)(tb + cP + TT_BD - rt_now()), 0) + rt_now() + TT_OD;
            gate(s, 0);
            if (s > 0) put(s - 1);
            rt_wait(to_);
            AT(0);
            req(s);
            AT(1);
            if (lane == 0 && r == 0 && s > 56) STAMP(300 + s - 57);
        }
        LBAR();
        times();
        if (!layer && s > 0) { PIN(rr); if (lane == 0) st16c(rfl, F_OF + ((s - 1) * NR_ + r) * FLP, (u32x4){cP, 1u, 1u, tb}); }
        if (s >= 0) { __builtin_amdgcn_s_sleep(OSL); comp(s); u3 = u3 == 2 ? 0 : u3 + 1; AT(2); }
        ATD(2, t == 42 && r == 0);
    }
    fin(NSEG_ - 1 - OLAG);
    }
    LBAR();
    }
}
__device__ __forceinline__ void ro_fill(KA& a, unsigned char* lds, const int r) {
    constexpr int wave = 3;
    const int lane = lane_id();
#pragma unroll 1
    for (int layer = 0; layer < 2; ++layer) {
    unsigned char* hop = (a.ws + WS_HOP) + layer * HOP_BYTES;
    const rsrc_t rfl = MKR(hop, F_ERR);
    unsigned tb = 0, cC1 = 0, cP = 0;
#pragma unroll 1
    for (int t = 0; t < RO_END; ++t) {
        ATV;
        if (t < NSEG_) {
            if (t == 0) wait_set(rfl, F_XP + (r % NXP_) * FLP, hop, 4);
            if (t > 1) rt_wait(tb + cC1 + TT_FP);
            wait_set(rfl, (r >= 40 && r < 64 && t > 2) ? F_UW + (t * 8 + (lane & 7)) * FLP : (r < 64 || (t == 0 && lane >= 32)) ? F_UX + (t * 32 + (lane & 31)) * FLP : F_PX + (t * 64 + (lane & 31) * 2 + ((r >> 3) & 1)) * FLP, hop, t > 2 ? 0 : 1);
        }
        LBAR();
        LBAR();
        if (t < NSEG_) { const unsigned tpv = *reinterpret_cast<const unsigned*>(lds + RO_TIM + lane * 4); tb = __builtin_amdgcn_readlane(tpv, 0); cC1 = __builtin_amdgcn_readlane(tpv, 1); cP = __builtin_amdgcn_readlane(tpv, 2); }
        ATD(2, t == 42 && r == 0);
    }
    LBAR();
    if (lane == 0) STAMP(512 + blockIdx.x);
    }
}
__device__ __forceinline__ void ro_part(KA& a, unsigned char* lds, const int r, const int wave) {
    if (wave == 0) { lds_u32* tim = (lds_u32*)(lds + RO_TIM + 256); tim[0] = 0; tim[1] = 0; tim[2] = 0; }
    if (wave == 0) { __builtin_amdgcn_s_setprio(3); ro_state(a, lds, r, page_pol(a)); }
    else if (wave == 1) ro_time(a, lds, r, page_pol(a));
    else if (wave == 2) ro_out<0>(a, lds, r, 0u);
    else if (wave == 3) { init_wait(a); ro_fill(a, lds, r); }
    else if (wave == 4) ro_out<1>(a, lds, r, 0u);
    else if (wave == 7) ro_out<2>(a, lds, r, 0u);
    else if (wave == 6) { const unsigned pol = page_pol(a); ro_out<3>(a, lds, r, um_base(um_sides(a), pol, r >> 1) + lane_id() * 16); }
    else {
        if (r == 1) { const rsrc_t rfl = MKR(a.ws + WS_HOP, F_ERR); wait_set(rfl, F_QK + lane_id() * FLP, a.ws + WS_HOP, 1); wait_set(rfl, F_QK + (64 + lane_id()) * FLP, a.ws + WS_HOP, 1); if (lane_id() < 8) st4c(rfl, F_IN + lane_id() * FLP, 1u); }
        const rsrc_t rz = MKR((a.ws + WS_HOP) + HOP_BYTES, HOP_BYTES);
        for (unsigned o = r * 1024 + lane_id() * 16; o < HOP_BYTES; o += NR_ * 1024) st16c(rz, o, (u32x4){0u, 0u, 0u, 0u});
        VMDRAIN();
        if (r == 0) ro_master(a); else for (int q = 0; q < 4 * NSEG_ + 2; ++q) __builtin_amdgcn_s_barrier(); }
}

#define QE 3
#define XKB 4
#define XSL 0
#define VMKNOWN5() __builtin_amdgcn_s_waitcnt(0x0F75)
#define PIM 60
#define XNP 3
constexpr int XB = 40960;
struct XWt { u32x4 w[2][20]; f32x4 cs[2]; };
template <int NB, int NR, class G, class H> __device__ __forceinline__ void x_load(XWt& x, unsigned char* lds, const int wave, const int pw, G&& src, H&& more) {
    const unsigned ldsb = LDSB(), lane = lane_id(), l15 = lane & 15, lg = lane >> 4;
    load_b<NB, 4, NR, 0, 0>(ldsb, wave, src, [&](int rd, int base) { if (rd >= 10) more(rd, base); else if (pw >= 0) {
#pragma unroll
        for (int u = 0; u < 2; ++u)
#pragma unroll
            for (int k = 0; k < 2; ++k) x.w[u][(2 * rd + k) % 20] = ld_frag(lds + base + (pw >> 1) * 8192, 32 * k + 8 * lg, 4, (pw & 1) * 2 + u, l15); } }, [](int) {});
    if (pw >= 0) {
        const u32x4 one = (u32x4){0x3F803F80u, 0x3F803F80u, 0x3F803F80u, 0x3F803F80u};
        x.cs[0] = Z4; x.cs[1] = Z4;
#pragma unroll
        for (int k = 0; k < 20; ++k) { x.cs[0] = dmf(x.w[0][k], one, x.cs[0]); x.cs[1] = dmf(x.w[1][k], one, x.cs[1]); }
        PIN(x.cs[0]); PIN(x.cs[1]);
    }
}
__device__ __forceinline__ void c_load(u32x2 (&sv)[4], rsrc_t rx, int t) {
    const int lane = lane_id();
    asm volatile("s_nop 4\n\tbuffer_load_dwordx2 %0, %4, %5, %6 offen sc1\n\tbuffer_load_dwordx2 %1, %4, %5, %6 offen offset:128 sc1\n\tbuffer_load_dwordx2 %2, %4, %5, %6 offen offset:256 sc1\n\tbuffer_load_dwordx2 %3, %4, %5, %6 offen offset:384 sc1"
        : "=&v"(sv[0]), "=&v"(sv[1]), "=&v"(sv[2]), "=&v"(sv[3]) : "v"((lane >> 4) * 512 + (lane & 15) * 8), "s"(rx), "s"(t * XI_PITCH + XI_S8) : "memory");
}
__device__ __forceinline__ void c_got(u32x2 (&sv)[4]) { asm volatile("" : "+v"(sv[0]), "+v"(sv[1]), "+v"(sv[2]), "+v"(sv[3])); }
__device__ __forceinline__ void c_put(const u32x2 (&sv)[4], unsigned char* dst) {
    float s[2];
#pragma unroll
    for (int q = 0; q < 2; ++q) s[q] = adds(adds(__uint_as_float(sv[0][q]), __uint_as_float(sv[1][q])), adds(__uint_as_float(sv[2][q]), __uint_as_float(sv[3][q])));
    asm volatile("s_nop 1" : "+v"(s[0]), "+v"(s[1]));
    const auto a1 = __builtin_amdgcn_permlane16_swap(__float_as_uint(s[0]), __float_as_uint(s[0]), false, false), a2 = __builtin_amdgcn_permlane16_swap(__float_as_uint(s[1]), __float_as_uint(s[1]), false, false);
    float m, i; ln_consts(adds(__uint_as_float(a1[0]), __uint_as_float(a1[1])), __uint_as_float(a2[0]) + __uint_as_float(a2[1]), m, i);
    const int lane = lane_id();
    *reinterpret_cast<f32x2*>(dst + (lane >> 5) * 128 + (lane & 15) * 8) = (f32x2){-m, i};
}
template <class F> __device__ __forceinline__ void x_proj(const XWt& x, const unsigned char* xb, const unsigned char* cb, u32x4 (&o)[2], F&& mid) {
    const int lane = lane_id();
    unsigned l16 = lane * 16, c8 = (lane & 15) * 8; PIN(l16); PIN(c8);
    const unsigned char* xp = xb + l16;
    f32x4 acc[2][2] = {{Z4, Z4}, {Z4, Z4}};
    u32x4 xf[3][2]; f32x2 ct[2];
#pragma unroll
    for (int k = 0; k < 2; ++k) { xf[k][0] = lds16(xp + k * 1024); SBAR(); xf[k][1] = lds16(xp + 20480 + k * 1024); SBAR(); }
#pragma unroll
    for (int k = 0; k < 20; ++k) {
#pragma unroll
        for (int j = 0; j < 4; ++j) { acc[j >> 1][j & 1] = dmf(x.w[j >> 1][k], xf[k % 3][j & 1], acc[j >> 1][j & 1]); PIN(acc[j >> 1][j & 1]); SBAR();
            if ((j == 1 || j == 2) && k + 2 < 20) { xf[(k + 2) % 3][j - 1] = lds16(xp + (j - 1) * 20480 + (k + 2) * 1024); SBAR(); }
            if (j == 1 && k == 18) { ct[0] = *reinterpret_cast<const f32x2*>(cb + c8); ct[1] = *reinterpret_cast<const f32x2*>(cb + c8 + 128); SBAR(); } }
        mid(k, l16);
    }
#pragma unroll
    for (int rt = 0; rt < 2; ++rt)
#pragma unroll
        for (int q = 0; q < 4; ++q) { const int u = q >> 1, e = 2 * (q & 1);
            o[rt][q] = pk2(__builtin_fmaf(ct[rt][0], x.cs[u][e], acc[u][rt][e]) * ct[rt][1], __builtin_fmaf(ct[rt][0], x.cs[u][e + 1], acc[u][rt][e + 1]) * ct[rt][1]); }
}
__device__ __forceinline__ void xp_part(KA& a, unsigned char* lds, const int j, const int wave) {
    const int lane = lane_id(), tid = wave * 64 + lane;
    const unsigned ldsb = LDSB();
    const int hp = j / 3, kind = j - 3 * hp;
    XWt x;
    {
        const rsrc_t rk = MKR(psel(a.wk, a.wv, kind >> 1), D_ * HE_ * 2), rv = MKR(a.wv, D_ * HE_ * 2);
        const unsigned row = 8 * wave + (lane >> 3), vo = row * (HE_ * 2) + ((lane & 7) ^ (wsw(row) << 1)) * 16 + hp * 768;
        x_load<4, 10>(x, lds, wave, wave, [&](int rd, int q, unsigned dst) { dma16(q < 3 ? rk : rv, vo, rd * (64 * HE_ * 2) + (kind < 2 ? kind * 384 + (q % 3) * 128 : (q >> 1) * 384 + 128 + (q & 1) * 128), dst); }, [](int, int) {});
    }
    const unsigned oo = kind < 2 ? (wave < 6 ? ST_KX + wave * 1024 : ST_VX + (wave - 6) * 1024) + (2 * hp + kind) * 12288 : ST_VX + (2 * hp + (wave >> 2)) * 12288 + (2 + (wave & 3)) * 1024;
    const rsrc_t rst = MKR((a.ws + WS_ST), ST_BYTES), rx = MKR((a.ws + WS_X), XI_BYTES);
    const int wr_ = (wave + (blockIdx.x >> 3) * ROT) & 7;
#pragma unroll 1
    for (int layer = 0; layer < 2; ++layer) {
    unsigned char* hop = (a.ws + WS_HOP) + layer * HOP_BYTES;
    const rsrc_t rfl = MKR(hop, F_ERR);
    auto piece = [&](int t, int b, int k, unsigned l16) { const unsigned o = (wr_ * 5 + k) * 1024; dma16c(rx, l16, (unsigned)t * XI_PITCH + XI_X + o, ldsb + b * XB + o); };
    auto x_dma = [&](int t, int b) {
#pragma unroll
        for (int k = 0; k < 5; ++k) piece(t, b, k, lane * 16); };
    if (tid == 0 && j == 0) STAMP(7);
    if (wave == 2) { if (!layer) init_wait(a); wait_set(rfl, F_AF + (lane & 15) * FLP, hop, XSL); }
    LBAR();
    if (tid == 0 && j == 0) STAMP(8);
    { u32x2 sv[4]; if (wave == 1) c_load(sv, rx, 0);
      x_dma(0, 0);
      if (wave == 1) { VMW(5); c_got(sv); c_put(sv, lds + 3 * XB); } }
    VMKNOWN(); LBAR();
    unsigned tnext = rt_now(); int b = 0;
#pragma unroll 1
    for (int t = 0; t < NSEG_; ++t) {
        ATV;
        AT(0);
        u32x4 fl; u32x2 sv[4];
        const unsigned fo = (lane < 16 && t >= XLD) ? F_KM + ((t - XLD) * 32 + 2 * hp) * FLP : F_AF + (min(t + (lane < 16 ? 1 : 2), NSEG_ - 1) * 16 + (lane & 15)) * FLP;
        if (wave == 2) { if (t > XLD) rt_wait(tnext);
            asm volatile("buffer_load_dwordx4 %0, %1, %2, 0 offen sc1" : "=&v"(fl) : "v"(fo), "s"(rfl) : "memory"); }
        const int bn = b == 0 ? 2 : b - 1;
        u32x4 o[2];
        x_proj(x, lds + b * XB, lds + 3 * XB + (t & 1) * 256, o, [&](int k, unsigned l16) {
            if (k == XKB) { if (wave == 2) VMW(1); else VMDRAIN();
                __builtin_amdgcn_s_barrier(); CBAR();
                if (wave == 0 && t > 0 && l16 == 0) { st4c(rfl, F_XP + ((t - 1) * NXP_ + j) * FLP, 1u); if (j == 0 && t < 4) STAMP(t); }
                AT(2); SBAR(); }
            if (k == XKB + 3) {
                if (wave == 2) { VMDRAIN(); asm volatile("" : "+v"(fl));
                    if (__builtin_expect(__all(fl.x != 0u), 1)) tnext = __builtin_amdgcn_readfirstlane(fl.w) + XNP * __builtin_amdgcn_readfirstlane(fl.x) + TT_KD; else { wait_timed(rfl, fo, hop, tnext, XNP, TT_KD, t < XLD); VMKNOWN(); } }
                __builtin_amdgcn_s_barrier(); CBAR();
                if (wave == 1) c_load(sv, rx, min(t + 1, NSEG_ - 1));
                if (t == 0) x_dma(1, 1);
                SBAR(); }
            if (k > XKB + 3 && k <= XKB + 13 && ((k - XKB) & 1) == 0 && t + 2 < NSEG_) { piece(t + 2, bn, (k - XKB - 4) >> 1, l16); SBAR(); } });
        const int ln = lane_id();
        st16s(o[0], rst, ln * 16, oo + (t & (KXR - 1)) * (32 * 12288)); st16s(o[1], rst, ln * 16, oo + (t & (KXR - 1)) * (32 * 12288) + 6144);
        AT(1);
        if (wave == 1) { if (t + 2 < NSEG_) VMW(7); else VMW(2); c_got(sv); c_put(sv, lds + 3 * XB + ((t + 1) & 1) * 256); asm volatile("s_waitcnt lgkmcnt(0)" ::: "memory"); }
        if (t == 0) { VMW(7); LBAR(); }
        ATD(3, t == 48 && j == 0);
        b = b == 2 ? 0 : b + 1;
    }
    VMDRAIN(); LBAR();
    if (tid == 0) st4c(rfl, F_XP + ((NSEG_ - 1) * NXP_ + j) * FLP, 1u);
    if (tid == 0) STAMP(512 + blockIdx.x);
    }
}

template <int AT_> __device__ __forceinline__ void xq_run(KA& a, unsigned char* lds, const int h, const int wave) {
    constexpr bool ATT = AT_ >= 0; constexpr int qt = ATT ? AT_ : 0;
    const int lane = lane_id(), tid = wave * 64 + lane;
    const unsigned ldsb = LDSB();
    constexpr int L_C = 2 * XB, L_Q = L_C + 512, L_VX = L_Q + (QA + 1) * 12288;
    rt_wait(rt_now() + XQD);
    XWt x; u32x4 wo[4][6];
    {
        const rsrc_t rq = MKR(a.wq, D_ * HE_ * 2), ro = MKR(a.wo, HE_ * D_ * 2);
        const unsigned row = 8 * wave + (lane >> 3), ch = ((lane & 7) ^ (wsw(row) << 1)) * 16, vo = row * (HE_ * 2) + ch + h * 384, oo = (h * 192 + row) * (D_ * 2) + ch;
        x_load<3, 12>(x, lds, wave, ATT ? -1 : wave - 2, [&](int rd, int q, unsigned dst) { if (rd < 10) dma16(rq, vo, rd * (64 * HE_ * 2) + q * 128, dst); else dma16(ro, oo, q * (64 * D_ * 2) + (rd - 2) * 128, dst); },
            [&](int rd, int base) { if (rd == 10 + AT_) {
#pragma unroll
                for (int q = 0; q < 24; ++q) wo[q / 6][q % 6] = ld_frag(lds + base + ((q % 6) >> 1) * 8192, (q & 1) * 32 + 4 * (lane >> 4), 16, q / 6, lane & 15); } });
    }
    const rsrc_t rst = MKR((a.ws + WS_ST), ST_BYTES), rx = MKR((a.ws + WS_X), XI_BYTES);
    const int wr_ = (wave - 2 + (blockIdx.x >> 3)) % 6, xj = xp_of(h), xj2 = 3 * (h >> 1) + 2;
    const int l15 = lane & 15, lg = lane >> 4;
    const bool cm[4] = {4 * lg <= l15, 4 * lg + 1 <= l15, 4 * lg + 2 <= l15, 4 * lg + 3 <= l15};
#pragma unroll 1
    for (int layer = 0; layer < 2; ++layer) {
    unsigned char* hop = (a.ws + WS_HOP) + layer * HOP_BYTES;
    const rsrc_t rfl = MKR(hop, F_ERR);
    auto piece = [&](int t, int k, unsigned l16) { const unsigned o = min(wr_ * 7 + k, 39) * 1024; dma16c(rx, l16, (unsigned)t * XI_PITCH + XI_X + o, ldsb + (t & 1) * XB + o); };
    if (wave == 0) { if (!layer) init_wait(a); wait_set(rfl, F_AF + (lane & 31) * FLP, hop, 2); }
    LBAR();
    if (!ATT) {
#pragma unroll
        for (int k = 0; k < 7; ++k) piece(0, k, lane * 16); }
    u32x4 kf[2][6];
    if (AT_ == 0) { u32x2 sv[4]; c_load(sv, rx, 0); VMDRAIN(); c_got(sv); c_put(sv, lds + L_C); }
    VMKNOWN();
    unsigned tnext = rt_now(); int qs = 0; bool rdy = false, owe = false;
#pragma unroll 1
    for (int i = 0; i < NSEG_ + QA; ++i) {
        ATV;
        if (i <= NSEG_) LBAR();
        AT(0);
        if (!ATT) {
            if (i < NSEG_) {
                u32x4 o[2];
                x_proj(x, lds + (i & 1) * XB, lds + L_C + (i & 1) * 256, o, [&](int k, unsigned l16) { if ((k & 1) && k < 14 && i + 1 < NSEG_) { piece(i + 1, k >> 1, l16); SBAR(); } });
                unsigned char* qp = lds + L_Q + qs * 12288 + (wave - 2) * 1024 + lane_id() * 16;
                *reinterpret_cast<u32x4*>(qp) = o[0]; *reinterpret_cast<u32x4*>(qp + 6144) = o[1];
                AT(1);
                VMDRAIN();
            }
        } else {
            const int t = i - QA;
            const unsigned fa = F_AF + (min(i + 2, NSEG_ - 1) * 16 + l15) * FLP, fx = F_XP + ((lane & 1) ? xj : xj2) * FLP, ub = ST_U + (((i & (URING - 1)) * 40 + 32 + 4 * qt) * 32 + h) * 1024;
            unsigned l16 = lane_id() * 16; PIN(l16);
            auto v_dma = [&]() { dma16cn<6>(rst, l16, ST_VX + (((i + 1) & (KXR - 1)) * 32 + h) * 12288 + qt * 6144, ldsb + L_VX + ((i + 1) & 1) * 12288 + qt * 6144); };
            auto next = [&]() {
                const unsigned kb = ST_KX + (((t + 1) & (KXR - 1)) * 32 + h) * 12288;
#pragma unroll
                for (int m = 0; m < (qt ? 12 : 6); ++m) asm volatile("buffer_load_dwordx4 %0, %1, %2, %3 offen offset:%4 sc1" : "=&v"(kf[m / 6][m % 6]) : "v"(l16), "s"(rst), "s"(kb + (m >> 2) * 4096), "i"((m & 3) * 1024) : "memory"); };
            auto got = [&]() {
#pragma unroll
                for (int m = 0; m < (qt ? 12 : 6); ++m) PIN(kf[m / 6][m % 6]); };
            u32x4 vf[12]; u32x2 sv[4];
            auto u_two = [&](int u0) {
                f32x4 acc[2][2] = {{Z4, Z4}, {Z4, Z4}};
#pragma unroll
                for (int j = 0; j < 24; ++j) { const int k = j >> 2, u = (j >> 1) & 1, r = j & 1; acc[u][r] = dmf(vf[r * 6 + k], wo[u0 + u][k], acc[u][r]); }
                SBAR();
#pragma unroll
                for (int u = 0; u < 2; ++u) st16s((u32x4){pk2(acc[u][0][0], acc[u][1][0]), pk2(acc[u][0][2], acc[u][1][2]), pk2(acc[u][0][1], acc[u][1][1]), pk2(acc[u][0][3], acc[u][1][3])}, rst, l16, ub + (u0 + u) * 32768);
                SBAR(); };
            if (t < 0) {
                if (!qt) { c_load(sv, rx, i + 1); VMDRAIN(); c_got(sv); c_put(sv, lds + L_C + ((i + 1) & 1) * 256); }
                wait_set(rfl, fx + i * (NXP_ * FLP), hop, 1);
#pragma unroll
                for (int m = 0; m < 12; ++m) vf[m] = __builtin_amdgcn_raw_buffer_load_b128(rst, l16 + (m & 3) * 1024, ST_VX + (i * 32 + h) * 12288 + (m >> 2) * 4096, 16);
                SBAR();
                u_two(0); u_two(2);
                VMDRAIN();
                if (l16 == 0) st4c(rfl, F_PX + (i * 64 + h * 2 + qt) * FLP, 1u);
                wait_set(rfl, (lane < 16 && t == -1) ? fx + (i + 1) * (NXP_ * FLP) : fa, hop, 1); v_dma(); next(); VMDRAIN(); got();
            } else {
                const unsigned char* qp = lds + L_Q + (qs == QA ? 0 : qs + 1) * 12288 + qt * 6144 + l16;
                u32x4 qf[6], km[6], fl;
                const bool uu = i < NSEG_;
#pragma unroll
                for (int m = 0; m < 12; ++m) vf[m] = lds16(lds + L_VX + (i & 1) * 12288 + l16 + m * 1024);
                SBAR();
                if (!qt) c_load(sv, rx, min(i + 1, NSEG_ - 1));
                const bool er = !rdy;
                if (uu && !er) { v_dma(); u_two(0); VMW(8); } else { if (uu) { u_two(0); VMW(2); } else VMDRAIN(); }
                got();
                if (!qt) c_got(sv);
                if (l16 == 0 && owe) st4c(rfl, F_PX + ((i - 1) * 64 + h * 2 + qt) * FLP, 1u);
                AT(1);
                const unsigned fo = lane < 16 ? F_KM + (t * 32 + h) * FLP : (lane < 32 ? fx + min(i + 2, NSEG_ - 1) * (NXP_ * FLP) : fa);
                const int dl = t + 2 == NSEG_ ? TT_QN : TT_QD;
#pragma unroll
                for (int m = 0; m < 6; ++m) qf[m] = lds16(qp + m * 1024);
                rt_wait(tnext);
                AT(5);
                asm volatile("buffer_load_dwordx4 %0, %1, %2, 0 offen sc1" : "=&v"(fl) : "v"(fo), "s"(rfl) : "memory");
                f32x4 sc[3] = {Z4, Z4, Z4};
#pragma unroll
                for (int m = 0; m < 6; ++m) { sc[0] = dmf(kf[0][m], qf[m], sc[0]); if (qt) sc[1] = dmf(kf[1][m], qf[m], sc[1]); }
                float s2[10]; float mx = -3.0e38f, scl = 0.07216878364870322f;
                auto cvt = [&](int q) {
                    const unsigned r0 = pk2(sc[q >> 1][2 * (q & 1)], sc[q >> 1][2 * (q & 1) + 1]), r1 = pk2(muls(lo2f(r0), scl), muls(hi2f(r0), scl));
                    s2[2 * q] = (q >> 1) != qt || cm[2 * (q & 1)] ? lo2f(r1) : -3.0e38f; s2[2 * q + 1] = (q >> 1) != qt || cm[2 * (q & 1) + 1] ? hi2f(r1) : -3.0e38f;
                    mx = fmaxf(mx, fmaxf(s2[2 * q], s2[2 * q + 1])); PIN(s2[2 * q]); PIN(s2[2 * q + 1]); };
#pragma unroll
                for (int q = 0; q < 4; ++q) { if (qt == 0 && q >= 2) { s2[2 * q] = 0.f; s2[2 * q + 1] = 0.f; } else cvt(q); }
                PIN(mx);
                if (!qt && uu) c_put(sv, lds + L_C + ((i + 1) & 1) * 256);
                asm volatile("s_waitcnt vmcnt(0)" : "+v"(fl) :: "memory");
                const unsigned long long fz = __ballot(fl.x == 0u);
                rdy = !(fz & 0xffff0000ull);
                if (!(fz & ~0xffff0000ull)) tnext = __builtin_amdgcn_readfirstlane(fl.w) + 2u * __builtin_amdgcn_readfirstlane(fl.x) + dl; else wait_timed(rfl, lg == 1 ? fa : fo, hop, tnext, 2, dl, t + 1 == NSEG_ || t < QE);
                AT(2);
                if (l16 == 0 && h == 0 && qt == 0 && t >= 56) STAMP(360 + t - 56);
                const unsigned mo = (lg * 8 + (((l15 >> 2) * 2 + (l15 & 1) + (lg & 1) * 4) & 7)) * 16;
#pragma unroll
                for (int m = 0; m < 6; ++m) asm volatile("buffer_load_dwordx4 %0, %1, %2, %3 offen offset:%4 sc1" : "=&v"(km[m]) : "v"(mo), "s"(rst), "s"(ST_KM + ((t & (PR - 1)) * 32 + h) * 3072), "i"(m * 512) : "memory");
                if (uu) { u_two(2); VMW(2); } else VMDRAIN();
                asm volatile("" : "+v"(km[0]), "+v"(km[1]), "+v"(km[2]), "+v"(km[3]), "+v"(km[4]), "+v"(km[5]));
#pragma unroll
                for (int m = 0; m < 6; ++m) sc[2] = dmf(km[m], qf[m], sc[2]);
                cvt(4);
                mx = xg_max(mx) * -1.4426950408889634f;
                float sum = 0.f, su1 = 0.f;
#pragma unroll
                for (int q = 0; q < 10; ++q) { if (qt == 0 && q >= 4 && q < 8) continue; s2[q] = __builtin_amdgcn_exp2f(__builtin_fmaf(s2[q], 1.4426950408889634f, mx)); if (q & 1) su1 += s2[q]; else sum += s2[q]; }
                float inv = __builtin_amdgcn_rcpf(xg_sum(sum + su1));
                asm volatile("s_nop 0" : "+v"(inv));
#pragma unroll
                for (int q = 0; q < 10; ++q) { if (qt == 0 && q >= 4 && q < 8) continue; s2[q] = muls(s2[q], inv); }
                const unsigned pb = ST_PX + ((t & (PR - 1)) * 2 + qt) * 40960;
                st16s((u32x4){pk2(s2[0], s2[4]), pk2(s2[2], s2[6]), pk2(s2[1], s2[5]), pk2(s2[3], s2[7])}, rst, l16, pb + h * 1024);
                __builtin_amdgcn_raw_buffer_store_b32(pk2(s2[8], s2[9]), rst, l15 * 16 + (lg & 1) * 8 + (lg >> 1) * 4, pb + 32768 + h * 256, 16);
                AT(3);
                owe = !(t >= PIM || er);
                if (!owe) { VMDRAIN(); if (l16 == 0) st4c(rfl, F_PX + (i * 64 + h * 2 + qt) * FLP, 1u); }
                if (er) { if (!rdy) wait_set(rfl, fx + min(i + 1, NSEG_ - 1) * (NXP_ * FLP), hop, 0); v_dma(); }
                next();
                if (er) VMW(qt ? 12 : 6);
                AT(4);
                if (l16 == 0 && h == 0 && qt == 1 && t >= 56) STAMP(340 + t - 56);
            }
        }
        ATD(4, i == 44 && h == 0);
        qs = qs == QA ? 0 : qs + 1;
    }
    VMDRAIN(); LBAR();
    if (tid == 0) STAMP(512 + blockIdx.x);
    }
}
__device__ __forceinline__ void xq_part(KA& a, unsigned char* lds, const int h, const int wave) {
    if (wave < 2) __builtin_amdgcn_s_setprio(1);
    if (wave == 0) xq_run<0>(a, lds, h, wave); else if (wave == 1) xq_run<1>(a, lds, h, wave); else xq_run<-1>(a, lds, h, wave);
}

__device__ __forceinline__ void q_part(KA& a, unsigned char* lds, const int rb, const int g, const int wave) {
    constexpr int STG = 26624, NS = 5, layer = 0;
    const int lane = lane_id(), l15 = lane & 15, lg = lane >> 4, rp = wave & 1, cg = wave >> 1;
    const unsigned ldsb = LDSB();
    const rsrc_t rw = MKR(a.wqkv, D_ * 1920 * 2), rx = MKR(a.x0, S_ * D_ * 2);
    unsigned vo[4];
    vo[0] = (rb * 128 + wave * 16 + (lane >> 2)) * (D_ * 2) + ((lane ^ -lg) & 3) * 16;
#pragma unroll
    for (int u = 0; u < 3; ++u) { const int p = wave + 8 * u, c = p * 16 + (lane >> 2), row = c / 9, e = c - row * 9, sg = e / 3;
        vo[u + 1] = p < 18 ? row * 3840 + sg * 1280 + ((160 * g) & ~63) + (e - sg * 3) * 64 + ((lane & 3) ^ ((row >> 2) & 2)) * 16 : OOB; }
    const unsigned m3 = wave < 2 ? ~0u : 0u, d3 = wave < 2 ? 24576 : NS * STG;
    const int rot = (rb + (g & 1) * 5) % 10;
    auto piece = [&](int st, unsigned sl, int j) { dma16(j ? rw : rx, vo[j], st * (j ? 32 * 3840 : 64), ldsb + wave * 1024 + (j < 3 ? sl + j * 8192 : (sl & m3) + d3)); };
#pragma unroll 1
    for (int st = 0; st < NS - 1; ++st) {
#pragma unroll
        for (int j = 0; j < 4; ++j) piece((st + 2 * rot) % 20, st * STG, j); }
    if (wave == 0 && lane == 0 && rb == 0 && g == 0) STAMP(17);
    f32x4 acc[4][4], gr = Z4, cs[4] = {Z4, Z4, Z4, Z4};
#pragma unroll
    for (int i = 0; i < 16; ++i) acc[i >> 2][i & 3] = Z4;
    u32x4 one = (u32x4){0x3F803F80u, 0x3F803F80u, 0x3F803F80u, 0x3F803F80u}; PIN(one);
    unsigned wp[4]; const unsigned xp = (rp * 4 + cg) * 1024 + l15 * 64 + ((lg ^ -(l15 >> 2)) & 3) * 16;
#pragma unroll
    for (int u = 0; u < 4; ++u) { const int T = min(4 * u + cg, 14); wp[u] = 8192 + (8 * lg + (l15 >> 2)) * 576 + (((T / 5) * 192 + (T % 5 + (g & 1)) * 32) ^ ((lg & 1) * 32)) + (l15 & 3) * 8; }
    u32x4 xf[2][4], wf[2][4];
    auto rw_ = [&](int kk, unsigned sl, int u) { const unsigned char* p = lds + sl + wp[u]; const u32x2 x0 = ldstr(p), x1 = ldstr(p + 4 * 576); wf[kk][u] = (u32x4){x0.x, x0.y, x1.x, x1.y}; SBAR(); };
    auto rx_ = [&](int kk, unsigned sl, int i) { xf[kk][i] = lds16(lds + sl + (xp ^ (i * 1024))); SBAR(); };
    VMW(12);
    LBAR();
    if (wave == 0 && lane == 0 && rb == 0 && g == 0) STAMP(14);
#pragma unroll
    for (int j = 0; j < 8; ++j) { if (j < 4) rx_(0, 0, j); else rw_(0, 0, j - 4); }
    unsigned sn = STG, sf = (NS - 1) * STG;
    int cf = (rot + 2) % 10; const int cb = (rb + 10 - rot) % 10 + (rb < 10 ? 0 : 16);
#pragma unroll 1
    for (int ch = 0; ch < 10; ++ch) {
#pragma unroll
        for (int kk = 0; kk < 2; ++kk) {
            AT(3 * kk);
            VMW(8);
            __builtin_amdgcn_s_barrier(); CBAR();
            AT(3 * kk + 1);
            gr = dmf(xf[kk][0], xf[kk][0], gr); PIN(gr); SBAR();
#pragma unroll
            for (int j = 0; j < 16; ++j) { const int u = j >> 2, i = j & 3;
                if (j == 12 && cg == 3) wf[kk][3] = one;
                acc[i][u] = dmf(wf[kk][u], xf[kk][i], acc[i][u]); PIN(acc[i][u]); SBAR();
                if (!(j & 1)) { if (j < 8) rx_(kk ^ 1, sn, j >> 1); else rw_(kk ^ 1, sn, (j >> 1) - 4); }
                if (i == 1) { piece(2 * cf + kk, sf, u); SBAR(); } }
            if (rp) { cs[0] = dmf(wf[kk][2], one, cs[0]); cs[1] = dmf(wf[kk][3], one, cs[1]); } else { cs[0] = dmf(wf[kk][0], one, cs[0]); cs[1] = dmf(wf[kk][1], one, cs[1]); }
            if (__builtin_expect(ch == cb && kk == rp, 0)) {
#pragma unroll
                for (int u = 0; u < 4; ++u) { const int T = 4 * u + cg; st16s(wf[kk][u], MKR(a.ws + WS_WF_AQKV, D_ * 1920 * 2), T < 15 ? lane * 16 : OOB, (((T / 5) * 40 + 5 * g + T % 5) * 20 + 2 * rb + kk) * 1024); } }
            SBAR();
            sn = sn == (NS - 1) * STG ? 0 : sn + STG; sf = sf == (NS - 1) * STG ? 0 : sf + STG;
        }
        cf = cf == 9 ? 0 : cf + 1;
        if (ch == 7) { vo[0] = vo[1] = vo[2] = vo[3] = OOB; }
        AT(6);
        ATD(6, ch == 5 && rb == 0 && g == 0);
    }
    {
        const rsrc_t rz = MKR(a.ws + WS_HOP + F_AF, HOP_BYTES - F_AF), rh = MKR(al2m(a.ws + WS_FR), FR_FILL); const unsigned w = (rb * 8 + g) * 8 + wave;
#pragma unroll
        for (int j = 0; j < 3; ++j) st16c(rz, (w + j * 1024) * 1024 + lane * 16, (u32x4){0u, 0u, 0u, 0u});
        static_assert(HOP_BYTES - F_AF <= 3u << 20, "flags");
        st16c(rh, (w >> 4) % 20u >= 2u ? w * 1024 + lane * 16 : OOB, (u32x4){~0u, ~0u, ~0u, ~0u});
    }
    if (wave == 0 && lane == 0 && rb == 0 && g == 0) STAMP(15);
    constexpr int KP = 176, L_K = 0, L_V = L_K + 128 * KP, L_Q = L_V + 128 * KP + 64, L_S = NS * STG + 8192, L_CS = L_S + 1024;
    const int row = rp * 64 + cg * 16 + l15;
    if (lg == (l15 >> 2)) *reinterpret_cast<float*>(lds + L_S + row * 8 + 4) = (l15 & 2) ? ((l15 & 1) ? gr[3] : gr[2]) : ((l15 & 1) ? gr[1] : gr[0]);
    if (cg == 3) *reinterpret_cast<float*>(lds + L_S + (row ^ (lg * 16)) * 8) = lg < 2 ? (lg ? acc[1][3][0] : acc[0][3][0]) : (lg == 2 ? acc[2][3][0] : acc[3][3][0]);
    if (l15 < 2) *reinterpret_cast<f32x4*>(lds + L_CS + wave * 128 + l15 * 64 + lg * 16) = l15 ? cs[1] : cs[0];
    VMW(4); LBAR();
    f32x2 ct[4];
    {
        const f32x2 sv = *reinterpret_cast<const f32x2*>(lds + L_S + (row ^ (lg * 16)) * 8);
#pragma unroll
        for (int u = 0; u < 4; ++u) cs[u] = *reinterpret_cast<const f32x4*>(lds + L_CS + cg * 256 + u * 64 + lg * 16);
        float m, iv; ln_consts(sv[0], sv[1], m, iv); m = -m;
        const auto m2 = __builtin_amdgcn_permlane32_swap(__float_as_uint(m), __float_as_uint(m), false, false), i2 = __builtin_amdgcn_permlane32_swap(__float_as_uint(iv), __float_as_uint(iv), false, false);
#pragma unroll
        for (int z = 0; z < 2; ++z) { const auto x = __builtin_amdgcn_permlane16_swap(m2[z], m2[z], false, false), y = __builtin_amdgcn_permlane16_swap(i2[z], i2[z], false, false);
            ct[2 * z] = (f32x2){__uint_as_float(x[0]), __uint_as_float(y[0])}; ct[2 * z + 1] = (f32x2){__uint_as_float(x[1]), __uint_as_float(y[1])}; }
    }
    if (wave == 0 && lane == 0 && rb == 0 && g == 0) STAMP(18);
    auto tile = [&](int u, bool ph, unsigned char* dst, int pitch) {
#pragma unroll
        for (int i = 0; i < 4; ++i) { f32x4 y = acc[i][u] + ct[i][0] * cs[u];
            if (ph) { y = y * ct[i][1] + 1e-6f; const f32x4 z = y * 1.4426950408889634f - 1.4426950408889634e-6f;
#pragma unroll
                for (int r = 0; r < 4; ++r) y[r] = __builtin_amdgcn_exp2f(__builtin_amdgcn_fmed3f(z[r], 0.f, -3.0e38f)) + __builtin_amdgcn_fmed3f(y[r], 1e-6f, 3.0e38f); }
            else y *= ct[i][1];
            *reinterpret_cast<u32x2*>(dst + (i ^ cg) * pitch) = (u32x2){pk2(y[0], y[1]), pk2(y[2], y[3])}; } };
    unsigned char* kp = lds + L_K + (rp * 64 + l15) * KP + 8 * lg + cg * 32;
    if (cg > 0) tile(1, true, kp - 32, 16 * KP);
    if (cg < 2) tile(2, true, kp + 96, 16 * KP); else tile(2, false, kp + (L_V - L_K) - 64, 16 * KP);
    if (cg < 3) tile(3, false, kp + (L_V - L_K) + 64, 16 * KP);
    LBAR();
    if (wave == 0 && lane == 0 && rb == 0 && g == 0) STAMP(19);
    if (wave >= 4) {
        f32x4 d4[4] = {Z4, Z4, Z4, Z4};
#pragma unroll
        for (int ks = 0; ks < 4; ++ks) {
            u32x4 A[2], B[2];
#pragma unroll
            for (int t = 0; t < 2; ++t) { const unsigned char* p = lds + L_K + (32 * ks + 8 * lg + (l15 >> 2)) * KP + 40 * (wave & 3) + 32 * t + 8 * (l15 & 3);
                const u32x2 a0 = ldstr(p), a1 = ldstr(p + 4 * KP), b0 = ldstr(p + (L_V - L_K)), b1 = ldstr(p + (L_V - L_K) + 4 * KP);
                A[t] = (u32x4){a0.x, a0.y, a1.x, a1.y}; B[t] = (u32x4){b0.x, b0.y, b1.x, b1.y}; }
            if (l15 >= 4) B[1] = one;
#pragma unroll
            for (int q = 0; q < 4; ++q) d4[q] = dmf(A[q >> 1], B[q & 1], d4[q]); }
        const rsrc_t rk = MKR(a.ws + WS_KVP, 32 * 16 * 4096);
#pragma unroll
        for (int q = 0; q < 4; ++q) st16c(rk, ((q < 2 || lg == 0) && (!(q & 1) || l15 < 8)) ? ((4 * g + (wave & 3)) * 16 + rb) * 4096 + q * 1024 + lane * 16 : OOB, __builtin_bit_cast(u32x4, d4[q]));
    }
    auto qdst = [&](int T) { const int gi = 4 * T + lg, hd = gi / 5, pt = gi - 5 * hd; return lds + L_Q + rp * 10240 + hd * 640 + (pt < 4 ? 128 + l15 * 32 + pt * 8 : l15 * 8); };
    tile(0, true, qdst(cg), 2560);
    if (cg == 0) tile(1, true, qdst(4), 2560);
    if (wave == 3) boot_wait(a);
    VMDRAIN(); LBAR();
    const rsrc_t rfl = MKR(a.ws + WS_HOP, F_ERR), rq = MKR(a.ws + WS_PQ, S_ * D_ * 2);
    if (wave == 3 && lane == 0) { st4c(rfl, F_QK + (rb * 8 + g) * FLP, 1u); if (rb == 0 && g == 0) STAMP(16); STAMP(1400 + rb * 8 + g); }
#pragma unroll
    for (int j = 0; j < 3; ++j) st16c(rq, (j < 2 || lane < 32) ? (rb * 8 + wave) * 20480 + g * 2560 + j * 1024 + lane * 16 : OOB, lds16(lds + L_Q + wave * 2560 + j * 1024 + lane * 16));
    VMDRAIN(); LBAR();
    if (wave == 3 && lane == 0) st4c(rfl, F_PQ + (rb * 8 + g) * FLP, 1u);
}
__device__ __forceinline__ void kv_part(KA& a, unsigned char* lds, const int i, const int wave) {
    const int lane = lane_id(), l15 = lane & 15, lg = lane >> 4, h = 2 * i + (wave >> 2);
    const rsrc_t rk = MKR(a.ws + WS_KVP, 32 * 16 * 4096);
    if (wave == 7) wait_set(MKR(a.ws + WS_HOP, F_ERR), F_QK + (l15 * 8 + (h >> 2)) * FLP, a.ws + WS_HOP, 0);
    LBAR();
    u32x4 v[4][4];
#pragma unroll
    for (int q = 0; q < 16; ++q) v[q >> 2][q & 3] = __builtin_amdgcn_raw_buffer_load_b128(rk, (((q & 2) == 0 || lg == 0) && (!(q & 1) || l15 < 8)) ? lane * 16 + (q & 3) * 1024 : OOB, (i * 32 + wave * 4 + (q >> 2)) * 4096, 16);
#pragma unroll
    for (int q = 0; q < 4; ++q) *reinterpret_cast<f32x4*>(lds + 32768 + ((wave >> 2) * 16 + q * 4 + (wave & 3)) * 1024 + lane * 16) = (__builtin_bit_cast(f32x4, v[0][q]) + __builtin_bit_cast(f32x4, v[1][q])) + (__builtin_bit_cast(f32x4, v[2][q]) + __builtin_bit_cast(f32x4, v[3][q]));
    LBAR();
    const f32x4* p = reinterpret_cast<const f32x4*>(lds + 32768 + wave * 4096 + lane * 16);
    const f32x4 s = (p[0] + p[64]) + (p[128] + p[192]);
    const int e = 16 * (wave & 1) + l15, d = 8 * (wave & 2) + 4 * lg;
    st8c(MKR(a.ws + WS_KVF, 32768), (d < 24 && e <= 20) ? h * 1024 + e * 48 + d * 2 : OOB, (u32x2){min(pk2(s[0], s[1]), 0xfffffffeu), min(pk2(s[2], s[3]), 0xfffffffeu)});
}
__device__ __forceinline__ void boot_part(KA& a, unsigned char* lds, const int i, const int wave) {
    const int lane = lane_id(), l15 = lane & 15, lg = lane >> 4, w = i * 8 + wave;
    const rsrc_t rb = MKR((a.ws + WS_HOP) - 2048, 2048), rz = MKR((a.ws + WS_HOP), F_AF), rh = MKR(al2m((a.ws + WS_FR)), FR_FILL), rf = MKR((a.ws + WS_WF_AO), D_ * D_ * 2);
    const u32x2 tag = ld8c(rb, 0);
    {
        const unsigned c = (w & 7) * 64 + lane, b = ((w >> 4) & 1) * 8 + (c >> 6), m = c & 15u;
        u32x4 v = (u32x4){~0u, ~0u, ~0u, ~0u};
        if (b < 10u) { const unsigned* mp = reinterpret_cast<const unsigned*>(a.mem0 + (2 * (m >> 2) + (m & 1)) * D_ + b * 64 + ((c >> 4) & 3u) * 16 + (m & 2));
            v = (u32x4){min(mp[0], 0xfffffffeu), min(mp[2], 0xfffffffeu), min(mp[4], 0xfffffffeu), min(mp[6], 0xfffffffeu)}; }
        else if (!(c & 3u)) v.x = c == 196u ? TT_E1 + TT_E2 + 40u : TT_E1 + 20u;
        st16c(rh, (w >> 5) * (20u << 14) + (w & 31) * 1024 + lane * 16, v);
    }
    st16c(rz, w * 1024 + lane * 16, (u32x4){0u, 0u, 0u, 0u});
    st16c(MKR(a.ws + WS_KVF, 65536), w * 1024 + lane * 16, (u32x4){~0u, ~0u, ~0u, ~0u});
    VMDRAIN(); LBAR();
    if (wave == 0 && lane == 0) st8c(rb, 128 + i * 128, (u32x2){tag.x + 1u, ~tag.y});
    if (w == 0 && lane == 0) { const int layer = 0; STAMP(20); }
    u32x4 v[4][4];
#pragma unroll
    for (int q = 0; q < 16; ++q) { const int u = min(w + 64 * (q >> 2), 199), kk = u / 10; v[q >> 2][q & 3] = *reinterpret_cast<const u32x4*>(a.wao + (kk * 32 + (q & 3) * 8 + (lane >> 3)) * D_ + (u - kk * 10) * 64 + (lane & 7) * 8); }
    unsigned char* t = lds + wave * 16384;
#pragma unroll
    for (int q = 0; q < 16; ++q) *reinterpret_cast<u32x4*>(t + q * 1024 + lane * 16) = v[q >> 2][q & 3];
    CBAR();
#pragma unroll
    for (int q = 0; q < 16; ++q) { const int u = w + 64 * (q >> 2), kk = u / 10; const unsigned char* p = t + (q >> 2) * 4096 + (8 * lg + (l15 >> 2)) * 128 + (q & 3) * 32 + (l15 & 3) * 8; const u32x2 x0 = ldstr(p), y0 = ldstr(p + 512);
        if (u < 200) st16c(rf, ((((u - kk * 10) * 4 + (q & 3)) * 20 + kk) * 64 + lane) * 16, (u32x4){x0.x, x0.y, y0.x, y0.y}); }
    boot_wait(a);
    VMDRAIN(); LBAR();
    if (wave == 0 && lane == 0) st4c(rz, F_WO + i * FLP, 1u);
    pol_vote(a, w);
    if (w == 0 && lane == 0) { const int layer = 0; STAMP(21); }
    LBAR();
}

#define KSL0 16
#define KSL 6
#define AU0 66
#define TT_AW 950
#define ATS 62
#define ATU 0
#define AIM 12
template <int K, bool W3, class F> __device__ __forceinline__ void a_proj(KA& a, unsigned char* lds, const int h, const int wave, F&& pre) {
    constexpr int YB = 51200, L_SK = 3 * YB, L_C = L_SK + 6144 + 64, L_KN = L_C + 256, KP = 48, layer = 0;
    const int lane = lane_id(), l15 = lane & 15, lg = lane >> 4, hd = 2 * h + (wave & 1);
    const unsigned ldsb = LDSB();
    unsigned char* hop = (a.ws + WS_HOP);
    const rsrc_t rfl = MKR(hop, F_ERR), rp = MKR((a.ws + WS_PQ), S_ * D_ * 2), rxa = MKR((a.ws + WS_X), XI_BYTES);
    u32x4 wq[2][20]; f32x4 cs[2] = {Z4, Z4}, d4[4] = {Z4, Z4, Z4, Z4};
    if (K) {
        const rsrc_t rw = MKR((a.ws + WS_WF_AQKV), 640 * 1920 * 2);
#pragma unroll
        for (int u = 0; u < 2; ++u) { const int c = 20 * hd + (K == 2 ? (1 + u) * 640 + l15 : (u ? min(l15 >> 2, 2) * 640 + 16 + (l15 & 3) : l15));
#pragma unroll
            for (int q = 0; q < 20; ++q) wq[u][q] = __builtin_amdgcn_raw_buffer_load_b128(rw, ((c >> 4) * 20 * 64 + lg * 16 + (c & 15)) * 16, q * 1024, 16); }
#pragma unroll
        for (int q = 0; q < 40; ++q) cs[q & 1] = dmf(wq[q & 1][q >> 1], (u32x4){0x3F803F80u, 0x3F803F80u, 0x3F803F80u, 0x3F803F80u}, cs[q & 1]);
        PIN(cs[0]); PIN(cs[1]); VMKNOWN();
    }
    const unsigned f0 = F_OF + lane * FLP, f1 = F_OF + (64 + l15 + (lane & 16) * 5) * FLP;
    unsigned tnext = rt_now(), per = 0;
    auto kv = [&]() {
        { u32x4 A[2], B[2]; const int z = wave & 1;
#pragma unroll
            for (int q = 0; q < 2; ++q) { const unsigned char* p = lds + L_SK + z * 64 * KP + (8 * lg + (l15 >> 2)) * KP + 32 * q + 8 * (l15 & 3);
                const u32x2 a0 = ldstr(p), a1 = ldstr(p + 4 * KP), b0 = ldstr(p + 32 * KP), b1 = ldstr(p + 36 * KP);
                A[q] = (u32x4){a0.x, a0.y, a1.x, a1.y}; B[q] = (u32x4){b0.x, b0.y, b1.x, b1.y}; }
            if (l15 >= 4) { A[1] = (u32x4){0u, 0u, 0u, 0u}; B[1] = (u32x4){0x3F803F80u, 0x3F803F80u, 0x3F803F80u, 0x3F803F80u}; }
#pragma unroll
            for (int q = 0; q < 4; ++q) d4[q] = dmf(A[q >> 1], B[q & 1], d4[q]); } };
    auto fill = [&](int n, unsigned dst, int g0) {
#pragma unroll
        for (int g = 0; g < 2; ++g) { const unsigned o = min(((g0 + g + (blockIdx.x >> 3) * AROT) & 3) * 13, 37) * 1024; dma16cn<13>(rxa, lane * 16, (unsigned)n * XI_PITCH + o, dst + o); } };
    LBAR();
    if (!K) __builtin_amdgcn_s_setprio(2);
    int nx = 0, kn = 0, yb = 0;
    auto seg = [&](const int t) {
        AT(0);
        if (!K && !W3 && wave && t) kv();
        if (nx == t) {
            if (W3) {
                if (kn == t) {
                    if (t < TF) rt_wait(tnext);
                    for (unsigned sp = 0;; ++sp) { CBAR(); const u32x4 v = ld16c(rfl, f0 + t * (NR_ * FLP)); const unsigned w = ld4c(rfl, F_OF + (64 + l15 + t * NR_) * FLP);
                        if (__all(v.x != 0u && w != 0u)) { per = __builtin_amdgcn_readfirstlane(v.x); tnext = __builtin_amdgcn_readfirstlane(v.w) + per + (t + 1 < TF - 6 ? TT_AD : TT_AN); break; }
                        if (t < TF) __builtin_amdgcn_s_sleep(12); else __builtin_amdgcn_s_sleep(3);
                        if (sp > SPIN_CAP_W) { set_err(hop); break; } } }
                fill(t, ldsb + yb, 0); fill(t, ldsb + yb, 2); VMKNOWN(); }
            kn = max(kn, t + 1); ++nx;
            LBAR();
        }
        AT(1);
        if (lane == 0 && wave == 4 && h == 0 && t >= 56) STAMP(330 + t - 56);
        const int up = min(kn, t + 3), n = nx + ((nx ^ wave) & 1);
        const unsigned nb = ldsb + (n == t + 1 ? (yb == 2 * YB ? 0 : yb + YB) : (yb == 0 ? 2 * YB : yb - YB));
        u32x4 fa, fb; unsigned fc; int pr = 0; f32x4 acc[2][2] = {{Z4, Z4}, {Z4, Z4}};
        if (K) {
            const unsigned char* yp = lds + yb + XI_X + lane * 16;
            u32x4 yf[4];
#pragma unroll
            for (int q = 0; q < 3; ++q) { yf[q] = lds16(yp + q * 1024); SBAR(); }
            f32x2 ct; float y[8], e[8]; u32x2 o0, o1;
            auto epi = [&](int rt, int z) {
                constexpr int NP = 8 / (K ? K : 1);
                if (z == 0) ct = *reinterpret_cast<const f32x2*>(lds + L_C + rt * 128 + l15 * 8);
                if (z >= 2 && z < 10) { y[z - 2] = __builtin_fmaf(ct[0], cs[(z - 2) >> 2][(z - 2) & 3], acc[rt][(z - 2) >> 2][(z - 2) & 3]); PIN(y[z - 2]); if (z > 2) { y[z - 3] *= ct[1]; PIN(y[z - 3]); } }
                if (z == 10) { y[7] *= ct[1]; PIN(y[7]); }
                if (z >= 10 && z < 10 + NP) { e[z - 10] = __builtin_amdgcn_fmed3f(y[z - 10], 0.f, -3.0e38f); PIN(e[z - 10]); }
                if (z >= 11 && z < 11 + NP) { e[z - 11] *= 1.4426950408889634f; PIN(e[z - 11]); }
                if (z >= 19 && z < 19 + NP) { e[z - 19] = __builtin_amdgcn_exp2f(e[z - 19]); PIN(e[z - 19]); }
                if (z >= 20 && z < 20 + NP) { const float m = __builtin_amdgcn_fmed3f(y[z - 20], 0.f, 3.0e38f); e[z - 20] = (e[z - 20] + 1e-6f) + m; PIN(e[z - 20]); }
                if (z >= 28 && z < 32) { e[z - 24] = (K == 2 || lg >= 2) ? y[z - 24] : e[z - 24]; PIN(e[z - 24]); }
                if (z == 30) { o0 = (u32x2){pk2(e[0], e[1]), pk2(e[2], e[3])}; PIN(o0); }
                if (z == 32) { o1 = (u32x2){pk2(e[4], e[5]), pk2(e[6], e[7])}; PIN(o1); }
                unsigned char* sk = lds + L_SK + (wave & 1) * 64 * KP + (rt * 16 + l15) * KP;
                if (z == 33) { if (K == 2) *reinterpret_cast<u32x2*>(sk + 8 * lg) = o0; else __builtin_amdgcn_raw_buffer_store_b64(o0, rp, 128 + l15 * 32 + lg * 8, (t * 2 + rt) * 20480 + hd * 640, 16); }
                if (z == 34) *reinterpret_cast<u32x2*>(sk + (K == 2 ? 32 * KP + 8 * lg : (lg >= 2 ? 32 * KP + 32 : 40 - 8 * lg))) = o1;
                if (z == 35 && K == 1) __builtin_amdgcn_raw_buffer_store_b64(o1, rp, lg ? OOB : l15 * 8, (t * 2 + rt) * 20480 + hd * 640, 16);
                SBAR(); };
#pragma unroll
            for (int q = 0; q < 40; ++q) { acc[q / 20][0] = dmf(wq[0][q % 20], yf[q & 3], acc[q / 20][0]); PIN(acc[q / 20][0]); SBAR(); if (q + 3 < 40) { yf[(q + 3) & 3] = lds16(yp + (q + 3) * 1024); SBAR(); }
                if (q > 21) epi(0, 2 * q - 44);
                acc[q / 20][1] = dmf(wq[1][q % 20], yf[q & 3], acc[q / 20][1]); PIN(acc[q / 20][1]); SBAR();
                if (q > 21) epi(0, 2 * q - 43);
                if (q == 21) { CBAR(); __builtin_amdgcn_s_barrier(); CBAR(); SBAR(); } }
#pragma unroll
            for (int z = 0; z < 36; ++z) epi(1, z);
            if (K == 1 && t == 8) VMDRAIN();
        } else if (!W3 && wave == 0) {
            const f32x2* sp = reinterpret_cast<const f32x2*>(lds + yb + lg * 2560 + l15 * 8);
            f32x2 sv[20];
#pragma unroll
            for (int q = 0; q < 20; ++q) sv[q] = sp[q * 16];
#pragma unroll
            for (int q = 0; q < 16; ++q) { sv[q & 3][0] = adds(sv[q & 3][0], sv[q + 4][0]); sv[q & 3][1] = adds(sv[q & 3][1], sv[q + 4][1]); }
            const u32x2 s4[4] = {__builtin_bit_cast(u32x2, sv[0]), __builtin_bit_cast(u32x2, sv[1]), __builtin_bit_cast(u32x2, sv[2]), __builtin_bit_cast(u32x2, sv[3])};
            c_put(s4, lds + L_C);
        } else if (W3) {
            const int d = (int)(rt_now() - tnext);
            pr = kn >= NSEG_ ? 0 : (kn >= TF || d >= (int)per ? 2 : (d >= 0 ? 1 : 0));
            if (kn + 1 >= NSEG_) pr = min(pr, 1);
            fa = __builtin_amdgcn_raw_buffer_load_b128(rfl, pr ? f0 : OOB, kn * (NR_ * FLP), 16); fb = __builtin_amdgcn_raw_buffer_load_b128(rfl, pr > 1 ? f0 : OOB, (kn + 1) * (NR_ * FLP), 16);
            fc = __builtin_amdgcn_raw_buffer_load_b32(rfl, pr > ((lane >> 4) & 1) ? f1 : OOB, kn * (NR_ * FLP), 16); SBAR();
        } else if (n < up) fill(n, nb, 0);
        AT(2);
        if (!K) LBAR();
        AT(3);
        if (K) {
        } else if (W3) {
            {
                const unsigned long long z = __ballot(fc == 0u);
                if (__all(fa.x != 0u) && !(z & 0xffffull)) { const bool two = __all(fb.x != 0u) && !z; kn += two ? 2 : 1;
                    per = __builtin_amdgcn_readfirstlane(two ? fb.x : fa.x); tnext = __builtin_amdgcn_readfirstlane(two ? fb.w : fa.w) + per + (kn < TF - 6 ? TT_AD : TT_AN); } }
            *reinterpret_cast<int*>(lds + L_KN + (t & 1) * 4) = kn;
            if (t == 40) wait_set(rfl, F_AQ + (lane & 15) * 4, hop, 4);
        } else if (wave) { if (n < up) fill(n, nb, 2); if (((t + 1) ^ wave) & 1) { } else VMDRAIN(); }
        nx = max(nx, up);
        AT(4);
        LBAR();
        AT(5);
        kn = __builtin_amdgcn_readfirstlane(*reinterpret_cast<const int*>(lds + L_KN + (t & 1) * 4));
        if (W3 && t == 8 && lane == 0) st4c(rfl, F_AQ + h * 4, 1u);
        yb = yb == 2 * YB ? 0 : yb + YB;
        AT(6); AT(7); ATD(5, t == ATS && h == 0);
    };
#pragma unroll 1
    for (int t = 0; t < 50; ++t) seg(t);
    pre();
#pragma unroll 1
    for (int t = 50; t < NSEG_; ++t) seg(t);
    __builtin_amdgcn_s_setprio(0);
    if (!K && !W3 && wave) {
        kv();
        const rsrc_t rkv = MKR(a.ws + WS_KVF + 32768, 32768);
#pragma unroll
        for (int q = 0; q < 4; ++q) { const int e = 16 * (q & 1) + l15, d = 8 * (q & 2) + 4 * lg;
            st8c(rkv, (d < 24 && e <= 20) ? hd * 1024 + e * 48 + d * 2 : OOB, (u32x2){min(pk2(d4[q][0], d4[q][1]), 0xfffffffeu), min(pk2(d4[q][2], d4[q][3]), 0xfffffffeu)}); }
    }
}
__device__ __forceinline__ void a_part(KA& a, unsigned char* lds, const int ai, const int wave) {
    const int lane = lane_id(), tid = wave * 64 + lane;
    constexpr int L_ST = 20480;
    const int cb = ai & 7, rep = ai >> 3, h = ai;
    const int nt = wave < 5 ? wave : 0;
    u32x4 b[20], tq[4]; u32x2 rres;
    const int ct = 5 * cb + nt;
    const rsrc_t rp = MKR((a.ws + WS_PQ), S_ * D_ * 2), rxa = MKR((a.ws + WS_X), XI_BYTES);
    auto fetch = [&](int u) { const int lane = lane_id(), l15 = lane & 15, lg = lane >> 4;
#pragma unroll
        for (int q = 0; q < 4; ++q) tq[q] = __builtin_amdgcn_raw_buffer_load_b128(rp, (lg < 2 ? 128 + l15 * 32 + lg * 16 : (lg == 2 ? l15 * 8 : OOB)) + q * 640, u * 20480 + wave * 2560, 16); };
    auto x_off = [&]() { const int lane = lane_id(), l15 = lane & 15, lg = lane >> 4; return XI_X + (ct >> 1) * 1024 + ((ct & 1) * 2 + (lg >> 1)) * 256 + l15 * 16 + (lg & 1) * 8; };
    auto b_load = [&]() { const int lane = lane_id();
#pragma unroll
        for (int q = 0; q < 20; ++q) b[q] = __builtin_amdgcn_raw_buffer_load_b128(MKR((a.ws + WS_WF_AO), D_ * D_ * 2), wave < 5 ? lane * 16 + (q & 3) * 1024 : OOB, ct * 20480 + (q >> 2) * 4096, 16); };
    auto outp = [&](const int layer) {
        const int lane = lane_id(), l15 = lane & 15, lg = lane >> 4;
        unsigned char* hop = (a.ws + WS_HOP) + layer * HOP_BYTES;
        const rsrc_t rfl = MKR(hop, F_ERR), rr_ = MKR(psel(a.x0, (a.ws + WS_X), layer), layer ? XI_BYTES : S_ * D_ * 2);
        const unsigned oo = x_off(), ro = layer ? oo : (unsigned)(l15 * D_ + 16 * ct + 4 * lg) * 2;
        auto res = [&](int u) { rres = __builtin_amdgcn_raw_buffer_load_b64(rr_, ro, (u >> 1) * (layer ? XI_PITCH : 40960) + (u & 1) * 20480, 16); };
        if (!layer) { boot_wait(a); wait_set(rfl, F_WO + (lane & 7) * FLP, hop, 1); b_load(); res(rep); if (ai < 16) { VMKNOWN(); kv_part(a, lds, ai, wave); } wait_set(rfl, F_PQ + (lane & 7) * FLP, hop, 1); fetch(rep); }
        unsigned so[5];
#pragma unroll
        for (int q = 0; q < 5; ++q) { const unsigned c = 80 * wave + (q < 4 ? 20 * q + 4 * lg : 20 * lg + 16); so[q] = (c >> 3) * 256 + l15 * 16 + (c & 4) * 2; }
        u32x4 k0[4], k1[4], kz[4];
        {
            const rsrc_t rkv = MKR(a.ws + WS_KVF + layer * 32768, 32768);
            const unsigned ko = (lg < 3 ? lg : 0) * 16;
            __builtin_amdgcn_s_sleep(KSL0);
            for (unsigned sp = 0; sp < SPIN_CAP_W; ++sp) { CBAR();
                unsigned mx = 0u;
#pragma unroll
                for (int q = 0; q < 4; ++q) { k0[q] = __builtin_amdgcn_raw_buffer_load_b128(rkv, ko + l15 * 48 + q * 1024, wave * 4096, 16);
                    k1[q] = __builtin_amdgcn_raw_buffer_load_b128(rkv, (l15 >> 2) == q ? ko + (16 + (l15 & 3)) * 48 + q * 1024 : OOB, wave * 4096, 16); kz[q] = __builtin_amdgcn_raw_buffer_load_b128(rkv, ko + 960 + q * 1024, wave * 4096, 16); }
#pragma unroll
                for (int q = 0; q < 4; ++q) mx = max(mx, max(umax4(k0[q]), max(umax4(k1[q]), umax4(kz[q]))));
                if (__all(mx != 0xffffffffu)) break;
                __builtin_amdgcn_s_sleep(KSL);
            }
            if (tid == 0 && ai == 0) STAMP(4);
        }
        const int ue = (!layer && rep < 2) ? AU0 : 2 * NSEG_; int up = -1;
#pragma unroll 1
        for (int u = rep, un; u < ue; up = u, u = un) {
            un = u + ((!layer && u + 1 >= AU0) ? 1 : 3);
            if (!layer && u >= 3 && u < 6) { wait_set(rfl, F_PQ + lane * FLP, hop, 1); wait_set(rfl, F_PQ + (64 + lane) * FLP, hop, 1); }
            if (layer && u >= 9 && u < 12) wait_set(rfl, F_AQ + (lane & 15) * 4, hop, 1);
            if (layer) AT(0);
            {
                f32x4 dz[4], d0[4], d1 = Z4;
#pragma unroll
                for (int q = 0; q < 4; ++q) dz[q] = dmf(kz[q], tq[q], Z4);
#pragma unroll
                for (int q = 0; q < 4; ++q) { d0[q] = dmf(k0[q], tq[q], Z4); d1 = dmf(k1[q], tq[q], d1); }
                SBAR(); fetch(min(un, 2 * NSEG_ - 1)); SBAR();
                float inv[4];
#pragma unroll
                for (int q = 0; q < 4; ++q) inv[q] = __builtin_amdgcn_rcpf(dz[q][0] + 1e-6f);
#pragma unroll
                for (int q = 0; q < 4; ++q) { d0[q] *= inv[q]; *reinterpret_cast<u32x2*>(lds + so[q]) = (u32x2){pk2(d0[q][0], d0[q][1]), pk2(d0[q][2], d0[q][3])}; }
                d1 *= lg < 2 ? (lg == 0 ? inv[0] : inv[1]) : (lg == 2 ? inv[2] : inv[3]);
                *reinterpret_cast<u32x2*>(lds + so[4]) = (u32x2){pk2(d1[0], d1[1]), pk2(d1[2], d1[3])};
            }
            f32x4 acc = (f32x4){lo2f(rres.x), hi2f(rres.x), lo2f(rres.y), hi2f(rres.y)}; PIN(acc);
            if (layer) AT(1);
            LBAR();
            if (layer) AT(2);
            if (layer && u == rep && tid == 448 && ai < 16) st4c(rfl, F_AQ + h * 4, 1u);
            if (wave < 5) {
                const unsigned char* ar = lds + lane * 16;
                u32x4 af[8];
#pragma unroll
                for (int q = 0; q < 5; ++q) { af[q] = lds16(ar + 1024 * q); SBAR(); }
#pragma unroll
                for (int q = 0; q < 20; ++q) { acc = dmf(b[q], af[q & 7], acc); PIN(acc); SBAR();
                    if (q + 5 < 20) { af[(q + 5) & 7] = lds16(ar + 1024 * (q + 5)); SBAR(); } }
                const u32x2 o = (u32x2){pk2(acc[0], acc[1]), pk2(acc[2], acc[3])};
                __builtin_amdgcn_raw_buffer_store_b64(o, rxa, oo, (u >> 1) * XI_PITCH + (u & 1) * 20480, 16);
                *reinterpret_cast<float*>(lds + L_ST + wave * 128 + l15 * 8 + (lg & 1) * 4) = row_sums(o);
            }
            if (layer) AT(3);
            LBAR();
            if (layer) AT(4);
            if (wave == 0) {
                if (lane < 32) { const float* sp = reinterpret_cast<const float*>(lds + L_ST + lane * 4); const float v = ((sp[0] + sp[32]) + (sp[64] + sp[96])) + sp[128];
                    __builtin_amdgcn_raw_buffer_store_b32(__float_as_uint(v), rxa, XI_S8 + cb * 128 + lane * 4, (u >> 1) * XI_PITCH + (u & 1) * 1024, 16); }
                if (lane == 0 && up >= AIM) st4c(rfl, F_AF + (up * 8 + cb) * FLP, 1u); }
            if (u < AIM || un >= ue) { VMKNOWN(); LBAR(); if (tid == 0) st4c(rfl, F_AF + (u * 8 + cb) * FLP, 1u); }
            if (tid == 0 && ai == 0 && u == 0) STAMP(5);
            if (tid == 0 && ai == 16 && un >= ue) STAMP(6);
            if (layer) { AT(5); AT(6); AT(7); }
            ATD(5, layer && u == ATU && ai == 0);
            CBAR(); res(min(un, 2 * NSEG_ - 1));
        }
    };
    outp(0);
    VMKNOWN();
    auto pre = [&]() { fetch(rep); rres = __builtin_amdgcn_raw_buffer_load_b64(rxa, x_off(), (rep >> 1) * XI_PITCH + (rep & 1) * 20480, 16); VMKNOWN(); };
    if (wave >= 4 && ai < 16) { if (wave >= 6) a_proj<2, false>(a, lds, h, wave, pre); else a_proj<1, false>(a, lds, h, wave, pre); b_load(); outp(1); }
    else { if (ai >= 16) {
            if (wave == 0) { const rsrc_t rfl = MKR(a.ws + WS_HOP, F_ERR); unsigned tn = rt_now();
                wait_set(rfl, F_AQ + (lane & 15) * 4, a.ws + WS_HOP, 60); wait_timed(rfl, F_KM + (50 * 32 + ai) * FLP, a.ws + WS_HOP, tn, 13, TT_AW, false); rt_wait(tn); }
            LBAR(); pre(); }
        else if (wave == 3) a_proj<0, true>(a, lds, h, wave, pre); else a_proj<0, false>(a, lds, h, wave, pre); outp(1); }
}

#define MAPV 2
#define WDL 700
#define WDL2 400
#define WDL3 400
#define XPERM 0x76543210u
__global__ void __launch_bounds__(512, 2) KATTR bmega_kernel(MegaArgs) {
    ATI();
    extern __shared__ __attribute__((aligned(16))) unsigned char lds[];
    const int c = blockIdx.x, x = (XPERM >> ((c & 7) * 4)) & 7, rr = c >> 3, q = x >> 1, wave = __builtin_amdgcn_readfirstlane(threadIdx.x >> 6);
    if (LDSB() != 0u) return;
    int role, id, qt = -1;
    const bool ev = (x & 1) == 0;
    if (rr < 8) { role = ev ? 0 : 1; id = q * 8 + rr; }
    else if (rr >= 28) { if (ev == (MAPV == 1)) { role = rr < 30 ? 5 : 6; id = (rr < 30 ? 16 : 0) + 2 * q + (rr & 1); } else { role = 5; id = (rr - 28) * 4 + q; } }
    else if (ev) { role = 2; id = q * 20 + rr - 8; } else if (rr < 20) { role = 3; id = q * 12 + rr - 8; } else { role = 4; id = q * 8 + rr - 20; }
    if (role == 2 || role == 4 || role == 6 || (role == 5 && id >= 16)) qt = (role == 4 ? (rr + 4) >> 1 : (rr - 8) >> 1) * 8 + 2 * q + (rr & 1);
    if (role == 0 || role == 1 || role == 3) rt_wait(rt_now() + (role == 0 ? WDL : (role == 1 ? WDL2 : WDL3)));
    if (role == 0) s_part(args(), lds, id, wave);
    else if (role == 1) { if (wave < 4) v_run<true>(args(), lds, id, wave); else v_run<false>(args(), lds, id, wave); }
    else if (role == 3) xp_part(args(), lds, id, wave);
    else {
        if (qt >= 0) q_part(args(), lds, qt >> 3, qt & 7, wave); else if (id >= 8) boot_part(args(), lds, id - 8, wave);
        if (role == 5) a_part(args(), lds, id, wave);
        else if (role == 6) w_part(args(), lds, id, wave);
        else if (role == 4) xq_part(args(), lds, id, wave);
        else {
            if (wave == 0 && !(id & 1) && lane_id() == 0) st4c(MKR(args().ws + WS_HOP, F_ERR), F_SD + (id >> 1) * 4, 1u + my_side());
            ro_part(args(), lds, id, wave); }
    }
}

extern "C" void kernel_launch(void* const* d_in, const int* in_sizes, int n_in, void* d_out, int out_size, void* d_ws, size_t ws_size, hipStream_t stream) {
    (void)hipFuncSetAttribute((const void*)bmega_kernel, hipFuncAttributeMaxDynamicSharedMemorySize, MG_LDS);
    MegaArgs ma; ma.wq = (const bf16_t*)d_in[5]; ma.wk = (const bf16_t*)d_in[6]; ma.wv = (const bf16_t*)d_in[7]; ma.wo = (const bf16_t*)d_in[8];
    ma.wqkv = (const bf16_t*)d_in[1]; ma.wao = (const bf16_t*)d_in[2]; ma.mem0 = (const bf16_t*)d_in[4];
    ma.x0 = (const bf16_t*)d_in[0]; ma.out = (bf16_t*)d_out; ma.ws = (unsigned char*)d_ws;
    bmega_kernel<<<256, 512, MG_LDS, stream>>>(ma);
}
```

```cpp
#include <hip/hip_runtime.h>
#include <hip/hip_bf16.h>
#include <stdint.h>
#include <type_traits>

typedef __attribute__((ext_vector_type(8))) short bf16x8;
typedef __attribute__((ext_vector_type(4))) float f32x4;
typedef __attribute__((ext_vector_type(4))) unsigned u32x4;
typedef __attribute__((ext_vector_type(2))) unsigned u32x2;
typedef uint16_t bf16_t;

typedef __attribute__((ext_vector_type(2))) __bf16 bf16x2_t;
typedef __attribute__((ext_vector_type(2))) float f32x2;
__device__ __forceinline__ unsigned pk2(float lo, float hi) { f32x2 v = {lo, hi}; return __builtin_bit_cast(unsigned, __builtin_convertvector(v, bf16x2_t)); }
__device__ __forceinline__ float rbf(float f) { return __uint_as_float(pk2(f, f) << 16); }
__device__ __forceinline__ float lo2f(unsigned p) { return __uint_as_float(p << 16); }
__device__ __forceinline__ float hi2f(unsigned p) { return __uint_as_float(p & 0xffff0000u); }

constexpr int S_ = 2048, D_ = 640, H_ = 32, E_ = 192, NSEG_ = 64;
constexpr int HE_ = H_ * E_;

constexpr int NR_ = 80;
constexpr int NXP_ = 48;
constexpr size_t al(size_t x) { return (x + 255) & ~(size_t)255; }
constexpr size_t WS_WF_AQKV = 0;
constexpr size_t WS_WF_AO   = WS_WF_AQKV + al((size_t)640 * 1920 * 2);
constexpr size_t WS_PQ      = WS_WF_AO + al((size_t)640 * 640 * 2);
constexpr unsigned XI_X = 10240, XI_S8 = XI_X + 40960, XI_PITCH = XI_S8 + 2048, XI_BYTES = NSEG_ * XI_PITCH;
constexpr size_t WS_X       = WS_PQ + al((size_t)S_ * D_ * 2);
constexpr size_t WS_KVP     = WS_X + al(XI_BYTES);
constexpr size_t WS_KVF     = WS_KVP + al((size_t)32 * 16 * 4096);
constexpr size_t WS_ST      = WS_KVF + al((size_t)32 * 1024 * 2);
#define KXR 32
#define PR 32
#define URING 32
constexpr unsigned ST_KX = 0;
constexpr unsigned ST_VX = ST_KX + KXR * 32u * 12288;
constexpr unsigned ST_KM = ST_VX + KXR * 32u * 12288;
constexpr unsigned ST_PX = ST_KM + PR * 32u * 3072;
constexpr unsigned ST_U  = ST_PX + PR * 2u * 40960;
constexpr unsigned ST_BYTES = ST_U + (unsigned)URING * 40 * 32768;
constexpr size_t WS_FR      = WS_ST + al(ST_BYTES);
constexpr unsigned FR_FILL = 40u << 14, FR_UM = FR_FILL, FR_USED = FR_UM + (160u << 14);
__device__ __forceinline__ unsigned char* al2m(unsigned char* p) { return (unsigned char*)(((uintptr_t)p + (((uintptr_t)1 << 21) - 1)) & ~(((uintptr_t)1 << 21) - 1)); }
constexpr unsigned M_STAMP = 16384 + 2048;
constexpr unsigned FLP = 128;
constexpr unsigned F_POL = 0, F_AQ = F_POL + 256, F_SD = F_AQ + 256, F_WO = F_SD + 256, F_IN = F_WO + 8 * FLP, F_QK = F_IN + 8 * FLP, F_PQ = F_QK + 128 * FLP, F_AF = 36864, F_XP = F_AF + 64 * 16 * FLP, F_PX = F_XP + 64 * NXP_ * FLP, F_KM = F_PX + 70 * 64 * FLP, F_UX = F_KM + 64 * 32 * FLP,
    F_UW = F_UX + 66 * 32 * FLP, F_OF = F_UW + 64 * 8 * FLP, F_RD = F_OF + 64 * NR_ * FLP, F_ERR = F_RD + 64 * NR_ * FLP, HOP_BYTES = F_ERR + 256;
constexpr size_t WS_BOOT    = WS_FR + ((size_t)6 << 20);
constexpr size_t WS_HOP     = WS_BOOT + 2048;
constexpr size_t WS_DBG     = WS_HOP + al(2 * HOP_BYTES);
constexpr size_t WS_END     = WS_DBG + al((size_t)2 << 20);
static_assert(WS_END <= (size_t)256 * 1024 * 1024, "ws too big");
static_assert(FR_USED <= (4u << 20), "hand-off pages");

__device__ __forceinline__ float phi1(float x) { return (__builtin_amdgcn_exp2f(fminf(x, 0.f) * 1.4426950408889634f) + 1e-6f) + fmaxf(x, 0.f); }
__device__ __forceinline__ unsigned phi2(float x0, float x1) { return pk2(phi1(x0), phi1(x1)); }
constexpr unsigned OOB = 0x40000000u;
#define CBAR() asm volatile("" ::: "memory")
#define VMDRAIN() asm volatile("s_waitcnt vmcnt(0)" ::: "memory")
#define PIN(x) asm volatile("" : "+v"(x))
#define VMKNOWN() __builtin_amdgcn_s_waitcnt(0x0F70)
#define LBAR() do { asm volatile("s_waitcnt lgkmcnt(0)" ::: "memory"); __builtin_amdgcn_s_barrier(); asm volatile("" ::: "memory"); } while (0)
typedef __amdgpu_buffer_rsrc_t rsrc_t;
__device__ __forceinline__ u32x4 ld16c(rsrc_t r, unsigned off) { return __builtin_amdgcn_raw_buffer_load_b128(r, off, 0, 16); }
__device__ __forceinline__ u32x2 ld8c(rsrc_t r, unsigned off) { return __builtin_amdgcn_raw_buffer_load_b64(r, off, 0, 16); }
__device__ __forceinline__ unsigned ld4c(rsrc_t r, unsigned off) { return __builtin_amdgcn_raw_buffer_load_b32(r, off, 0, 16); }
__device__ __forceinline__ void st16c(rsrc_t r, unsigned off, u32x4 v) { __builtin_amdgcn_raw_buffer_store_b128(v, r, off, 0, 16); }
__device__ __forceinline__ void st8c(rsrc_t r, unsigned off, u32x2 v) { __builtin_amdgcn_raw_buffer_store_b64(v, r, off, 0, 16); }
__device__ __forceinline__ void st4c(rsrc_t r, unsigned off, unsigned v) { __builtin_amdgcn_raw_buffer_store_b32(v, r, off, 0, 16); }
template <int CTRL> __device__ __forceinline__ unsigned dppu(unsigned x) { return (unsigned)__builtin_amdgcn_mov_dpp((int)x, CTRL, 0xf, 0xf, true); }
template <int CTRL> __device__ __forceinline__ float dppf(float x) { return __uint_as_float(dppu<CTRL>(__float_as_uint(x))); }
__device__ __forceinline__ float xg_max(float x) {
    auto s = __builtin_amdgcn_permlane16_swap(__float_as_uint(x), __float_as_uint(x), false, false); x = fmaxf(__uint_as_float(s[0]), __uint_as_float(s[1]));
    auto t = __builtin_amdgcn_permlane32_swap(__float_as_uint(x), __float_as_uint(x), false, false); return fmaxf(__uint_as_float(t[0]), __uint_as_float(t[1])); }
__device__ __forceinline__ float xg_sum(float x) {
    auto s = __builtin_amdgcn_permlane16_swap(__float_as_uint(x), __float_as_uint(x), false, false); x = __uint_as_float(s[0]) + __uint_as_float(s[1]);
    auto t = __builtin_amdgcn_permlane32_swap(__float_as_uint(x), __float_as_uint(x), false, false); return __uint_as_float(t[0]) + __uint_as_float(t[1]); }

typedef __attribute__((ext_vector_type(8))) unsigned u32x8;
typedef __attribute__((ext_vector_type(8))) __bf16 bfv8;
typedef __attribute__((ext_vector_type(16))) __bf16 bfv16;
typedef short v4i16_t __attribute__((ext_vector_type(4)));
typedef __attribute__((address_space(3))) volatile unsigned lds_u32;
#define SBAR() __builtin_amdgcn_sched_barrier(0)
__device__ __forceinline__ int lane_id() { int l; asm volatile("v_mbcnt_lo_u32_b32 %0, -1, 0\n\tv_mbcnt_hi_u32_b32 %0, -1, %0" : "=v"(l)); return l; }
#define Z4 ((f32x4){0.f, 0.f, 0.f, 0.f})
__device__ __forceinline__ f32x4 smf(u32x4 A, u32x8 B, f32x4 c, unsigned idx) { return __builtin_amdgcn_smfmac_f32_16x16x64_bf16(__builtin_bit_cast(bfv8, A), __builtin_bit_cast(bfv16, B), c, (int)idx, 0, 0); }
__device__ __forceinline__ f32x4 dmf(u32x4 A, u32x4 B, f32x4 c) { return __builtin_amdgcn_mfma_f32_16x16x32_bf16(__builtin_bit_cast(bfv8, A), __builtin_bit_cast(bfv8, B), c, 0, 0, 0); }
__device__ __forceinline__ u32x4 lo4(u32x8 v) { return (u32x4){v[0], v[1], v[2], v[3]}; }
__device__ __forceinline__ u32x4 hi4(u32x8 v) { return (u32x4){v[4], v[5], v[6], v[7]}; }
__device__ __forceinline__ u32x8 cat8(u32x4 a, u32x4 b) { return (u32x8){a.x, a.y, a.z, a.w, b.x, b.y, b.z, b.w}; }
__device__ __forceinline__ u32x4 lds16(const unsigned char* p) { return *reinterpret_cast<const u32x4*>(p); }
__device__ __forceinline__ u32x4 lds16a(int ad) { return *(const __attribute__((address_space(3))) u32x4*)(uintptr_t)(unsigned)ad; }
__device__ __forceinline__ unsigned lds4a(int ad) { return *(const __attribute__((address_space(3))) unsigned*)(uintptr_t)(unsigned)ad; }
__device__ __forceinline__ f32x2 lds8a(int ad) { return *(const __attribute__((address_space(3))) f32x2*)(uintptr_t)(unsigned)ad; }
__device__ __forceinline__ void sts8a(int ad, f32x2 v) { *(__attribute__((address_space(3))) f32x2*)(uintptr_t)(unsigned)ad = v; }
__device__ __forceinline__ void sts4a(int ad, unsigned v) { *(__attribute__((address_space(3))) unsigned*)(uintptr_t)(unsigned)ad = v; }
__device__ __forceinline__ void sts16a(int ad, u32x4 v) { *(__attribute__((address_space(3))) u32x4*)(uintptr_t)(unsigned)ad = v; }
__device__ __forceinline__ u32x2 ldstr(const unsigned char* p) { return __builtin_bit_cast(u32x2, __builtin_amdgcn_ds_read_tr16_b64_v4i16((__attribute__((address_space(3))) v4i16_t*)p)); }
__device__ __forceinline__ unsigned umax4(u32x4 v) { return max(max(v.x, v.y), max(v.z, v.w)); }
__device__ __forceinline__ void dma16(rsrc_t r, unsigned voff, unsigned soff, unsigned ldsd) {
    unsigned keep;
    asm volatile("s_mov_b32 %0, m0\n\ts_mov_b32 m0, %1\n\ts_nop 2\n\tbuffer_load_dwordx4 %2, %3, %4 offen lds\n\ts_mov_b32 m0, %0" : "=&s"(keep) : "s"(ldsd), "v"(voff), "s"(r), "s"(soff) : "memory");
}
__device__ __forceinline__ void dma16c(rsrc_t r, unsigned voff, unsigned soff, unsigned ldsd) {
    unsigned keep;
    asm volatile("s_mov_b32 %0, m0\n\ts_mov_b32 m0, %1\n\ts_nop 2\n\tbuffer_load_dwordx4 %2, %3, %4 offen sc1 lds\n\ts_mov_b32 m0, %0" : "=&s"(keep) : "s"(ldsd), "v"(voff), "s"(r), "s"(soff) : "memory");
}
__device__ __forceinline__ void dma4c(rsrc_t r, unsigned voff, unsigned soff, unsigned ldsd) {
    unsigned keep;
    asm volatile("s_mov_b32 %0, m0\n\ts_mov_b32 m0, %1\n\ts_nop 2\n\tbuffer_load_dword %2, %3, %4 offen sc1 lds\n\ts_mov_b32 m0, %0" : "=&s"(keep) : "s"(ldsd), "v"(voff), "s"(r), "s"(soff) : "memory");
}
template <int N> __device__ __forceinline__ void dma16cn(rsrc_t r, unsigned voff, unsigned soff, unsigned ldsd) {
    unsigned keep;
    if (N >= 4) asm volatile("s_mov_b32 %0, m0\n\ts_mov_b32 m0, %1\n\ts_nop 2\n\tbuffer_load_dwordx4 %2, %3, %4 offen sc1 lds\n\tbuffer_load_dwordx4 %2, %3, %4 offen offset:1024 sc1 lds\n\t"
        "buffer_load_dwordx4 %2, %3, %4 offen offset:2048 sc1 lds\n\tbuffer_load_dwordx4 %2, %3, %4 offen offset:3072 sc1 lds\n\ts_mov_b32 m0, %0" : "=&s"(keep) : "s"(ldsd), "v"(voff), "s"(r), "s"(soff) : "memory");
    else if (N == 3) asm volatile("s_mov_b32 %0, m0\n\ts_mov_b32 m0, %1\n\ts_nop 2\n\tbuffer_load_dwordx4 %2, %3, %4 offen sc1 lds\n\tbuffer_load_dwordx4 %2, %3, %4 offen offset:1024 sc1 lds\n\t"
        "buffer_load_dwordx4 %2, %3, %4 offen offset:2048 sc1 lds\n\ts_mov_b32 m0, %0" : "=&s"(keep) : "s"(ldsd), "v"(voff), "s"(r), "s"(soff) : "memory");
    else if (N >= 2) asm volatile("s_mov_b32 %0, m0\n\ts_mov_b32 m0, %1\n\ts_nop 2\n\tbuffer_load_dwordx4 %2, %3, %4 offen sc1 lds\n\tbuffer_load_dwordx4 %2, %3, %4 offen offset:1024 sc1 lds\n\ts_mov_b32 m0, %0"
        : "=&s"(keep) : "s"(ldsd), "v"(voff), "s"(r), "s"(soff) : "memory");
    else dma16c(r, voff, soff, ldsd);
    constexpr int D = N >= 4 ? 4 : N;
    if constexpr (N > D) dma16cn<N - D>(r, voff, soff + D * 1024, ldsd + D * 1024);
}
__device__ __forceinline__ void st16s(u32x4 v, rsrc_t r, unsigned voff, unsigned soff) { __builtin_amdgcn_raw_buffer_store_b128(v, r, voff, soff, 16); asm volatile("s_nop 1" :: "v"(v) : "memory"); }
#define VMW(n) asm volatile("s_waitcnt vmcnt(%0)" :: "i"(n) : "memory")

struct MegaArgs { const bf16_t* wq; const bf16_t* wk; const bf16_t* wv; const bf16_t* wo; const bf16_t* wqkv; const bf16_t* wao; const bf16_t* mem0; const bf16_t* x0; bf16_t* out; unsigned char* ws; };
typedef const __attribute__((address_space(4))) MegaArgs KA;
__device__ __forceinline__ KA& args() { KA* p = (KA*)__builtin_amdgcn_kernarg_segment_ptr(); asm volatile("" : "+s"(p)); return *p; }
#define STAMP(idx) do { } while (0)
#define KATTR
#define ATI() do { } while (0)
#define AT(n) do { } while (0)
#define ATV do { } while (0)
#define ATD(role, on) do { } while (0)
#define SPINB 17
constexpr unsigned SPIN_CAP = 2u << SPINB;
constexpr unsigned SPIN_CAP_W = 1u << SPINB;
constexpr int MG_LDS = 163840;

#define PARAM(n, v) constexpr int n = v
#define TT_M1  14
#define TT_M2  13
#define TT_V   100
#define TT_V1 TT_V
#define TT_DEC 2
#define TT_BUMP 2
#define TT_DCY 3
#define TT_XS 5
#define TT_XD 1
#define TT_RT 34
#define TT_UM -2
#define TT_TW 8
#define TT_MD 60
#define TT_BD 12
#define TT_KD 95
#define TT_QD 5
#define TT_QN -40
#define TT_AD 250
#define XQD 0
#define SPRIO 2
#define PFD 6
#define DPS 5
#define PF0 2
#define KIM 6
#define XLD 9
#define NEAR 60
#define ROT 1
#define TF 60
#define RTS 12
#define RTL 8
#define R0S 4
#define VPRIO 0
#define XPD 4
#define TT_FP 13
#define TT_OD 30
#define TT_VD 3
#define YP1 0
#define QA 3
#define OIM 63
#define TT_UX -2
#define RLATE 0
#define PLATE 0
#define SLATE1 0
#define SLATE2 0
#define TT_TL 250
#define TT_AN 130
#define RROT 0
#define AROT 0
#define RFD 9
#define SFD 8
#define STK 0
#define VFD 7
#define VTK 2
#define VH 0
#define OSL 0
#define YP2 1
#define YA2 1
#define YD 0
#define YNB 6
#define TT_UP 2
#define TT_DN 3
#define XP2 2
#define YP3 0
#define TT_X1 10
#define VNH 0
#define ASL 10
#define RSL 1
#define TT_S1 155
#define TT_S2 40
#define OLAG 3
constexpr int TT_E1 = 132, TT_E2 = 48;
__device__ __forceinline__ unsigned rt_now() { return (unsigned)__builtin_amdgcn_s_memrealtime(); }
__device__ __forceinline__ void rt_wait(unsigned t) {
    unsigned d;
    asm volatile("s_branch 2f\n"
        "1:\n\ts_cmp_lt_u32 %0, 12\n\ts_cbranch_scc1 2f\n\ts_sleep 2\n\ts_cmp_lt_u32 %0, 30\n\ts_cbranch_scc1 2f\n\ts_sleep 5\n"
        "2:\n\ts_memrealtime vcc\n\ts_waitcnt lgkmcnt(0)\n\ts_sub_u32 %0, %1, vcc_lo\n\ts_cmp_lt_u32 %0, 20000\n\ts_cbranch_scc1 1b" : "=&s"(d) : "s"(t - 1u) : "scc", "vcc", "memory");
}
#define FINE 1
#define FC 100
template <int K> __device__ __forceinline__ void rt_fine(unsigned t) {
    if (!FINE) { rt_wait(t); return; }
    asm volatile("s_branch 2f\n"
        "1:\n\ts_cmp_lt_u32 s70, 12\n\ts_cbranch_scc1 2f\n\ts_sleep 2\n\ts_cmp_lt_u32 s70, 30\n\ts_cbranch_scc1 2f\n\ts_sleep 5\n"
        "2:\n\ts_memrealtime vcc\n\ts_waitcnt lgkmcnt(0)\n\ts_sub_u32 s70, %0, vcc_lo\n\ts_cmp_lt_u32 s70, 20000\n\ts_cbranch_scc1 1b\n\t"
        "s_cmp_lt_u32 s70, -8\n\ts_cbranch_scc1 5f\n\t"
        "s_memrealtime s[70:71]\n\ts_nop 4\n\ts_memrealtime s[72:73]\n\ts_nop 4\n\ts_memrealtime s[74:75]\n\ts_nop 4\n\ts_memrealtime s[76:77]\n\ts_nop 4\n\ts_memrealtime vcc\n\ts_waitcnt lgkmcnt(0)\n\t"
        "s_add_u32 s72, s72, s74\n\ts_add_u32 s76, s76, vcc_lo\n\ts_add_u32 s72, s72, s76\n\ts_lshl_b32 s71, s70, 2\n\ts_sub_u32 s72, s72, s71\n\ts_mul_i32 s72, s72, 6\n\t"
        "s_sub_u32 s70, %0, s70\n\ts_mul_i32 s70, s70, %1\n\ts_sub_i32 s70, s70, s72\n\ts_add_i32 s70, s70, %2\n\ts_max_i32 s70, s70, 0\n\ts_min_i32 s70, s70, 255\n\ts_lshr_b32 s70, s70, 3\n\ts_lshl_b32 s70, s70, 2\n\t"
        "s_getpc_b64 vcc\n3:\n\ts_sub_u32 s70, 4f-3b, s70\n\ts_add_u32 vcc_lo, vcc_lo, s70\n\ts_addc_u32 vcc_hi, vcc_hi, 0\n\ts_setpc_b64 vcc\n"
        "5:\n\ts_add_u32 s70, s70, 18\n\ts_cmp_gt_u32 s70, 18\n\ts_cbranch_scc1 4f\n\ts_memrealtime vcc\n\ts_waitcnt lgkmcnt(0)\n\ts_sub_u32 s70, %0, vcc_lo\n\ts_branch 5b\n"
        "\t.rept 32\n\ts_nop 1\n\t.endr\n4:"
        :: "s"(t - 19u), "i"(K), "i"(15 * K - FC) : "scc", "vcc", "s70", "s71", "s72", "s73", "s74", "s75", "s76", "s77", "memory");
}
template <int N> __device__ __forceinline__ void nops() { if (N > 0) asm volatile(".rept %0\n\ts_nop 1\n\t.endr" :: "i"(N > 0 ? N : 0) : "memory"); }
__device__ __forceinline__ const void* psel(const void* p0, const void* p1, int l) { return (const void*)((uintptr_t)p0 + (((uintptr_t)p1 - (uintptr_t)p0) & (uintptr_t)(-(long)l))); }
#define MKR(p, n) __builtin_amdgcn_make_buffer_rsrc((void*)(p), 0, (int)(n), 0x00020000)
__device__ __forceinline__ unsigned my_side() { return (__builtin_amdgcn_s_getreg((3 << 11) | 20) & 15u) >= 4u ? 1u : 0u; }
__device__ __forceinline__ void tt_step(unsigned tw, int s, unsigned t_poll, unsigned& tb, unsigned& cC1, unsigned& cP) {
    const unsigned tgt0 = tb + cP, pc2 = cP - cC1;
    int d = (int)(tw + pc2 - tgt0);
    d = max(d, (int)dppu<0x128>((unsigned)d)); d = max(d, (int)dppu<0x124>((unsigned)d)); d = max(d, (int)dppu<0x122>((unsigned)d)); d = max(d, (int)dppu<0x121>((unsigned)d));
    d = __builtin_amdgcn_readlane(d, 0);
    tb = s == 0 ? t_poll : ((s == 1 || d > 0) ? tgt0 + (unsigned)d - pc2 + (s == 1 ? TT_V1 : TT_V) : tgt0);
    cC1 = __builtin_amdgcn_readlane(tw, 16); cP = __builtin_amdgcn_readlane(tw, 17);
}
__device__ __forceinline__ void set_err(unsigned char* hop) { unsigned z, v; asm volatile("v_mov_b32 %0, 0\n\tv_mov_b32 %1, 1\n\ts_nop 4\n\tglobal_store_dword %0, %1, %2\n\ts_nop 1" : "=&v"(z), "=&v"(v) : "s"(hop + F_ERR) : "memory"); }
__device__ __forceinline__ void wait_set(rsrc_t rfl, unsigned off, unsigned char* hop, int slp) {
    for (unsigned sp = 0;; ++sp) { CBAR();
        const unsigned v = ld4c(rfl, off);
        if (__all(v != 0u)) break;
        for (int q = 0; q < slp; ++q) __builtin_amdgcn_s_sleep(8);
        if (sp > SPIN_CAP_W) { set_err(hop); break; } }
}
__device__ __forceinline__ void boot_wait(KA& a) {
    const rsrc_t r = MKR((a.ws + WS_HOP) - 2048, 2048); const unsigned off = 128 + (lane_id() & 7) * 128;
    for (unsigned sp = 0; sp < SPIN_CAP_W; ++sp) { CBAR();
        const u32x2 t = ld8c(r, 0), v = ld8c(r, off);
        if (__all(v.x == t.x + 1u && v.y == ~t.y)) break;
        __builtin_amdgcn_s_sleep(16); }
}
__device__ __forceinline__ void init_wait(KA& a) { boot_wait(a); wait_set(MKR(a.ws + WS_HOP, F_ERR), F_IN + (blockIdx.x & 7) * FLP, a.ws + WS_HOP, 3); }
__device__ __forceinline__ void pol_vote(KA& a, const int v) {
    const rsrc_t r = MKR(al2m((a.ws + WS_FR)), 1 << 22);
    const unsigned lane = lane_id(), b0 = (48u + v + (v & 1) * 128u) << 14;
    if (lane < 16) st4c(r, b0 + (lane & 1) * 8192 + (lane >> 1) * 896, 0u);
    VMDRAIN();
    unsigned best[2] = {~0u, ~0u}, acc = 0;
#pragma unroll 1
    for (int q = 0; q < 12; ++q) {
        const unsigned long long t0 = __builtin_amdgcn_s_memtime();
        unsigned x = ld4c(r, b0 + ((q ^ (v >> 1)) & 1) * 8192 + (q >> 1) * 896 + acc);
        asm volatile("s_waitcnt vmcnt(0)" : "+v"(x) :: "memory");
        const unsigned dt = (unsigned)(__builtin_amdgcn_s_memtime() - t0);
        acc += x; if ((q ^ (v >> 1)) & 1) best[1] = min(best[1], dt); else best[0] = min(best[0], dt);
    }
    const int df = max(-300, min(300, (int)best[0] - (int)best[1]));
    if (lane == 0) st4c(MKR((a.ws + WS_HOP), F_ERR), F_POL + v * 4, (unsigned)((my_side() ? -df : df) | 1));
}
__device__ __forceinline__ unsigned page_pol(KA& a) {
    const rsrc_t rfl = MKR((a.ws + WS_HOP), F_ERR); const unsigned off = F_POL + lane_id() * 4;
    init_wait(a); wait_set(rfl, off, (a.ws + WS_HOP), 2);
    const unsigned long long b = __ballot((int)ld4c(rfl, off) > 0);
    return (__popcll(b & 0x5555555555555555ull) > 16 ? 1u : 0u) | (__popcll(b & 0xAAAAAAAAAAAAAAAAull) > 16 ? 2u : 0u);
}
__device__ __forceinline__ unsigned wait_timed(rsrc_t rfl, unsigned off, unsigned char* hop, unsigned& tnext, int nper, int dly, bool fast) {
    if (!fast && (int)(tnext - rt_now()) < 4500) rt_wait(tnext);
    for (unsigned sp = 0;; ++sp) { CBAR();
        const u32x4 v = ld16c(rfl, off);
        if (__all(v.x != 0u)) { tnext = __builtin_amdgcn_readfirstlane(v.w) + (unsigned)nper * __builtin_amdgcn_readfirstlane(v.x) + (unsigned)dly; return __builtin_amdgcn_readfirstlane(v.x); }
        if (fast) __builtin_amdgcn_s_sleep(2); else __builtin_amdgcn_s_sleep(40);
        if (sp > SPIN_CAP_W) { set_err(hop); return 0u; } }
}
__device__ __forceinline__ unsigned blk_of(int j, unsigned nr) { return (j < 8 ? j & 3 : 4) + 5u * (((j < 8 ? j >> 2 : j) & 1u) ^ nr); }
__device__ __forceinline__ unsigned blk_off(int j, unsigned nr) { const unsigned b = blk_of(j, nr); return b * 1024 + (b < 8 ? 0 : 8192); }
template <int NRND, class F> __device__ __forceinline__ void load_w(rsrc_t r0, rsrc_t r1, unsigned nr, unsigned col0, unsigned char* lds, unsigned ldsb, int wave, int lane, F&& put) {
    unsigned vo[3];
#pragma unroll
    for (int i = 0; i < 3; ++i) { const int c = 64 * i + lane, row = c / 24, ch = c - row * 24; vo[i] = (unsigned)((8 * wave + row) * HE_ + col0 + ch * 8) * 2; }
    auto issue = [&](int rd) {
#pragma unroll
        for (int i = 0; i < 3; ++i) { dma16(r0, vo[i], blk_of(rd, nr) * (64 * HE_ * 2), ldsb + (rd % 3) * 49152 + wave * 3072 + i * 1024);
            dma16(r1, vo[i], blk_of(rd, nr) * (64 * HE_ * 2), ldsb + (rd % 3) * 49152 + 24576 + wave * 3072 + i * 1024); }
    };
    issue(0); issue(1); issue(2);
    const unsigned char* rp = lds + (wave >> 2) * 24576 + (8 * (lane >> 4) + ((lane & 15) >> 2)) * 384 + (wave & 3) * 96 + (lane & 3) * 8;
#pragma unroll
    for (int rd = 0; rd < NRND; ++rd) {
        if (rd + 2 < NRND) VMW(12); else if (rd + 1 < NRND) VMW(6); else VMW(0);
        LBAR();
#pragma unroll
        for (int u = 0; u < 3; ++u) { const unsigned char* p = rp + (rd % 3) * 49152 + u * 32;
            const u32x2 a0 = ldstr(p), a1 = ldstr(p + 4 * 384), b0 = ldstr(p + 32 * 384), b1 = ldstr(p + 36 * 384);
            put(rd, u, (u32x8){a0.x, a0.y, a1.x, a1.y, b0.x, b0.y, b1.x, b1.y}); }
        LBAR();
        if (rd + 3 < NRND) issue(rd + 3);
    }
}

__device__ __forceinline__ unsigned wsw(unsigned row) { return ((row >> 1) & 1u) | ((((row >> 2) ^ (row >> 3)) & 1u) << 1); }
template <int NB, int RB, int NRND, int LB, int R0, class G, class F, class H> __device__ __forceinline__ void load_b(unsigned ldsb, int wave, G&& src, F&& take, H&& back) {
    auto issue = [&](int rd) {
#pragma unroll
        for (int j = 0; j < NB; ++j) src(rd, j, ldsb + LB + ((rd + R0) % RB) * (NB * 8192) + j * 8192 + wave * 1024); };
#pragma unroll
    for (int rd = 0; rd < RB; ++rd) issue(rd);
#pragma unroll
    for (int rd = 0; rd < NRND; ++rd) {
        VMW((NRND - 1 - rd < RB - 1 ? NRND - 1 - rd : RB - 1) * NB);
        LBAR();
        take(rd, LB + ((rd + R0) % RB) * (NB * 8192));
        LBAR();
        back(rd);
        if (rd + RB < NRND) issue(rd + RB);
    }
}
__device__ __forceinline__ u32x4 ld_frag(const unsigned char* blk, unsigned r0, unsigned dr, unsigned win, unsigned l15) {
    const unsigned ra = r0 + (l15 >> 2), rb = ra + dr;
    const u32x2 x = ldstr(blk + ra * 128 + ((win ^ wsw(ra)) << 5) + (l15 & 3) * 8), y = ldstr(blk + rb * 128 + ((win ^ wsw(rb)) << 5) + (l15 & 3) * 8);
    return (u32x4){x.x, x.y, y.x, y.y};
}
__device__ __forceinline__ void ln_consts(float s1, float s2, float& mb, float& inv) {
    const float mean = s1 * (1.0f / D_), var = fmaxf(__builtin_fmaf(-mean, mean, s2 * (1.0f / D_)), 0.f);
    mb = rbf(mean); inv = __builtin_amdgcn_rcpf(rbf(__builtin_amdgcn_sqrtf(rbf(rbf(var) + 1e-5f))));
}
__device__ __forceinline__ float row_sums(u32x2 o) {
    const float y0 = lo2f(o.x), y1 = hi2f(o.x), y2 = lo2f(o.y), y3 = hi2f(o.y);
    const auto e = __builtin_amdgcn_permlane16_swap(__float_as_uint((y0 + y1) + (y2 + y3)), __float_as_uint(__builtin_fmaf(y0, y0, y1 * y1) + __builtin_fmaf(y2, y2, y3 * y3)), false, false);
    const float x = __uint_as_float(e[0]) + __uint_as_float(e[1]);
    const auto f = __builtin_amdgcn_permlane32_swap(__float_as_uint(x), __float_as_uint(x), false, false);
    return __uint_as_float(f[0]) + __uint_as_float(f[1]);
}
__device__ __forceinline__ unsigned m_idx(int ln) { return (ln & 2) ? 0xEEEEu : 0x4444u; }
__device__ __forceinline__ f32x4 zero4() { f32x2 a, b; asm volatile("v_mov_b64 %0, 0\n\tv_mov_b64 %1, 0\n\ts_nop 0" : "=v"(a), "=v"(b)); return (f32x4){a[0], a[1], b[0], b[1]}; }
__device__ __forceinline__ u32x4 ones4() { u32x2 a, b; asm volatile("v_mov_b64 %0, -1\n\tv_mov_b64 %1, -1" : "=v"(a), "=v"(b)); return (u32x4){a[0], a[1], b[0], b[1]}; }
__device__ __forceinline__ f32x2 fold2(f32x4 v) { return (f32x2){v[0], v[1]} + (f32x2){v[2], v[3]}; }
__device__ __forceinline__ unsigned fold_pk(f32x4 v) { float x = v[0] + v[2]; PIN(x); return pk2(x, v[1] + v[3]); }
__device__ __forceinline__ float max3f(float x, float y, float z) { return __builtin_fmaxf(__builtin_fmaxf(x, y), z); }
__device__ __forceinline__ float adds(float x, float y) { float r; asm("v_add_f32 %0, %1, %2" : "=v"(r) : "v"(x), "v"(y)); return r; }
__device__ __forceinline__ float muls(float x, float y) { float r; asm("v_mul_f32 %0, %1, %2" : "=v"(r) : "v"(x), "v"(y)); return r; }
#define LDSB() __builtin_amdgcn_readfirstlane((unsigned)(uintptr_t)(__attribute__((address_space(3))) unsigned char*)lds)
__device__ __forceinline__ int xp_of(int h) { return 3 * (h >> 1) + (h & 1); }
__device__ __forceinline__ unsigned um_base(unsigned long long sd, unsigned pol, unsigned c) {
    const unsigned s = (unsigned)(sd >> c) & 1u; const unsigned long long same = (s ? sd : ~sd) & ((1ull << c) - 1ull);
    const unsigned o = FR_UM + ((unsigned)__popcll(same) << 16);
    return o + (((s ^ (pol >> (o >> 21))) & 1u) << 13);
}
__device__ __forceinline__ unsigned long long um_sides(KA& a) {
    const rsrc_t rfl = MKR((a.ws + WS_HOP), F_ERR); const unsigned l = lane_id(), off = F_SD + (l < 40 ? l : 0) * 4;
    wait_set(rfl, off, (a.ws + WS_HOP), 2);
    return __ballot(ld4c(rfl, off) == 2u);
}

#define KME 1
#define SPS 1
#define KXW 2
#define RAP 0
#define SW2 3
#define KPR 0
template <int W> __device__ __forceinline__ void s_run(KA& a, unsigned char* lds, const int h, const u32x8 (&wr)[8][3]) {
    constexpr int wave = W; const int lane = lane_id();
    const unsigned ldsb = LDSB();
    const rsrc_t rhx = MKR(al2m((a.ws + WS_FR)), FR_USED);
    constexpr int L_MEM = 0, L_Q = 10240, L_K = 13312, L_KX = 16384, L_TIM = 28672, L_W = 32768;
    lds_u32* tim = (lds_u32*)(lds + L_TIM + 256);
    if (W == 0) { tim[0] = 0; tim[1] = 0; tim[2] = 0; tim[3] = 0; tim[4] = 0; tim[5] = 0; }
    const unsigned pol = page_pol(a), sx = ((my_side() ^ pol) & 1u) << 13;
    unsigned po = (2 + (lane >> 5)) * 16384 + h * 256 + ((lane >> 4) & 1) * 128 + (lane & 8) * 4 + (lane & 2) * 8 + (lane & 4) * 2 + (lane & 1) * 8192;
    unsigned pm = 4 * 16384 + (lane >> 5) * 2048 + (h >> 4) * 1024 + ((h >> 1) & 3) * 256 + ((lane >> 3) & 3) * 64 + (h & 8) * 4 + (lane & 4) * 4 + (h & 1) * 8 + (lane & 2) * 2 + (lane & 1) * 8192;
    const int xj = xp_of(h);
    unsigned idx = m_idx(lane);
    unsigned pn = ((lane >> 3) & 1) != (W ? 1 : 0) ? pm | OOB : pm;
    PIN(idx); if (W < 2 || W == SW2) PIN(po); if (W < 2 || W == SW2) PIN(pn);
#pragma unroll 1
    for (int layer = 0; layer < 2; ++layer) {
    unsigned char* hop = (a.ws + WS_HOP) + layer * HOP_BYTES;
    const rsrc_t rfl = MKR(hop, F_ERR), rst = MKR((a.ws + WS_ST), ST_BYTES);
    const unsigned sb0 = (unsigned)layer * (20u << 14);
    auto kx_fetch = [&](int t, bool wait) {
        int ln = lane_id();
        if (wait) wait_set(rfl, F_XP + (t * NXP_ + xj) * FLP, hop, 0);
        dma16cn<12>(rst, ln * 16, ST_KX + ((t & (KXR - 1)) * 32 + h) * 12288, ldsb + L_KX);
    };
    if (W == 3 && lane_id() == 0) STAMP(100 + h);
    bool early = false;
    if (W == 2) { early = __all(ld4c(rfl, F_XP + xj * FLP) != 0u); if (early) kx_fetch(0, false); }
    VMKNOWN();
    unsigned tb = 0, cC1 = 0, cP = 0, kdur = 0, krt = 0;
#pragma unroll 1
    for (int s = 0; s < NSEG_; ++s) {
        int ln = lane_id();
        ATV;
        const unsigned sb = sb0 + (unsigned)(s & 3) * (5u << 14);
        unsigned qo = (W < 4 ? L_Q + ((ln >> 3) & 1) * 256 + (ln >> 4) * 64 + ((ln >> 1) & 1) * 32 + (ln & 1) * 16 + ((ln >> 2) & 1) * 8
                             : L_K + ((ln & 15) >> 2) * 128 + (((ln >> 4) * 2 + (ln & 1) + (ln & 4)) & 7) * 16 + (ln & 2) * 2);
        unsigned sel = (ln & 1) ? 0x03020706u : 0x05040100u, w2o = L_W + 65536 + wave * 2048 + ln * 16;
        PIN(qo); PIN(sel); PIN(w2o);
        unsigned char* qp = lds + qo;
        u32x8 wl[3];
        const unsigned char* wp = lds + L_W + wave * 2048 + ln * 16;
        auto w_lds = [&](int f) { const unsigned char* p = f < 4 ? wp + f * 16384 : lds + w2o + (f - 4) * 16384; return cat8(lds16(p), lds16(p + 1024)); };
#pragma unroll
        for (int u = 0; u < 3; ++u) wl[u] = w_lds(1 + u);
        f32x4 acc[3] = {zero4(), zero4(), zero4()};
        unsigned tp = s == 0 ? rt_now() : tb + cP;
#define G3(A, Wt) do { acc[0] = smf(A, Wt[0], acc[0], idx); acc[1] = smf(A, Wt[1], acc[1], idx); acc[2] = smf(A, Wt[2], acc[2], idx); PIN(acc[0]); PIN(acc[1]); PIN(acc[2]); SBAR(); } while (0)
#define S1(u, A, B) do { acc[u] = smf(A, B, acc[u], idx); PIN(acc[u]); SBAR(); } while (0)
        u32x4 m0, n0, n1; unsigned nw = 0;
        unsigned char* mp = lds + L_MEM + ln * 16;
        constexpr int B0 = W == 4 ? 8 : (W == 5 ? 1 : W), B1 = W == 3 ? 9 : 2 * W - 8; constexpr bool ER = W != 1 && W < 6, LT = W == 2 || W == 3 || W >= 6;
        unsigned mo = sx + ln * 16, to = sx + M_STAMP + min(ln, 17) * 64 + (s >> 2) * 4, s0 = sb + blk_off(B0, h >> 4), s1 = sb + blk_off(B1, h >> 4), s2 = sb + blk_off(B1 + 1, h >> 4);
        auto late = [&]() { CBAR();
            if (W == 2) nw = __builtin_amdgcn_raw_buffer_load_b32(rhx, to, sb, 16); else n0 = __builtin_amdgcn_raw_buffer_load_b128(rhx, mo, s1, 16);
            if (W >= 6) n1 = __builtin_amdgcn_raw_buffer_load_b128(rhx, mo, s2, 16);
            CBAR(); SBAR(); };
        auto stuck = [&](unsigned sp) { if ((sp & 63u) == 63u && (sp > SPIN_CAP || tim[0])) { tim[0] = 1; return true; } if (sp > RTS) __builtin_amdgcn_s_sleep(RTL); return false; };
        if (W != 1) {
            PIN(wl[0]); PIN(wl[1]); PIN(wl[2]); PIN(mo); if (W == 2) PIN(to); if (ER) asm volatile("" : "+s"(s0)); if (LT && W != 2) asm volatile("" : "+s"(s1)); if (W >= 6) asm volatile("" : "+s"(s2));
            rt_fine<23>(ER ? tp : tp + SFD);
            if (ER) { CBAR(); m0 = __builtin_amdgcn_raw_buffer_load_b128(rhx, mo, s0, 16); CBAR(); SBAR(); if (LT) nops<(SFD * 23 - 12) / 8>(); }
            if (LT) late();
            if (ER) { *reinterpret_cast<u32x4*>(mp + B0 * 1024) = m0;
                if (__builtin_expect(!__all(umax4(m0) != 0xffffffffu), 0)) {
                    tim[2] = (unsigned)s + 1u;
                    for (unsigned sp = 0;; ++sp) { CBAR(); m0 = __builtin_amdgcn_raw_buffer_load_b128(rhx, mo, s0, 16); if (__all(umax4(m0) != 0xffffffffu) || stuck(sp)) break; }
                    *reinterpret_cast<u32x4*>(mp + B0 * 1024) = m0; } }
        }
        if (W == 0 || W == SW2) __builtin_amdgcn_s_setprio(SPRIO);
        AT(2);
        LBAR();
        AT(3);
        {
            constexpr int F = (ER && W != 4) ? B0 : 0, R1 = F == 0 ? 1 : 0, R2 = F <= 1 ? 2 : 1, R3 = F <= 2 ? 3 : 2, R6 = 6;
            if (!ER) { m0 = lds16(mp); SBAR(); }
            u32x4 A0 = lds16(mp + (W == 4 ? 0 : 8) * 1024); SBAR(); u32x4 A1 = lds16(mp + R1 * 1024); SBAR(); u32x4 A2 = lds16(mp + R2 * 1024); SBAR(); u32x4 A3 = lds16(mp + R3 * 1024); SBAR();
            auto take = [&]() { if (LT) {
                auto put_ = [&]() { if (W == 2) *reinterpret_cast<unsigned*>(lds + L_TIM + ln * 4) = nw; else *reinterpret_cast<u32x4*>(mp + B1 * 1024) = n0; if (W >= 6) *reinterpret_cast<u32x4*>(mp + B1 * 1024 + 1024) = n1;
                    return __all((W == 2 ? nw : max(umax4(n0), W >= 6 ? umax4(n1) : 0u)) != 0xffffffffu); };
                if (__builtin_expect(!put_(), 0)) { tim[2] = (unsigned)s + 1u; for (unsigned sp = 0;; ++sp) { late(); if (put_() || stuck(sp)) break; } }
                SBAR(); } };
            if (W != 4) G3(m0, wr[F]);
            if (STK == 0) take();
            { const u32x4 a8 = W == 4 ? m0 : A0;
              S1(0, a8, wl[0]); wl[0] = w_lds(4); SBAR(); S1(1, a8, wl[1]); wl[1] = w_lds(5); SBAR(); S1(2, a8, wl[2]); wl[2] = w_lds(6); SBAR(); }
            if (W == 4) G3(A0, wr[F]);
            if (STK == 1) take();
            G3(A1, wr[R1]);
            if (STK == 2) take();
            G3(A2, wr[R2]);
            if (LT) asm volatile("s_waitcnt lgkmcnt(0)" ::: "memory");
            CBAR(); __builtin_amdgcn_s_barrier(); CBAR(); SBAR();
            if (W == 1 && RAP == 2)
                {
                    const unsigned sr = sb0 + (unsigned)((s + 3) & 3) * (5u << 14);
                    const u32x4 on = ones4(); PIN(pn);
                    __builtin_amdgcn_raw_buffer_store_b64((u32x2){on.x, on.y}, rhx, po, sr, 16); __builtin_amdgcn_raw_buffer_store_b64((u32x2){on.x, on.y}, rhx, po, sr + 64, 16);
                    __builtin_amdgcn_raw_buffer_store_b32(on.x, rhx, pn & ~OOB, sr, 16);
                }
            SBAR();
            if (W >= 4 && KPR) __builtin_amdgcn_s_setprio(KPR);
            m0 = lds16(mp + 9 * 1024); SBAR(); A1 = lds16(mp + 4 * 1024); SBAR();
            S1(0, A3, wr[R3][0]); A2 = lds16(mp + 5 * 1024); SBAR(); S1(1, A3, wr[R3][1]); A0 = lds16(mp + 7 * 1024); SBAR(); S1(2, A3, wr[R3][2]); A3 = lds16(mp + 6 * 1024); SBAR();
            S1(1, m0, wl[1]); wl[1] = w_lds(0); SBAR(); S1(2, m0, wl[2]); wl[2] = w_lds(7); SBAR(); S1(0, m0, wl[0]);
            G3(A1, wr[4]); G3(A2, wr[5]);
            auto put = [&](int u) { const int T = 3 * (W & 3) + u; unsigned o; if (W < 4) { constexpr float c1 = 0.07216878364870322f * 1.4426950408889634f; float x = (acc[u][0] + acc[u][2]) * c1; PIN(x); o = pk2(x, (acc[u][1] + acc[u][3]) * c1); } else o = fold_pk(acc[u]);
                *reinterpret_cast<unsigned*>(qp + (T >> 1) * 512 + (T & 1) * (W < 4 ? 4 : 8)) = __builtin_amdgcn_perm(dppu<0xB1>(o), o, sel); };
            acc[0] = smf(A3, wr[R6][0], acc[0], idx); acc[1] = smf(A3, wr[R6][1], acc[1], idx); acc[0] = smf(A0, wr[7][0], acc[0], idx);
            acc[2] = smf(A3, wr[R6][2], acc[2], idx); acc[1] = smf(A0, wl[1], acc[1], idx); PIN(acc[0]); PIN(acc[1]); PIN(acc[2]); SBAR();
            put(0); SBAR();
            acc[2] = smf(A0, wl[2], acc[2], idx); PIN(acc[2]); SBAR();
            put(1); SBAR(); put(2); SBAR(); AT(4);
        }
        if (s == 0) { if (W == 2 && !early) { kx_fetch(0, true); VMDRAIN(); } LBAR(); }
        if ((W == 0 || W == SW2) && s + 1 < NSEG_) {
            PIN(ln);
            unsigned kp = ldsb + L_Q + ln * 16, mk = ldsb + L_K + (ln >> 4) * 128 + ((ln + ((ln >> 2) & 4)) & 7) * 16;
            constexpr int KO = L_KX - L_Q;
            u32x8 ka = cat8(lds16a(kp + KO), lds16a(kp + KO + 1024)), kb = cat8(lds16a(kp + KO + 6144), lds16a(kp + KO + 7168)), kc = cat8(lds16a(kp + KO + 2048), lds16a(kp + KO + 3072)), kd = cat8(lds16a(kp + KO + 8192), lds16a(kp + KO + 9216));
            f32x4 sc[3] = {zero4(), zero4(), zero4()};
            PIN(ka); PIN(kb); PIN(kc); PIN(kd); PIN(sc[0]); PIN(sc[1]); PIN(sc[2]); PIN(kp); PIN(mk);
            unsigned sq = sb + (W ? 64 : 0); asm volatile("" : "+s"(sq));
            __builtin_amdgcn_s_setprio(3);
            LBAR();
            AT(5);
            const u32x4 q0 = lds16a(kp); SBAR(); u32x8 ke = cat8(lds16a(mk), lds16a(mk + 512)); SBAR(); const u32x4 q1 = lds16a(kp + 1024); SBAR(); const u32x4 q2 = lds16a(kp + 2048); SBAR();
            sc[0] = smf(q0, ka, sc[0], idx); sc[1] = smf(q0, kb, sc[1], idx); PIN(sc[0]); PIN(sc[1]); SBAR();
            ka = cat8(lds16a(mk + 1024), lds16a(mk + 1536)); SBAR(); kb = cat8(lds16a(kp + KO + 4096), lds16a(kp + KO + 5120)); SBAR();
            sc[2] = smf(q0, ke, sc[2], idx); PIN(sc[2]); SBAR();
            ke = cat8(lds16a(kp + KO + 10240), lds16a(kp + KO + 11264)); SBAR();
            sc[0] = smf(q1, kc, sc[0], idx); PIN(sc[0]); SBAR();
            kc = cat8(lds16a(mk + 2048), lds16a(mk + 2560)); SBAR();
            sc[1] = smf(q1, kd, sc[1], idx); sc[2] = smf(q1, ka, sc[2], idx); sc[0] = smf(q2, kb, sc[0], idx); sc[1] = smf(q2, ke, sc[1], idx); sc[2] = smf(q2, kc, sc[2], idx); SBAR();
            AT(6);
            constexpr int R = W ? 1 : 0;
            float s0 = sc[0][R] + sc[0][R + 2], s1 = sc[1][R] + sc[1][R + 2]; PIN(s0); PIN(s1);
            float s2 = sc[2][R] + sc[2][R + 2], mx = max3f(s0, s1, s2), g0, g1, g2, g3, g4, g5;
            asm volatile("s_nop 1\n\tv_max_f32_dpp %0, %0, %0 row_ror:8 row_mask:0xf bank_mask:0xf\n\ts_nop 1\n\tv_max_f32_dpp %0, %0, %0 row_ror:4 row_mask:0xf bank_mask:0xf\n\ts_nop 1\n\t"
                "v_max_f32_dpp %0, %0, %0 row_ror:2 row_mask:0xf bank_mask:0xf\n\ts_nop 1\n\tv_max_f32_dpp %0, %0, %0 row_ror:1 row_mask:0xf bank_mask:0xf\n\t"
                "v_sub_f32 %1, %1, %0\n\tv_sub_f32 %2, %2, %0\n\tv_sub_f32 %3, %3, %0\n\tv_exp_f32 %1, %1\n\tv_exp_f32 %2, %2\n\tv_exp_f32 %3, %3\n\t"
                "v_add_f32 %0, %1, %2\n\tv_fmac_f32 %0, 0.5, %3\n\t"
                "v_mov_b32_dpp %4, %1 quad_perm:[0,0,2,2] row_mask:0xf bank_mask:0xf\n\tv_mov_b32_dpp %5, %2 quad_perm:[0,0,2,2] row_mask:0xf bank_mask:0xf\n\tv_add_f32_dpp %0, %0, %0 row_ror:8 row_mask:0xf bank_mask:0xf\n\t"
                "v_mov_b32_dpp %6, %1 quad_perm:[1,1,3,3] row_mask:0xf bank_mask:0xf\n\tv_mov_b32_dpp %7, %2 quad_perm:[1,1,3,3] row_mask:0xf bank_mask:0xf\n\tv_add_f32_dpp %0, %0, %0 row_ror:4 row_mask:0xf bank_mask:0xf\n\t"
                "v_mov_b32_dpp %8, %3 quad_perm:[0,0,2,2] row_mask:0xf bank_mask:0xf\n\tv_mov_b32_dpp %9, %3 quad_perm:[1,1,3,3] row_mask:0xf bank_mask:0xf\n\tv_add_f32_dpp %0, %0, %0 row_ror:2 row_mask:0xf bank_mask:0xf\n\t"
                "s_nop 1\n\tv_add_f32_dpp %0, %0, %0 row_ror:1 row_mask:0xf bank_mask:0xf"
                : "+v"(mx), "+v"(s0), "+v"(s1), "+v"(s2), "=&v"(g0), "=&v"(g1), "=&v"(g2), "=&v"(g3), "=&v"(g4), "=&v"(g5));
            const float z = __builtin_amdgcn_rcpf(mx);
            __builtin_amdgcn_raw_buffer_store_b64((u32x2){pk2(g0 * z, g1 * z), pk2(g2 * z, g3 * z)}, rhx, po, sq, 16); SBAR();
            __builtin_amdgcn_raw_buffer_store_b32(pk2(g4 * z, g5 * z), rhx, pn, sb, 16);
            SBAR(); AT(7);
            tim[1 + 2 * R] = rt_now() - (tb + cP);
            tim[4 + R] = (unsigned)s + 1u;
            __builtin_amdgcn_s_setprio(0);
            PIN(ln);
            tt_step(*reinterpret_cast<const unsigned*>(lds + L_TIM + ln * 4), s, s == 0 ? rt_now() - (W == 0 || W == SW2 ? TT_E1 : TT_E1 - 35) : 0u, tb, cC1, cP);
        } else {
            LBAR();
            if (W == 1 && KME) {
#pragma unroll
                for (int m = 0; m < 3; ++m) st16s(lds16(lds + L_K + m * 1024 + ln * 16), rst, ln * 16, ST_KM + ((s & (PR - 1)) * 32 + h) * 3072 + m * 1024);
            }
            if (W >= 4 && KPR) __builtin_amdgcn_s_setprio(0);
            AT(5);
            tt_step(*reinterpret_cast<const unsigned*>(lds + L_TIM + ln * 4), s, s == 0 ? rt_now() - (W == 0 || W == SW2 ? TT_E1 : TT_E1 - 35) : 0u, tb, cC1, cP);
        }
        {
            if (W == KXW && s + 1 < NSEG_) wait_set(rfl, F_XP + ((s + 1) * NXP_ + xj) * FLP, hop, 0);
            if ((W == 1 || W == KXW) && s + 1 < NSEG_) { for (unsigned sp = 0; *(volatile __attribute__((address_space(3))) unsigned long long*)(tim + 4) != ((unsigned long long)s + 1u) * 0x100000001ull && sp < SPIN_CAP; ++sp) __builtin_amdgcn_s_sleep(SPS); CBAR(); if (W == 1 && SLATE1) __builtin_amdgcn_s_sleep(SLATE1); if (W == 2 && SLATE2) __builtin_amdgcn_s_sleep(SLATE2); }
            if (W == KXW && s + 1 < NSEG_) dma16cn<12>(rst, ln * 16, ST_KX + (((s + 1) & (KXR - 1)) * 32 + h) * 12288, ldsb + L_KX);
            if (W == 1) {
                kdur = min(__builtin_amdgcn_readfirstlane(max((unsigned)tim[1], (unsigned)tim[3])), 4095u); krt = __builtin_amdgcn_readfirstlane(tim[2]) == (unsigned)s + 1u ? 1u : 0u;
                if (!KME) {
#pragma unroll
                for (int m = 0; m < 3; ++m) st16s(lds16(lds + L_K + m * 1024 + ln * 16), rst, ln * 16, ST_KM + ((s & (PR - 1)) * 32 + h) * 3072 + m * 1024);
                }
                if (RAP == 0)
                {
                    const unsigned sr = sb0 + (unsigned)((s + 3) & 3) * (5u << 14);
                    const u32x4 on = ones4(); PIN(pn);
                    __builtin_amdgcn_raw_buffer_store_b64((u32x2){on.x, on.y}, rhx, po, sr, 16); __builtin_amdgcn_raw_buffer_store_b64((u32x2){on.x, on.y}, rhx, po, sr + 64, 16);
                    __builtin_amdgcn_raw_buffer_store_b32(on.x, rhx, pn & ~OOB, sr, 16);
                }
                VMW(RAP == 0 ? 3 : 0); if (ln == 0) st16c(rfl, F_KM + (s * 32 + h) * FLP, (u32x4){cP, kdur, krt, tb}); if (ln == 0 && h == 0 && s >= 56) STAMP(370 + s - 56);
                if (RAP == 1)
                {
                    const unsigned sr = sb0 + (unsigned)((s + 3) & 3) * (5u << 14);
                    const u32x4 on = ones4(); PIN(pn);
                    __builtin_amdgcn_raw_buffer_store_b64((u32x2){on.x, on.y}, rhx, po, sr, 16); __builtin_amdgcn_raw_buffer_store_b64((u32x2){on.x, on.y}, rhx, po, sr + 64, 16);
                    __builtin_amdgcn_raw_buffer_store_b32(on.x, rhx, pn & ~OOB, sr, 16);
                }
            }
        }
        ATD(0, s == 41 && h == 0);
    }
    if (W == 0) { tim[2] = 0; tim[3] = 0; tim[4] = 0; tim[5] = 0; }
    if (W == 1 && lane_id() == 0) STAMP(512 + blockIdx.x);
    }
    if (W == 0 && tim[0]) set_err((a.ws + WS_HOP));
}

__device__ __forceinline__ void s_part(KA& a, unsigned char* lds, const int h, const int wave) {
    const int lane = lane_id();
    const unsigned ldsb = LDSB();
    u32x8 wr[8][3];
    auto keep = [&](int f, u32x8 v) { unsigned char* p = lds + 32768 + f * 16384 + wave * 2048 + lane * 16; *reinterpret_cast<u32x4*>(p) = lo4(v); *reinterpret_cast<u32x4*>(p + 1024) = hi4(v); };
    load_w<10>(MKR(a.wq, D_ * HE_ * 2), MKR(a.wk, D_ * HE_ * 2), h >> 4, h * 192, lds, ldsb, wave, lane, [&](int rd, int u, u32x8 v) { if (rd >= 8) keep(3 * rd - 23 + u, v); else if (rd == 7 && u == 2) keep(7, v); else wr[rd][u] = v; });
    keep(0, wr[7][1]);
    if (wave == 7) s_run<7>(a, lds, h, wr); else if (wave == 6) s_run<6>(a, lds, h, wr); else if (wave == 5) s_run<5>(a, lds, h, wr); else if (wave == 4) s_run<4>(a, lds, h, wr);
    else if (wave == 3) s_run<3>(a, lds, h, wr); else if (wave == 2) s_run<2>(a, lds, h, wr); else if (wave == 1) s_run<1>(a, lds, h, wr); else s_run<0>(a, lds, h, wr);
}

#define VSL0 1
#define WLD 4
#define TT_WD 45
#define ATH 0
#define LBAR_NONE() do { } while (0)
template <int N, class W, class O, class F> __device__ __forceinline__ void u_tiles(u32x4 (&xf)[6][2], rsrc_t rst, unsigned l16, W&& w, O&& off, F&& side) {
    f32x4 acc[2][2]; u32x4 o; unsigned so;
#pragma unroll
    for (int i = 0; i < N; ++i) {
#pragma unroll
        for (int j = 0; j < 12; ++j) { const int k = j >> 1, r = j & 1;
            acc[i & 1][r] = dmf(xf[k][r], w(i, k), k ? acc[i & 1][r] : Z4); PIN(acc[i & 1][r]); SBAR();
            side(i, j);
            if (i > 0) { const f32x4 c0 = acc[(i - 1) & 1][0], c1 = acc[(i - 1) & 1][1];
                if (j == 2) { so = off(i - 1); asm volatile("" : "+s"(so)); }
                if (j == 3) { o.x = pk2(c0[0], c1[0]); PIN(o.x); } if (j == 4) { o.y = pk2(c0[2], c1[2]); PIN(o.y); } if (j == 5) { o.z = pk2(c0[1], c1[1]); PIN(o.z); } if (j == 6) { o.w = pk2(c0[3], c1[3]); PIN(o); }
                if (j == 7) __builtin_amdgcn_raw_buffer_store_b128(o, rst, l16, so, 16);
                if (j == 9) asm volatile("" :: "v"(o)); }
            SBAR(); }
    }
    const f32x4 c0 = acc[(N - 1) & 1][0], c1 = acc[(N - 1) & 1][1];
    st16s((u32x4){pk2(c0[0], c1[0]), pk2(c0[2], c1[2]), pk2(c0[1], c1[1]), pk2(c0[3], c1[3])}, rst, l16, off(N - 1));
}
__device__ __forceinline__ unsigned vblk(int j, unsigned nr) { const unsigned v = (unsigned)(0x9873265410ull >> (4 * j)) & 15u; return nr ? (v < 5 ? v + 5 : v - 5) : v; }
template <bool XW> __device__ __forceinline__ void v_run(KA& a, unsigned char* lds, const int h, const int wave) {
    const int lane = lane_id(), tid = wave * 64 + lane;
    const unsigned ldsb = LDSB();
    constexpr int L_MEM = 0, L_V = 10240, L_VX = 13312, L_TIM = 25600, L_E = 26624, L_F = L_E + 2048, L_T = L_F + 1024, L_U = L_T + 1024, L_W = 16384, NO = XW ? 4 : 6, NV = XW ? 2 : 1, KR = XW ? 8 : 4;
    constexpr int NBK = XW ? 2 : 6;
    u32x8 wo[NBK][3], wv[KR][NV];
    const int lw0 = XW ? 65536 + wave * 8192 : wave * 8192, lw1 = XW ? 114688 + wave * 12288 : 81920 + wave * 4096;
    const int vt = XW ? 2 * wave : 4 + wave;
    {
        const rsrc_t rv = MKR(a.wv, D_ * HE_ * 2), ro = MKR(a.wo, HE_ * D_ * 2);
        const unsigned l15 = lane & 15, lg = lane >> 4, row = 8 * wave + (lane >> 3), ch = ((lane & 7) ^ (wsw(row) << 1)) * 16;
        const unsigned vo = row * (HE_ * 2) + h * 384 + ch, oo = (h * 192 + row) * (D_ * 2) + ch;
        u32x8 tmp[6];
        load_b<6, 3, 10, L_W, 2>(ldsb, wave,
            [&](int rd, int j, unsigned dst) { if (rd < 4 || rd == 9) dma16(ro, oo, (j >> 1) * (64 * D_ * 2) + (rd % 5) * 256 + (j & 1) * 128, dst); else dma16(rv, vo, vblk((rd - 4) * 2 + j / 3, h >> 4) * (64 * HE_ * 2) + (j % 3) * 128, dst); },
            [&](int rd, int base) {
                if (rd < 4 || rd == 9) {
#pragma unroll
                    for (int i = 0; i < (rd < 2 ? 1 : (XW == (rd == 9) ? 2 : 0)); ++i) { const int tl = rd < 2 ? wave : (rd == 2 ? (wave & 3) + 4 * i : 2 * (wave & 3) + i);
#pragma unroll
                        for (int k = 0; k < 3; ++k) { const unsigned char* p = lds + base + (2 * k + (tl >> 2)) * 8192; const u32x8 v = cat8(ld_frag(p, 4 * lg, 16, tl & 3, l15), ld_frag(p, 32 + 4 * lg, 16, tl & 3, l15));
                            if (rd == 9) tmp[3 * i + k] = v; else wo[(rd < 2 ? rd : 2 * rd - 2 + i) % NBK][k] = v; } }
                } else {
#pragma unroll
                    for (int k = 0; k < 2; ++k)
#pragma unroll
                        for (int u = 0; u < NV; ++u) { const unsigned char* p = lds + base + (3 * k + (vt >> 2)) * 8192; const u32x8 v = cat8(ld_frag(p, 8 * lg, 4, (vt & 3) + u, l15), ld_frag(p, 32 + 8 * lg, 4, (vt & 3) + u, l15));
                            const int j = 2 * (rd - 4) + k; if (j < KR) wv[j % KR][u] = v; else tmp[XW ? 2 * k + u : j & 3] = v; } } },
            [&](int rd) {
                const int n = XW ? (rd == 8 ? 4 : (rd == 9 ? 6 : 0)) : (rd == 7 ? 4 : (rd == 8 ? 2 : 0));
#pragma unroll
                for (int q = 0; q < n; ++q) { unsigned char* p = lds + (n == 4 ? lw0 : lw1) + q * 2048 + lane * 16; *reinterpret_cast<u32x4*>(p) = lo4(tmp[q]); *reinterpret_cast<u32x4*>(p + 1024) = hi4(tmp[q]); } });
    }
    const rsrc_t rhx = MKR(al2m((a.ws + WS_FR)), FR_USED), rst = MKR((a.ws + WS_ST), ST_BYTES);
    lds_u32* tim = (lds_u32*)(lds + L_TIM);
    if (tid == 0) { tim[0] = 0; tim[2] = 0; }
    const unsigned pol = page_pol(a), sx = ((my_side() ^ pol) & 1u) << 13;
    unsigned uo0, uo1 = 0, l16 = lane * 16;
    { const unsigned long long sd = um_sides(a);
      uo0 = um_base(sd, pol, lane < 32 ? 8 * (lane >> 4) + wave : (XW ? 30 + 2 * wave + (lane >> 4) : 4 + wave + 4 * (lane >> 4))) + h * 256 + (lane & 15) * 16;
      if (!XW) uo1 = um_base(sd, pol, 16 + 2 * wave + (lane >> 5)) + h * 256 + (lane & 15) * 16 + (lane & 16) / 2; }
    auto arm = [&](unsigned so) { const u32x4 on = ones4(); st16s(on, rhx, uo0, so); if (!XW) __builtin_amdgcn_raw_buffer_store_b64((u32x2){on.x, on.y}, rhx, uo1, so, 16); SBAR(); };
    arm(16384); arm(32768); arm(49152);
    const int xj = xp_of(h), xj2 = 3 * (h >> 1) + 2;
    if (XW) uo1 = wave < 2 ? l16 : M_STAMP + min(lane, 17) * 64;
    const int j0 = (0x6510 >> (4 * (wave & 3))) & 15, j1 = (0x9842 >> (4 * (wave & 3))) & 15, j2 = wave ? 7 : 3;
    auto voff = [&](int j) { const unsigned b = vblk(j, h >> 4); return b * 1024 + (b < 8 ? 0 : 8192); };
    const unsigned o0 = voff(j0), o1 = voff(j1), o2 = wave < 2 ? voff(j2) : 0u;
    PIN(uo0); PIN(uo1); PIN(l16);
    *reinterpret_cast<u32x4*>(lds + L_T + l16) = (u32x4){L_V + ((lane >> 3) & 1) * 256u + (lane >> 4) * 64u + ((lane >> 1) & 1) * 32u + (lane & 1) * 16u + ((lane >> 2) & 1) * 8u,
        (lane & 1) ? 0x03020706u : 0x05040100u, m_idx(lane), F_XP + ((lane & 1) ? xj : xj2) * FLP};
    VMKNOWN();
    ATV;
#define la ((int)l16)
    auto u_so = [&](int t, int i) { return ST_U + (((t & (URING - 1)) * 40 + (i < 2 ? 8 * i + wave : (i < 4 ? 4 + wave + 4 * i : 12 + 2 * wave + i))) * 32 + h) * 1024; };
    u32x4 xf[6][2];
    auto back0 = [&](int q) { xf[q >> 1][q & 1] = lds16a(la + L_VX + (q & 1) * 6144 + (q >> 1) * 1024); SBAR(); };
    auto back = [&](auto N, int t, auto&& side) { u_tiles<decltype(N)::value>(xf, rst, l16, [&](int i, int k) { return (k & 1) ? hi4(wo[i][k >> 1]) : lo4(wo[i][k >> 1]); }, [&](int i) { return u_so(t, i); }, [&](int i, int j) { if (i == 0) { side(j); if (j + 4 < 12) back0(j + 4); } }); };
    auto v_so = [&](int t) { return ST_VX + ((t & (KXR - 1)) * 32 + h) * 12288 + (wave & 3) * 3072; };
#pragma unroll 1
    for (int layer = 0; layer < 2; ++layer) {
    unsigned char* hop = (a.ws + WS_HOP) + layer * HOP_BYTES;
    const rsrc_t rfl = MKR(hop, F_ERR);
    const unsigned sb0 = (unsigned)layer * (20u << 14) + sx;
    if (wave == 0 && l16 == 0) STAMP(140 + h);
    unsigned tb = rt_now(), cC1 = 0, cP = 0;
#pragma unroll 1
    for (int s = 0; s < NSEG_; ++s) {
        PIN(l16);
        int w0o = la + lw0, w1o = la + lw1; unsigned us = (s & 3) << 14;
        PIN(w0o); PIN(w1o); asm volatile("" : "+s"(us));
        const u32x4 lc = lds16a(la + L_T);
        unsigned fw = 1u, d0 = 0, d1 = 0;
        if (!XW) fw = lds4a(L_F + (l16 >> 2));
        SBAR();
        constexpr int NL = 4;
        u32x4 wl[NL][2];
#pragma unroll
        for (int u = 0; u < 2 * NL; ++u) wl[u >> 1][u & 1] = lds16a(w0o + u * 1024);
        f32x4 acc[NV];
#pragma unroll
        for (int u = 0; u < NV; ++u) acc[u] = zero4();
        const unsigned sel = lc.y, idx = lc.z;
        SBAR();
        u32x4 m1, m2; unsigned s1 = 0, s2 = 0; int w1 = 0, w2 = 0;
        auto late = [&]() { CBAR(); m1 = __builtin_amdgcn_raw_buffer_load_b128(rhx, l16, s1, 16); SBAR(); m2 = __builtin_amdgcn_raw_buffer_load_b128(rhx, uo1, s2, 16); CBAR(); SBAR(); };
        auto stuck = [&](unsigned sp) { if ((sp & 63u) == 63u && (sp > SPIN_CAP || tim[0])) { tim[0] = sp; return true; } if (sp > 8) __builtin_amdgcn_s_sleep(RTL); return false; };
        if (XW) {
            const unsigned sb = sb0 + (unsigned)(s & 3) * (5u << 14);
            unsigned s0 = sb + o0; s1 = sb + o1; s2 = sb + (wave < 2 ? o2 : (s >> 2) * 4);
            int w0 = la + j0 * 1024; w1 = la + j1 * 1024; w2 = la + (wave < 2 ? j2 * 1024 : L_E - 2048 + wave * 1024);
            PIN(w0); PIN(w1); PIN(w2); asm volatile("" : "+s"(s0), "+s"(s1), "+s"(s2));
            AT(0);
            rt_fine<21>(tb + cP + TT_VD);
            AT(6);
            CBAR(); u32x4 m0 = __builtin_amdgcn_raw_buffer_load_b128(rhx, l16, s0, 16); CBAR(); SBAR();
            nops<(VFD * 21 - 12) / 8>();
            late();
            sts16a(w0, m0);
            if (__builtin_expect(!__all(umax4(m0) != 0xffffffffu), 0)) {
                tim[2] = (unsigned)s + 1u;
                for (unsigned sp = 0;; ++sp) { CBAR(); m0 = __builtin_amdgcn_raw_buffer_load_b128(rhx, l16, s0, 16); if (__all(umax4(m0) != 0xffffffffu) || stuck(sp)) break; }
                sts16a(w0, m0); }
        } else {
            if (__builtin_expect(s > 0 && !__all(fw != 0u), 0)) { wait_set(rfl, lc.w + min(s + 2, NSEG_ - 1) * (NXP_ * FLP), hop, 0); VMKNOWN(); }
            d0 = v_so(s + 2); d1 = min(s + 3, NSEG_ - 1) * (NXP_ * FLP); asm volatile("" : "+s"(d0), "+s"(d1));
        }
        AT(1);
        if (XW) { LBAR(); __builtin_amdgcn_s_setprio(0); } else { __builtin_amdgcn_s_setprio(YP1); CBAR(); __builtin_amdgcn_s_barrier(); CBAR(); }
        AT(2);
        f32x4 um[NO];
        {
#define VS(u, A, B) do { acc[(u) % NV] = smf(A, B, acc[(u) % NV], idx); PIN(acc[(u) % NV]); SBAR(); } while (0)
#define VL(u) cat8(wl[(u) % NL][0], wl[(u) % NL][1])
#define VA(k) lds16a(la + L_MEM + (k) * 1024); SBAR()
#define VW(u, f, q) wl[u][q] = lds16a(w1o + (f) * 2048 + (q) * 1024); SBAR()
            int qo;
            if (XW) {
                u32x4 A0 = VA(5); u32x4 A1 = VA(6); u32x4 A2 = VA(0);
                auto take = [&]() { const unsigned em = wave < 2 ? ~0u : 0u;
                    auto put_ = [&]() { sts16a(w1, m1); sts16a(w2, m2); SBAR(); return __all(max(umax4(m1), max(m2.x, max(max(m2.y, m2.z), m2.w) & em)) != 0xffffffffu); };
                    if (__builtin_expect(!put_(), 0)) { tim[2] = (unsigned)s + 1u; for (unsigned sp = 0;; ++sp) { late(); if (put_() || stuck(sp)) break; } }
                    SBAR(); };
#define V2(A, j) VS(0, A, wv[(j) % KR][0]); VS(1, A, wv[(j) % KR][NV - 1])
                V2(A0, 5);
                take();
                u32x4 A3 = VA(1);
                V2(A1, 6);
                LBAR(); SBAR();
                A0 = VA(8); A1 = VA(9);
                V2(A2, 0); A2 = VA(2); V2(A3, 1); A3 = VA(3);
                VS(0, A0, VL(0)); VS(1, A0, VL(1)); A0 = VA(4); VS(0, A1, VL(2)); VS(1, A1, VL(3)); A1 = VA(7);
                VS(0, A2, wv[2][0]); VW(0, 0, 0); VS(1, A2, wv[2][NV - 1]); VW(0, 0, 1);
                VS(0, A3, wv[3][0]); VW(1, 1, 0); um[0] = zero4(); SBAR(); VS(1, A3, wv[3][NV - 1]); VW(1, 1, 1);
                VS(0, A0, wv[4 % KR][0]); VW(2, 2, 0); um[1] = zero4(); qo = lc.x + (vt >> 1) * 512; PIN(qo); SBAR(); VS(0, A1, wv[7 % KR][0]); VW(2, 2, 1);
                VS(1, A0, wv[4 % KR][NV - 1]); VS(1, A1, wv[7 % KR][NV - 1]);
            } else {
                u32x4 A0 = VA(5); u32x4 A1 = VA(6); u32x4 A2 = VA(0); u32x4 A3 = VA(1);
                dma16cn<3>(rst, l16, d0, L_VX + (wave & 3) * 3072); SBAR();
                VS(0, A0, VL(1)); VS(0, A1, VL(2));
                CBAR(); __builtin_amdgcn_s_barrier(); CBAR(); SBAR();
                if (YP2) __builtin_amdgcn_s_setprio(YP2);
                A0 = VA(4); A1 = VA(7);
                VS(0, A2, wv[0][0]); A2 = VA(2);
                VS(0, A3, wv[1][0]); A3 = VA(3); qo = lc.x + ((vt >> 1) * 512 + (vt & 1) * 4); PIN(qo); SBAR();
                VW(1, 0, 0); VW(1, 0, 1); VS(0, A0, VL(0)); A0 = VA(8); VW(2, 1, 0); VS(0, A1, VL(3)); A1 = VA(9); VW(2, 1, 1);
                VS(0, A2, wv[2][0]); um[0] = zero4(); um[1] = zero4(); SBAR(); VS(0, A3, wv[3][0]); um[2] = zero4(); um[3] = zero4(); SBAR();
                VS(0, A0, VL(1)); um[NO - 2] = zero4(); um[NO - 1] = zero4(); SBAR(); VS(0, A1, VL(2));
            }
            unsigned o[NV];
#pragma unroll
            for (int u = 0; u < NV; ++u) { o[u] = fold_pk(acc[u]); o[u] = __builtin_amdgcn_perm(dppu<0xB1>(o[u]), o[u], sel); }
            if (XW) { *(__attribute__((address_space(3))) u32x2*)(uintptr_t)(unsigned)qo = (u32x2){o[0], o[NV - 1]}; SBAR(); um[2] = zero4(); um[3] = zero4();
            } else { *(__attribute__((address_space(3))) unsigned*)(uintptr_t)(unsigned)qo = o[0]; __builtin_amdgcn_s_setprio(2); }
        }
        AT(3);
        VMDRAIN(); LBAR();
        AT(4);
        {
            const u32x4 q0 = lds16a(la + L_V), q1 = lds16a(la + L_V + 1024), q2 = lds16a(la + L_V + 2048); SBAR();
#define VU(u, q, k) do { um[u] = smf(q, (XW && (u) >= 2) ? VL(k) : wo[(u) % NBK][k], um[u], idx); PIN(um[u]); SBAR(); } while (0)
#define VO(u) min(fold_pk(um[u]), 0xfffffffeu)
            if (!XW) { VU(NO - 2, q0, 0); VU(NO - 1, q0, 0); VU(NO - 2, q1, 1); VU(NO - 1, q1, 1); VU(NO - 2, q2, 2); VU(NO - 1, q2, 2); }
            VU(0, q0, 0); VU(2, q0, 0);
            unsigned e0 = 0, e1 = 0;
            if (!XW) { e0 = VO(NO - 2); PIN(e0); SBAR(); }
            VU(0, q1, 1);
            if (XW) { VW(0, 3, 0); VW(0, 3, 1); }
            if (!XW) { e1 = VO(NO - 1); PIN(e1); SBAR(); }
            VU(2, q1, 1);
            if (!XW) { const auto z = __builtin_amdgcn_permlane32_swap(e0, e1, false, false); __builtin_amdgcn_raw_buffer_store_b64((u32x2){z[0], z[1]}, rhx, uo1, us, 16); SBAR(); }
            VU(0, q2, 2); if (XW) { VW(1, 4, 0); VW(1, 4, 1); } else back0(2); VU(2, q2, 2); if (!XW) back0(3); VU(1, q0, 0); if (XW) { VW(2, 5, 0); VW(2, 5, 1); } back0(0); VU(1, q1, 1); back0(1);
            e0 = VO(0); PIN(e0); SBAR();
            VU(1, q2, 2);
            e1 = VO(2); PIN(e1); SBAR();
            VU(3, q0, 0);
            const auto x02 = __builtin_amdgcn_permlane32_swap(e0, e1, false, false); SBAR();
            VU(3, q1, 1); if (XW) { back0(2); back0(3); } VU(3, q2, 2);
            e0 = VO(1); PIN(e0); SBAR();
            const auto x13 = __builtin_amdgcn_permlane32_swap(e0, VO(3), false, false);
            const auto y01 = __builtin_amdgcn_permlane16_swap(x02[0], x13[0], false, false), y23 = __builtin_amdgcn_permlane16_swap(x02[1], x13[1], false, false);
            st16s((u32x4){y01[0], y23[0], y01[1], y23[1]}, rhx, uo0, us);
            SBAR();
            AT(5);
            __builtin_amdgcn_s_setprio(XW ? 2 : 0);
        }
        if (__builtin_expect(s == 0, 0)) {
            if (!XW) sts4a(L_F + (l16 >> 2), 0u);
#pragma unroll 1
            for (int t = 0; t < 3; ++t) {
                if (XW) { wait_set(rfl, lds4a(la + L_T + 12) + t * (NXP_ * FLP), hop, t ? 0 : VSL0); dma16cn<3>(rst, l16, v_so(t), L_VX + wave * 3072); VMDRAIN(); }
                LBAR();
                back0(0); back0(1); back0(2); back0(3);
                back(std::integral_constant<int, NBK>{}, t, [](int) {});
                if (t == 2) break;
                VMDRAIN(); LBAR();
                if (wave == 0 && l16 == 0) st4c(rfl, F_UX + (t * 32 + h) * FLP, (unsigned)(t + 1) << 16);
                if (wave == 0 && l16 == 0 && h == 0) STAMP(10 + t);
            }
        } else {
            unsigned tw, tr, te; int d;
            back(std::integral_constant<int, XW ? 2 : 3>{}, s + 2, [&](int j) {
                if (XW) {
                    if (j == 0) { tw = lds4a(la + L_E); tr = tim[2]; te = rt_now(); asm volatile("" : "+s"(te)); }
                    if (j == 2) { d = (int)(tw - tb - cC1); PIN(d); }
                    if (j == 3) { d = max(d, (int)dppu<0x128>((unsigned)d)); PIN(d); } if (j == 4) { d = max(d, (int)dppu<0x124>((unsigned)d)); PIN(d); }
                    if (j == 5) { d = max(d, (int)dppu<0x122>((unsigned)d)); PIN(d); } if (j == 6) { d = max(d, (int)dppu<0x121>((unsigned)d)); PIN(d); }
                    if (j == 7) tim[4 + (s & 1)] = min(te - (tb + cP), 4095u) + (__builtin_amdgcn_readfirstlane(tr) == (unsigned)s + 1u ? 4096u : 0u);
                    if (j == 8) { const int e = __builtin_amdgcn_readlane(d, 0); tb = (s == 1 || e > 0) ? tb + cC1 + (unsigned)e + (s == 1 ? TT_V1 : TT_V) : tb + cP; cC1 = __builtin_amdgcn_readlane(tw, 16); cP = __builtin_amdgcn_readlane(tw, 17); asm volatile("" : "+s"(tb), "+s"(cC1), "+s"(cP)); }
                }
                if (!XW && j == 0) tw = tim[4 + ((s + 1) & 1)];
                if (!XW && j == 2) __builtin_amdgcn_raw_buffer_store_b32(tw | 65536u, rfl, wave == 7 ? l16 << 20 : OOB, F_UX + ((s + 1) * 32 + h) * FLP, 16);
                if (!XW && j == 5) dma4c(rfl, lds4a(la + L_T + 12), d1, L_F);
                });
            if (!XW) VMW(3);
        }
        AT(7);
        ATD(1, s == 41 && h == ATH);
    }
    if (wave == 0 && l16 == 0) { tim[2] = 0; STAMP(512 + blockIdx.x); }
    }
    if (wave == 0 && l16 == 0 && tim[0]) set_err((a.ws + WS_HOP));
#undef la
}

__device__ __forceinline__ void w_part(KA& a, unsigned char* lds, const int w, const int wave) {
    const int lane = lane_id(), hw = wave >> 1, t0 = (wave & 1) * 7;
    const unsigned ldsb = LDSB();
    u32x4 wo[7][6];
    {
        const rsrc_t ro = MKR(a.wo, HE_ * D_ * 2);
        const unsigned row = 8 * wave + (lane >> 3), oo = ((4 * w + (row >> 5)) * 192 + (row & 31)) * (D_ * 2) + 640 + ((lane & 7) ^ (wsw(row) << 1)) * 16;
        load_b<8, 2, 6, 0, 0>(ldsb, wave, [&](int rd, int j, unsigned dst) { dma16(ro, oo, (rd * 32 + (j >> 2) * 384) * (D_ * 2) + (j & 3) * 128, dst); },
            [&](int rd, int base) {
#pragma unroll
                for (int i = 0; i < 7; ++i) wo[i][rd] = ld_frag(lds + base + ((hw >> 1) * 4 + ((t0 + i) >> 2)) * 8192, (hw & 1) * 32 + 4 * (lane >> 4), 16, (t0 + i) & 3, lane & 15); }, [](int) {});
    }
    const rsrc_t rst = MKR(a.ws + WS_ST, ST_BYTES);
    if (wave == 0) init_wait(a);
#pragma unroll 1
    for (int layer = 0; layer < 2; ++layer) {
    unsigned char* hop = a.ws + WS_HOP + layer * HOP_BYTES;
    const rsrc_t rfl = MKR(hop, F_ERR);
    unsigned l16 = lane_id() * 16; PIN(l16);
    const unsigned fx = F_XP + (6 * w + (lane & 7) % 6) * FLP;
    auto v_so = [&](int t) { return ST_VX + ((t & (KXR - 1)) * 32 + 4 * w) * 12288 + wave * 6144; };
    if (wave == 0) { wait_set(rfl, fx + NXP_ * FLP, hop, 8); wait_set(rfl, fx + 3 * (NXP_ * FLP), hop, 1); }
    LBAR();
    dma16cn<6>(rst, l16, v_so(3), ldsb + 49152 + wave * 6144);
    VMKNOWN(); LBAR();
    unsigned tnext = rt_now();
#pragma unroll 1
    for (int t = 3; t < NSEG_; ++t) {
        u32x4 fl, xf[6][2];
        const unsigned fo = (lane < 8 && t >= WLD) ? F_KM + ((t - WLD) * 32 + 4 * w) * FLP : fx + min(t + 1, NSEG_ - 1) * (NXP_ * FLP);
        if (wave == 0) asm volatile("buffer_load_dwordx4 %0, %1, %2, 0 offen sc1" : "=&v"(fl) : "v"(fo), "s"(rfl) : "memory");
        const int xp = ldsb + (t & 1) * 49152 + hw * 12288 + l16;
        auto rd = [&](int q) { xf[q >> 1][q & 1] = lds16a(xp + (q & 1) * 6144 + (q >> 1) * 1024); SBAR(); };
        rd(0); rd(1); rd(2); rd(3);
        u_tiles<7>(xf, rst, l16, [&](int i, int k) { return wo[i][k]; }, [&](int i) { return ST_U + (((t & (URING - 1)) * 40 + 20 + t0 + i) * 32 + 4 * w + hw) * 1024; }, [&](int i, int j) {
            if (i == 0 && j + 4 < 12) rd(j + 4);
            if (i == 1 && j == 0) { if (wave == 0) VMW(1); else VMDRAIN(); __builtin_amdgcn_s_barrier(); CBAR(); if (wave == 1 && t > 3 && l16 == 0) st4c(rfl, F_UW + ((t - 1) * 8 + w) * FLP, 1u); }
            if (i == 2 && j == 0) {
                if (wave == 0) { asm volatile("s_waitcnt vmcnt(1)" : "+v"(fl) :: "memory");
                    if (__builtin_expect(__all(fl.x != 0u), 1)) tnext = __builtin_amdgcn_readfirstlane(fl.w) + 2u * __builtin_amdgcn_readfirstlane(fl.x) + TT_WD; else { wait_timed(rfl, fo, hop, tnext, 2, TT_WD, t < WLD); VMKNOWN(); } }
                __builtin_amdgcn_s_barrier(); CBAR(); }
            if ((i == 2 || i == 3) && (j == 1 || j == 8 || j == 11)) { const int p = (i - 2) * 3 + (j > 1) + (j > 8); dma16c(rst, l16, v_so(t + 1) + p * 1024, ldsb + ((t + 1) & 1) * 49152 + wave * 6144 + p * 1024); } });
        VMW(4);
        if (wave == 0 && t >= WLD) rt_wait(tnext);
        LBAR();
    }
    VMDRAIN(); LBAR();
    if (wave == 1 && l16 == 0) st4c(rfl, F_UW + ((NSEG_ - 1) * 8 + w) * FLP, 1u);
    }
}

constexpr int RO_UM = 131072, RO_TIM = RO_UM + 3 * 8192, RO_END = NSEG_;
__device__ __forceinline__ void ro_state(KA& a, unsigned char* lds, const int r, const unsigned pol) {
    constexpr int wave = 0;
    const int lane = lane_id();
    const rsrc_t rhx = MKR(al2m((a.ws + WS_FR)), FR_USED), rst = MKR(a.ws + WS_ST, ST_BYTES);
    lds_u32* tim = (lds_u32*)(lds + RO_TIM + 256);
    const unsigned sx = ((my_side() ^ pol) & 1u) << 13;
    unsigned po = sx + (2 + (r & 1)) * 16384 + lane * 16, uo = um_base(um_sides(a), pol, r >> 1) + lane * 16;
    unsigned ho = (r < 64 ? 0 : 8192) + r * 128 + (lane >> 5) * 64 + (lane & 2) * 16 + (lane & 16) + (lane & 12) + (lane & 1) * 8192;
    unsigned idx = (lane & 1) ? 0xEEEEu : 0x4444u;
    const unsigned pmo = (2 - (r & 1)) * 16384 + (r & 1) * 2048;
    const unsigned nr = r < 40 ? 0u : 1u;
    unsigned so = (r % 5 == 0 && lane < 2) ? M_STAMP + (r / 5) * 64 + lane * 8192 : OOB;
    PIN(po); PIN(uo); PIN(ho); PIN(idx); PIN(so);
#pragma unroll 1
    for (int layer = 0; layer < 2; ++layer) {
    const unsigned sb0 = (unsigned)layer * (20u << 14);
    unsigned tb = 0, cC1 = 0, cP = 0; int u3 = 0;
    u32x8 ux[16];
#pragma unroll 1
    for (int t = 0; t < RO_END; ++t) {
        const int s = t - 1;
        ATV;
        if (t > 0) {
            const unsigned sp_ = sb0 + (unsigned)(s & 3) * (5u << 14);
            unsigned sb = sb0 + (unsigned)(t & 3) * (5u << 14), ss = sb + (t >> 2) * 4, sp0 = sp_ + nr * 4096, sp1 = sp0 ^ 4096, sm0 = sp_ + pmo + nr * 1024, sm1 = sm0 ^ 1024, su0 = ((s & 3) << 14) + nr * 4096, su1 = su0 ^ 4096;
            asm volatile("" : "+s"(sb), "+s"(ss), "+s"(sp0), "+s"(sp1), "+s"(sm0), "+s"(sm1), "+s"(su0), "+s"(su1));
            u32x4 um[8], p[10];
            auto u_bad = [&]() { unsigned m = 0;
#pragma unroll
                for (int q = 0; q < 8; ++q) m = max(m, umax4(um[q]));
                return m; };
            unsigned tp = tb + cC1;
            {
                auto p_half = [&](int z) { CBAR();
#pragma unroll
                    for (int q = 0; q < 4; ++q) { p[4 * z + q] = __builtin_amdgcn_raw_buffer_load_b128(rhx, po + q * 1024, z ? sp1 : sp0, 16); SBAR(); }
                    p[8 + z] = __builtin_amdgcn_raw_buffer_load_b128(rhx, po, z ? sm1 : sm0, 16); SBAR();
#pragma unroll
                    for (int q = 0; q < 4; ++q) { um[4 * z + q] = __builtin_amdgcn_raw_buffer_load_b128(rhx, uo + q * 1024, z ? su1 : su0, 16); SBAR(); }
                    CBAR(); };
                unsigned o;
                auto p_use = [&](f32x4 c0, f32x4 c1) {
#pragma unroll
                    for (int z = 0; z < 2; ++z) {
#pragma unroll
                        for (int q = 4 * z; q < 4 * z + 4; ++q) { c0 = smf(p[q], ux[2 * q], c0, idx); c1 = smf(p[q], ux[2 * q + 1], c1, idx); SBAR(); }
                        c0 = smf(p[8 + z], cat8(um[4 * z], um[4 * z + 1]), c0, idx); SBAR(); c1 = smf(p[8 + z], cat8(um[4 * z + 2], um[4 * z + 3]), c1, idx); SBAR(); }
                    SBAR();
                    float v0 = c0[0] + c0[1]; PIN(v0);
                    const float v = v0 + (c1[2] + c1[3]);
                    o = pk2(dppf<0xA0>(v), dppf<0xF5>(v));
                    return !__any(v != v); };
                f32x4 c0 = zero4(), c1 = zero4();
                { const unsigned n = rt_now(); if ((int)(n - tp) > 4) { tp = n; tim[2] = (unsigned)t; } }
                unsigned tv = min(tp, 0xfffffffeu); PIN(tv);
                AT(0);
                rt_fine<23>(tp);
                AT(1); p_half(0); nops<(RFD * 23 - 100) / 8>(); p_half(1);
                if (__builtin_expect(!p_use(c0, c1), 0)) {
                    tim[2] = (unsigned)t;
                    for (unsigned sp = 0;; ++sp) {
                        p_half(0); p_half(1);
                        if (p_use(zero4(), zero4())) break;
                        unsigned m = u_bad();
#pragma unroll
                        for (int q = 0; q < 10; ++q) m = max(m, umax4(p[q]));
                        if (__all(m != 0xffffffffu)) break;
                        if ((sp & 63u) == 63u && (sp > SPIN_CAP || tim[0])) { tim[0] = 1; break; }
                        if (sp > 3 || s == 0) __builtin_amdgcn_s_sleep(R0S);
                    }
                    o = min(o, 0xfffffffeu);
                    tv = min(rt_now() - TT_RT, 0xfffffffeu);
                }
                __builtin_amdgcn_raw_buffer_store_b32(o, rhx, ho, sb, 16); SBAR();
                __builtin_amdgcn_raw_buffer_store_b32(tv, rhx, so, ss, 16);
                SBAR(); AT(2);
                tim[1] = rt_now() - (tb + cC1);
            }
            LBAR();
            unsigned char* up = lds + RO_UM + u3 * 8192 + lane_id() * 16;
#pragma unroll
            for (int q = 0; q < 8; ++q) *reinterpret_cast<u32x4*>(up + ((q >> 2) ^ nr) * 4096 + (q & 3) * 1024) = um[q];
            u3 = u3 == 2 ? 0 : u3 + 1;
        } else LBAR();
        {
            const int ln = lane_id();
            if (t > 1) rt_wait(tb + cP + TT_BD);
            AT(3);
            const unsigned ub = ST_U + ((t & (URING - 1)) * 40 + (r >> 1)) * 32768, lb = (t & 3) * 32768, o = nr * 16384;
            dma16cn<16>(rst, ln * 16, ub + o, lb + o); dma16cn<16>(rst, ln * 16, ub + (o ^ 16384), lb + (o ^ 16384));
            const unsigned char* xp = lds + lb + (ln >> 5) * 1024 + (ln & 31) * 16;
#pragma unroll
            for (int q = 0; q < 16; ++q) { if (!(q & 1)) VMW(28 - 2 * q); const unsigned char* p = xp + ((q >> 3) ^ nr) * 16384 + ((q >> 1) & 3) * 4096 + (q & 1) * 512; ux[q] = cat8(lds16(p), lds16(p + 2048)); }
            AT(4);
            if (ln == 0 && r == 0 && t == 0) STAMP(13);
            LBAR();
            AT(5);
            const unsigned tpv = *reinterpret_cast<const unsigned*>(lds + RO_TIM + ln * 4);
            tb = __builtin_amdgcn_readlane(tpv, 0); cC1 = __builtin_amdgcn_readlane(tpv, 1); cP = __builtin_amdgcn_readlane(tpv, 2);
            if (t == 0) tb = rt_now() - cC1;
#pragma unroll
            for (int q = 0; q < 16; ++q) PIN(ux[q]);
        }
        ATD(2, t == 42 && r == 0);
    }
    if (lane == 0) tim[2] = 0;
    LBAR();
    }
    if (lane == 0 && tim[0]) set_err((a.ws + WS_HOP));
}
__device__ __forceinline__ void ro_time(KA& a, unsigned char* lds, const int r, const unsigned pol) {
    const int lane = lane_id();
    const rsrc_t rhx = MKR(al2m((a.ws + WS_FR)), FR_USED);
    lds_u32* tim = (lds_u32*)(lds + RO_TIM + 256);
    const unsigned sx = ((my_side() ^ pol) & 1u) << 13, to = sx + M_STAMP + min(lane, 17) * 64;
#pragma unroll 1
    for (int layer = 0; layer < 2; ++layer) {
    const rsrc_t rfl = MKR((a.ws + WS_HOP) + layer * HOP_BYTES, F_ERR);
    const unsigned sb0 = (unsigned)layer * (20u << 14);
    unsigned tb = 0, cC1 = 0, cP = 0;
#pragma unroll 1
    for (int s = 0; s < RO_END; ++s) {
        LBAR();
        if (s < NSEG_) {
            if (s > 1) rt_wait(tb + cP + TT_TW);
            unsigned tw;
            for (unsigned sp = 0;; ++sp) { CBAR();
                tw = __builtin_amdgcn_raw_buffer_load_b32(rhx, to, sb0 + (unsigned)(s & 3) * (5u << 14) + (s >> 2) * 4, 16);
                if (__all(tw != 0xffffffffu)) break;
                __builtin_amdgcn_s_sleep(6);
                if (sp > SPIN_CAP_W || tim[0]) { tim[0] = 1; break; } }
            tt_step(tw, s, rt_now(), tb, cC1, cP);
            if (lane == 0) *reinterpret_cast<u32x4*>(lds + RO_TIM) = (u32x4){tb, cC1, cP, 0u};
            if (lane == 0 && s > 1) st16c(rfl, F_RD + (s * NR_ + r) * FLP, (u32x4){1u, min((unsigned)tim[1], 4095u), tim[2] == (unsigned)s ? 1u : 0u, 0u});
        }
        LBAR();
    }
    LBAR();
    }
}
__device__ __forceinline__ void ro_master(KA& a) {
    const int lane = lane_id();
    const rsrc_t rhx = MKR(al2m((a.ws + WS_FR)), FR_USED);
    int e1 = TT_E1 * 16, e2 = TT_E2 * 16, x1 = 0, x2 = 0; bool pr2 = true;
#pragma unroll 1
    for (int layer = 0; layer < 2; ++layer) {
    const rsrc_t rfl = MKR((a.ws + WS_HOP) + layer * HOP_BYTES, F_ERR);
    const unsigned sb0 = (unsigned)layer * (20u << 14);
#pragma unroll 1
    for (int s = 0; s < RO_END; ++s) {
        LBAR();
        const int xs = max(TT_XS - s * TT_XD, 0);
        const unsigned c1 = max(e1 >> 4, TT_E1 + TT_X1 - 2 * s) + TT_M1 + x1 + (s == 0 ? TT_S1 : 0), c2 = c1 + (e2 >> 4) + TT_M2 + x2 + xs;
        const int q = max(s, 2);
        u32x4 f = ld16c(rfl, lane < 32 ? F_KM + ((q - 2) * 32 + lane) * FLP : F_UX + (q * 32 + lane - 32) * FLP), g = ld16c(rfl, F_RD + ((q - 1) * NR_ + lane) * FLP), k = ld16c(rfl, F_RD + ((q - 1) * NR_ + 64 + (lane & 15)) * FLP);
        if (lane < 4 && s + 1 < NSEG_) __builtin_amdgcn_raw_buffer_store_b32((lane & 2) ? c2 : c1, rhx, M_STAMP + 1024 + (lane & 2) * 32 + (lane & 1) * 8192, sb0 + (unsigned)((s + 1) & 3) * (5u << 14) + ((s + 1) >> 2) * 4, 16);
        SBAR();
        LBAR();
        SBAR();
        if (lane >= 32) { f.y = f.x & 4095u; f.z = (f.x >> 12) & 1u; }
        auto wmax = [&](unsigned v) { v = max(v, dppu<0x128>(v)); v = max(v, dppu<0x124>(v)); v = max(v, dppu<0x122>(v)); v = max(v, dppu<0x121>(v));
            return (int)max(max(__builtin_amdgcn_readlane(v, 0), __builtin_amdgcn_readlane(v, 16)), max(__builtin_amdgcn_readlane(v, 32), __builtin_amdgcn_readlane(v, 48))); };
        auto upd = [&](int& e, int m) { if (m > 0) { const int d = min(m * 16, e + 16 * 12) - e; e += d >> (d > 0 ? TT_UP : (s < 12 && !layer ? TT_DEC : TT_DN)); } };
        const bool rs = __any(f.z != 0u), r2 = __any((g.z | k.z) != 0u);
        if (s > 3) {
            if (!rs) upd(e1, wmax(lane < 32 ? f.y : (unsigned)max((int)f.y + TT_UM + TT_UX, 0)));
            if (!r2) upd(e2, wmax(max(g.y, k.y)));
            if (rs && !pr2) x2 += TT_BUMP; else if (r2 && !rs) x1 += TT_BUMP;
            if (!rs && !r2 && (s & TT_DCY) == 0) { x1 -= x1 > 0; x2 -= x2 > 0; }
        }
        pr2 = r2 || s < 3;
    }
    LBAR();
    if (!layer && lane == 0) { const rsrc_t rb = MKR((a.ws + WS_HOP) - 2048, 2048); const u32x2 t = ld8c(rb, 0); st8c(rb, 0, (u32x2){t.x + 1u, t.y}); }
    }
}
template <int TAIL> __device__ __forceinline__ void ro_out(KA& a, unsigned char* lds, const int r, const unsigned uo) {
    constexpr int wave = 2;
    const int lane = lane_id(), l15 = lane & 15, lg = lane >> 4, c = r >> 1, rt = r & 1, cr = (RROT & 2) ? c : 0;
    const rsrc_t rst = MKR((a.ws + WS_ST), ST_BYTES), rx = MKR((a.ws + WS_X), XI_BYTES), ro = MKR(a.out, S_ * D_ * 2);
    const unsigned xo = XI_X + rt * 20480 + (c >> 1) * 1024 + ((c & 1) * 2 + (lg >> 1)) * 256 + l15 * 16 + (lg & 1) * 8;
#pragma unroll 1
    for (int layer = 0; layer < 2; ++layer) {
    unsigned char* hop = (a.ws + WS_HOP) + layer * HOP_BYTES;
    const rsrc_t rfl = MKR(hop, F_ERR);
    unsigned tb = 0, cC1 = 0, cP = 1; int u3 = 0;
    u32x2 o = (u32x2){0u, 0u}, rr; float sq = 0.f; u32x4 p[40], um[8];
    auto put = [&](int s) {
        if (layer) __builtin_amdgcn_raw_buffer_store_b64(o, ro, ((rt * 16 + l15) * D_ + 16 * c + 4 * lg) * 2, s * (32 * D_ * 2), 0);
        else { __builtin_amdgcn_raw_buffer_store_b64(o, rx, xo, s * XI_PITCH, 16); if (lane < 32) __builtin_amdgcn_raw_buffer_store_b32(__float_as_uint(sq), rx, (rt * 40 + c) * 128 + l15 * 8 + lg * 4, s * XI_PITCH, 16); } };
    auto gate = [&](int s, int slp) { wait_set(rfl, F_PX + ((s + QA) * 64 + (lane & 31) * 2 + rt) * FLP, hop, slp); };
    auto req = [&](int s) { const int ln = lane_id();
        rr = __builtin_amdgcn_raw_buffer_load_b64(rx, xo, s * XI_PITCH, 16); SBAR();
#pragma unroll
        for (int q = 0; q < 40; ++q) { p[q] = __builtin_amdgcn_raw_buffer_load_b128(rst, ln * 16 + (q & 3) * 1024, ST_PX + ((s & (PR - 1)) * 2 + rt) * 40960 + (q < 32 ? ((q >> 2) + cr) & 7 : q >> 2) * 4096, 16); SBAR(); }
        CBAR(); };
    auto comp = [&](int s) { const int ln = lane_id();
        const unsigned char* xp = lds + (s & 3) * 32768 + ln * 16; const unsigned char* mp = lds + RO_UM + u3 * 8192 + ln * 16;
        u32x4 uf[8];
#pragma unroll
        for (int q = 0; q < 7; ++q) uf[q] = lds16(xp + (((q >> 2) + cr) & 7) * 4096 + (q & 3) * 1024);
        SBAR();
        f32x4 acc = (f32x4){lo2f(rr.x), hi2f(rr.x), lo2f(rr.y), hi2f(rr.y)};
#pragma unroll
        for (int q = 0; q < 40; ++q) { acc = dmf((TAIL == OLAG && q >= 32) ? um[q & 7] : uf[q & 7], p[q], acc); PIN(acc); SBAR();
            if (q + 7 < (TAIL == OLAG ? 32 : 40)) { uf[(q + 7) & 7] = q + 7 < 32 ? lds16(xp + ((((q + 7) >> 2) + cr) & 7) * 4096 + ((q + 7) & 3) * 1024) : lds16(mp + (q - 25) * 1024); SBAR(); } }
        o = (u32x2){pk2(acc[0], acc[1]), pk2(acc[2], acc[3])};
        sq = row_sums(o); };
    auto fin = [&](int s) { put(s); if (!layer) { VMDRAIN(); if (lane == 0) st16c(rfl, F_OF + (s * NR_ + r) * FLP, (u32x4){cP, 1u, 1u, tb}); } if (lane == 0 && r == 0) STAMP(300 + s - 56); };
    auto times = [&]() { const unsigned tpv = *reinterpret_cast<const unsigned*>(lds + RO_TIM + lane * 4); tb = __builtin_amdgcn_readlane(tpv, 0); cC1 = __builtin_amdgcn_readlane(tpv, 1); cP = __builtin_amdgcn_readlane(tpv, 2); };
    if (TAIL) {
        const int s = NSEG_ - 1 - OLAG + TAIL;
        for (int t = 0; t < NSEG_; ++t) {
            __builtin_amdgcn_s_barrier();
            if (TAIL == OLAG) __builtin_amdgcn_s_sleep(ASL);
            if (TAIL == OLAG && t > 0) { const unsigned sr = (unsigned)layer * (20u << 14) + (unsigned)((t + 2) & 3) * (5u << 14);
                __builtin_amdgcn_raw_buffer_store_b32(~0u, MKR(al2m(a.ws + WS_FR), FR_USED), (r < 64 ? 0 : 8192) + r * 128 + (lane >> 5) * 8192 + (lane & 31) * 4, sr, 16);
                __builtin_amdgcn_raw_buffer_store_b32(~0u, MKR(al2m(a.ws + WS_FR), FR_USED), (r % 5 == 0 && lane < 2 && t + 2 < NSEG_) ? M_STAMP + (r / 5) * 64 + lane * 8192 : OOB, sr + ((t + 2) >> 2) * 4, 16); }
            if (TAIL == OLAG && (t >= 2 || layer)) {
#pragma unroll
                for (int q = 0; q < 4; ++q) st16s((u32x4){~0u, ~0u, ~0u, ~0u}, MKR(al2m(a.ws + WS_FR), FR_USED), uo + q * 1024, (((t + 2) & 3) << 14) + rt * 4096); }
            if (TAIL == OLAG) VMDRAIN();
            __builtin_amdgcn_s_barrier(); }
        CBAR();
        u3 = s % 3;
        if (TAIL == OLAG) {
            const rsrc_t rhx = MKR(al2m((a.ws + WS_FR)), FR_USED);
            times(); rt_wait(tb + cC1);
            for (unsigned sp = 0;; ++sp) { unsigned m = 0; CBAR();
#pragma unroll
                for (int q = 0; q < 8; ++q) um[q] = __builtin_amdgcn_raw_buffer_load_b128(rhx, uo + (q & 3) * 1024, ((s & 3) << 14) + (q >> 2) * 4096, 16);
#pragma unroll
                for (int q = 0; q < 8; ++q) m = max(m, umax4(um[q]));
                if (__all(m != 0xffffffffu)) break;
                if (sp > SPIN_CAP_W) { set_err(hop); break; }
                __builtin_amdgcn_s_sleep(8); }
            rt_wait(tb + TT_TL);
        }
        gate(s, 0); req(s); comp(s); fin(s);
    } else {
#pragma unroll 1
    for (int t = 0; t < NSEG_; ++t) {
        const int s = t - OLAG;
        ATV;
        LBAR();
        if (s >= 0) {
            const unsigned to_ = max((int)(tb + cP + TT_BD - rt_now()), 0) + rt_now() + TT_OD;
            gate(s, 0);
            if (s > 0) put(s - 1);
            rt_wait(to_);
            AT(0);
            req(s);
            AT(1);
            if (lane == 0 && r == 0 && s > 56) STAMP(300 + s - 57);
        }
        LBAR();
        times();
        if (!layer && s > 0) { PIN(rr); if (lane == 0) st16c(rfl, F_OF + ((s - 1) * NR_ + r) * FLP, (u32x4){cP, 1u, 1u, tb}); }
        if (s >= 0) { __builtin_amdgcn_s_sleep(OSL); comp(s); u3 = u3 == 2 ? 0 : u3 + 1; AT(2); }
        ATD(2, t == 42 && r == 0);
    }
    fin(NSEG_ - 1 - OLAG);
    }
    LBAR();
    }
}
__device__ __forceinline__ void ro_fill(KA& a, unsigned char* lds, const int r) {
    constexpr int wave = 3;
    const int lane = lane_id();
#pragma unroll 1
    for (int layer = 0; layer < 2; ++layer) {
    unsigned char* hop = (a.ws + WS_HOP) + layer * HOP_BYTES;
    const rsrc_t rfl = MKR(hop, F_ERR);
    unsigned tb = 0, cC1 = 0, cP = 0;
#pragma unroll 1
    for (int t = 0; t < RO_END; ++t) {
        ATV;
        if (t < NSEG_) {
            if (t == 0) wait_set(rfl, F_XP + (r % NXP_) * FLP, hop, 4);
            if (t > 1) rt_wait(tb + cC1 + TT_FP);
            wait_set(rfl, (r >= 40 && r < 68 && t > 2) ? F_UW + (t * 8 + (lane & 7)) * FLP : (r < 64 || (t == 0 && lane >= 32)) ? F_UX + (t * 32 + (lane & 31)) * FLP : F_PX + (t * 64 + (lane & 31) * 2 + ((r >> 1) & 1)) * FLP, hop, t > 2 ? 0 : RSL);
        }
        LBAR();
        LBAR();
        if (t < NSEG_) { const unsigned tpv = *reinterpret_cast<const unsigned*>(lds + RO_TIM + lane * 4); tb = __builtin_amdgcn_readlane(tpv, 0); cC1 = __builtin_amdgcn_readlane(tpv, 1); cP = __builtin_amdgcn_readlane(tpv, 2); }
        ATD(2, t == 42 && r == 0);
    }
    LBAR();
    if (lane == 0) STAMP(512 + blockIdx.x);
    }
}
__device__ __forceinline__ void ro_part(KA& a, unsigned char* lds, const int r, const int wave) {
    if (wave == 0) { lds_u32* tim = (lds_u32*)(lds + RO_TIM + 256); tim[0] = 0; tim[1] = 0; tim[2] = 0; }
    if (wave == 0) { __builtin_amdgcn_s_setprio(3); ro_state(a, lds, r, page_pol(a)); }
    else if (wave == 1) ro_time(a, lds, r, page_pol(a));
    else if (wave == 2) ro_out<0>(a, lds, r, 0u);
    else if (wave == 3) { init_wait(a); ro_fill(a, lds, r); }
    else if (wave == 4) ro_out<1>(a, lds, r, 0u);
    else if (wave == 7) ro_out<2>(a, lds, r, 0u);
    else if (wave == 6) { const unsigned pol = page_pol(a); ro_out<3>(a, lds, r, um_base(um_sides(a), pol, r >> 1) + lane_id() * 16); }
    else {
        if (r == 1) { const rsrc_t rfl = MKR(a.ws + WS_HOP, F_ERR); wait_set(rfl, F_QK + lane_id() * FLP, a.ws + WS_HOP, 1); wait_set(rfl, F_QK + (64 + lane_id()) * FLP, a.ws + WS_HOP, 1); if (lane_id() < 8) st4c(rfl, F_IN + lane_id() * FLP, 1u); }
        const rsrc_t rz = MKR((a.ws + WS_HOP) + HOP_BYTES, HOP_BYTES);
        for (unsigned o = r * 1024 + lane_id() * 16; o < HOP_BYTES; o += NR_ * 1024) st16c(rz, o, (u32x4){0u, 0u, 0u, 0u});
        VMDRAIN();
        if (r == 0) ro_master(a); else for (int q = 0; q < 4 * NSEG_ + 2; ++q) __builtin_amdgcn_s_barrier(); }
}

#define AW1 1
#define AWS 0
#define QE 3
#define XKB 4
#define XSL 0
#define VMKNOWN5() __builtin_amdgcn_s_waitcnt(0x0F75)
#define PIM 60
#define XNP 3
constexpr int XB = 40960;
struct XWt { u32x4 w[2][20]; f32x4 cs[2]; };
template <int NB, int NR, class G, class H> __device__ __forceinline__ void x_load(XWt& x, unsigned char* lds, const int wave, const int pw, G&& src, H&& more) {
    const unsigned ldsb = LDSB(), lane = lane_id(), l15 = lane & 15, lg = lane >> 4;
    load_b<NB, 4, NR, 0, 0>(ldsb, wave, src, [&](int rd, int base) { if (rd >= 10) more(rd, base); else if (pw >= 0) {
#pragma unroll
        for (int u = 0; u < 2; ++u)
#pragma unroll
            for (int k = 0; k < 2; ++k) x.w[u][(2 * rd + k) % 20] = ld_frag(lds + base + (pw >> 1) * 8192, 32 * k + 8 * lg, 4, (pw & 1) * 2 + u, l15); } }, [](int) {});
    if (pw >= 0) {
        const u32x4 one = (u32x4){0x3F803F80u, 0x3F803F80u, 0x3F803F80u, 0x3F803F80u};
        x.cs[0] = Z4; x.cs[1] = Z4;
#pragma unroll
        for (int k = 0; k < 20; ++k) { x.cs[0] = dmf(x.w[0][k], one, x.cs[0]); x.cs[1] = dmf(x.w[1][k], one, x.cs[1]); }
        PIN(x.cs[0]); PIN(x.cs[1]);
    }
}
__device__ __forceinline__ void c_load(u32x2 (&sv)[4], rsrc_t rx, int t) {
    const int lane = lane_id();
    asm volatile("s_nop 4\n\tbuffer_load_dwordx2 %0, %4, %5, %6 offen sc1\n\tbuffer_load_dwordx2 %1, %4, %5, %6 offen offset:128 sc1\n\tbuffer_load_dwordx2 %2, %4, %5, %6 offen offset:256 sc1\n\tbuffer_load_dwordx2 %3, %4, %5, %6 offen offset:384 sc1"
        : "=&v"(sv[0]), "=&v"(sv[1]), "=&v"(sv[2]), "=&v"(sv[3]) : "v"((lane >> 4) * 512 + (lane & 15) * 8), "s"(rx), "s"(t * XI_PITCH + XI_S8) : "memory");
}
__device__ __forceinline__ void c_got(u32x2 (&sv)[4]) { asm volatile("" : "+v"(sv[0]), "+v"(sv[1]), "+v"(sv[2]), "+v"(sv[3])); }
__device__ __forceinline__ void c_put(const u32x2 (&sv)[4], unsigned char* dst) {
    float s[2];
#pragma unroll
    for (int q = 0; q < 2; ++q) s[q] = adds(adds(__uint_as_float(sv[0][q]), __uint_as_float(sv[1][q])), adds(__uint_as_float(sv[2][q]), __uint_as_float(sv[3][q])));
    asm volatile("s_nop 1" : "+v"(s[0]), "+v"(s[1]));
    const auto a1 = __builtin_amdgcn_permlane16_swap(__float_as_uint(s[0]), __float_as_uint(s[0]), false, false), a2 = __builtin_amdgcn_permlane16_swap(__float_as_uint(s[1]), __float_as_uint(s[1]), false, false);
    float m, i; ln_consts(adds(__uint_as_float(a1[0]), __uint_as_float(a1[1])), __uint_as_float(a2[0]) + __uint_as_float(a2[1]), m, i);
    const int lane = lane_id();
    *reinterpret_cast<f32x2*>(dst + (lane >> 5) * 128 + (lane & 15) * 8) = (f32x2){-m, i};
}
template <class F> __device__ __forceinline__ void x_proj(const XWt& x, const unsigned char* xb, const unsigned char* cb, u32x4 (&o)[2], F&& mid) {
    const int lane = lane_id();
    unsigned l16 = lane * 16, c8 = (lane & 15) * 8; PIN(l16); PIN(c8);
    const unsigned char* xp = xb + l16;
    f32x4 acc[2][2] = {{Z4, Z4}, {Z4, Z4}};
    u32x4 xf[3][2]; f32x2 ct[2];
#pragma unroll
    for (int k = 0; k < 2; ++k) { xf[k][0] = lds16(xp + k * 1024); SBAR(); xf[k][1] = lds16(xp + 20480 + k * 1024); SBAR(); }
#pragma unroll
    for (int k = 0; k < 20; ++k) {
#pragma unroll
        for (int j = 0; j < 4; ++j) { acc[j >> 1][j & 1] = dmf(x.w[j >> 1][k], xf[k % 3][j & 1], acc[j >> 1][j & 1]); PIN(acc[j >> 1][j & 1]); SBAR();
            if ((j == 1 || j == 2) && k + 2 < 20) { xf[(k + 2) % 3][j - 1] = lds16(xp + (j - 1) * 20480 + (k + 2) * 1024); SBAR(); }
            if (j == 1 && k == 18) { ct[0] = *reinterpret_cast<const f32x2*>(cb + c8); ct[1] = *reinterpret_cast<const f32x2*>(cb + c8 + 128); SBAR(); } }
        mid(k, l16);
    }
#pragma unroll
    for (int rt = 0; rt < 2; ++rt)
#pragma unroll
        for (int q = 0; q < 4; ++q) { const int u = q >> 1, e = 2 * (q & 1);
            o[rt][q] = pk2(__builtin_fmaf(ct[rt][0], x.cs[u][e], acc[u][rt][e]) * ct[rt][1], __builtin_fmaf(ct[rt][0], x.cs[u][e + 1], acc[u][rt][e + 1]) * ct[rt][1]); }
}
__device__ __forceinline__ void xp_part(KA& a, unsigned char* lds, const int j, const int wave) {
    const int lane = lane_id(), tid = wave * 64 + lane;
    const unsigned ldsb = LDSB();
    const int hp = j / 3, kind = j - 3 * hp;
    XWt x;
    {
        const rsrc_t rk = MKR(psel(a.wk, a.wv, kind >> 1), D_ * HE_ * 2), rv = MKR(a.wv, D_ * HE_ * 2);
        const unsigned row = 8 * wave + (lane >> 3), vo = row * (HE_ * 2) + ((lane & 7) ^ (wsw(row) << 1)) * 16 + hp * 768;
        x_load<4, 10>(x, lds, wave, wave, [&](int rd, int q, unsigned dst) { dma16(q < 3 ? rk : rv, vo, rd * (64 * HE_ * 2) + (kind < 2 ? kind * 384 + (q % 3) * 128 : (q >> 1) * 384 + 128 + (q & 1) * 128), dst); }, [](int, int) {});
    }
    const unsigned oo = kind < 2 ? (wave < 6 ? ST_KX + wave * 1024 : ST_VX + (wave - 6) * 1024) + (2 * hp + kind) * 12288 : ST_VX + (2 * hp + (wave >> 2)) * 12288 + (2 + (wave & 3)) * 1024;
    const rsrc_t rst = MKR((a.ws + WS_ST), ST_BYTES), rx = MKR((a.ws + WS_X), XI_BYTES);
    const int wr_ = (wave + (blockIdx.x >> 3) * ROT) & 7;
#pragma unroll 1
    for (int layer = 0; layer < 2; ++layer) {
    unsigned char* hop = (a.ws + WS_HOP) + layer * HOP_BYTES;
    const rsrc_t rfl = MKR(hop, F_ERR);
    auto piece = [&](int t, int b, int k, unsigned l16) { const unsigned o = (wr_ * 5 + k) * 1024; dma16c(rx, l16, (unsigned)t * XI_PITCH + XI_X + o, ldsb + b * XB + o); };
    auto x_dma = [&](int t, int b) {
#pragma unroll
        for (int k = 0; k < 5; ++k) piece(t, b, k, lane * 16); };
    if (tid == 0 && j == 0) STAMP(7);
    if (wave == 2) { if (!layer) init_wait(a); wait_set(rfl, F_AF + (lane & 15) * FLP, hop, XSL); }
    LBAR();
    if (tid == 0 && j == 0) STAMP(8);
    { u32x2 sv[4]; if (wave == 1) c_load(sv, rx, 0);
      x_dma(0, 0);
      if (wave == 1) { VMW(5); c_got(sv); c_put(sv, lds + 3 * XB); } }
    VMKNOWN(); LBAR();
    unsigned tnext = rt_now(); int b = 0;
#pragma unroll 1
    for (int t = 0; t < NSEG_; ++t) {
        ATV;
        AT(0);
        u32x4 fl; u32x2 sv[4];
        const unsigned fo = (lane < 16 && t >= XLD) ? F_KM + ((t - XLD) * 32 + 2 * hp) * FLP : F_AF + (min(t + (lane < 16 ? 1 : 2), NSEG_ - 1) * 16 + (lane & 15)) * FLP;
        if (wave == 2) { if (t > XLD) rt_wait(tnext);
            asm volatile("buffer_load_dwordx4 %0, %1, %2, 0 offen sc1" : "=&v"(fl) : "v"(fo), "s"(rfl) : "memory"); }
        const int bn = b == 0 ? 2 : b - 1;
        u32x4 o[2];
        x_proj(x, lds + b * XB, lds + 3 * XB + (t & 1) * 256, o, [&](int k, unsigned l16) {
            if (k == XKB) { if (wave == 2) VMW(1); else VMDRAIN();
                __builtin_amdgcn_s_barrier(); CBAR();
                if (wave == 0 && t > 0 && l16 == 0) { st4c(rfl, F_XP + ((t - 1) * NXP_ + j) * FLP, 1u); if (j == 0 && t < 4) STAMP(t); if (j == 7 && t < 12) STAMP(2100 + t); }
                AT(2); SBAR(); }
            if (k == XKB + 3) {
                if (wave == 2) { VMDRAIN(); asm volatile("" : "+v"(fl));
                    if (__builtin_expect(__all(fl.x != 0u), 1)) tnext = __builtin_amdgcn_readfirstlane(fl.w) + XNP * __builtin_amdgcn_readfirstlane(fl.x) + TT_KD; else { wait_timed(rfl, fo, hop, tnext, XNP, TT_KD, t < XLD); VMKNOWN(); } }
                __builtin_amdgcn_s_barrier(); CBAR();
                if (wave == 1) c_load(sv, rx, min(t + 1, NSEG_ - 1));
                if (t == 0) x_dma(1, 1);
                SBAR(); }
            if (k > XKB + 3 && k <= XKB + 13 && ((k - XKB) & 1) == 0 && t + 2 < NSEG_) { piece(t + 2, bn, (k - XKB - 4) >> 1, l16); SBAR(); } });
        const int ln = lane_id();
        st16s(o[0], rst, ln * 16, oo + (t & (KXR - 1)) * (32 * 12288)); st16s(o[1], rst, ln * 16, oo + (t & (KXR - 1)) * (32 * 12288) + 6144);
        AT(1);
        if (wave == 1) { if (t + 2 < NSEG_) VMW(7); else VMW(2); c_got(sv); c_put(sv, lds + 3 * XB + ((t + 1) & 1) * 256); asm volatile("s_waitcnt lgkmcnt(0)" ::: "memory"); }
        if (t == 0) { VMW(7); LBAR(); }
        ATD(3, t == 48 && j == 0);
        b = b == 2 ? 0 : b + 1;
    }
    VMDRAIN(); LBAR();
    if (tid == 0) st4c(rfl, F_XP + ((NSEG_ - 1) * NXP_ + j) * FLP, 1u);
    if (tid == 0) STAMP(512 + blockIdx.x);
    }
}

template <int AT_> __device__ __forceinline__ void xq_run(KA& a, unsigned char* lds, const int h, const int wave) {
    constexpr bool ATT = AT_ >= 0; constexpr int qt = ATT ? AT_ : 0;
    const int lane = lane_id(), tid = wave * 64 + lane;
    const unsigned ldsb = LDSB();
    constexpr int L_C = 2 * XB, L_Q = L_C + 512, L_VX = L_Q + (QA + 1) * 12288;
    const int pw = AW1 == 1 ? wave - 2 : wave - 1 - (wave >> 2);
    XWt x; u32x4 wo[4][6];
    {
        const rsrc_t rq = MKR(a.wq, D_ * HE_ * 2), ro = MKR(a.wo, HE_ * D_ * 2);
        const unsigned row = 8 * wave + (lane >> 3), ch = ((lane & 7) ^ (wsw(row) << 1)) * 16, vo = row * (HE_ * 2) + ch + h * 384, oo = (h * 192 + row) * (D_ * 2) + ch;
        x_load<3, 12>(x, lds, wave, ATT ? -1 : pw, [&](int rd, int q, unsigned dst) { if (rd < 10) dma16(rq, vo, rd * (64 * HE_ * 2) + q * 128, dst); else dma16(ro, oo, q * (64 * D_ * 2) + (rd - 2) * 128, dst); },
            [&](int rd, int base) { if (ATT) {
#pragma unroll
                for (int q = 0; q < 12; ++q) wo[2 * (rd & 1) + q / 6][q % 6] = ld_frag(lds + base + ((q % 6) >> 1) * 8192, (q & 1) * 32 + 4 * (lane >> 4), 16, qt + 2 * (q / 6), lane & 15); } });
    }
    const rsrc_t rst = MKR((a.ws + WS_ST), ST_BYTES), rx = MKR((a.ws + WS_X), XI_BYTES);
    const int wr_ = (pw + (blockIdx.x >> 3)) % 6, xj = xp_of(h), xj2 = 3 * (h >> 1) + 2;
    const int l15 = lane & 15, lg = lane >> 4;
    const bool cm[4] = {4 * lg <= l15, 4 * lg + 1 <= l15, 4 * lg + 2 <= l15, 4 * lg + 3 <= l15};
#pragma unroll 1
    for (int layer = 0; layer < 2; ++layer) {
    unsigned char* hop = (a.ws + WS_HOP) + layer * HOP_BYTES;
    const rsrc_t rfl = MKR(hop, F_ERR);
    auto piece = [&](int t, int k, unsigned l16) { const unsigned o = min(wr_ * 7 + k, 39) * 1024; dma16c(rx, l16, (unsigned)t * XI_PITCH + XI_X + o, ldsb + (t & 1) * XB + o); };
    if (wave == 0) { if (!layer) init_wait(a); wait_set(rfl, F_AF + (lane & 31) * FLP, hop, 2); }
    LBAR();
    if (!ATT) {
#pragma unroll
        for (int k = 0; k < 7; ++k) piece(0, k, lane * 16); }
    u32x4 kf[2][6];
    if (AT_ == 0) { u32x2 sv[4]; c_load(sv, rx, 0); VMDRAIN(); c_got(sv); c_put(sv, lds + L_C); }
    VMKNOWN();
    unsigned tnext = rt_now(); int qs = 0; bool rdy = false, owe = false;
#pragma unroll 1
    for (int i = 0; i < NSEG_ + QA; ++i) {
        ATV;
        if (i <= NSEG_) LBAR();
        AT(0);
        if (AT_ == 1 && h == 5 && i < 10 && lane == 0) STAMP(2000 + i * 4);
        if (!ATT) {
            if (i < NSEG_) {
                u32x4 o[2];
                x_proj(x, lds + (i & 1) * XB, lds + L_C + (i & 1) * 256, o, [&](int k, unsigned l16) { if ((k & 1) && k < 14 && i + 1 < NSEG_) { piece(i + 1, k >> 1, l16); SBAR(); } });
                unsigned char* qp = lds + L_Q + qs * 12288 + pw * 1024 + lane_id() * 16;
                *reinterpret_cast<u32x4*>(qp) = o[0]; *reinterpret_cast<u32x4*>(qp + 6144) = o[1];
                AT(1);
                VMDRAIN();
            }
        } else {
            const int t = i - QA;
            const unsigned fa = F_AF + (min(i + 2, NSEG_ - 1) * 16 + l15) * FLP, fx = F_XP + ((lane & 1) ? xj : xj2) * FLP, ub = ST_U + (((i & (URING - 1)) * 40 + 32 + qt) * 32 + h) * 1024;
            unsigned l16 = lane_id() * 16; PIN(l16);
            auto v_dma = [&]() { dma16cn<6>(rst, l16, ST_VX + (((i + 1) & (KXR - 1)) * 32 + h) * 12288 + qt * 6144, ldsb + L_VX + ((i + 1) & 1) * 12288 + qt * 6144); };
            auto next = [&]() {
                const unsigned kb = ST_KX + (((t + 1) & (KXR - 1)) * 32 + h) * 12288;
#pragma unroll
                for (int m = 0; m < (qt ? 12 : 6); ++m) asm volatile("buffer_load_dwordx4 %0, %1, %2, %3 offen offset:%4 sc1" : "=&v"(kf[m / 6][m % 6]) : "v"(l16), "s"(rst), "s"(kb + (m >> 2) * 4096), "i"((m & 3) * 1024) : "memory"); };
            auto got = [&]() {
#pragma unroll
                for (int m = 0; m < (qt ? 12 : 6); ++m) PIN(kf[m / 6][m % 6]); };
            u32x4 vf[12]; u32x2 sv[4];
            auto u_from = [&](int u0) {
                f32x4 acc[4][2];
#pragma unroll
                for (int j = 0; j < 48; ++j) { const int k = j >> 3, u = (j >> 1) & 3, r = j & 1; if (u >= u0) acc[u][r] = dmf(vf[r * 6 + k], wo[u][k], k ? acc[u][r] : Z4); }
                SBAR();
#pragma unroll
                for (int u = u0; u < 4; ++u) st16s((u32x4){pk2(acc[u][0][0], acc[u][1][0]), pk2(acc[u][0][2], acc[u][1][2]), pk2(acc[u][0][1], acc[u][1][1]), pk2(acc[u][0][3], acc[u][1][3])}, rst, l16, ub + u * 65536);
                SBAR(); };
            auto out_then = [&](int n) {
                const unsigned v = ld4c(rfl, fx + n * (NXP_ * FLP)); VMDRAIN();
                if (l16 == 0) st4c(rfl, F_PX + (i * 64 + h * 2 + qt) * FLP, 1u);
                if (!__all(v != 0u)) wait_set(rfl, fx + n * (NXP_ * FLP), hop, 0); };
            if (t < 0) {
                if (!qt) c_load(sv, rx, i + 1);
                wait_set(rfl, lane < 16 ? fx + i * (NXP_ * FLP) : fa, hop, 1);
#pragma unroll
                for (int m = 0; m < 12; ++m) vf[m] = __builtin_amdgcn_raw_buffer_load_b128(rst, l16 + (m & 3) * 1024, ST_VX + (i * 32 + h) * 12288 + (m >> 2) * 4096, 16);
                SBAR();
                if (!qt) { c_got(sv); c_put(sv, lds + L_C + ((i + 1) & 1) * 256); SBAR(); }
                u_from(0);
                next();
                out_then(t == -1 ? i + 1 : i);
                if (t == -1) { v_dma(); VMDRAIN(); }
                got();
            } else {
                const unsigned char* qp = lds + L_Q + (qs == QA ? 0 : qs + 1) * 12288 + qt * 6144 + l16;
                u32x4 qf[6], km[6], fl;
                const bool uu = i < NSEG_;
                if (!qt) c_load(sv, rx, min(i + 1, NSEG_ - 1));
                const bool er = !rdy;
                if (uu && !er) { v_dma(); VMW(6); } else VMDRAIN();
                got();
                if (!qt) c_got(sv);
                if (l16 == 0 && owe) st4c(rfl, F_PX + ((i - 1) * 64 + h * 2 + qt) * FLP, 1u);
                AT(1);
                const unsigned fo = lane < 16 ? F_KM + (t * 32 + h) * FLP : (lane < 32 ? fx + min(i + 2, NSEG_ - 1) * (NXP_ * FLP) : fa);
                const int dl = t + 2 == NSEG_ ? TT_QN : TT_QD;
#pragma unroll
                for (int m = 0; m < 6; ++m) qf[m] = lds16(qp + m * 1024);
                rt_wait(tnext);
                AT(5);
                asm volatile("buffer_load_dwordx4 %0, %1, %2, 0 offen sc1" : "=&v"(fl) : "v"(fo), "s"(rfl) : "memory");
                f32x4 sc[3] = {Z4, Z4, Z4};
#pragma unroll
                for (int m = 0; m < 6; ++m) { sc[0] = dmf(kf[0][m], qf[m], sc[0]); if (qt) sc[1] = dmf(kf[1][m], qf[m], sc[1]); }
                float s2[10];
#pragma unroll
                for (int q = 0; q < 8; ++q) s2[q] = (q >> 2) > qt ? 0.f : (((q >> 2) < qt || cm[q & 3]) ? sc[q >> 2][q & 3] : -3.0e38f);
                float mx = max3f(max3f(s2[0], s2[1], s2[2]), s2[3], qt ? max3f(max3f(s2[4], s2[5], s2[6]), s2[7], s2[7]) : s2[3]);
#pragma unroll
                for (int q = 0; q < (qt ? 8 : 4); ++q) PIN(s2[q]);
                PIN(mx);
                if (!qt && uu) c_put(sv, lds + L_C + ((i + 1) & 1) * 256);
#pragma unroll
                for (int m = 0; m < 12; ++m) vf[m] = lds16(lds + L_VX + (i & 1) * 12288 + l16 + m * 1024);
                SBAR();
                asm volatile("s_waitcnt vmcnt(0)" : "+v"(fl) :: "memory");
                const unsigned long long fz = __ballot(fl.x == 0u);
                rdy = !(fz & 0xffff0000ull);
                if (!(fz & ~0xffff0000ull)) tnext = __builtin_amdgcn_readfirstlane(fl.w) + 2u * __builtin_amdgcn_readfirstlane(fl.x) + dl; else wait_timed(rfl, lg == 1 ? fa : fo, hop, tnext, 2, dl, t + 1 == NSEG_ || t < QE);
                AT(2);
                if (l16 == 0 && h == 0 && qt == 0 && t >= 56) STAMP(360 + t - 56);
                const unsigned mo = (lg * 8 + (((l15 >> 2) * 2 + (l15 & 1) + (lg & 1) * 4) & 7)) * 16;
#pragma unroll
                for (int m = 0; m < 6; ++m) asm volatile("buffer_load_dwordx4 %0, %1, %2, %3 offen offset:%4 sc1" : "=&v"(km[m]) : "v"(mo), "s"(rst), "s"(ST_KM + ((t & (PR - 1)) * 32 + h) * 3072), "i"(m * 512) : "memory");
                if (uu) { u_from(1); VMW(3); } else VMDRAIN();
                asm volatile("" : "+v"(km[0]), "+v"(km[1]), "+v"(km[2]), "+v"(km[3]), "+v"(km[4]), "+v"(km[5]));
#pragma unroll
                for (int m = 0; m < 6; ++m) sc[2] = dmf(km[m], qf[m], sc[2]);
                s2[8] = sc[2][0]; s2[9] = sc[2][1]; mx = max3f(mx, s2[8], s2[9]);
                constexpr float c1 = 0.07216878364870322f * 1.4426950408889634f;
                mx = xg_max(mx) * -c1;
                float sum = 0.f, su1 = 0.f;
#pragma unroll
                for (int q = 0; q < 10; ++q) { if (qt == 0 && q >= 4 && q < 8) continue; s2[q] = __builtin_amdgcn_exp2f(__builtin_fmaf(s2[q], c1, mx)); if (q & 1) su1 += s2[q]; else sum += s2[q]; }
                float inv = __builtin_amdgcn_rcpf(xg_sum(sum + su1));
                asm volatile("s_nop 0" : "+v"(inv));
#pragma unroll
                for (int q = 0; q < 10; ++q) { if (qt == 0 && q >= 4 && q < 8) continue; s2[q] = muls(s2[q], inv); }
                const unsigned pb = ST_PX + ((t & (PR - 1)) * 2 + qt) * 40960;
                st16s((u32x4){pk2(s2[0], s2[4]), pk2(s2[2], s2[6]), pk2(s2[1], s2[5]), pk2(s2[3], s2[7])}, rst, l16, pb + h * 1024);
                __builtin_amdgcn_raw_buffer_store_b32(pk2(s2[8], s2[9]), rst, l15 * 16 + (lg & 1) * 8 + (lg >> 1) * 4, pb + 32768 + h * 256, 16);
                AT(3);
                owe = !(t >= PIM || (er && !rdy));
                if (!owe) out_then(min(i + 1, NSEG_ - 1));
                if (er) v_dma();
                next();
                if (er) VMW(qt ? 12 : 6);
                AT(4);
                if (l16 == 0 && h == 0 && qt == 1 && t >= 56) STAMP(340 + t - 56);
            }
        }
        ATD(4, i == 44 && h == 0);
        qs = qs == QA ? 0 : qs + 1;
    }
    VMDRAIN(); LBAR();
    if (tid == 0) STAMP(512 + blockIdx.x);
    }
}
__device__ __forceinline__ void xq_part(KA& a, unsigned char* lds, const int h, const int wave) {
    if (wave == 0 || wave == AW1) __builtin_amdgcn_s_setprio(1);
    if (wave == (AWS ? AW1 : 0)) xq_run<0>(a, lds, h, wave); else if (wave == (AWS ? 0 : AW1)) xq_run<1>(a, lds, h, wave); else xq_run<-1>(a, lds, h, wave);
}

__device__ __forceinline__ void q_part(KA& a, unsigned char* lds, const int rb, const int g, const int wave) {
    constexpr int STG = 26624, NS = 5, layer = 0;
    const int lane = lane_id(), l15 = lane & 15, lg = lane >> 4, rp = wave & 1, cg = wave >> 1;
    const unsigned ldsb = LDSB();
    const rsrc_t rw = MKR(a.wqkv, D_ * 1920 * 2), rx = MKR(a.x0, S_ * D_ * 2);
    unsigned vo[4];
    vo[0] = (rb * 128 + wave * 16 + (lane >> 2)) * (D_ * 2) + ((lane ^ -lg) & 3) * 16;
#pragma unroll
    for (int u = 0; u < 3; ++u) { const int p = wave + 8 * u, c = p * 16 + (lane >> 2), row = c / 9, e = c - row * 9, sg = e / 3;
        vo[u + 1] = p < 18 ? row * 3840 + sg * 1280 + ((160 * g) & ~63) + (e - sg * 3) * 64 + ((lane & 3) ^ ((row >> 2) & 2)) * 16 : OOB; }
    const unsigned m3 = wave < 2 ? ~0u : 0u, d3 = wave < 2 ? 24576 : NS * STG;
    const int rot = (rb + (g & 1) * 5) % 10;
    auto piece = [&](int st, unsigned sl, int j) { dma16(j ? rw : rx, vo[j], st * (j ? 32 * 3840 : 64), ldsb + wave * 1024 + (j < 3 ? sl + j * 8192 : (sl & m3) + d3)); };
#pragma unroll 1
    for (int st = 0; st < NS - 1; ++st) {
#pragma unroll
        for (int j = 0; j < 4; ++j) piece((st + 2 * rot) % 20, st * STG, j); }
    if (wave == 0 && lane == 0 && rb == 0 && g == 0) STAMP(17);
    f32x4 acc[4][4], gr = Z4, cs[4] = {Z4, Z4, Z4, Z4};
#pragma unroll
    for (int i = 0; i < 16; ++i) acc[i >> 2][i & 3] = Z4;
    u32x4 one = (u32x4){0x3F803F80u, 0x3F803F80u, 0x3F803F80u, 0x3F803F80u}; PIN(one);
    unsigned wp[4]; const unsigned xp = (rp * 4 + cg) * 1024 + l15 * 64 + ((lg ^ -(l15 >> 2)) & 3) * 16;
#pragma unroll
    for (int u = 0; u < 4; ++u) { const int T = min(4 * u + cg, 14); wp[u] = 8192 + (8 * lg + (l15 >> 2)) * 576 + (((T / 5) * 192 + (T % 5 + (g & 1)) * 32) ^ ((lg & 1) * 32)) + (l15 & 3) * 8; }
    u32x4 xf[2][4], wf[2][4];
    auto rw_ = [&](int kk, unsigned sl, int u) { const unsigned char* p = lds + sl + wp[u]; const u32x2 x0 = ldstr(p), x1 = ldstr(p + 4 * 576); wf[kk][u] = (u32x4){x0.x, x0.y, x1.x, x1.y}; SBAR(); };
    auto rx_ = [&](int kk, unsigned sl, int i) { xf[kk][i] = lds16(lds + sl + (xp ^ (i * 1024))); SBAR(); };
    VMW(12);
    LBAR();
    if (wave == 0 && lane == 0 && rb == 0 && g == 0) STAMP(14);
#pragma unroll
    for (int j = 0; j < 8; ++j) { if (j < 4) rx_(0, 0, j); else rw_(0, 0, j - 4); }
    unsigned sn = STG, sf = (NS - 1) * STG;
    int cf = (rot + 2) % 10; const int cb = (rb + 10 - rot) % 10 + (rb < 10 ? 0 : 16);
#pragma unroll 1
    for (int ch = 0; ch < 10; ++ch) {
#pragma unroll
        for (int kk = 0; kk < 2; ++kk) {
            AT(3 * kk);
            VMW(8);
            __builtin_amdgcn_s_barrier(); CBAR();
            AT(3 * kk + 1);
            gr = dmf(xf[kk][0], xf[kk][0], gr); PIN(gr); SBAR();
#pragma unroll
            for (int j = 0; j < 16; ++j) { const int u = j >> 2, i = j & 3;
                if (j == 12 && cg == 3) wf[kk][3] = one;
                acc[i][u] = dmf(wf[kk][u], xf[kk][i], acc[i][u]); PIN(acc[i][u]); SBAR();
                if (!(j & 1)) { if (j < 8) rx_(kk ^ 1, sn, j >> 1); else rw_(kk ^ 1, sn, (j >> 1) - 4); }
                if (i == 1) { piece(2 * cf + kk, sf, u); SBAR(); } }
            if (rp) { cs[0] = dmf(wf[kk][2], one, cs[0]); cs[1] = dmf(wf[kk][3], one, cs[1]); } else { cs[0] = dmf(wf[kk][0], one, cs[0]); cs[1] = dmf(wf[kk][1], one, cs[1]); }
            if (__builtin_expect(ch == cb && kk == rp, 0)) {
#pragma unroll
                for (int u = 0; u < 4; ++u) { const int T = 4 * u + cg; st16s(wf[kk][u], MKR(a.ws + WS_WF_AQKV, D_ * 1920 * 2), T < 15 ? lane * 16 : OOB, (((T / 5) * 40 + 5 * g + T % 5) * 20 + 2 * rb + kk) * 1024); } }
            SBAR();
            sn = sn == (NS - 1) * STG ? 0 : sn + STG; sf = sf == (NS - 1) * STG ? 0 : sf + STG;
        }
        cf = cf == 9 ? 0 : cf + 1;
        if (ch == 7) { vo[0] = vo[1] = vo[2] = vo[3] = OOB; }
        AT(6);
        ATD(6, ch == 5 && rb == 0 && g == 0);
    }
    {
        const rsrc_t rz = MKR(a.ws + WS_HOP + F_AF, HOP_BYTES - F_AF), rh = MKR(al2m(a.ws + WS_FR), FR_FILL); const unsigned w = (rb * 8 + g) * 8 + wave;
#pragma unroll
        for (int j = 0; j < 3; ++j) st16c(rz, (w + j * 1024) * 1024 + lane * 16, (u32x4){0u, 0u, 0u, 0u});
        static_assert(HOP_BYTES - F_AF <= 3u << 20, "flags");
        st16c(rh, (w >> 4) % 20u >= 2u ? w * 1024 + lane * 16 : OOB, (u32x4){~0u, ~0u, ~0u, ~0u});
    }
    if (wave == 0 && lane == 0 && rb == 0 && g == 0) STAMP(15);
    constexpr int KP = 176, L_K = 0, L_V = L_K + 128 * KP, L_Q = L_V + 128 * KP + 64, L_S = NS * STG + 8192, L_CS = L_S + 1024;
    const int row = rp * 64 + cg * 16 + l15;
    if (lg == (l15 >> 2)) *reinterpret_cast<float*>(lds + L_S + row * 8 + 4) = (l15 & 2) ? ((l15 & 1) ? gr[3] : gr[2]) : ((l15 & 1) ? gr[1] : gr[0]);
    if (cg == 3) *reinterpret_cast<float*>(lds + L_S + (row ^ (lg * 16)) * 8) = lg < 2 ? (lg ? acc[1][3][0] : acc[0][3][0]) : (lg == 2 ? acc[2][3][0] : acc[3][3][0]);
    if (l15 < 2) *reinterpret_cast<f32x4*>(lds + L_CS + wave * 128 + l15 * 64 + lg * 16) = l15 ? cs[1] : cs[0];
    VMW(4); LBAR();
    f32x2 ct[4];
    {
        const f32x2 sv = *reinterpret_cast<const f32x2*>(lds + L_S + (row ^ (lg * 16)) * 8);
#pragma unroll
        for (int u = 0; u < 4; ++u) cs[u] = *reinterpret_cast<const f32x4*>(lds + L_CS + cg * 256 + u * 64 + lg * 16);
        float m, iv; ln_consts(sv[0], sv[1], m, iv); m = -m;
        const auto m2 = __builtin_amdgcn_permlane32_swap(__float_as_uint(m), __float_as_uint(m), false, false), i2 = __builtin_amdgcn_permlane32_swap(__float_as_uint(iv), __float_as_uint(iv), false, false);
#pragma unroll
        for (int z = 0; z < 2; ++z) { const auto x = __builtin_amdgcn_permlane16_swap(m2[z], m2[z], false, false), y = __builtin_amdgcn_permlane16_swap(i2[z], i2[z], false, false);
            ct[2 * z] = (f32x2){__uint_as_float(x[0]), __uint_as_float(y[0])}; ct[2 * z + 1] = (f32x2){__uint_as_float(x[1]), __uint_as_float(y[1])}; }
    }
    if (wave == 0 && lane == 0 && rb == 0 && g == 0) STAMP(18);
    auto tile = [&](int u, bool ph, unsigned char* dst, int pitch) {
#pragma unroll
        for (int i = 0; i < 4; ++i) { f32x4 y = acc[i][u] + ct[i][0] * cs[u];
            if (ph) { y = y * ct[i][1] + 1e-6f; const f32x4 z = y * 1.4426950408889634f - 1.4426950408889634e-6f;
#pragma unroll
                for (int r = 0; r < 4; ++r) y[r] = __builtin_amdgcn_exp2f(__builtin_amdgcn_fmed3f(z[r], 0.f, -3.0e38f)) + __builtin_amdgcn_fmed3f(y[r], 1e-6f, 3.0e38f); }
            else y *= ct[i][1];
            *reinterpret_cast<u32x2*>(dst + (i ^ cg) * pitch) = (u32x2){pk2(y[0], y[1]), pk2(y[2], y[3])}; } };
    unsigned char* kp = lds + L_K + (rp * 64 + l15) * KP + 8 * lg + cg * 32;
    if (cg > 0) tile(1, true, kp - 32, 16 * KP);
    if (cg < 2) tile(2, true, kp + 96, 16 * KP); else tile(2, false, kp + (L_V - L_K) - 64, 16 * KP);
    if (cg < 3) tile(3, false, kp + (L_V - L_K) + 64, 16 * KP);
    LBAR();
    if (wave == 0 && lane == 0 && rb == 0 && g == 0) STAMP(19);
    if (wave >= 4) {
        f32x4 d4[4] = {Z4, Z4, Z4, Z4};
#pragma unroll
        for (int ks = 0; ks < 4; ++ks) {
            u32x4 A[2], B[2];
#pragma unroll
            for (int t = 0; t < 2; ++t) { const unsigned char* p = lds + L_K + (32 * ks + 8 * lg + (l15 >> 2)) * KP + 40 * (wave & 3) + 32 * t + 8 * (l15 & 3);
                const u32x2 a0 = ldstr(p), a1 = ldstr(p + 4 * KP), b0 = ldstr(p + (L_V - L_K)), b1 = ldstr(p + (L_V - L_K) + 4 * KP);
                A[t] = (u32x4){a0.x, a0.y, a1.x, a1.y}; B[t] = (u32x4){b0.x, b0.y, b1.x, b1.y}; }
            if (l15 >= 4) B[1] = one;
#pragma unroll
            for (int q = 0; q < 4; ++q) d4[q] = dmf(A[q >> 1], B[q & 1], d4[q]); }
        const rsrc_t rk = MKR(a.ws + WS_KVP, 32 * 16 * 4096);
#pragma unroll
        for (int q = 0; q < 4; ++q) st16c(rk, ((q < 2 || lg == 0) && (!(q & 1) || l15 < 8)) ? ((4 * g + (wave & 3)) * 16 + rb) * 4096 + q * 1024 + lane * 16 : OOB, __builtin_bit_cast(u32x4, d4[q]));
    }
    auto qdst = [&](int T) { const int gi = 4 * T + lg, hd = gi / 5, pt = gi - 5 * hd; return lds + L_Q + rp * 10240 + hd * 640 + (pt < 4 ? 128 + l15 * 32 + pt * 8 : l15 * 8); };
    tile(0, true, qdst(cg), 2560);
    if (cg == 0) tile(1, true, qdst(4), 2560);
    if (wave == 3) boot_wait(a);
    VMDRAIN(); LBAR();
    const rsrc_t rfl = MKR(a.ws + WS_HOP, F_ERR), rq = MKR(a.ws + WS_PQ, S_ * D_ * 2);
    if (wave == 3 && lane == 0) { st4c(rfl, F_QK + (rb * 8 + g) * FLP, 1u); if (rb == 0 && g == 0) STAMP(16); STAMP(1400 + rb * 8 + g); }
#pragma unroll
    for (int j = 0; j < 3; ++j) st16c(rq, (j < 2 || lane < 32) ? (rb * 8 + wave) * 20480 + g * 2560 + j * 1024 + lane * 16 : OOB, lds16(lds + L_Q + wave * 2560 + j * 1024 + lane * 16));
    VMDRAIN(); LBAR();
    if (wave == 3 && lane == 0) st4c(rfl, F_PQ + (rb * 8 + g) * FLP, 1u);
}
__device__ __forceinline__ void kv_part(KA& a, unsigned char* lds, const int i, const int wave) {
    const int lane = lane_id(), l15 = lane & 15, lg = lane >> 4, h = 2 * i + (wave >> 2);
    const rsrc_t rk = MKR(a.ws + WS_KVP, 32 * 16 * 4096);
    if (wave == 7) wait_set(MKR(a.ws + WS_HOP, F_ERR), F_QK + (l15 * 8 + (h >> 2)) * FLP, a.ws + WS_HOP, 0);
    LBAR();
    u32x4 v[4][4];
#pragma unroll
    for (int q = 0; q < 16; ++q) v[q >> 2][q & 3] = __builtin_amdgcn_raw_buffer_load_b128(rk, (((q & 2) == 0 || lg == 0) && (!(q & 1) || l15 < 8)) ? lane * 16 + (q & 3) * 1024 : OOB, (i * 32 + wave * 4 + (q >> 2)) * 4096, 16);
#pragma unroll
    for (int q = 0; q < 4; ++q) *reinterpret_cast<f32x4*>(lds + 32768 + ((wave >> 2) * 16 + q * 4 + (wave & 3)) * 1024 + lane * 16) = (__builtin_bit_cast(f32x4, v[0][q]) + __builtin_bit_cast(f32x4, v[1][q])) + (__builtin_bit_cast(f32x4, v[2][q]) + __builtin_bit_cast(f32x4, v[3][q]));
    LBAR();
    const f32x4* p = reinterpret_cast<const f32x4*>(lds + 32768 + wave * 4096 + lane * 16);
    const f32x4 s = (p[0] + p[64]) + (p[128] + p[192]);
    const int e = 16 * (wave & 1) + l15, d = 8 * (wave & 2) + 4 * lg;
    st8c(MKR(a.ws + WS_KVF, 32768), (d < 24 && e <= 20) ? h * 1024 + e * 48 + d * 2 : OOB, (u32x2){min(pk2(s[0], s[1]), 0xfffffffeu), min(pk2(s[2], s[3]), 0xfffffffeu)});
}
__device__ __forceinline__ void boot_part(KA& a, unsigned char* lds, const int i, const int wave) {
    const int lane = lane_id(), l15 = lane & 15, lg = lane >> 4, w = i * 8 + wave;
    const rsrc_t rb = MKR((a.ws + WS_HOP) - 2048, 2048), rz = MKR((a.ws + WS_HOP), F_AF), rh = MKR(al2m((a.ws + WS_FR)), FR_FILL), rf = MKR((a.ws + WS_WF_AO), D_ * D_ * 2);
    const u32x2 tag = ld8c(rb, 0);
    {
        const unsigned c = (w & 7) * 64 + lane, b = ((w >> 4) & 1) * 8 + (c >> 6), m = c & 15u;
        u32x4 v = (u32x4){~0u, ~0u, ~0u, ~0u};
        if (b < 10u) { const unsigned* mp = reinterpret_cast<const unsigned*>(a.mem0 + (2 * (m >> 2) + (m & 1)) * D_ + b * 64 + ((c >> 4) & 3u) * 16 + (m & 2));
            v = (u32x4){min(mp[0], 0xfffffffeu), min(mp[2], 0xfffffffeu), min(mp[4], 0xfffffffeu), min(mp[6], 0xfffffffeu)}; }
        else if (!(c & 3u)) v.x = c == 196u ? TT_E1 + TT_E2 + 40u : TT_E1 + 20u;
        st16c(rh, (w >> 5) * (20u << 14) + (w & 31) * 1024 + lane * 16, v);
    }
    st16c(rz, w * 1024 + lane * 16, (u32x4){0u, 0u, 0u, 0u});
    st16c(MKR(a.ws + WS_KVF, 65536), w * 1024 + lane * 16, (u32x4){~0u, ~0u, ~0u, ~0u});
    VMDRAIN(); LBAR();
    if (wave == 0 && lane == 0) st8c(rb, 128 + i * 128, (u32x2){tag.x + 1u, ~tag.y});
    if (w == 0 && lane == 0) { const int layer = 0; STAMP(20); }
    u32x4 v[4][4];
#pragma unroll
    for (int q = 0; q < 16; ++q) { const int u = min(w + 64 * (q >> 2), 199), kk = u / 10; v[q >> 2][q & 3] = *reinterpret_cast<const u32x4*>(a.wao + (kk * 32 + (q & 3) * 8 + (lane >> 3)) * D_ + (u - kk * 10) * 64 + (lane & 7) * 8); }
    unsigned char* t = lds + wave * 16384;
#pragma unroll
    for (int q = 0; q < 16; ++q) *reinterpret_cast<u32x4*>(t + q * 1024 + lane * 16) = v[q >> 2][q & 3];
    CBAR();
#pragma unroll
    for (int q = 0; q < 16; ++q) { const int u = w + 64 * (q >> 2), kk = u / 10; const unsigned char* p = t + (q >> 2) * 4096 + (8 * lg + (l15 >> 2)) * 128 + (q & 3) * 32 + (l15 & 3) * 8; const u32x2 x0 = ldstr(p), y0 = ldstr(p + 512);
        if (u < 200) st16c(rf, ((((u - kk * 10) * 4 + (q & 3)) * 20 + kk) * 64 + lane) * 16, (u32x4){x0.x, x0.y, y0.x, y0.y}); }
    boot_wait(a);
    VMDRAIN(); LBAR();
    if (wave == 0 && lane == 0) st4c(rz, F_WO + i * FLP, 1u);
    pol_vote(a, w);
    if (w == 0 && lane == 0) { const int layer = 0; STAMP(21); }
    LBAR();
}

#define KSL0 16
#define KSL 6
#define AU0 66
#define TT_AW 950
#define ATS 62
#define ATU 0
#define AIM 12
template <int K, bool W3, class F> __device__ __forceinline__ void a_proj(KA& a, unsigned char* lds, const int h, const int wave, F&& pre) {
    constexpr int YB = 51200, L_SK = 3 * YB, L_C = L_SK + 6144 + 64, L_KN = L_C + 256, KP = 48, layer = 0;
    const int lane = lane_id(), l15 = lane & 15, lg = lane >> 4, hd = 2 * h + (wave & 1);
    const unsigned ldsb = LDSB();
    unsigned char* hop = (a.ws + WS_HOP);
    const rsrc_t rfl = MKR(hop, F_ERR), rp = MKR((a.ws + WS_PQ), S_ * D_ * 2), rxa = MKR((a.ws + WS_X), XI_BYTES);
    u32x4 wq[2][20]; f32x4 cs[2] = {Z4, Z4}, d4[4] = {Z4, Z4, Z4, Z4};
    if (K) {
        const rsrc_t rw = MKR((a.ws + WS_WF_AQKV), 640 * 1920 * 2);
#pragma unroll
        for (int u = 0; u < 2; ++u) { const int c = 20 * hd + (K == 2 ? (1 + u) * 640 + l15 : (u ? min(l15 >> 2, 2) * 640 + 16 + (l15 & 3) : l15));
#pragma unroll
            for (int q = 0; q < 20; ++q) wq[u][q] = __builtin_amdgcn_raw_buffer_load_b128(rw, ((c >> 4) * 20 * 64 + lg * 16 + (c & 15)) * 16, q * 1024, 16); }
#pragma unroll
        for (int q = 0; q < 40; ++q) cs[q & 1] = dmf(wq[q & 1][q >> 1], (u32x4){0x3F803F80u, 0x3F803F80u, 0x3F803F80u, 0x3F803F80u}, cs[q & 1]);
        PIN(cs[0]); PIN(cs[1]); VMKNOWN();
    }
    const unsigned f0 = F_OF + lane * FLP, f1 = F_OF + (64 + l15 + (lane & 16) * 5) * FLP;
    unsigned tnext = rt_now(), per = 0;
    auto kv = [&]() {
        { u32x4 A[2], B[2]; const int z = wave & 1;
#pragma unroll
            for (int q = 0; q < 2; ++q) { const unsigned char* p = lds + L_SK + z * 64 * KP + (8 * lg + (l15 >> 2)) * KP + 32 * q + 8 * (l15 & 3);
                const u32x2 a0 = ldstr(p), a1 = ldstr(p + 4 * KP), b0 = ldstr(p + 32 * KP), b1 = ldstr(p + 36 * KP);
                A[q] = (u32x4){a0.x, a0.y, a1.x, a1.y}; B[q] = (u32x4){b0.x, b0.y, b1.x, b1.y}; }
            if (l15 >= 4) { A[1] = (u32x4){0u, 0u, 0u, 0u}; B[1] = (u32x4){0x3F803F80u, 0x3F803F80u, 0x3F803F80u, 0x3F803F80u}; }
#pragma unroll
            for (int q = 0; q < 4; ++q) d4[q] = dmf(A[q >> 1], B[q & 1], d4[q]); } };
    auto fill = [&](int n, unsigned dst, int g0) {
#pragma unroll
        for (int g = 0; g < 2; ++g) { const unsigned z = (g0 + g + (h >> 2) * AROT) & 3, o = (z * 13 - (z + 1) / 4 * 2) * 1024; dma16cn<13>(rxa, lane * 16, (unsigned)n * XI_PITCH + o, dst + o); } };
    LBAR();
    if (!K) __builtin_amdgcn_s_setprio(2);
    int nx = 0, kn = 0, yb = 0;
    auto need = [&](int t, unsigned dst) {
        if (kn == t) {
            if (t < TF) rt_wait(tnext);
            for (unsigned sp = 0;; ++sp) { CBAR(); const u32x4 v = ld16c(rfl, f0 + t * (NR_ * FLP)); const unsigned w = ld4c(rfl, F_OF + (64 + l15 + t * NR_) * FLP);
                if (__all(v.x != 0u && w != 0u)) { per = __builtin_amdgcn_readfirstlane(v.x); tnext = __builtin_amdgcn_readfirstlane(v.w) + per + (t + 1 < TF - 6 ? TT_AD : TT_AN); break; }
                if (t < TF) __builtin_amdgcn_s_sleep(12); else __builtin_amdgcn_s_sleep(3);
                if (sp > SPIN_CAP_W) { set_err(hop); break; } }
            ++kn; }
        fill(t, dst, 0); fill(t, dst, 2);
        if (kn >= TF && kn < NSEG_) { const int k1 = min(kn + 1, NSEG_ - 1);
            const unsigned x = ld4c(rfl, f0 + kn * (NR_ * FLP)), y = ld4c(rfl, f0 + k1 * (NR_ * FLP)), z = ld4c(rfl, F_OF + (64 + l15 + ((lane & 16) ? k1 : kn) * NR_) * FLP);
            const unsigned long long zz = __ballot(z == 0u);
            if (__all(x != 0u) && !(zz & 0xffffull)) kn = (__all(y != 0u) && !zz) ? k1 + 1 : kn + 1; }
        VMKNOWN(); };
    if (W3) need(0, ldsb);
    nx = kn = 1;
    LBAR();
    auto seg = [&](const int t) {
        AT(0);
        if (!K && !W3 && wave && t) kv();
        AT(1);
        if (lane == 0 && wave == 4 && h == 0 && t >= 56) STAMP(330 + t - 56);
        const int up = min(kn, t + 3), n = nx + ((nx ^ wave) & 1);
        const unsigned nb = ldsb + (n == t + 1 ? (yb == 2 * YB ? 0 : yb + YB) : (yb == 0 ? 2 * YB : yb - YB));
        u32x4 fa, fb; unsigned fc; int pr = 0; f32x4 acc[2][2] = {{Z4, Z4}, {Z4, Z4}};
        if (K) {
            const unsigned char* yp = lds + yb + XI_X + lane * 16;
            u32x4 yf[4];
#pragma unroll
            for (int q = 0; q < 3; ++q) { yf[q] = lds16(yp + q * 1024); SBAR(); }
            f32x2 ct; float y[8], e[8]; u32x2 o0, o1;
            auto epi = [&](int rt, int z) {
                constexpr int NP = 8 / (K ? K : 1);
                if (z == 0) ct = *reinterpret_cast<const f32x2*>(lds + L_C + rt * 128 + l15 * 8);
                if (z >= 2 && z < 10) { y[z - 2] = __builtin_fmaf(ct[0], cs[(z - 2) >> 2][(z - 2) & 3], acc[rt][(z - 2) >> 2][(z - 2) & 3]); PIN(y[z - 2]); if (z > 2) { y[z - 3] *= ct[1]; PIN(y[z - 3]); } }
                if (z == 10) { y[7] *= ct[1]; PIN(y[7]); }
                if (z >= 10 && z < 10 + NP) { e[z - 10] = __builtin_amdgcn_fmed3f(y[z - 10], 0.f, -3.0e38f); PIN(e[z - 10]); }
                if (z >= 11 && z < 11 + NP) { e[z - 11] *= 1.4426950408889634f; PIN(e[z - 11]); }
                if (z >= 19 && z < 19 + NP) { e[z - 19] = __builtin_amdgcn_exp2f(e[z - 19]); PIN(e[z - 19]); }
                if (z >= 20 && z < 20 + NP) { const float m = __builtin_amdgcn_fmed3f(y[z - 20], 0.f, 3.0e38f); e[z - 20] = (e[z - 20] + 1e-6f) + m; PIN(e[z - 20]); }
                if (z >= 28 && z < 32) { e[z - 24] = (K == 2 || lg >= 2) ? y[z - 24] : e[z - 24]; PIN(e[z - 24]); }
                if (z == 30) { o0 = (u32x2){pk2(e[0], e[1]), pk2(e[2], e[3])}; PIN(o0); }
                if (z == 32) { o1 = (u32x2){pk2(e[4], e[5]), pk2(e[6], e[7])}; PIN(o1); }
                unsigned char* sk = lds + L_SK + (wave & 1) * 64 * KP + (rt * 16 + l15) * KP;
                if (z == 33) { if (K == 2) *reinterpret_cast<u32x2*>(sk + 8 * lg) = o0; else __builtin_amdgcn_raw_buffer_store_b64(o0, rp, 128 + l15 * 32 + lg * 8, (t * 2 + rt) * 20480 + hd * 640, 16); }
                if (z == 34) *reinterpret_cast<u32x2*>(sk + (K == 2 ? 32 * KP + 8 * lg : (lg >= 2 ? 32 * KP + 32 : 40 - 8 * lg))) = o1;
                if (z == 35 && K == 1) __builtin_amdgcn_raw_buffer_store_b64(o1, rp, lg ? OOB : l15 * 8, (t * 2 + rt) * 20480 + hd * 640, 16);
                SBAR(); };
#pragma unroll
            for (int q = 0; q < 40; ++q) { acc[q / 20][0] = dmf(wq[0][q % 20], yf[q & 3], acc[q / 20][0]); PIN(acc[q / 20][0]); SBAR(); if (q + 3 < 40) { yf[(q + 3) & 3] = lds16(yp + (q + 3) * 1024); SBAR(); }
                if (q > 21) epi(0, 2 * q - 44);
                acc[q / 20][1] = dmf(wq[1][q % 20], yf[q & 3], acc[q / 20][1]); PIN(acc[q / 20][1]); SBAR();
                if (q > 21) epi(0, 2 * q - 43);
                if (q == 21) { CBAR(); __builtin_amdgcn_s_barrier(); CBAR(); SBAR(); } }
#pragma unroll
            for (int z = 0; z < 36; ++z) epi(1, z);
            if (K == 1 && t == 8) VMDRAIN();
        } else if (!W3 && wave == 0) {
            const f32x2* sp = reinterpret_cast<const f32x2*>(lds + yb + lg * 2560 + l15 * 8);
            f32x2 sv[20];
#pragma unroll
            for (int q = 0; q < 20; ++q) sv[q] = sp[q * 16];
#pragma unroll
            for (int q = 0; q < 16; ++q) { sv[q & 3][0] = adds(sv[q & 3][0], sv[q + 4][0]); sv[q & 3][1] = adds(sv[q & 3][1], sv[q + 4][1]); }
            const u32x2 s4[4] = {__builtin_bit_cast(u32x2, sv[0]), __builtin_bit_cast(u32x2, sv[1]), __builtin_bit_cast(u32x2, sv[2]), __builtin_bit_cast(u32x2, sv[3])};
            c_put(s4, lds + L_C);
        } else if (W3) {
            const int d = (int)(rt_now() - tnext);
            pr = kn >= NSEG_ ? 0 : (kn >= TF || d >= (int)per ? 2 : (d >= 0 ? 1 : 0));
            if (kn + 1 >= NSEG_) pr = min(pr, 1);
            fa = __builtin_amdgcn_raw_buffer_load_b128(rfl, pr ? f0 : OOB, kn * (NR_ * FLP), 16); fb = __builtin_amdgcn_raw_buffer_load_b128(rfl, pr > 1 ? f0 : OOB, (kn + 1) * (NR_ * FLP), 16);
            fc = __builtin_amdgcn_raw_buffer_load_b32(rfl, pr > ((lane >> 4) & 1) ? f1 : OOB, kn * (NR_ * FLP), 16); SBAR();
        } else if (n < up) fill(n, nb, 0);
        AT(2);
        if (!K) LBAR();
        AT(3);
        if (K) {
        } else if (W3) {
            {
                const unsigned long long z = __ballot(fc == 0u);
                if (__all(fa.x != 0u) && !(z & 0xffffull)) { const bool two = __all(fb.x != 0u) && !z; kn += two ? 2 : 1;
                    per = __builtin_amdgcn_readfirstlane(two ? fb.x : fa.x); tnext = __builtin_amdgcn_readfirstlane(two ? fb.w : fa.w) + per + (kn < TF - 6 ? TT_AD : TT_AN); } }
            if (up <= t + 1 && t + 1 < NSEG_) need(t + 1, ldsb + (yb == 2 * YB ? 0 : yb + YB));
            *reinterpret_cast<int*>(lds + L_KN + (t & 1) * 4) = kn;
            if (t == 40) wait_set(rfl, F_AQ + (lane & 15) * 4, hop, 4);
        } else if (wave) { if (n < up) fill(n, nb, 2); if (((t + 1) ^ wave) & 1) { } else VMDRAIN(); }
        nx = max(up, t + 2);
        AT(4);
        LBAR();
        AT(5);
        kn = __builtin_amdgcn_readfirstlane(*reinterpret_cast<const int*>(lds + L_KN + (t & 1) * 4));
        if (W3 && t == 8 && lane == 0) st4c(rfl, F_AQ + h * 4, 1u);
        yb = yb == 2 * YB ? 0 : yb + YB;
        AT(6); AT(7); ATD(5, t == ATS && h == 0);
    };
#pragma unroll 1
    for (int t = 0; t < 50; ++t) seg(t);
    pre();
#pragma unroll 1
    for (int t = 50; t < NSEG_; ++t) seg(t);
    __builtin_amdgcn_s_setprio(0);
    if (!K && !W3 && wave) {
        kv();
        const rsrc_t rkv = MKR(a.ws + WS_KVF + 32768, 32768);
#pragma unroll
        for (int q = 0; q < 4; ++q) { const int e = 16 * (q & 1) + l15, d = 8 * (q & 2) + 4 * lg;
            st8c(rkv, (d < 24 && e <= 20) ? hd * 1024 + e * 48 + d * 2 : OOB, (u32x2){min(pk2(d4[q][0], d4[q][1]), 0xfffffffeu), min(pk2(d4[q][2], d4[q][3]), 0xfffffffeu)}); }
    }
}
__device__ __forceinline__ void a_part(KA& a, unsigned char* lds, const int ai, const int wave) {
    const int lane = lane_id(), tid = wave * 64 + lane;
    constexpr int L_ST = 20480;
    const int cb = ai & 7, rep = ai >> 3, h = ai;
    const int nt = wave < 5 ? wave : 0;
    u32x4 b[20], tq[4]; u32x2 rres;
    const int ct = 5 * cb + nt;
    const rsrc_t rp = MKR((a.ws + WS_PQ), S_ * D_ * 2), rxa = MKR((a.ws + WS_X), XI_BYTES);
    auto fetch = [&](int u) { const int lane = lane_id(), l15 = lane & 15, lg = lane >> 4;
#pragma unroll
        for (int q = 0; q < 4; ++q) tq[q] = __builtin_amdgcn_raw_buffer_load_b128(rp, (lg < 2 ? 128 + l15 * 32 + lg * 16 : (lg == 2 ? l15 * 8 : OOB)) + q * 640, u * 20480 + wave * 2560, 16); };
    auto x_off = [&]() { const int lane = lane_id(), l15 = lane & 15, lg = lane >> 4; return XI_X + (ct >> 1) * 1024 + ((ct & 1) * 2 + (lg >> 1)) * 256 + l15 * 16 + (lg & 1) * 8; };
    auto b_load = [&]() { const int lane = lane_id();
#pragma unroll
        for (int q = 0; q < 20; ++q) b[q] = __builtin_amdgcn_raw_buffer_load_b128(MKR((a.ws + WS_WF_AO), D_ * D_ * 2), wave < 5 ? lane * 16 + (q & 3) * 1024 : OOB, ct * 20480 + (q >> 2) * 4096, 16); };
    auto outp = [&](const int layer) {
        const int lane = lane_id(), l15 = lane & 15, lg = lane >> 4;
        unsigned char* hop = (a.ws + WS_HOP) + layer * HOP_BYTES;
        const rsrc_t rfl = MKR(hop, F_ERR), rr_ = MKR(psel(a.x0, (a.ws + WS_X), layer), layer ? XI_BYTES : S_ * D_ * 2);
        const unsigned oo = x_off(), ro = layer ? oo : (unsigned)(l15 * D_ + 16 * ct + 4 * lg) * 2;
        auto res = [&](int u) { rres = __builtin_amdgcn_raw_buffer_load_b64(rr_, ro, (u >> 1) * (layer ? XI_PITCH : 40960) + (u & 1) * 20480, 16); };
        if (!layer) { boot_wait(a); wait_set(rfl, F_WO + (lane & 7) * FLP, hop, 1); b_load(); res(rep); if (ai < 16) { VMKNOWN(); kv_part(a, lds, ai, wave); } wait_set(rfl, F_PQ + (lane & 7) * FLP, hop, 1); fetch(rep); }
        unsigned so[5];
#pragma unroll
        for (int q = 0; q < 5; ++q) { const unsigned c = 80 * wave + (q < 4 ? 20 * q + 4 * lg : 20 * lg + 16); so[q] = (c >> 3) * 256 + l15 * 16 + (c & 4) * 2; }
        u32x4 k0[4], k1[4], kz[4];
        {
            const rsrc_t rkv = MKR(a.ws + WS_KVF + layer * 32768, 32768);
            const unsigned ko = (lg < 3 ? lg : 0) * 16;
            __builtin_amdgcn_s_sleep(KSL0);
            for (unsigned sp = 0; sp < SPIN_CAP_W; ++sp) { CBAR();
                unsigned mx = 0u;
#pragma unroll
                for (int q = 0; q < 4; ++q) { k0[q] = __builtin_amdgcn_raw_buffer_load_b128(rkv, ko + l15 * 48 + q * 1024, wave * 4096, 16);
                    k1[q] = __builtin_amdgcn_raw_buffer_load_b128(rkv, (l15 >> 2) == q ? ko + (16 + (l15 & 3)) * 48 + q * 1024 : OOB, wave * 4096, 16); kz[q] = __builtin_amdgcn_raw_buffer_load_b128(rkv, ko + 960 + q * 1024, wave * 4096, 16); }
#pragma unroll
                for (int q = 0; q < 4; ++q) mx = max(mx, max(umax4(k0[q]), max(umax4(k1[q]), umax4(kz[q]))));
                if (__all(mx != 0xffffffffu)) break;
                __builtin_amdgcn_s_sleep(KSL);
            }
            if (tid == 0 && ai == 0) STAMP(4);
        }
        const int ue = (!layer && rep < 2) ? AU0 : 2 * NSEG_; int up = -1;
#pragma unroll 1
        for (int u = rep, un; u < ue; up = u, u = un) {
            un = u + ((!layer && u + 1 >= AU0) ? 1 : 3);
            if (!layer && u >= 3 && u < 6) { wait_set(rfl, F_PQ + lane * FLP, hop, 1); wait_set(rfl, F_PQ + (64 + lane) * FLP, hop, 1); }
            if (layer && u >= 9 && u < 12) wait_set(rfl, F_AQ + (lane & 15) * 4, hop, 1);
            if (layer) AT(0);
            {
                f32x4 dz[4], d0[4], d1 = Z4;
#pragma unroll
                for (int q = 0; q < 4; ++q) dz[q] = dmf(kz[q], tq[q], Z4);
#pragma unroll
                for (int q = 0; q < 4; ++q) { d0[q] = dmf(k0[q], tq[q], Z4); d1 = dmf(k1[q], tq[q], d1); }
                SBAR(); fetch(min(un, 2 * NSEG_ - 1)); SBAR();
                float inv[4];
#pragma unroll
                for (int q = 0; q < 4; ++q) inv[q] = __builtin_amdgcn_rcpf(dz[q][0] + 1e-6f);
#pragma unroll
                for (int q = 0; q < 4; ++q) { d0[q] *= inv[q]; *reinterpret_cast<u32x2*>(lds + so[q]) = (u32x2){pk2(d0[q][0], d0[q][1]), pk2(d0[q][2], d0[q][3])}; }
                d1 *= lg < 2 ? (lg == 0 ? inv[0] : inv[1]) : (lg == 2 ? inv[2] : inv[3]);
                *reinterpret_cast<u32x2*>(lds + so[4]) = (u32x2){pk2(d1[0], d1[1]), pk2(d1[2], d1[3])};
            }
            f32x4 acc = (f32x4){lo2f(rres.x), hi2f(rres.x), lo2f(rres.y), hi2f(rres.y)}; PIN(acc);
            if (layer) AT(1);
            LBAR();
            if (layer) AT(2);
            if (layer && u == rep && tid == 448 && ai < 16) st4c(rfl, F_AQ + h * 4, 1u);
            if (wave < 5) {
                const unsigned char* ar = lds + lane * 16;
                u32x4 af[8];
#pragma unroll
                for (int q = 0; q < 5; ++q) { af[q] = lds16(ar + 1024 * q); SBAR(); }
#pragma unroll
                for (int q = 0; q < 20; ++q) { acc = dmf(b[q], af[q & 7], acc); PIN(acc); SBAR();
                    if (q + 5 < 20) { af[(q + 5) & 7] = lds16(ar + 1024 * (q + 5)); SBAR(); } }
                const u32x2 o = (u32x2){pk2(acc[0], acc[1]), pk2(acc[2], acc[3])};
                __builtin_amdgcn_raw_buffer_store_b64(o, rxa, oo, (u >> 1) * XI_PITCH + (u & 1) * 20480, 16);
                *reinterpret_cast<float*>(lds + L_ST + wave * 128 + l15 * 8 + (lg & 1) * 4) = row_sums(o);
            }
            if (layer) AT(3);
            LBAR();
            if (layer) AT(4);
            if (wave == 0) {
                if (lane < 32) { const float* sp = reinterpret_cast<const float*>(lds + L_ST + lane * 4); const float v = ((sp[0] + sp[32]) + (sp[64] + sp[96])) + sp[128];
                    __builtin_amdgcn_raw_buffer_store_b32(__float_as_uint(v), rxa, XI_S8 + cb * 128 + lane * 4, (u >> 1) * XI_PITCH + (u & 1) * 1024, 16); }
                if (lane == 0 && up >= AIM) st4c(rfl, F_AF + (up * 8 + cb) * FLP, 1u); }
            if (u < AIM || un >= ue) { VMKNOWN(); LBAR(); if (tid == 0) st4c(rfl, F_AF + (u * 8 + cb) * FLP, 1u); }
            if (tid == 0 && ai == 0 && u == 0) STAMP(5);
            if (tid == 0 && ai == 16 && un >= ue) STAMP(6);
            if (layer) { AT(5); AT(6); AT(7); }
            ATD(5, layer && u == ATU && ai == 0);
            CBAR(); res(min(un, 2 * NSEG_ - 1));
        }
    };
    outp(0);
    VMKNOWN();
    auto pre = [&]() { fetch(rep); rres = __builtin_amdgcn_raw_buffer_load_b64(rxa, x_off(), (rep >> 1) * XI_PITCH + (rep & 1) * 20480, 16); VMKNOWN(); };
    if (wave >= 4 && ai < 16) { if (wave >= 6) a_proj<2, false>(a, lds, h, wave, pre); else a_proj<1, false>(a, lds, h, wave, pre); b_load(); outp(1); }
    else { if (ai >= 16) {
            if (wave == 0) { const rsrc_t rfl = MKR(a.ws + WS_HOP, F_ERR); unsigned tn = rt_now();
                wait_set(rfl, F_AQ + (lane & 15) * 4, a.ws + WS_HOP, 60); wait_timed(rfl, F_KM + (50 * 32 + ai) * FLP, a.ws + WS_HOP, tn, 13, TT_AW, false); rt_wait(tn); }
            LBAR(); pre(); }
        else if (wave == 3) a_proj<0, true>(a, lds, h, wave, pre); else a_proj<0, false>(a, lds, h, wave, pre); outp(1); }
}

#define MAPV 2
#define WDL 700
#define WDL2 400
#define WDL3 400
#define XPERM 0x76543210u
__global__ void __launch_bounds__(512, 2) KATTR bmega_kernel(MegaArgs) {
    ATI();
    extern __shared__ __attribute__((aligned(16))) unsigned char lds[];
    const int c = blockIdx.x, x = (XPERM >> ((c & 7) * 4)) & 7, rr = c >> 3, q = x >> 1, wave = __builtin_amdgcn_readfirstlane(threadIdx.x >> 6);
    if (LDSB() != 0u) return;
    int role, id, qt = -1;
    const bool ev = (x & 1) == 0;
    if (rr < 8) { role = ev ? 0 : 1; id = q * 8 + rr; }
    else if (rr >= 28) { if (ev == (MAPV == 1)) { role = rr < 30 ? 5 : 6; id = (rr < 30 ? 16 : 0) + 2 * q + (rr & 1); } else { role = 5; id = (rr - 28) * 4 + q; } }
    else if (ev) { role = 2; id = q * 20 + rr - 8; } else if (rr < 20) { role = 3; id = q * 12 + rr - 8; } else { role = 4; id = q * 8 + rr - 20; }
    if (role == 2 || role == 4 || role == 6 || (role == 5 && id >= 16)) qt = (role == 4 ? (rr + 4) >> 1 : (rr - 8) >> 1) * 8 + 2 * q + (rr & 1);
    if (role == 0 || role == 1 || role == 3) rt_wait(rt_now() + (role == 0 ? WDL : (role == 1 ? WDL2 : WDL3)));
    if (role == 0) s_part(args(), lds, id, wave);
    else if (role == 1) { if (wave < 4) v_run<true>(args(), lds, id, wave); else v_run<false>(args(), lds, id, wave); }
    else if (role == 3) xp_part(args(), lds, id, wave);
    else {
        if (qt >= 0) q_part(args(), lds, qt >> 3, qt & 7, wave); else if (id >= 8) boot_part(args(), lds, id - 8, wave);
        if (role == 5) a_part(args(), lds, id, wave);
        else if (role == 6) w_part(args(), lds, id, wave);
        else if (role == 4) xq_part(args(), lds, id, wave);
        else {
            if (wave == 0 && !(id & 1) && lane_id() == 0) st4c(MKR(args().ws + WS_HOP, F_ERR), F_SD + (id >> 1) * 4, 1u + my_side());
            ro_part(args(), lds, id, wave); }
    }
}

extern "C" void kernel_launch(void* const* d_in, const int* in_sizes, int n_in, void* d_out, int out_size, void* d_ws, size_t ws_size, hipStream_t stream) {
    (void)hipFuncSetAttribute((const void*)bmega_kernel, hipFuncAttributeMaxDynamicSharedMemorySize, MG_LDS);
    MegaArgs ma; ma.wq = (const bf16_t*)d_in[5]; ma.wk = (const bf16_t*)d_in[6]; ma.wv = (const bf16_t*)d_in[7]; ma.wo = (const bf16_t*)d_in[8];
    ma.wqkv = (const bf16_t*)d_in[1]; ma.wao = (const bf16_t*)d_in[2]; ma.mem0 = (const bf16_t*)d_in[4];
    ma.x0 = (const bf16_t*)d_in[0]; ma.out = (bf16_t*)d_out; ma.ws = (unsigned char*)d_ws;
    bmega_kernel<<<256, 512, MG_LDS, stream>>>(ma);
}
```

```cpp
#include <hip/hip_runtime.h>
#include <hip/hip_bf16.h>
#include <stdint.h>
#include <type_traits>

typedef __attribute__((ext_vector_type(8))) short bf16x8;
typedef __attribute__((ext_vector_type(4))) float f32x4;
typedef __attribute__((ext_vector_type(4))) unsigned u32x4;
typedef __attribute__((ext_vector_type(2))) unsigned u32x2;
typedef uint16_t bf16_t;

typedef __attribute__((ext_vector_type(2))) __bf16 bf16x2_t;
typedef __attribute__((ext_vector_type(2))) float f32x2;
__device__ __forceinline__ unsigned pk2(float lo, float hi) { f32x2 v = {lo, hi}; return __builtin_bit_cast(unsigned, __builtin_convertvector(v, bf16x2_t)); }
__device__ __forceinline__ float rbf(float f) { return __uint_as_float(pk2(f, f) << 16); }
__device__ __forceinline__ float lo2f(unsigned p) { return __uint_as_float(p << 16); }
__device__ __forceinline__ float hi2f(unsigned p) { return __uint_as_float(p & 0xffff0000u); }

constexpr int S_ = 2048, D_ = 640, H_ = 32, E_ = 192, NSEG_ = 64;
constexpr int HE_ = H_ * E_;

#define YS 1
constexpr int NR_ = 80;
constexpr int NXP_ = 48;
constexpr size_t al(size_t x) { return (x + 255) & ~(size_t)255; }
constexpr size_t WS_WF_AQKV = 0;
constexpr size_t WS_WF_AO   = WS_WF_AQKV + al((size_t)640 * 1920 * 2);
constexpr size_t WS_PQ      = WS_WF_AO + al((size_t)640 * 640 * 2);
constexpr unsigned XI_X = 10240, XI_S8 = XI_X + 40960, XI_PITCH = XI_S8 + 2048, XI_BYTES = NSEG_ * XI_PITCH;
constexpr size_t WS_X       = WS_PQ + al((size_t)S_ * D_ * 2);
constexpr size_t WS_Y       = WS_X + YS * al(XI_BYTES);
constexpr size_t WS_KVP     = WS_Y + al(XI_BYTES);
constexpr size_t WS_KVF     = WS_KVP + al((size_t)32 * 16 * 2048);
constexpr size_t WS_ST      = WS_KVF + al((size_t)32 * 1024 * 2);
#define KXR 32
#define PR 32
#define URING 32
constexpr unsigned ST_KX = 0;
constexpr unsigned ST_VX = ST_KX + KXR * 32u * 12288;
constexpr unsigned ST_KM = ST_VX + KXR * 32u * 12288;
constexpr unsigned ST_PX = ST_KM + PR * 32u * 3072;
constexpr unsigned ST_U  = ST_PX + PR * 2u * 40960;
constexpr unsigned ST_BYTES = ST_U + (unsigned)URING * 40 * 32768;
constexpr size_t WS_FR      = WS_ST + al(ST_BYTES);
constexpr unsigned FR_FILL = 40u << 14, FR_UM = FR_FILL, FR_USED = FR_UM + (160u << 14);
__device__ __forceinline__ unsigned char* al2m(unsigned char* p) { return (unsigned char*)(((uintptr_t)p + (((uintptr_t)1 << 21) - 1)) & ~(((uintptr_t)1 << 21) - 1)); }
constexpr unsigned M_STAMP = 16384 + 2048;
constexpr unsigned FLP = 128;
constexpr unsigned F_POL = 0, F_AQ = F_POL + 256, F_SD = F_AQ + 256, F_WO = F_SD + 256, F_IN = F_WO + 8 * FLP, F_QK = F_IN + 8 * FLP, F_PQ = F_QK + 128 * FLP, F_ZR = F_PQ + 128 * FLP, F_AF = 53248, F_XP = F_AF + 64 * 16 * FLP, F_PX = F_XP + 64 * NXP_ * FLP, F_KM = F_PX + 70 * 64 * FLP, F_UX = F_KM + 64 * 32 * FLP,
    F_UW = F_UX + 66 * 32 * FLP, F_OF = F_UW + 64 * 8 * FLP, F_RD = F_OF + 64 * NR_ * FLP, F_ERR = F_RD + 64 * NR_ * FLP, HOP_BYTES = F_ERR + 256;
constexpr size_t WS_BOOT    = WS_FR + ((size_t)6 << 20);
constexpr size_t WS_HOP     = WS_BOOT + 2048;
constexpr size_t WS_DBG     = WS_HOP + al(2 * HOP_BYTES);
constexpr size_t WS_END     = WS_DBG + al((size_t)2 << 20);
static_assert(WS_END <= (size_t)256 * 1024 * 1024, "ws too big");
static_assert(FR_USED <= (4u << 20), "hand-off pages");

__device__ __forceinline__ float phi1(float x) { return (__builtin_amdgcn_exp2f(fminf(x, 0.f) * 1.4426950408889634f) + 1e-6f) + fmaxf(x, 0.f); }
__device__ __forceinline__ unsigned phi2(float x0, float x1) { return pk2(phi1(x0), phi1(x1)); }
constexpr unsigned OOB = 0x40000000u;
#define CBAR() asm volatile("" ::: "memory")
#define VMDRAIN() asm volatile("s_waitcnt vmcnt(0)" ::: "memory")
#define PIN(x) asm volatile("" : "+v"(x))
#define VMKNOWN() __builtin_amdgcn_s_waitcnt(0x0F70)
#define LBAR() do { asm volatile("s_waitcnt lgkmcnt(0)" ::: "memory"); __builtin_amdgcn_s_barrier(); asm volatile("" ::: "memory"); } while (0)
typedef __amdgpu_buffer_rsrc_t rsrc_t;
__device__ __forceinline__ u32x4 ld16c(rsrc_t r, unsigned off) { return __builtin_amdgcn_raw_buffer_load_b128(r, off, 0, 16); }
__device__ __forceinline__ u32x2 ld8c(rsrc_t r, unsigned off) { return __builtin_amdgcn_raw_buffer_load_b64(r, off, 0, 16); }
__device__ __forceinline__ unsigned ld4c(rsrc_t r, unsigned off) { return __builtin_amdgcn_raw_buffer_load_b32(r, off, 0, 16); }
__device__ __forceinline__ void st16c(rsrc_t r, unsigned off, u32x4 v) { __builtin_amdgcn_raw_buffer_store_b128(v, r, off, 0, 16); }
__device__ __forceinline__ void st8c(rsrc_t r, unsigned off, u32x2 v) { __builtin_amdgcn_raw_buffer_store_b64(v, r, off, 0, 16); }
__device__ __forceinline__ void st4c(rsrc_t r, unsigned off, unsigned v) { __builtin_amdgcn_raw_buffer_store_b32(v, r, off, 0, 16); }
template <int CTRL> __device__ __forceinline__ unsigned dppu(unsigned x) { return (unsigned)__builtin_amdgcn_mov_dpp((int)x, CTRL, 0xf, 0xf, true); }
template <int CTRL> __device__ __forceinline__ float dppf(float x) { return __uint_as_float(dppu<CTRL>(__float_as_uint(x))); }
__device__ __forceinline__ float xg_max(float x) {
    auto s = __builtin_amdgcn_permlane16_swap(__float_as_uint(x), __float_as_uint(x), false, false); x = fmaxf(__uint_as_float(s[0]), __uint_as_float(s[1]));
    auto t = __builtin_amdgcn_permlane32_swap(__float_as_uint(x), __float_as_uint(x), false, false); return fmaxf(__uint_as_float(t[0]), __uint_as_float(t[1])); }
__device__ __forceinline__ float xg_sum(float x) {
    auto s = __builtin_amdgcn_permlane16_swap(__float_as_uint(x), __float_as_uint(x), false, false); x = __uint_as_float(s[0]) + __uint_as_float(s[1]);
    auto t = __builtin_amdgcn_permlane32_swap(__float_as_uint(x), __float_as_uint(x), false, false); return __uint_as_float(t[0]) + __uint_as_float(t[1]); }

typedef __attribute__((ext_vector_type(8))) unsigned u32x8;
typedef __attribute__((ext_vector_type(8))) __bf16 bfv8;
typedef __attribute__((ext_vector_type(16))) __bf16 bfv16;
typedef short v4i16_t __attribute__((ext_vector_type(4)));
typedef __attribute__((address_space(3))) volatile unsigned lds_u32;
#define SBAR() __builtin_amdgcn_sched_barrier(0)
__device__ __forceinline__ int lane_id() { int l; asm volatile("v_mbcnt_lo_u32_b32 %0, -1, 0\n\tv_mbcnt_hi_u32_b32 %0, -1, %0" : "=v"(l)); return l; }
#define Z4 ((f32x4){0.f, 0.f, 0.f, 0.f})
__device__ __forceinline__ f32x4 smf(u32x4 A, u32x8 B, f32x4 c, unsigned idx) { return __builtin_amdgcn_smfmac_f32_16x16x64_bf16(__builtin_bit_cast(bfv8, A), __builtin_bit_cast(bfv16, B), c, (int)idx, 0, 0); }
__device__ __forceinline__ f32x4 dmf(u32x4 A, u32x4 B, f32x4 c) { return __builtin_amdgcn_mfma_f32_16x16x32_bf16(__builtin_bit_cast(bfv8, A), __builtin_bit_cast(bfv8, B), c, 0, 0, 0); }
__device__ __forceinline__ u32x4 lo4(u32x8 v) { return (u32x4){v[0], v[1], v[2], v[3]}; }
__device__ __forceinline__ u32x4 hi4(u32x8 v) { return (u32x4){v[4], v[5], v[6], v[7]}; }
__device__ __forceinline__ u32x8 cat8(u32x4 a, u32x4 b) { return (u32x8){a.x, a.y, a.z, a.w, b.x, b.y, b.z, b.w}; }
__device__ __forceinline__ u32x4 lds16(const unsigned char* p) { return *reinterpret_cast<const u32x4*>(p); }
__device__ __forceinline__ u32x4 lds16a(int ad) { return *(const __attribute__((address_space(3))) u32x4*)(uintptr_t)(unsigned)ad; }
__device__ __forceinline__ unsigned lds4a(int ad) { return *(const __attribute__((address_space(3))) unsigned*)(uintptr_t)(unsigned)ad; }
__device__ __forceinline__ f32x2 lds8a(int ad) { return *(const __attribute__((address_space(3))) f32x2*)(uintptr_t)(unsigned)ad; }
__device__ __forceinline__ void sts8a(int ad, f32x2 v) { *(__attribute__((address_space(3))) f32x2*)(uintptr_t)(unsigned)ad = v; }
__device__ __forceinline__ void sts4a(int ad, unsigned v) { *(__attribute__((address_space(3))) unsigned*)(uintptr_t)(unsigned)ad = v; }
__device__ __forceinline__ void sts16a(int ad, u32x4 v) { *(__attribute__((address_space(3))) u32x4*)(uintptr_t)(unsigned)ad = v; }
__device__ __forceinline__ u32x2 ldstr(const unsigned char* p) { return __builtin_bit_cast(u32x2, __builtin_amdgcn_ds_read_tr16_b64_v4i16((__attribute__((address_space(3))) v4i16_t*)p)); }
__device__ __forceinline__ unsigned umax4(u32x4 v) { return max(max(v.x, v.y), max(v.z, v.w)); }
#define DNOP "0"
__device__ __forceinline__ void dma16(rsrc_t r, unsigned voff, unsigned soff, unsigned ldsd) {
    asm volatile("s_mov_b32 m0, %0\n\ts_nop " DNOP "\n\tbuffer_load_dwordx4 %1, %2, %3 offen lds" :: "s"(ldsd), "v"(voff), "s"(r), "s"(soff) : "m0", "memory");
}
__device__ __forceinline__ void dma16c(rsrc_t r, unsigned voff, unsigned soff, unsigned ldsd) {
    asm volatile("s_mov_b32 m0, %0\n\ts_nop " DNOP "\n\tbuffer_load_dwordx4 %1, %2, %3 offen sc1 lds" :: "s"(ldsd), "v"(voff), "s"(r), "s"(soff) : "m0", "memory");
}
__device__ __forceinline__ void dma4c(rsrc_t r, unsigned voff, unsigned soff, unsigned ldsd) {
    asm volatile("s_mov_b32 m0, %0\n\ts_nop " DNOP "\n\tbuffer_load_dword %1, %2, %3 offen sc1 lds" :: "s"(ldsd), "v"(voff), "s"(r), "s"(soff) : "m0", "memory");
}
template <int N> __device__ __forceinline__ void dma16cn(rsrc_t r, unsigned voff, unsigned soff, unsigned ldsd) {
    if (N >= 4) asm volatile("s_mov_b32 m0, %0\n\ts_nop " DNOP "\n\tbuffer_load_dwordx4 %1, %2, %3 offen sc1 lds\n\tbuffer_load_dwordx4 %1, %2, %3 offen offset:1024 sc1 lds\n\t"
        "buffer_load_dwordx4 %1, %2, %3 offen offset:2048 sc1 lds\n\tbuffer_load_dwordx4 %1, %2, %3 offen offset:3072 sc1 lds" :: "s"(ldsd), "v"(voff), "s"(r), "s"(soff) : "m0", "memory");
    else if (N == 3) asm volatile("s_mov_b32 m0, %0\n\ts_nop " DNOP "\n\tbuffer_load_dwordx4 %1, %2, %3 offen sc1 lds\n\tbuffer_load_dwordx4 %1, %2, %3 offen offset:1024 sc1 lds\n\t"
        "buffer_load_dwordx4 %1, %2, %3 offen offset:2048 sc1 lds" :: "s"(ldsd), "v"(voff), "s"(r), "s"(soff) : "m0", "memory");
    else if (N >= 2) asm volatile("s_mov_b32 m0, %0\n\ts_nop " DNOP "\n\tbuffer_load_dwordx4 %1, %2, %3 offen sc1 lds\n\tbuffer_load_dwordx4 %1, %2, %3 offen offset:1024 sc1 lds"
        :: "s"(ldsd), "v"(voff), "s"(r), "s"(soff) : "m0", "memory");
    else dma16c(r, voff, soff, ldsd);
    constexpr int D = N >= 4 ? 4 : N;
    if constexpr (N > D) dma16cn<N - D>(r, voff, soff + D * 1024, ldsd + D * 1024);
}
__device__ __forceinline__ void st16s(u32x4 v, rsrc_t r, unsigned voff, unsigned soff) { __builtin_amdgcn_raw_buffer_store_b128(v, r, voff, soff, 16); asm volatile("s_nop 1" :: "v"(v) : "memory"); }
#define VMW(n) asm volatile("s_waitcnt vmcnt(%0)" :: "i"(n) : "memory")

struct MegaArgs { const bf16_t* wq; const bf16_t* wk; const bf16_t* wv; const bf16_t* wo; const bf16_t* wqkv; const bf16_t* wao; const bf16_t* mem0; const bf16_t* x0; bf16_t* out; unsigned char* ws; };
typedef const __attribute__((address_space(4))) MegaArgs KA;
__device__ __forceinline__ KA& args() { KA* p = (KA*)__builtin_amdgcn_kernarg_segment_ptr(); asm volatile("" : "+s"(p)); return *p; }
#define STAMP(idx) do { } while (0)
#define KATTR
#define ATI() do { } while (0)
#define AT(n) do { } while (0)
#define ATV do { } while (0)
#define ATD(role, on) do { } while (0)
#define SPINB 17
constexpr unsigned SPIN_CAP = 2u << SPINB;
constexpr unsigned SPIN_CAP_W = 1u << SPINB;
constexpr int MG_LDS = 163840;

#define PARAM(n, v) constexpr int n = v
#define TT_M1  14
#define TT_M2  13
#define TT_V   100
#define TT_V1 TT_V
#define TT_DEC 2
#define TT_BUMP 2
#define TT_DCY 3
#define TT_XS 5
#define TT_XD 1
#define TT_RT 34
#define TT_UM -2
#define TT_TW 8
#define TT_MD 60
#define TT_BD 12
#define TT_KD 95
#define TT_QD 5
#define TT_QN -35
#define TT_AD 250
#define XQD 0
#define SPRIO 2
#define PFD 6
#define DPS 5
#define PF0 2
#define KIM 6
#define XLD 9
#define NEAR 60
#define ROT 1
#define TF 60
#define RTS 12
#define RTL 8
#define R0S 4
#define VPRIO 0
#define XPD 4
#define TT_FP 13
#define TT_OD 30
#define TT_VD 3
#define YP1 0
#define QA 3
#define OIM 63
#define TT_UX -2
#define RLATE 0
#define PLATE 0
#define SLATE1 0
#define SLATE2 0
#define TT_TL 250
#define TT_AN 110
#define RROT 0
#define AROT 0
#define RFD 9
#define SFD 8
#define STK 0
#define VFD 7
#define VTK 2
#define VH 0
#define OSL 0
#define YP2 1
#define YA2 1
#define YD 0
#define YNB 6
#define TT_UP 2
#define TT_DN 3
#define XP2 2
#define YP3 0
#define TT_X1 10
#define VNH 0
#define ASL 10
#define RSL 1
#define TT_S1 155
#define TT_S2 40
#define OLAG 3
constexpr int TT_E1 = 132, TT_E2 = 48;
__device__ __forceinline__ unsigned rt_now() { return (unsigned)__builtin_amdgcn_s_memrealtime(); }
__device__ __forceinline__ void rt_wait(unsigned t) {
    unsigned d;
    asm volatile("s_branch 2f\n"
        "1:\n\ts_cmp_lt_u32 %0, 12\n\ts_cbranch_scc1 2f\n\ts_sleep 2\n\ts_cmp_lt_u32 %0, 30\n\ts_cbranch_scc1 2f\n\ts_sleep 5\n"
        "2:\n\ts_memrealtime vcc\n\ts_waitcnt lgkmcnt(0)\n\ts_sub_u32 %0, %1, vcc_lo\n\ts_cmp_lt_u32 %0, 20000\n\ts_cbranch_scc1 1b" : "=&s"(d) : "s"(t - 1u) : "scc", "vcc", "memory");
}
#define FINE 1
#define FC 100
template <int K> __device__ __forceinline__ void rt_fine(unsigned t) {
    if (!FINE) { rt_wait(t); return; }
    asm volatile("s_branch 2f\n"
        "1:\n\ts_cmp_lt_u32 s70, 12\n\ts_cbranch_scc1 2f\n\ts_sleep 2\n\ts_cmp_lt_u32 s70, 30\n\ts_cbranch_scc1 2f\n\ts_sleep 5\n"
        "2:\n\ts_memrealtime vcc\n\ts_waitcnt lgkmcnt(0)\n\ts_sub_u32 s70, %0, vcc_lo\n\ts_cmp_lt_u32 s70, 20000\n\ts_cbranch_scc1 1b\n\t"
        "s_cmp_lt_u32 s70, -8\n\ts_cbranch_scc1 5f\n\t"
        "s_memrealtime s[70:71]\n\ts_nop 4\n\ts_memrealtime s[72:73]\n\ts_nop 4\n\ts_memrealtime s[74:75]\n\ts_nop 4\n\ts_memrealtime s[76:77]\n\ts_nop 4\n\ts_memrealtime vcc\n\ts_waitcnt lgkmcnt(0)\n\t"
        "s_add_u32 s72, s72, s74\n\ts_add_u32 s76, s76, vcc_lo\n\ts_add_u32 s72, s72, s76\n\ts_lshl_b32 s71, s70, 2\n\ts_sub_u32 s72, s72, s71\n\ts_mul_i32 s72, s72, 6\n\t"
        "s_sub_u32 s70, %0, s70\n\ts_mul_i32 s70, s70, %1\n\ts_sub_i32 s70, s70, s72\n\ts_add_i32 s70, s70, %2\n\ts_max_i32 s70, s70, 0\n\ts_min_i32 s70, s70, 255\n\ts_lshr_b32 s70, s70, 3\n\ts_lshl_b32 s70, s70, 2\n\t"
        "s_getpc_b64 vcc\n3:\n\ts_sub_u32 s70, 4f-3b, s70\n\ts_add_u32 vcc_lo, vcc_lo, s70\n\ts_addc_u32 vcc_hi, vcc_hi, 0\n\ts_setpc_b64 vcc\n"
        "5:\n\ts_add_u32 s70, s70, 18\n\ts_cmp_gt_u32 s70, 18\n\ts_cbranch_scc1 4f\n\ts_memrealtime vcc\n\ts_waitcnt lgkmcnt(0)\n\ts_sub_u32 s70, %0, vcc_lo\n\ts_branch 5b\n"
        "\t.rept 32\n\ts_nop 1\n\t.endr\n4:"
        :: "s"(t - 19u), "i"(K), "i"(15 * K - FC) : "scc", "vcc", "s70", "s71", "s72", "s73", "s74", "s75", "s76", "s77", "memory");
}
template <int N> __device__ __forceinline__ void nops() { if (N > 0) asm volatile(".rept %0\n\ts_nop 1\n\t.endr" :: "i"(N > 0 ? N : 0) : "memory"); }
__device__ __forceinline__ const void* psel(const void* p0, const void* p1, int l) { return (const void*)((uintptr_t)p0 + (((uintptr_t)p1 - (uintptr_t)p0) & (uintptr_t)(-(long)l))); }
#define MKR(p, n) __builtin_amdgcn_make_buffer_rsrc((void*)(p), 0, (int)(n), 0x00020000)
__device__ __forceinline__ unsigned my_side() { return (__builtin_amdgcn_s_getreg((3 << 11) | 20) & 15u) >= 4u ? 1u : 0u; }
__device__ __forceinline__ void tt_step(unsigned tw, int s, unsigned t_poll, unsigned& tb, unsigned& cC1, unsigned& cP) {
    const unsigned tgt0 = tb + cP, pc2 = cP - cC1;
    int d = (int)(tw + pc2 - tgt0);
    d = max(d, (int)dppu<0x128>((unsigned)d)); d = max(d, (int)dppu<0x124>((unsigned)d)); d = max(d, (int)dppu<0x122>((unsigned)d)); d = max(d, (int)dppu<0x121>((unsigned)d));
    d = __builtin_amdgcn_readlane(d, 0);
    tb = s == 0 ? t_poll : ((s == 1 || d > 0) ? tgt0 + (unsigned)d - pc2 + (s == 1 ? TT_V1 : TT_V) : tgt0);
    cC1 = __builtin_amdgcn_readlane(tw, 16); cP = __builtin_amdgcn_readlane(tw, 17);
}
__device__ __forceinline__ void set_err(unsigned char* hop) { unsigned z, v; asm volatile("v_mov_b32 %0, 0\n\tv_mov_b32 %1, 1\n\ts_nop 4\n\tglobal_store_dword %0, %1, %2\n\ts_nop 1" : "=&v"(z), "=&v"(v) : "s"(hop + F_ERR) : "memory"); }
__device__ __forceinline__ void wait_set(rsrc_t rfl, unsigned off, unsigned char* hop, int slp) {
    for (unsigned sp = 0;; ++sp) { CBAR();
        const unsigned v = ld4c(rfl, off);
        if (__all(v != 0u)) break;
        for (int q = 0; q < slp; ++q) __builtin_amdgcn_s_sleep(8);
        if (sp > SPIN_CAP_W) { set_err(hop); break; } }
}
__device__ __forceinline__ void boot_wait(KA& a) {
    const rsrc_t r = MKR((a.ws + WS_HOP) - 2048, 2048); const unsigned off = 128 + (lane_id() & 7) * 128;
    for (unsigned sp = 0; sp < SPIN_CAP_W; ++sp) { CBAR();
        const u32x2 t = ld8c(r, 0), v = ld8c(r, off);
        if (__all(v.x == t.x + 1u && v.y == ~t.y)) break;
        __builtin_amdgcn_s_sleep(16); }
}
__device__ __forceinline__ void init_wait(KA& a) { boot_wait(a); wait_set(MKR(a.ws + WS_HOP, F_ERR), F_IN + (blockIdx.x & 7) * FLP, a.ws + WS_HOP, 3); }
__device__ __forceinline__ void pol_vote(KA& a, const int v) {
    const rsrc_t r = MKR(al2m((a.ws + WS_FR)), 1 << 22);
    const unsigned lane = lane_id(), b0 = (48u + v + (v & 1) * 128u) << 14;
    if (lane < 16) st4c(r, b0 + (lane & 1) * 8192 + (lane >> 1) * 896, 0u);
    VMDRAIN();
    unsigned best[2] = {~0u, ~0u}, acc = 0;
#pragma unroll 1
    for (int q = 0; q < 12; ++q) {
        const unsigned long long t0 = __builtin_amdgcn_s_memtime();
        unsigned x = ld4c(r, b0 + ((q ^ (v >> 1)) & 1) * 8192 + (q >> 1) * 896 + acc);
        asm volatile("s_waitcnt vmcnt(0)" : "+v"(x) :: "memory");
        const unsigned dt = (unsigned)(__builtin_amdgcn_s_memtime() - t0);
        acc += x; if ((q ^ (v >> 1)) & 1) best[1] = min(best[1], dt); else best[0] = min(best[0], dt);
    }
    const int df = max(-300, min(300, (int)best[0] - (int)best[1]));
    if (lane == 0) st4c(MKR((a.ws + WS_HOP), F_ERR), F_POL + v * 4, (unsigned)((my_side() ? -df : df) | 1));
}
__device__ __forceinline__ unsigned page_pol(KA& a) {
    const rsrc_t rfl = MKR((a.ws + WS_HOP), F_ERR); const unsigned off = F_POL + lane_id() * 4;
    init_wait(a); wait_set(rfl, off, (a.ws + WS_HOP), 2);
    const unsigned long long b = __ballot((int)ld4c(rfl, off) > 0);
    return (__popcll(b & 0x5555555555555555ull) > 16 ? 1u : 0u) | (__popcll(b & 0xAAAAAAAAAAAAAAAAull) > 16 ? 2u : 0u);
}
__device__ __forceinline__ unsigned wait_timed(rsrc_t rfl, unsigned off, unsigned char* hop, unsigned& tnext, int nper, int dly, bool fast) {
    if (!fast && (int)(tnext - rt_now()) < 4500) rt_wait(tnext);
    for (unsigned sp = 0;; ++sp) { CBAR();
        const u32x4 v = ld16c(rfl, off);
        if (__all(v.x != 0u)) { tnext = __builtin_amdgcn_readfirstlane(v.w) + (unsigned)nper * __builtin_amdgcn_readfirstlane(v.x) + (unsigned)dly; return __builtin_amdgcn_readfirstlane(v.x); }
        if (fast) __builtin_amdgcn_s_sleep(2); else __builtin_amdgcn_s_sleep(40);
        if (sp > SPIN_CAP_W) { set_err(hop); return 0u; } }
}
__device__ __forceinline__ unsigned blk_of(int j, unsigned nr) { return (j < 8 ? j & 3 : 4) + 5u * (((j < 8 ? j >> 2 : j) & 1u) ^ nr); }
__device__ __forceinline__ unsigned blk_off(int j, unsigned nr) { const unsigned b = blk_of(j, nr); return b * 1024 + (b < 8 ? 0 : 8192); }
template <int NRND, class F> __device__ __forceinline__ void load_w(rsrc_t r0, rsrc_t r1, unsigned nr, unsigned col0, unsigned char* lds, unsigned ldsb, int wave, int lane, F&& put) {
    unsigned vo[3];
#pragma unroll
    for (int i = 0; i < 3; ++i) { const int c = 64 * i + lane, row = c / 24, ch = c - row * 24; vo[i] = (unsigned)((8 * wave + row) * HE_ + col0 + ch * 8) * 2; }
    auto issue = [&](int rd) {
#pragma unroll
        for (int i = 0; i < 3; ++i) { dma16(r0, vo[i], blk_of(rd, nr) * (64 * HE_ * 2), ldsb + (rd % 3) * 49152 + wave * 3072 + i * 1024);
            dma16(r1, vo[i], blk_of(rd, nr) * (64 * HE_ * 2), ldsb + (rd % 3) * 49152 + 24576 + wave * 3072 + i * 1024); }
    };
    issue(0); issue(1); issue(2);
    const unsigned char* rp = lds + (wave >> 2) * 24576 + (8 * (lane >> 4) + ((lane & 15) >> 2)) * 384 + (wave & 3) * 96 + (lane & 3) * 8;
#pragma unroll
    for (int rd = 0; rd < NRND; ++rd) {
        if (rd + 2 < NRND) VMW(12); else if (rd + 1 < NRND) VMW(6); else VMW(0);
        LBAR();
#pragma unroll
        for (int u = 0; u < 3; ++u) { const unsigned char* p = rp + (rd % 3) * 49152 + u * 32;
            const u32x2 a0 = ldstr(p), a1 = ldstr(p + 4 * 384), b0 = ldstr(p + 32 * 384), b1 = ldstr(p + 36 * 384);
            put(rd, u, (u32x8){a0.x, a0.y, a1.x, a1.y, b0.x, b0.y, b1.x, b1.y}); }
        LBAR();
        if (rd + 3 < NRND) issue(rd + 3);
    }
}

__device__ __forceinline__ unsigned wsw(unsigned row) { return ((row >> 1) & 1u) | ((((row >> 2) ^ (row >> 3)) & 1u) << 1); }
template <int NB, int RB, int NRND, int LB, int R0, class G, class F, class H> __device__ __forceinline__ void load_b(unsigned ldsb, int wave, G&& src, F&& take, H&& back) {
    auto issue = [&](int rd) {
#pragma unroll
        for (int j = 0; j < NB; ++j) src(rd, j, ldsb + LB + ((rd + R0) % RB) * (NB * 8192) + j * 8192 + wave * 1024); };
#pragma unroll
    for (int rd = 0; rd < RB; ++rd) issue(rd);
#pragma unroll
    for (int rd = 0; rd < NRND; ++rd) {
        VMW((NRND - 1 - rd < RB - 1 ? NRND - 1 - rd : RB - 1) * NB);
        LBAR();
        take(rd, LB + ((rd + R0) % RB) * (NB * 8192));
        LBAR();
        back(rd);
        if (rd + RB < NRND) issue(rd + RB);
    }
}
__device__ __forceinline__ u32x4 ld_frag(const unsigned char* blk, unsigned r0, unsigned dr, unsigned win, unsigned l15) {
    const unsigned ra = r0 + (l15 >> 2), rb = ra + dr;
    const u32x2 x = ldstr(blk + ra * 128 + ((win ^ wsw(ra)) << 5) + (l15 & 3) * 8), y = ldstr(blk + rb * 128 + ((win ^ wsw(rb)) << 5) + (l15 & 3) * 8);
    return (u32x4){x.x, x.y, y.x, y.y};
}
__device__ __forceinline__ void ln_consts(float s1, float s2, float& mb, float& inv) {
    const float mean = s1 * (1.0f / D_), var = fmaxf(__builtin_fmaf(-mean, mean, s2 * (1.0f / D_)), 0.f);
    mb = rbf(mean); inv = __builtin_amdgcn_rcpf(rbf(__builtin_amdgcn_sqrtf(rbf(rbf(var) + 1e-5f))));
}
__device__ __forceinline__ float row_sums(u32x2 o) {
    const float y0 = lo2f(o.x), y1 = hi2f(o.x), y2 = lo2f(o.y), y3 = hi2f(o.y);
    const auto e = __builtin_amdgcn_permlane16_swap(__float_as_uint((y0 + y1) + (y2 + y3)), __float_as_uint(__builtin_fmaf(y0, y0, y1 * y1) + __builtin_fmaf(y2, y2, y3 * y3)), false, false);
    const float x = __uint_as_float(e[0]) + __uint_as_float(e[1]);
    const auto f = __builtin_amdgcn_permlane32_swap(__float_as_uint(x), __float_as_uint(x), false, false);
    return __uint_as_float(f[0]) + __uint_as_float(f[1]);
}
__device__ __forceinline__ unsigned m_idx(int ln) { return (ln & 2) ? 0xEEEEu : 0x4444u; }
__device__ __forceinline__ f32x4 zero4() { f32x2 a, b; asm volatile("v_mov_b64 %0, 0\n\tv_mov_b64 %1, 0\n\ts_nop 0" : "=v"(a), "=v"(b)); return (f32x4){a[0], a[1], b[0], b[1]}; }
__device__ __forceinline__ u32x4 ones4() { u32x2 a, b; asm volatile("v_mov_b64 %0, -1\n\tv_mov_b64 %1, -1" : "=v"(a), "=v"(b)); return (u32x4){a[0], a[1], b[0], b[1]}; }
__device__ __forceinline__ f32x2 fold2(f32x4 v) { return (f32x2){v[0], v[1]} + (f32x2){v[2], v[3]}; }
__device__ __forceinline__ unsigned fold_pk(f32x4 v) { float x = v[0] + v[2]; PIN(x); return pk2(x, v[1] + v[3]); }
__device__ __forceinline__ float max3f(float x, float y, float z) { return __builtin_fmaxf(__builtin_fmaxf(x, y), z); }
__device__ __forceinline__ float adds(float x, float y) { float r; asm("v_add_f32 %0, %1, %2" : "=v"(r) : "v"(x), "v"(y)); return r; }
__device__ __forceinline__ float muls(float x, float y) { float r; asm("v_mul_f32 %0, %1, %2" : "=v"(r) : "v"(x), "v"(y)); return r; }
#define LDSB() __builtin_amdgcn_readfirstlane((unsigned)(uintptr_t)(__attribute__((address_space(3))) unsigned char*)lds)
__device__ __forceinline__ int xp_of(int h) { return 3 * (h >> 1) + (h & 1); }
__device__ __forceinline__ unsigned um_base(unsigned long long sd, unsigned pol, unsigned c) {
    const unsigned s = (unsigned)(sd >> c) & 1u; const unsigned long long same = (s ? sd : ~sd) & ((1ull << c) - 1ull);
    const unsigned o = FR_UM + ((unsigned)__popcll(same) << 16);
    return o + (((s ^ (pol >> (o >> 21))) & 1u) << 13);
}
__device__ __forceinline__ unsigned long long um_sides(KA& a) {
    const rsrc_t rfl = MKR((a.ws + WS_HOP), F_ERR); const unsigned l = lane_id(), off = F_SD + (l < 40 ? l : 0) * 4;
    wait_set(rfl, off, (a.ws + WS_HOP), 2);
    return __ballot(ld4c(rfl, off) == 2u);
}

#define KME 1
#define SPS 1
#define KXW 2
#define RAP 0
#define SW2 3
#define KPR 0
template <int W> __device__ __forceinline__ void s_run(KA& a, unsigned char* lds, const int h, const u32x8 (&wr)[8][3]) {
    constexpr int wave = W; const int lane = lane_id();
    const unsigned ldsb = LDSB();
    const rsrc_t rhx = MKR(al2m((a.ws + WS_FR)), FR_USED);
    constexpr int L_MEM = 0, L_Q = 10240, L_K = 13312, L_KX = 16384, L_TIM = 28672, L_W = 32768;
    lds_u32* tim = (lds_u32*)(lds + L_TIM + 256);
    if (W == 0) { tim[0] = 0; tim[1] = 0; tim[2] = 0; tim[3] = 0; tim[4] = 0; tim[5] = 0; }
    const unsigned pol = page_pol(a), sx = ((my_side() ^ pol) & 1u) << 13;
    unsigned po = (2 + (lane >> 5)) * 16384 + h * 256 + ((lane >> 4) & 1) * 128 + (lane & 8) * 4 + (lane & 2) * 8 + (lane & 4) * 2 + (lane & 1) * 8192;
    unsigned pm = 4 * 16384 + (lane >> 5) * 2048 + (h >> 4) * 1024 + ((h >> 1) & 3) * 256 + ((lane >> 3) & 3) * 64 + (h & 8) * 4 + (lane & 4) * 4 + (h & 1) * 8 + (lane & 2) * 2 + (lane & 1) * 8192;
    const int xj = xp_of(h);
    unsigned idx = m_idx(lane);
    unsigned pn = ((lane >> 3) & 1) != (W ? 1 : 0) ? pm | OOB : pm;
    PIN(idx); if (W < 2 || W == SW2) PIN(po); if (W < 2 || W == SW2) PIN(pn);
#pragma unroll 1
    for (int layer = 0; layer < 2; ++layer) {
    unsigned char* hop = (a.ws + WS_HOP) + layer * HOP_BYTES;
    const rsrc_t rfl = MKR(hop, F_ERR), rst = MKR((a.ws + WS_ST), ST_BYTES);
    const unsigned sb0 = (unsigned)layer * (20u << 14);
    auto kx_fetch = [&](int t, bool wait) {
        int ln = lane_id();
        if (wait) wait_set(rfl, F_XP + (t * NXP_ + xj) * FLP, hop, 0);
        dma16cn<12>(rst, ln * 16, ST_KX + ((t & (KXR - 1)) * 32 + h) * 12288, ldsb + L_KX);
    };
    if (W == 3 && lane_id() == 0) STAMP(100 + h);
    bool early = false;
    if (W == 2) { early = __all(ld4c(rfl, F_XP + xj * FLP) != 0u); if (early) kx_fetch(0, false); }
    VMKNOWN();
    unsigned tb = 0, cC1 = 0, cP = 0, kdur = 0, krt = 0;
#pragma unroll 1
    for (int s = 0; s < NSEG_; ++s) {
        int ln = lane_id();
        ATV;
        const unsigned sb = sb0 + (unsigned)(s & 3) * (5u << 14);
        unsigned qo = (W < 4 ? L_Q + ((ln >> 3) & 1) * 256 + (ln >> 4) * 64 + ((ln >> 1) & 1) * 32 + (ln & 1) * 16 + ((ln >> 2) & 1) * 8
                             : L_K + ((ln & 15) >> 2) * 128 + (((ln >> 4) * 2 + (ln & 1) + (ln & 4)) & 7) * 16 + (ln & 2) * 2);
        unsigned sel = (ln & 1) ? 0x03020706u : 0x05040100u, w2o = L_W + 65536 + wave * 2048 + ln * 16;
        PIN(qo); PIN(sel); PIN(w2o);
        unsigned char* qp = lds + qo;
        u32x8 wl[3];
        const unsigned char* wp = lds + L_W + wave * 2048 + ln * 16;
        auto w_lds = [&](int f) { const unsigned char* p = f < 4 ? wp + f * 16384 : lds + w2o + (f - 4) * 16384; return cat8(lds16(p), lds16(p + 1024)); };
#pragma unroll
        for (int u = 0; u < 3; ++u) wl[u] = w_lds(1 + u);
        f32x4 acc[3] = {zero4(), zero4(), zero4()};
        unsigned tp = s == 0 ? rt_now() : tb + cP;
#define G3(A, Wt) do { acc[0] = smf(A, Wt[0], acc[0], idx); acc[1] = smf(A, Wt[1], acc[1], idx); acc[2] = smf(A, Wt[2], acc[2], idx); PIN(acc[0]); PIN(acc[1]); PIN(acc[2]); SBAR(); } while (0)
#define S1(u, A, B) do { acc[u] = smf(A, B, acc[u], idx); PIN(acc[u]); SBAR(); } while (0)
        u32x4 m0, n0, n1; unsigned nw = 0;
        unsigned char* mp = lds + L_MEM + ln * 16;
        constexpr int B0 = W == 4 ? 8 : (W == 5 ? 1 : W), B1 = W == 3 ? 9 : 2 * W - 8; constexpr bool ER = W != 1 && W < 6, LT = W == 2 || W == 3 || W >= 6;
        unsigned mo = sx + ln * 16, to = sx + M_STAMP + min(ln, 17) * 64 + (s >> 2) * 4, s0 = sb + blk_off(B0, h >> 4), s1 = sb + blk_off(B1, h >> 4), s2 = sb + blk_off(B1 + 1, h >> 4);
        auto late = [&]() { CBAR();
            if (W == 2) nw = __builtin_amdgcn_raw_buffer_load_b32(rhx, to, sb, 16); else n0 = __builtin_amdgcn_raw_buffer_load_b128(rhx, mo, s1, 16);
            if (W >= 6) n1 = __builtin_amdgcn_raw_buffer_load_b128(rhx, mo, s2, 16);
            CBAR(); SBAR(); };
        auto stuck = [&](unsigned sp) { if ((sp & 63u) == 63u && (sp > SPIN_CAP || tim[0])) { tim[0] = 1; return true; } if (sp > RTS) __builtin_amdgcn_s_sleep(RTL); return false; };
        if (W != 1) {
            PIN(wl[0]); PIN(wl[1]); PIN(wl[2]); PIN(mo); if (W == 2) PIN(to); if (ER) asm volatile("" : "+s"(s0)); if (LT && W != 2) asm volatile("" : "+s"(s1)); if (W >= 6) asm volatile("" : "+s"(s2));
            rt_fine<23>(ER ? tp : tp + SFD);
            if (ER) { CBAR(); m0 = __builtin_amdgcn_raw_buffer_load_b128(rhx, mo, s0, 16); CBAR(); SBAR(); if (LT) nops<(SFD * 23 - 12) / 8>(); }
            if (LT) late();
            if (ER) { *reinterpret_cast<u32x4*>(mp + B0 * 1024) = m0;
                if (__builtin_expect(!__all(umax4(m0) != 0xffffffffu), 0)) {
                    tim[2] = (unsigned)s + 1u;
                    for (unsigned sp = 0;; ++sp) { CBAR(); m0 = __builtin_amdgcn_raw_buffer_load_b128(rhx, mo, s0, 16); if (__all(umax4(m0) != 0xffffffffu) || stuck(sp)) break; }
                    *reinterpret_cast<u32x4*>(mp + B0 * 1024) = m0; } }
        }
        if (W == 0 || W == SW2) __builtin_amdgcn_s_setprio(SPRIO);
        AT(2);
        LBAR();
        AT(3);
        {
            constexpr int F = (ER && W != 4) ? B0 : 0, R1 = F == 0 ? 1 : 0, R2 = F <= 1 ? 2 : 1, R3 = F <= 2 ? 3 : 2, R6 = 6;
            if (!ER) { m0 = lds16(mp); SBAR(); }
            u32x4 A0 = lds16(mp + (W == 4 ? 0 : 8) * 1024); SBAR(); u32x4 A1 = lds16(mp + R1 * 1024); SBAR(); u32x4 A2 = lds16(mp + R2 * 1024); SBAR(); u32x4 A3 = lds16(mp + R3 * 1024); SBAR();
            auto take = [&]() { if (LT) {
                auto put_ = [&]() { if (W == 2) *reinterpret_cast<unsigned*>(lds + L_TIM + ln * 4) = nw; else *reinterpret_cast<u32x4*>(mp + B1 * 1024) = n0; if (W >= 6) *reinterpret_cast<u32x4*>(mp + B1 * 1024 + 1024) = n1;
                    return __all((W == 2 ? nw : max(umax4(n0), W >= 6 ? umax4(n1) : 0u)) != 0xffffffffu); };
                if (__builtin_expect(!put_(), 0)) { tim[2] = (unsigned)s + 1u; for (unsigned sp = 0;; ++sp) { late(); if (put_() || stuck(sp)) break; } }
                SBAR(); } };
            if (W != 4) G3(m0, wr[F]);
            if (STK == 0) take();
            { const u32x4 a8 = W == 4 ? m0 : A0;
              S1(0, a8, wl[0]); wl[0] = w_lds(4); SBAR(); S1(1, a8, wl[1]); wl[1] = w_lds(5); SBAR(); S1(2, a8, wl[2]); wl[2] = w_lds(6); SBAR(); }
            if (W == 4) G3(A0, wr[F]);
            if (STK == 1) take();
            G3(A1, wr[R1]);
            if (STK == 2) take();
            G3(A2, wr[R2]);
            if (LT) asm volatile("s_waitcnt lgkmcnt(0)" ::: "memory");
            CBAR(); __builtin_amdgcn_s_barrier(); CBAR(); SBAR();
            if (W == 1 && RAP == 2)
                {
                    const unsigned sr = sb0 + (unsigned)((s + 3) & 3) * (5u << 14);
                    const u32x4 on = ones4(); PIN(pn);
                    __builtin_amdgcn_raw_buffer_store_b64((u32x2){on.x, on.y}, rhx, po, sr, 16); __builtin_amdgcn_raw_buffer_store_b64((u32x2){on.x, on.y}, rhx, po, sr + 64, 16);
                    __builtin_amdgcn_raw_buffer_store_b32(on.x, rhx, pn & ~OOB, sr, 16);
                }
            SBAR();
            if (W >= 4 && KPR) __builtin_amdgcn_s_setprio(KPR);
            m0 = lds16(mp + 9 * 1024); SBAR(); A1 = lds16(mp + 4 * 1024); SBAR();
            S1(0, A3, wr[R3][0]); A2 = lds16(mp + 5 * 1024); SBAR(); S1(1, A3, wr[R3][1]); A0 = lds16(mp + 7 * 1024); SBAR(); S1(2, A3, wr[R3][2]); A3 = lds16(mp + 6 * 1024); SBAR();
            S1(1, m0, wl[1]); wl[1] = w_lds(0); SBAR(); S1(2, m0, wl[2]); wl[2] = w_lds(7); SBAR(); S1(0, m0, wl[0]);
            G3(A1, wr[4]); G3(A2, wr[5]);
            auto put = [&](int u) { const int T = 3 * (W & 3) + u; unsigned o; if (W < 4) { constexpr float c1 = 0.07216878364870322f * 1.4426950408889634f; float x = (acc[u][0] + acc[u][2]) * c1; PIN(x); o = pk2(x, (acc[u][1] + acc[u][3]) * c1); } else o = fold_pk(acc[u]);
                *reinterpret_cast<unsigned*>(qp + (T >> 1) * 512 + (T & 1) * (W < 4 ? 4 : 8)) = __builtin_amdgcn_perm(dppu<0xB1>(o), o, sel); };
            acc[0] = smf(A3, wr[R6][0], acc[0], idx); acc[1] = smf(A3, wr[R6][1], acc[1], idx); acc[0] = smf(A0, wr[7][0], acc[0], idx);
            acc[2] = smf(A3, wr[R6][2], acc[2], idx); acc[1] = smf(A0, wl[1], acc[1], idx); PIN(acc[0]); PIN(acc[1]); PIN(acc[2]); SBAR();
            put(0); SBAR();
            acc[2] = smf(A0, wl[2], acc[2], idx); PIN(acc[2]); SBAR();
            put(1); SBAR(); put(2); SBAR(); AT(4);
        }
        if (s == 0) { if (W == 2 && !early) { kx_fetch(0, true); VMDRAIN(); } LBAR(); }
        if ((W == 0 || W == SW2) && s + 1 < NSEG_) {
            PIN(ln);
            unsigned kp = ldsb + L_Q + ln * 16, mk = ldsb + L_K + (ln >> 4) * 128 + ((ln + ((ln >> 2) & 4)) & 7) * 16;
            constexpr int KO = L_KX - L_Q;
            u32x8 ka = cat8(lds16a(kp + KO), lds16a(kp + KO + 1024)), kb = cat8(lds16a(kp + KO + 6144), lds16a(kp + KO + 7168)), kc = cat8(lds16a(kp + KO + 2048), lds16a(kp + KO + 3072)), kd = cat8(lds16a(kp + KO + 8192), lds16a(kp + KO + 9216));
            f32x4 sc[3] = {zero4(), zero4(), zero4()};
            PIN(ka); PIN(kb); PIN(kc); PIN(kd); PIN(sc[0]); PIN(sc[1]); PIN(sc[2]); PIN(kp); PIN(mk);
            unsigned sq = sb + (W ? 64 : 0); asm volatile("" : "+s"(sq));
            __builtin_amdgcn_s_setprio(3);
            LBAR();
            AT(5);
            const u32x4 q0 = lds16a(kp); SBAR(); u32x8 ke = cat8(lds16a(mk), lds16a(mk + 512)); SBAR(); const u32x4 q1 = lds16a(kp + 1024); SBAR(); const u32x4 q2 = lds16a(kp + 2048); SBAR();
            sc[0] = smf(q0, ka, sc[0], idx); sc[1] = smf(q0, kb, sc[1], idx); PIN(sc[0]); PIN(sc[1]); SBAR();
            ka = cat8(lds16a(mk + 1024), lds16a(mk + 1536)); SBAR(); kb = cat8(lds16a(kp + KO + 4096), lds16a(kp + KO + 5120)); SBAR();
            sc[2] = smf(q0, ke, sc[2], idx); PIN(sc[2]); SBAR();
            ke = cat8(lds16a(kp + KO + 10240), lds16a(kp + KO + 11264)); SBAR();
            sc[0] = smf(q1, kc, sc[0], idx); PIN(sc[0]); SBAR();
            kc = cat8(lds16a(mk + 2048), lds16a(mk + 2560)); SBAR();
            sc[1] = smf(q1, kd, sc[1], idx); sc[2] = smf(q1, ka, sc[2], idx); sc[0] = smf(q2, kb, sc[0], idx); sc[1] = smf(q2, ke, sc[1], idx); sc[2] = smf(q2, kc, sc[2], idx); SBAR();
            AT(6);
            constexpr int R = W ? 1 : 0;
            float s0 = sc[0][R] + sc[0][R + 2], s1 = sc[1][R] + sc[1][R + 2]; PIN(s0); PIN(s1);
            float s2 = sc[2][R] + sc[2][R + 2], mx = max3f(s0, s1, s2), g0, g1, g2, g3, g4, g5;
            asm volatile("s_nop 1\n\tv_max_f32_dpp %0, %0, %0 row_ror:8 row_mask:0xf bank_mask:0xf\n\ts_nop 1\n\tv_max_f32_dpp %0, %0, %0 row_ror:4 row_mask:0xf bank_mask:0xf\n\ts_nop 1\n\t"
                "v_max_f32_dpp %0, %0, %0 row_ror:2 row_mask:0xf bank_mask:0xf\n\ts_nop 1\n\tv_max_f32_dpp %0, %0, %0 row_ror:1 row_mask:0xf bank_mask:0xf\n\t"
                "v_sub_f32 %1, %1, %0\n\tv_sub_f32 %2, %2, %0\n\tv_sub_f32 %3, %3, %0\n\tv_exp_f32 %1, %1\n\tv_exp_f32 %2, %2\n\tv_exp_f32 %3, %3\n\t"
                "v_add_f32 %0, %1, %2\n\tv_fmac_f32 %0, 0.5, %3\n\t"
                "v_mov_b32_dpp %4, %1 quad_perm:[0,0,2,2] row_mask:0xf bank_mask:0xf\n\tv_mov_b32_dpp %5, %2 quad_perm:[0,0,2,2] row_mask:0xf bank_mask:0xf\n\tv_add_f32_dpp %0, %0, %0 row_ror:8 row_mask:0xf bank_mask:0xf\n\t"
                "v_mov_b32_dpp %6, %1 quad_perm:[1,1,3,3] row_mask:0xf bank_mask:0xf\n\tv_mov_b32_dpp %7, %2 quad_perm:[1,1,3,3] row_mask:0xf bank_mask:0xf\n\tv_add_f32_dpp %0, %0, %0 row_ror:4 row_mask:0xf bank_mask:0xf\n\t"
                "v_mov_b32_dpp %8, %3 quad_perm:[0,0,2,2] row_mask:0xf bank_mask:0xf\n\tv_mov_b32_dpp %9, %3 quad_perm:[1,1,3,3] row_mask:0xf bank_mask:0xf\n\tv_add_f32_dpp %0, %0, %0 row_ror:2 row_mask:0xf bank_mask:0xf\n\t"
                "s_nop 1\n\tv_add_f32_dpp %0, %0, %0 row_ror:1 row_mask:0xf bank_mask:0xf"
                : "+v"(mx), "+v"(s0), "+v"(s1), "+v"(s2), "=&v"(g0), "=&v"(g1), "=&v"(g2), "=&v"(g3), "=&v"(g4), "=&v"(g5));
            const float z = __builtin_amdgcn_rcpf(mx);
            __builtin_amdgcn_raw_buffer_store_b64((u32x2){pk2(g0 * z, g1 * z), pk2(g2 * z, g3 * z)}, rhx, po, sq, 16); SBAR();
            __builtin_amdgcn_raw_buffer_store_b32(pk2(g4 * z, g5 * z), rhx, pn, sb, 16);
            SBAR(); AT(7);
            tim[1 + 2 * R] = rt_now() - (tb + cP);
            tim[4 + R] = (unsigned)s + 1u;
            __builtin_amdgcn_s_setprio(0);
            PIN(ln);
            tt_step(*reinterpret_cast<const unsigned*>(lds + L_TIM + ln * 4), s, s == 0 ? rt_now() - (W == 0 || W == SW2 ? TT_E1 : TT_E1 - 35) : 0u, tb, cC1, cP);
        } else {
            LBAR();
            if (W == 1 && KME) {
#pragma unroll
                for (int m = 0; m < 3; ++m) st16s(lds16(lds + L_K + m * 1024 + ln * 16), rst, ln * 16, ST_KM + ((s & (PR - 1)) * 32 + h) * 3072 + m * 1024);
            }
            if (W >= 4 && KPR) __builtin_amdgcn_s_setprio(0);
            AT(5);
            tt_step(*reinterpret_cast<const unsigned*>(lds + L_TIM + ln * 4), s, s == 0 ? rt_now() - (W == 0 || W == SW2 ? TT_E1 : TT_E1 - 35) : 0u, tb, cC1, cP);
        }
        {
            if (W == KXW && s + 1 < NSEG_) wait_set(rfl, F_XP + ((s + 1) * NXP_ + xj) * FLP, hop, 0);
            if ((W == 1 || W == KXW) && s + 1 < NSEG_) { for (unsigned sp = 0; *(volatile __attribute__((address_space(3))) unsigned long long*)(tim + 4) != ((unsigned long long)s + 1u) * 0x100000001ull && sp < SPIN_CAP; ++sp) __builtin_amdgcn_s_sleep(SPS); CBAR(); if (W == 1 && SLATE1) __builtin_amdgcn_s_sleep(SLATE1); if (W == 2 && SLATE2) __builtin_amdgcn_s_sleep(SLATE2); }
            if (W == KXW && s + 1 < NSEG_) dma16cn<12>(rst, ln * 16, ST_KX + (((s + 1) & (KXR - 1)) * 32 + h) * 12288, ldsb + L_KX);
            if (W == 1) {
                kdur = min(__builtin_amdgcn_readfirstlane(max((unsigned)tim[1], (unsigned)tim[3])), 4095u); krt = __builtin_amdgcn_readfirstlane(tim[2]) == (unsigned)s + 1u ? 1u : 0u;
                if (!KME) {
#pragma unroll
                for (int m = 0; m < 3; ++m) st16s(lds16(lds + L_K + m * 1024 + ln * 16), rst, ln * 16, ST_KM + ((s & (PR - 1)) * 32 + h) * 3072 + m * 1024);
                }
                if (RAP == 0)
                {
                    const unsigned sr = sb0 + (unsigned)((s + 3) & 3) * (5u << 14);
                    const u32x4 on = ones4(); PIN(pn);
                    __builtin_amdgcn_raw_buffer_store_b64((u32x2){on.x, on.y}, rhx, po, sr, 16); __builtin_amdgcn_raw_buffer_store_b64((u32x2){on.x, on.y}, rhx, po, sr + 64, 16);
                    __builtin_amdgcn_raw_buffer_store_b32(on.x, rhx, pn & ~OOB, sr, 16);
                }
                VMW(RAP == 0 ? 3 : 0); if (ln == 0) st16c(rfl, F_KM + (s * 32 + h) * FLP, (u32x4){cP, kdur, krt, tb}); if (ln == 0 && h == 0 && s >= 56) STAMP(370 + s - 56);
                if (RAP == 1)
                {
                    const unsigned sr = sb0 + (unsigned)((s + 3) & 3) * (5u << 14);
                    const u32x4 on = ones4(); PIN(pn);
                    __builtin_amdgcn_raw_buffer_store_b64((u32x2){on.x, on.y}, rhx, po, sr, 16); __builtin_amdgcn_raw_buffer_store_b64((u32x2){on.x, on.y}, rhx, po, sr + 64, 16);
                    __builtin_amdgcn_raw_buffer_store_b32(on.x, rhx, pn & ~OOB, sr, 16);
                }
            }
        }
        ATD(0, s == 41 && h == 0);
    }
    if (W == 0) { tim[2] = 0; tim[3] = 0; tim[4] = 0; tim[5] = 0; }
    if (W == 1 && lane_id() == 0) STAMP(512 + blockIdx.x);
    }
    if (W == 0 && tim[0]) set_err((a.ws + WS_HOP));
}

__device__ __forceinline__ void s_part(KA& a, unsigned char* lds, const int h, const int wave) {
    const int lane = lane_id();
    const unsigned ldsb = LDSB();
    u32x8 wr[8][3];
    auto keep = [&](int f, u32x8 v) { unsigned char* p = lds + 32768 + f * 16384 + wave * 2048 + lane * 16; *reinterpret_cast<u32x4*>(p) = lo4(v); *reinterpret_cast<u32x4*>(p + 1024) = hi4(v); };
    load_w<10>(MKR(a.wq, D_ * HE_ * 2), MKR(a.wk, D_ * HE_ * 2), h >> 4, h * 192, lds, ldsb, wave, lane, [&](int rd, int u, u32x8 v) { if (rd >= 8) keep(3 * rd - 23 + u, v); else if (rd == 7 && u == 2) keep(7, v); else wr[rd][u] = v; });
    keep(0, wr[7][1]);
    if (wave == 7) s_run<7>(a, lds, h, wr); else if (wave == 6) s_run<6>(a, lds, h, wr); else if (wave == 5) s_run<5>(a, lds, h, wr); else if (wave == 4) s_run<4>(a, lds, h, wr);
    else if (wave == 3) s_run<3>(a, lds, h, wr); else if (wave == 2) s_run<2>(a, lds, h, wr); else if (wave == 1) s_run<1>(a, lds, h, wr); else s_run<0>(a, lds, h, wr);
}

#define VSL0 1
#define WLD 4
#define TT_WD 45
#define ATH 0
#define LBAR_NONE() do { } while (0)
template <int N, class W, class O, class F> __device__ __forceinline__ void u_tiles(u32x4 (&xf)[6][2], rsrc_t rst, unsigned l16, W&& w, O&& off, F&& side) {
    f32x4 acc[2][2]; u32x4 o; unsigned so;
#pragma unroll
    for (int i = 0; i < N; ++i) {
#pragma unroll
        for (int j = 0; j < 12; ++j) { const int k = j >> 1, r = j & 1;
            acc[i & 1][r] = dmf(xf[k][r], w(i, k), k ? acc[i & 1][r] : Z4); PIN(acc[i & 1][r]); SBAR();
            side(i, j);
            if (i > 0) { const f32x4 c0 = acc[(i - 1) & 1][0], c1 = acc[(i - 1) & 1][1];
                if (j == 2) { so = off(i - 1); asm volatile("" : "+s"(so)); }
                if (j == 3) { o.x = pk2(c0[0], c1[0]); PIN(o.x); } if (j == 4) { o.y = pk2(c0[2], c1[2]); PIN(o.y); } if (j == 5) { o.z = pk2(c0[1], c1[1]); PIN(o.z); } if (j == 6) { o.w = pk2(c0[3], c1[3]); PIN(o); }
                if (j == 7) __builtin_amdgcn_raw_buffer_store_b128(o, rst, l16, so, 16);
                if (j == 9) asm volatile("" :: "v"(o)); }
            SBAR(); }
    }
    const f32x4 c0 = acc[(N - 1) & 1][0], c1 = acc[(N - 1) & 1][1];
    st16s((u32x4){pk2(c0[0], c1[0]), pk2(c0[2], c1[2]), pk2(c0[1], c1[1]), pk2(c0[3], c1[3])}, rst, l16, off(N - 1));
}
__device__ __forceinline__ unsigned vblk(int j, unsigned nr) { const unsigned v = (unsigned)(0x9873265410ull >> (4 * j)) & 15u; return nr ? (v < 5 ? v + 5 : v - 5) : v; }
template <bool XW> __device__ __forceinline__ void v_run(KA& a, unsigned char* lds, const int h, const int wave) {
    const int lane = lane_id(), tid = wave * 64 + lane;
    const unsigned ldsb = LDSB();
    constexpr int L_MEM = 0, L_V = 10240, L_VX = 13312, L_TIM = 25600, L_E = 26624, L_F = L_E + 2048, L_T = L_F + 1024, L_U = L_T + 1024, L_W = 16384, NO = XW ? 4 : 6, NV = XW ? 2 : 1, KR = XW ? 8 : 4;
    constexpr int NBK = XW ? 2 : 6;
    u32x8 wo[NBK][3], wv[KR][NV];
    const int lw0 = XW ? 65536 + wave * 8192 : wave * 8192, lw1 = XW ? 114688 + wave * 12288 : 81920 + wave * 4096;
    const int vt = XW ? 2 * wave : 4 + wave;
    {
        const rsrc_t rv = MKR(a.wv, D_ * HE_ * 2), ro = MKR(a.wo, HE_ * D_ * 2);
        const unsigned l15 = lane & 15, lg = lane >> 4, row = 8 * wave + (lane >> 3), ch = ((lane & 7) ^ (wsw(row) << 1)) * 16;
        const unsigned vo = row * (HE_ * 2) + h * 384 + ch, oo = (h * 192 + row) * (D_ * 2) + ch;
        u32x8 tmp[6];
        load_b<6, 3, 10, L_W, 2>(ldsb, wave,
            [&](int rd, int j, unsigned dst) { if (rd < 4 || rd == 9) dma16(ro, oo, (j >> 1) * (64 * D_ * 2) + (rd % 5) * 256 + (j & 1) * 128, dst); else dma16(rv, vo, vblk((rd - 4) * 2 + j / 3, h >> 4) * (64 * HE_ * 2) + (j % 3) * 128, dst); },
            [&](int rd, int base) {
                if (rd < 4 || rd == 9) {
#pragma unroll
                    for (int i = 0; i < (rd < 2 ? 1 : (XW == (rd == 9) ? 2 : 0)); ++i) { const int tl = rd < 2 ? wave : (rd == 2 ? (wave & 3) + 4 * i : 2 * (wave & 3) + i);
#pragma unroll
                        for (int k = 0; k < 3; ++k) { const unsigned char* p = lds + base + (2 * k + (tl >> 2)) * 8192; const u32x8 v = cat8(ld_frag(p, 4 * lg, 16, tl & 3, l15), ld_frag(p, 32 + 4 * lg, 16, tl & 3, l15));
                            if (rd == 9) tmp[3 * i + k] = v; else wo[(rd < 2 ? rd : 2 * rd - 2 + i) % NBK][k] = v; } }
                } else {
#pragma unroll
                    for (int k = 0; k < 2; ++k)
#pragma unroll
                        for (int u = 0; u < NV; ++u) { const unsigned char* p = lds + base + (3 * k + (vt >> 2)) * 8192; const u32x8 v = cat8(ld_frag(p, 8 * lg, 4, (vt & 3) + u, l15), ld_frag(p, 32 + 8 * lg, 4, (vt & 3) + u, l15));
                            const int j = 2 * (rd - 4) + k; if (j < KR) wv[j % KR][u] = v; else tmp[XW ? 2 * k + u : j & 3] = v; } } },
            [&](int rd) {
                const int n = XW ? (rd == 8 ? 4 : (rd == 9 ? 6 : 0)) : (rd == 7 ? 4 : (rd == 8 ? 2 : 0));
#pragma unroll
                for (int q = 0; q < n; ++q) { unsigned char* p = lds + (n == 4 ? lw0 : lw1) + q * 2048 + lane * 16; *reinterpret_cast<u32x4*>(p) = lo4(tmp[q]); *reinterpret_cast<u32x4*>(p + 1024) = hi4(tmp[q]); } });
    }
    const rsrc_t rhx = MKR(al2m((a.ws + WS_FR)), FR_USED), rst = MKR((a.ws + WS_ST), ST_BYTES);
    lds_u32* tim = (lds_u32*)(lds + L_TIM);
    if (tid == 0) { tim[0] = 0; tim[2] = 0; }
    const unsigned pol = page_pol(a), sx = ((my_side() ^ pol) & 1u) << 13;
    unsigned uo0, uo1 = 0, l16 = lane * 16;
    { const unsigned long long sd = um_sides(a);
      uo0 = um_base(sd, pol, lane < 32 ? 8 * (lane >> 4) + wave : (XW ? 30 + 2 * wave + (lane >> 4) : 4 + wave + 4 * (lane >> 4))) + h * 256 + (lane & 15) * 16;
      if (!XW) uo1 = um_base(sd, pol, 16 + 2 * wave + (lane >> 5)) + h * 256 + (lane & 15) * 16 + (lane & 16) / 2; }
    auto arm = [&](unsigned so) { const u32x4 on = ones4(); st16s(on, rhx, uo0, so); if (!XW) __builtin_amdgcn_raw_buffer_store_b64((u32x2){on.x, on.y}, rhx, uo1, so, 16); SBAR(); };
    arm(16384); arm(32768); arm(49152);
    const int xj = xp_of(h), xj2 = 3 * (h >> 1) + 2;
    if (XW) uo1 = wave < 2 ? l16 : M_STAMP + min(lane, 17) * 64;
    const int j0 = (0x6510 >> (4 * (wave & 3))) & 15, j1 = (0x9842 >> (4 * (wave & 3))) & 15, j2 = wave ? 7 : 3;
    auto voff = [&](int j) { const unsigned b = vblk(j, h >> 4); return b * 1024 + (b < 8 ? 0 : 8192); };
    const unsigned o0 = voff(j0), o1 = voff(j1), o2 = wave < 2 ? voff(j2) : 0u;
    PIN(uo0); PIN(uo1); PIN(l16);
    *reinterpret_cast<u32x4*>(lds + L_T + l16) = (u32x4){L_V + ((lane >> 3) & 1) * 256u + (lane >> 4) * 64u + ((lane >> 1) & 1) * 32u + (lane & 1) * 16u + ((lane >> 2) & 1) * 8u,
        (lane & 1) ? 0x03020706u : 0x05040100u, m_idx(lane), F_XP + ((lane & 1) ? xj : xj2) * FLP};
    VMKNOWN();
    ATV;
#define la ((int)l16)
    auto u_so = [&](int t, int i) { return ST_U + (((t & (URING - 1)) * 40 + (i < 2 ? 8 * i + wave : (i < 4 ? 4 + wave + 4 * i : 12 + 2 * wave + i))) * 32 + h) * 1024; };
    u32x4 xf[6][2];
    auto back0 = [&](int q) { xf[q >> 1][q & 1] = lds16a(la + L_VX + (q & 1) * 6144 + (q >> 1) * 1024); SBAR(); };
    auto back = [&](auto N, int t, auto&& side) { u_tiles<decltype(N)::value>(xf, rst, l16, [&](int i, int k) { return (k & 1) ? hi4(wo[i][k >> 1]) : lo4(wo[i][k >> 1]); }, [&](int i) { return u_so(t, i); }, [&](int i, int j) { if (i == 0) { side(j); if (j + 4 < 12) back0(j + 4); } }); };
    auto v_so = [&](int t) { return ST_VX + ((t & (KXR - 1)) * 32 + h) * 12288 + (wave & 3) * 3072; };
#pragma unroll 1
    for (int layer = 0; layer < 2; ++layer) {
    unsigned char* hop = (a.ws + WS_HOP) + layer * HOP_BYTES;
    const rsrc_t rfl = MKR(hop, F_ERR);
    const unsigned sb0 = (unsigned)layer * (20u << 14) + sx;
    if (wave == 0 && l16 == 0) STAMP(140 + h);
    unsigned tb = rt_now(), cC1 = 0, cP = 0;
#pragma unroll 1
    for (int s = 0; s < NSEG_; ++s) {
        PIN(l16);
        int w0o = la + lw0, w1o = la + lw1; unsigned us = (s & 3) << 14;
        PIN(w0o); PIN(w1o); asm volatile("" : "+s"(us));
        const u32x4 lc = lds16a(la + L_T);
        unsigned fw = 1u, d0 = 0, d1 = 0;
        if (!XW) fw = lds4a(L_F + (l16 >> 2));
        SBAR();
        constexpr int NL = 4;
        u32x4 wl[NL][2];
#pragma unroll
        for (int u = 0; u < 2 * NL; ++u) wl[u >> 1][u & 1] = lds16a(w0o + u * 1024);
        f32x4 acc[NV];
#pragma unroll
        for (int u = 0; u < NV; ++u) acc[u] = zero4();
        const unsigned sel = lc.y, idx = lc.z;
        SBAR();
        u32x4 m1, m2; unsigned s1 = 0, s2 = 0; int w1 = 0, w2 = 0;
        auto late = [&]() { CBAR(); m1 = __builtin_amdgcn_raw_buffer_load_b128(rhx, l16, s1, 16); SBAR(); m2 = __builtin_amdgcn_raw_buffer_load_b128(rhx, uo1, s2, 16); CBAR(); SBAR(); };
        auto stuck = [&](unsigned sp) { if ((sp & 63u) == 63u && (sp > SPIN_CAP || tim[0])) { tim[0] = sp; return true; } if (sp > 8) __builtin_amdgcn_s_sleep(RTL); return false; };
        if (XW) {
            const unsigned sb = sb0 + (unsigned)(s & 3) * (5u << 14);
            unsigned s0 = sb + o0; s1 = sb + o1; s2 = sb + (wave < 2 ? o2 : (s >> 2) * 4);
            int w0 = la + j0 * 1024; w1 = la + j1 * 1024; w2 = la + (wave < 2 ? j2 * 1024 : L_E - 2048 + wave * 1024);
            PIN(w0); PIN(w1); PIN(w2); asm volatile("" : "+s"(s0), "+s"(s1), "+s"(s2));
            AT(0);
            rt_fine<21>(tb + cP + TT_VD);
            AT(6);
            CBAR(); u32x4 m0 = __builtin_amdgcn_raw_buffer_load_b128(rhx, l16, s0, 16); CBAR(); SBAR();
            nops<(VFD * 21 - 12) / 8>();
            late();
            sts16a(w0, m0);
            if (__builtin_expect(!__all(umax4(m0) != 0xffffffffu), 0)) {
                tim[2] = (unsigned)s + 1u;
                for (unsigned sp = 0;; ++sp) { CBAR(); m0 = __builtin_amdgcn_raw_buffer_load_b128(rhx, l16, s0, 16); if (__all(umax4(m0) != 0xffffffffu) || stuck(sp)) break; }
                sts16a(w0, m0); }
        } else {
            if (__builtin_expect(s > 0 && !__all(fw != 0u), 0)) { wait_set(rfl, lc.w + min(s + 2, NSEG_ - 1) * (NXP_ * FLP), hop, 0); VMKNOWN(); }
            d0 = v_so(s + 2); d1 = min(s + 3, NSEG_ - 1) * (NXP_ * FLP); asm volatile("" : "+s"(d0), "+s"(d1));
        }
        AT(1);
        if (XW) { LBAR(); __builtin_amdgcn_s_setprio(0); } else { __builtin_amdgcn_s_setprio(YP1); CBAR(); __builtin_amdgcn_s_barrier(); CBAR(); }
        AT(2);
        f32x4 um[NO];
        {
#define VS(u, A, B) do { acc[(u) % NV] = smf(A, B, acc[(u) % NV], idx); PIN(acc[(u) % NV]); SBAR(); } while (0)
#define VL(u) cat8(wl[(u) % NL][0], wl[(u) % NL][1])
#define VA(k) lds16a(la + L_MEM + (k) * 1024); SBAR()
#define VW(u, f, q) wl[u][q] = lds16a(w1o + (f) * 2048 + (q) * 1024); SBAR()
            int qo;
            if (XW) {
                u32x4 A0 = VA(5); u32x4 A1 = VA(6); u32x4 A2 = VA(0);
                auto take = [&]() { const unsigned em = wave < 2 ? ~0u : 0u;
                    auto put_ = [&]() { sts16a(w1, m1); sts16a(w2, m2); SBAR(); return __all(max(umax4(m1), max(m2.x, max(max(m2.y, m2.z), m2.w) & em)) != 0xffffffffu); };
                    if (__builtin_expect(!put_(), 0)) { tim[2] = (unsigned)s + 1u; for (unsigned sp = 0;; ++sp) { late(); if (put_() || stuck(sp)) break; } }
                    SBAR(); };
#define V2(A, j) VS(0, A, wv[(j) % KR][0]); VS(1, A, wv[(j) % KR][NV - 1])
                V2(A0, 5);
                take();
                u32x4 A3 = VA(1);
                V2(A1, 6);
                LBAR(); SBAR();
                A0 = VA(8); A1 = VA(9);
                V2(A2, 0); A2 = VA(2); V2(A3, 1); A3 = VA(3);
                VS(0, A0, VL(0)); VS(1, A0, VL(1)); A0 = VA(4); VS(0, A1, VL(2)); VS(1, A1, VL(3)); A1 = VA(7);
                VS(0, A2, wv[2][0]); VW(0, 0, 0); VS(1, A2, wv[2][NV - 1]); VW(0, 0, 1);
                VS(0, A3, wv[3][0]); VW(1, 1, 0); um[0] = zero4(); SBAR(); VS(1, A3, wv[3][NV - 1]); VW(1, 1, 1);
                VS(0, A0, wv[4 % KR][0]); VW(2, 2, 0); um[1] = zero4(); qo = lc.x + (vt >> 1) * 512; PIN(qo); SBAR(); VS(0, A1, wv[7 % KR][0]); VW(2, 2, 1);
                VS(1, A0, wv[4 % KR][NV - 1]); VS(1, A1, wv[7 % KR][NV - 1]);
            } else {
                u32x4 A0 = VA(5); u32x4 A1 = VA(6); u32x4 A2 = VA(0); u32x4 A3 = VA(1);
                dma16cn<3>(rst, l16, d0, L_VX + (wave & 3) * 3072); SBAR();
                VS(0, A0, VL(1)); VS(0, A1, VL(2));
                CBAR(); __builtin_amdgcn_s_barrier(); CBAR(); SBAR();
                if (YP2) __builtin_amdgcn_s_setprio(YP2);
                A0 = VA(4); A1 = VA(7);
                VS(0, A2, wv[0][0]); A2 = VA(2);
                VS(0, A3, wv[1][0]); A3 = VA(3); qo = lc.x + ((vt >> 1) * 512 + (vt & 1) * 4); PIN(qo); SBAR();
                VW(1, 0, 0); VW(1, 0, 1); VS(0, A0, VL(0)); A0 = VA(8); VW(2, 1, 0); VS(0, A1, VL(3)); A1 = VA(9); VW(2, 1, 1);
                VS(0, A2, wv[2][0]); um[0] = zero4(); um[1] = zero4(); SBAR(); VS(0, A3, wv[3][0]); um[2] = zero4(); um[3] = zero4(); SBAR();
                VS(0, A0, VL(1)); um[NO - 2] = zero4(); um[NO - 1] = zero4(); SBAR(); VS(0, A1, VL(2));
            }
            unsigned o[NV];
#pragma unroll
            for (int u = 0; u < NV; ++u) { o[u] = fold_pk(acc[u]); o[u] = __builtin_amdgcn_perm(dppu<0xB1>(o[u]), o[u], sel); }
            if (XW) { *(__attribute__((address_space(3))) u32x2*)(uintptr_t)(unsigned)qo = (u32x2){o[0], o[NV - 1]}; SBAR(); um[2] = zero4(); um[3] = zero4();
            } else { *(__attribute__((address_space(3))) unsigned*)(uintptr_t)(unsigned)qo = o[0]; __builtin_amdgcn_s_setprio(2); }
        }
        AT(3);
        VMDRAIN(); LBAR();
        AT(4);
        {
            const u32x4 q0 = lds16a(la + L_V), q1 = lds16a(la + L_V + 1024), q2 = lds16a(la + L_V + 2048); SBAR();
#define VU(u, q, k) do { um[u] = smf(q, (XW && (u) >= 2) ? VL(k) : wo[(u) % NBK][k], um[u], idx); PIN(um[u]); SBAR(); } while (0)
#define VO(u) min(fold_pk(um[u]), 0xfffffffeu)
            if (!XW) { VU(NO - 2, q0, 0); VU(NO - 1, q0, 0); VU(NO - 2, q1, 1); VU(NO - 1, q1, 1); VU(NO - 2, q2, 2); VU(NO - 1, q2, 2); }
            VU(0, q0, 0); VU(2, q0, 0);
            unsigned e0 = 0, e1 = 0;
            if (!XW) { e0 = VO(NO - 2); PIN(e0); SBAR(); }
            VU(0, q1, 1);
            if (XW) { VW(0, 3, 0); VW(0, 3, 1); }
            if (!XW) { e1 = VO(NO - 1); PIN(e1); SBAR(); }
            VU(2, q1, 1);
            if (!XW) { const auto z = __builtin_amdgcn_permlane32_swap(e0, e1, false, false); __builtin_amdgcn_raw_buffer_store_b64((u32x2){z[0], z[1]}, rhx, uo1, us, 16); SBAR(); }
            VU(0, q2, 2); if (XW) { VW(1, 4, 0); VW(1, 4, 1); } else back0(2); VU(2, q2, 2); if (!XW) back0(3); VU(1, q0, 0); if (XW) { VW(2, 5, 0); VW(2, 5, 1); } back0(0); VU(1, q1, 1); back0(1);
            e0 = VO(0); PIN(e0); SBAR();
            VU(1, q2, 2);
            e1 = VO(2); PIN(e1); SBAR();
            VU(3, q0, 0);
            const auto x02 = __builtin_amdgcn_permlane32_swap(e0, e1, false, false); SBAR();
            VU(3, q1, 1); if (XW) { back0(2); back0(3); } VU(3, q2, 2);
            e0 = VO(1); PIN(e0); SBAR();
            const auto x13 = __builtin_amdgcn_permlane32_swap(e0, VO(3), false, false);
            const auto y01 = __builtin_amdgcn_permlane16_swap(x02[0], x13[0], false, false), y23 = __builtin_amdgcn_permlane16_swap(x02[1], x13[1], false, false);
            st16s((u32x4){y01[0], y23[0], y01[1], y23[1]}, rhx, uo0, us);
            SBAR();
            AT(5);
            __builtin_amdgcn_s_setprio(XW ? 2 : 0);
        }
        if (__builtin_expect(s == 0, 0)) {
            if (!XW) sts4a(L_F + (l16 >> 2), 0u);
#pragma unroll 1
            for (int t = 0; t < 3; ++t) {
                if (XW) { wait_set(rfl, lds4a(la + L_T + 12) + t * (NXP_ * FLP), hop, t ? 0 : VSL0); dma16cn<3>(rst, l16, v_so(t), L_VX + wave * 3072); VMDRAIN(); }
                LBAR();
                back0(0); back0(1); back0(2); back0(3);
                back(std::integral_constant<int, NBK>{}, t, [](int) {});
                if (t == 2) break;
                VMDRAIN(); LBAR();
                if (wave == 0 && l16 == 0) st4c(rfl, F_UX + (t * 32 + h) * FLP, (unsigned)(t + 1) << 16);
                if (wave == 0 && l16 == 0 && h == 0) STAMP(10 + t);
            }
        } else {
            unsigned tw, tr, te; int d;
            back(std::integral_constant<int, XW ? 2 : 3>{}, s + 2, [&](int j) {
                if (XW) {
                    if (j == 0) { tw = lds4a(la + L_E); tr = tim[2]; te = rt_now(); asm volatile("" : "+s"(te)); }
                    if (j == 2) { d = (int)(tw - tb - cC1); PIN(d); }
                    if (j == 3) { d = max(d, (int)dppu<0x128>((unsigned)d)); PIN(d); } if (j == 4) { d = max(d, (int)dppu<0x124>((unsigned)d)); PIN(d); }
                    if (j == 5) { d = max(d, (int)dppu<0x122>((unsigned)d)); PIN(d); } if (j == 6) { d = max(d, (int)dppu<0x121>((unsigned)d)); PIN(d); }
                    if (j == 7) tim[4 + (s & 1)] = min(te - (tb + cP), 4095u) + (__builtin_amdgcn_readfirstlane(tr) == (unsigned)s + 1u ? 4096u : 0u);
                    if (j == 8) { const int e = __builtin_amdgcn_readlane(d, 0); tb = (s == 1 || e > 0) ? tb + cC1 + (unsigned)e + (s == 1 ? TT_V1 : TT_V) : tb + cP; cC1 = __builtin_amdgcn_readlane(tw, 16); cP = __builtin_amdgcn_readlane(tw, 17); asm volatile("" : "+s"(tb), "+s"(cC1), "+s"(cP)); }
                }
                if (!XW && j == 0) tw = tim[4 + ((s + 1) & 1)];
                if (!XW && j == 2) __builtin_amdgcn_raw_buffer_store_b32(tw | 65536u, rfl, wave == 7 ? l16 << 20 : OOB, F_UX + ((s + 1) * 32 + h) * FLP, 16);
                if (!XW && j == 5) dma4c(rfl, lds4a(la + L_T + 12), d1, L_F);
                });
            if (!XW) VMW(3);
        }
        AT(7);
        ATD(1, s == 41 && h == ATH);
    }
    if (wave == 0 && l16 == 0) { tim[2] = 0; STAMP(512 + blockIdx.x); }
    }
    if (wave == 0 && l16 == 0 && tim[0]) set_err((a.ws + WS_HOP));
#undef la
}

__device__ __forceinline__ void w_part(KA& a, unsigned char* lds, const int w, const int wave) {
    const int lane = lane_id(), hw = wave >> 1, t0 = (wave & 1) * 7;
    const unsigned ldsb = LDSB();
    u32x4 wo[7][6];
    {
        const rsrc_t ro = MKR(a.wo, HE_ * D_ * 2);
        const unsigned row = 8 * wave + (lane >> 3), oo = ((4 * w + (row >> 5)) * 192 + (row & 31)) * (D_ * 2) + 640 + ((lane & 7) ^ (wsw(row) << 1)) * 16;
        load_b<8, 2, 6, 0, 0>(ldsb, wave, [&](int rd, int j, unsigned dst) { dma16(ro, oo, (rd * 32 + (j >> 2) * 384) * (D_ * 2) + (j & 3) * 128, dst); },
            [&](int rd, int base) {
#pragma unroll
                for (int i = 0; i < 7; ++i) wo[i][rd] = ld_frag(lds + base + ((hw >> 1) * 4 + ((t0 + i) >> 2)) * 8192, (hw & 1) * 32 + 4 * (lane >> 4), 16, (t0 + i) & 3, lane & 15); }, [](int) {});
    }
    const rsrc_t rst = MKR(a.ws + WS_ST, ST_BYTES);
    if (wave == 0) init_wait(a);
#pragma unroll 1
    for (int layer = 0; layer < 2; ++layer) {
    unsigned char* hop = a.ws + WS_HOP + layer * HOP_BYTES;
    const rsrc_t rfl = MKR(hop, F_ERR);
    unsigned l16 = lane_id() * 16; PIN(l16);
    const unsigned fx = F_XP + (6 * w + (lane & 7) % 6) * FLP;
    auto v_so = [&](int t) { return ST_VX + ((t & (KXR - 1)) * 32 + 4 * w) * 12288 + wave * 6144; };
    if (wave == 0) { wait_set(rfl, fx + NXP_ * FLP, hop, 8); wait_set(rfl, fx + 3 * (NXP_ * FLP), hop, 1); }
    LBAR();
    dma16cn<6>(rst, l16, v_so(3), ldsb + 49152 + wave * 6144);
    VMKNOWN(); LBAR();
    unsigned tnext = rt_now();
#pragma unroll 1
    for (int t = 3; t < NSEG_; ++t) {
        u32x4 fl, xf[6][2];
        const unsigned fo = (lane < 8 && t >= WLD) ? F_KM + ((t - WLD) * 32 + 4 * w) * FLP : fx + min(t + 1, NSEG_ - 1) * (NXP_ * FLP);
        if (wave == 0) asm volatile("buffer_load_dwordx4 %0, %1, %2, 0 offen sc1" : "=&v"(fl) : "v"(fo), "s"(rfl) : "memory");
        const int xp = ldsb + (t & 1) * 49152 + hw * 12288 + l16;
        auto rd = [&](int q) { xf[q >> 1][q & 1] = lds16a(xp + (q & 1) * 6144 + (q >> 1) * 1024); SBAR(); };
        rd(0); rd(1); rd(2); rd(3);
        u_tiles<7>(xf, rst, l16, [&](int i, int k) { return wo[i][k]; }, [&](int i) { return ST_U + (((t & (URING - 1)) * 40 + 20 + t0 + i) * 32 + 4 * w + hw) * 1024; }, [&](int i, int j) {
            if (i == 0 && j + 4 < 12) rd(j + 4);
            if (i == 1 && j == 0) { if (wave == 0) VMW(1); else VMDRAIN(); __builtin_amdgcn_s_barrier(); CBAR(); if (wave == 1 && t > 3 && l16 == 0) st4c(rfl, F_UW + ((t - 1) * 8 + w) * FLP, 1u); }
            if (i == 2 && j == 0) {
                if (wave == 0) { asm volatile("s_waitcnt vmcnt(1)" : "+v"(fl) :: "memory");
                    if (__builtin_expect(__all(fl.x != 0u), 1)) tnext = __builtin_amdgcn_readfirstlane(fl.w) + 2u * __builtin_amdgcn_readfirstlane(fl.x) + TT_WD; else { wait_timed(rfl, fo, hop, tnext, 2, TT_WD, t < WLD); VMKNOWN(); } }
                __builtin_amdgcn_s_barrier(); CBAR(); }
            if ((i == 2 || i == 3) && (j == 1 || j == 8 || j == 11)) { const int p = (i - 2) * 3 + (j > 1) + (j > 8); dma16c(rst, l16, v_so(t + 1) + p * 1024, ldsb + ((t + 1) & 1) * 49152 + wave * 6144 + p * 1024); } });
        VMW(4);
        if (wave == 0 && t >= WLD) rt_wait(tnext);
        LBAR();
    }
    VMDRAIN(); LBAR();
    if (wave == 1 && l16 == 0) st4c(rfl, F_UW + ((NSEG_ - 1) * 8 + w) * FLP, 1u);
    }
}

#define PSPAN 40000u
#define OAUX 0
constexpr int RO_UM = 131072, RO_TIM = RO_UM + 3 * 8192, RO_END = NSEG_;
__device__ __forceinline__ void ro_state(KA& a, unsigned char* lds, const int r, const unsigned pol) {
    constexpr int wave = 0;
    const int lane = lane_id();
    const rsrc_t rhx = MKR(al2m((a.ws + WS_FR)), FR_USED), rst = MKR(a.ws + WS_ST, ST_BYTES);
    lds_u32* tim = (lds_u32*)(lds + RO_TIM + 256);
    const unsigned sx = ((my_side() ^ pol) & 1u) << 13;
    unsigned po = sx + (2 + (r & 1)) * 16384 + lane * 16, uo = um_base(um_sides(a), pol, r >> 1) + lane * 16;
    unsigned ho = (r < 64 ? 0 : 8192) + r * 128 + (lane >> 5) * 64 + (lane & 2) * 16 + (lane & 16) + (lane & 12) + (lane & 1) * 8192;
    unsigned idx = (lane & 1) ? 0xEEEEu : 0x4444u;
    const unsigned pmo = (2 - (r & 1)) * 16384 + (r & 1) * 2048;
    const unsigned nr = r < 40 ? 0u : 1u;
    unsigned so = (r % 5 == 0 && lane < 2) ? M_STAMP + (r / 5) * 64 + lane * 8192 : OOB;
    PIN(po); PIN(uo); PIN(ho); PIN(idx); PIN(so);
#pragma unroll 1
    for (int layer = 0; layer < 2; ++layer) {
    const unsigned sb0 = (unsigned)layer * (20u << 14);
    unsigned tb = 0, cC1 = 0, cP = 0; int u3 = 0;
    u32x8 ux[16];
#pragma unroll 1
    for (int t = 0; t < RO_END; ++t) {
        const int s = t - 1;
        ATV;
        if (t > 0) {
            const unsigned sp_ = sb0 + (unsigned)(s & 3) * (5u << 14);
            unsigned sb = sb0 + (unsigned)(t & 3) * (5u << 14), ss = sb + (t >> 2) * 4, sp0 = sp_ + nr * 4096, sp1 = sp0 ^ 4096, sm0 = sp_ + pmo + nr * 1024, sm1 = sm0 ^ 1024, su0 = ((s & 3) << 14) + nr * 4096, su1 = su0 ^ 4096;
            asm volatile("" : "+s"(sb), "+s"(ss), "+s"(sp0), "+s"(sp1), "+s"(sm0), "+s"(sm1), "+s"(su0), "+s"(su1));
            u32x4 um[8], p[10];
            auto u_bad = [&]() { unsigned m = 0;
#pragma unroll
                for (int q = 0; q < 8; ++q) m = max(m, umax4(um[q]));
                return m; };
            unsigned tp = tb + cC1;
            {
                auto p_half = [&](int z) { CBAR();
#pragma unroll
                    for (int q = 0; q < 4; ++q) { p[4 * z + q] = __builtin_amdgcn_raw_buffer_load_b128(rhx, po + q * 1024, z ? sp1 : sp0, 16); SBAR(); }
                    p[8 + z] = __builtin_amdgcn_raw_buffer_load_b128(rhx, po, z ? sm1 : sm0, 16); SBAR();
#pragma unroll
                    for (int q = 0; q < 4; ++q) { um[4 * z + q] = __builtin_amdgcn_raw_buffer_load_b128(rhx, uo + q * 1024, z ? su1 : su0, 16); SBAR(); }
                    CBAR(); };
                unsigned o;
                auto p_use = [&](f32x4 c0, f32x4 c1) {
#pragma unroll
                    for (int z = 0; z < 2; ++z) {
#pragma unroll
                        for (int q = 4 * z; q < 4 * z + 4; ++q) { c0 = smf(p[q], ux[2 * q], c0, idx); c1 = smf(p[q], ux[2 * q + 1], c1, idx); SBAR(); }
                        c0 = smf(p[8 + z], cat8(um[4 * z], um[4 * z + 1]), c0, idx); SBAR(); c1 = smf(p[8 + z], cat8(um[4 * z + 2], um[4 * z + 3]), c1, idx); SBAR(); }
                    SBAR();
                    float v0 = c0[0] + c0[1]; PIN(v0);
                    const float v = v0 + (c1[2] + c1[3]);
                    o = pk2(dppf<0xA0>(v), dppf<0xF5>(v));
                    return !__any(v != v); };
                f32x4 c0 = zero4(), c1 = zero4();
                { const unsigned n = rt_now(); if ((int)(n - tp) > 4) { tp = n; tim[2] = (unsigned)t; } }
                unsigned tv = min(tp, 0xfffffffeu); PIN(tv);
                AT(0);
                rt_fine<23>(tp);
                AT(1); p_half(0); nops<(RFD * 23 - 100) / 8>(); p_half(1);
                if (__builtin_expect(!p_use(c0, c1), 0)) {
                    tim[2] = (unsigned)t;
                    for (unsigned sp = 0;; ++sp) {
                        p_half(0); p_half(1);
                        if (p_use(zero4(), zero4())) break;
                        unsigned m = u_bad();
#pragma unroll
                        for (int q = 0; q < 10; ++q) m = max(m, umax4(p[q]));
                        if (__all(m != 0xffffffffu)) break;
                        if ((sp & 63u) == 63u && (sp > SPIN_CAP || tim[0])) { tim[0] = 1; break; }
                        if (sp > 3 || s == 0) __builtin_amdgcn_s_sleep(R0S);
                    }
                    o = min(o, 0xfffffffeu);
                    tv = min(rt_now() - TT_RT, 0xfffffffeu);
                }
                __builtin_amdgcn_raw_buffer_store_b32(o, rhx, ho, sb, 16); SBAR();
                __builtin_amdgcn_raw_buffer_store_b32(tv, rhx, so, ss, 16);
                SBAR(); AT(2);
                tim[1] = rt_now() - (tb + cC1);
            }
            LBAR();
            unsigned char* up = lds + RO_UM + u3 * 8192 + lane_id() * 16;
#pragma unroll
            for (int q = 0; q < 8; ++q) *reinterpret_cast<u32x4*>(up + ((q >> 2) ^ nr) * 4096 + (q & 3) * 1024) = um[q];
            u3 = u3 == 2 ? 0 : u3 + 1;
        } else LBAR();
        {
            const int ln = lane_id();
            if (t > 1) rt_wait(tb + cP + TT_BD);
            AT(3);
            const unsigned ub = ST_U + ((t & (URING - 1)) * 40 + (r >> 1)) * 32768, lb = (t & 3) * 32768, o = nr * 16384;
            dma16cn<16>(rst, ln * 16, ub + o, lb + o); dma16cn<16>(rst, ln * 16, ub + (o ^ 16384), lb + (o ^ 16384));
            const unsigned char* xp = lds + lb + (ln >> 5) * 1024 + (ln & 31) * 16;
#pragma unroll
            for (int q = 0; q < 16; ++q) { if (!(q & 1)) VMW(28 - 2 * q); const unsigned char* p = xp + ((q >> 3) ^ nr) * 16384 + ((q >> 1) & 3) * 4096 + (q & 1) * 512; ux[q] = cat8(lds16(p), lds16(p + 2048)); }
            AT(4);
            if (ln == 0 && r == 0 && t == 0) STAMP(13);
            LBAR();
            AT(5);
            const unsigned tpv = *reinterpret_cast<const unsigned*>(lds + RO_TIM + ln * 4);
            tb = __builtin_amdgcn_readlane(tpv, 0); cC1 = __builtin_amdgcn_readlane(tpv, 1); cP = __builtin_amdgcn_readlane(tpv, 2);
            if (t == 0) tb = rt_now() - cC1;
#pragma unroll
            for (int q = 0; q < 16; ++q) PIN(ux[q]);
        }
        ATD(2, t == 42 && r == 0);
    }
    if (lane == 0) tim[2] = 0;
    LBAR();
    }
    if (lane == 0 && tim[0]) set_err((a.ws + WS_HOP));
}
__device__ __forceinline__ void ro_time(KA& a, unsigned char* lds, const int r, const unsigned pol) {
    const int lane = lane_id();
    const rsrc_t rhx = MKR(al2m((a.ws + WS_FR)), FR_USED);
    lds_u32* tim = (lds_u32*)(lds + RO_TIM + 256);
    const unsigned sx = ((my_side() ^ pol) & 1u) << 13, to = sx + M_STAMP + min(lane, 17) * 64;
#pragma unroll 1
    for (int layer = 0; layer < 2; ++layer) {
    const rsrc_t rfl = MKR((a.ws + WS_HOP) + layer * HOP_BYTES, F_ERR);
    const unsigned sb0 = (unsigned)layer * (20u << 14);
    unsigned tb = 0, cC1 = 0, cP = 0;
#pragma unroll 1
    for (int s = 0; s < RO_END; ++s) {
        LBAR();
        if (s < NSEG_) {
            if (s > 1) rt_wait(tb + cP + TT_TW);
            unsigned tw;
            for (unsigned sp = 0;; ++sp) { CBAR();
                tw = __builtin_amdgcn_raw_buffer_load_b32(rhx, to, sb0 + (unsigned)(s & 3) * (5u << 14) + (s >> 2) * 4, 16);
                if (__all(tw != 0xffffffffu)) break;
                __builtin_amdgcn_s_sleep(6);
                if (sp > SPIN_CAP_W || tim[0]) { tim[0] = 1; break; } }
            tt_step(tw, s, rt_now(), tb, cC1, cP);
            if (lane == 0) *reinterpret_cast<u32x4*>(lds + RO_TIM) = (u32x4){tb, cC1, cP, 0u};
            if (lane == 0 && s > 1) st16c(rfl, F_RD + (s * NR_ + r) * FLP, (u32x4){1u, min((unsigned)tim[1], 4095u), tim[2] == (unsigned)s ? 1u : 0u, 0u});
        }
        LBAR();
    }
    LBAR();
    }
}
__device__ __forceinline__ void ro_master(KA& a) {
    const int lane = lane_id();
    const rsrc_t rhx = MKR(al2m((a.ws + WS_FR)), FR_USED);
    int e1 = TT_E1 * 16, e2 = TT_E2 * 16, x1 = 0, x2 = 0; bool pr2 = true;
#pragma unroll 1
    for (int layer = 0; layer < 2; ++layer) {
    const rsrc_t rfl = MKR((a.ws + WS_HOP) + layer * HOP_BYTES, F_ERR);
    const unsigned sb0 = (unsigned)layer * (20u << 14);
#pragma unroll 1
    for (int s = 0; s < RO_END; ++s) {
        LBAR();
        const int xs = max(TT_XS - s * TT_XD, 0);
        const unsigned c1 = max(e1 >> 4, TT_E1 + TT_X1 - 2 * s) + TT_M1 + x1 + (s == 0 ? TT_S1 : 0), c2 = c1 + (e2 >> 4) + TT_M2 + x2 + xs;
        const int q = max(s, 2);
        u32x4 f = ld16c(rfl, lane < 32 ? F_KM + ((q - 2) * 32 + lane) * FLP : F_UX + (q * 32 + lane - 32) * FLP), g = ld16c(rfl, F_RD + ((q - 1) * NR_ + lane) * FLP), k = ld16c(rfl, F_RD + ((q - 1) * NR_ + 64 + (lane & 15)) * FLP);
        if (lane < 4 && s + 1 < NSEG_) __builtin_amdgcn_raw_buffer_store_b32((lane & 2) ? c2 : c1, rhx, M_STAMP + 1024 + (lane & 2) * 32 + (lane & 1) * 8192, sb0 + (unsigned)((s + 1) & 3) * (5u << 14) + ((s + 1) >> 2) * 4, 16);
        SBAR();
        LBAR();
        SBAR();
        if (lane >= 32) { f.y = f.x & 4095u; f.z = (f.x >> 12) & 1u; }
        auto wmax = [&](unsigned v) { v = max(v, dppu<0x128>(v)); v = max(v, dppu<0x124>(v)); v = max(v, dppu<0x122>(v)); v = max(v, dppu<0x121>(v));
            return (int)max(max(__builtin_amdgcn_readlane(v, 0), __builtin_amdgcn_readlane(v, 16)), max(__builtin_amdgcn_readlane(v, 32), __builtin_amdgcn_readlane(v, 48))); };
        auto upd = [&](int& e, int m) { if (m > 0) { const int d = min(m * 16, e + 16 * 12) - e; e += d >> (d > 0 ? TT_UP : (s < 12 && !layer ? TT_DEC : TT_DN)); } };
        const bool rs = __any(f.z != 0u), r2 = __any((g.z | k.z) != 0u);
        if (s > 3) {
            if (!rs) upd(e1, wmax(lane < 32 ? f.y : (unsigned)max((int)f.y + TT_UM + TT_UX, 0)));
            if (!r2) upd(e2, wmax(max(g.y, k.y)));
            if (rs && !pr2) x2 += TT_BUMP; else if (r2 && !rs) x1 += TT_BUMP;
            if (!rs && !r2 && (s & TT_DCY) == 0) { x1 -= x1 > 0; x2 -= x2 > 0; }
        }
        pr2 = r2 || s < 3;
    }
    LBAR();
    if (!layer && lane == 0) { const rsrc_t rb = MKR((a.ws + WS_HOP) - 2048, 2048); const u32x2 t = ld8c(rb, 0); st8c(rb, 0, (u32x2){t.x + 1u, t.y}); }
    }
}
template <int TAIL> __device__ __forceinline__ void ro_out(KA& a, unsigned char* lds, const int r, const unsigned uo) {
    constexpr int wave = 2;
    const int lane = lane_id(), l15 = lane & 15, lg = lane >> 4, c = r >> 1, rt = r & 1, cr = (RROT & 2) ? c : 0;
    const rsrc_t rst = MKR((a.ws + WS_ST), ST_BYTES), rx = MKR((a.ws + WS_X), XI_BYTES), ro = MKR(a.out, S_ * D_ * 2);
    const unsigned xo = XI_X + rt * 20480 + (c >> 1) * 1024 + ((c & 1) * 2 + (lg >> 1)) * 256 + l15 * 16 + (lg & 1) * 8;
#pragma unroll 1
    for (int layer = 0; layer < 2; ++layer) {
    unsigned char* hop = (a.ws + WS_HOP) + layer * HOP_BYTES;
    const rsrc_t rfl = MKR(hop, F_ERR);
    unsigned tb = 0, cC1 = 0, cP = 1; int u3 = 0;
    u32x2 o = (u32x2){0u, 0u}, rr; float sq = 0.f; u32x4 p[40], um[8];
    auto put = [&](int s) {
        if (layer) __builtin_amdgcn_raw_buffer_store_b64(o, ro, ((rt * 16 + l15) * D_ + 16 * c + 4 * lg) * 2, s * (32 * D_ * 2), OAUX);
        else { const rsrc_t ry = MKR(a.ws + WS_Y, XI_BYTES); __builtin_amdgcn_raw_buffer_store_b64(o, ry, xo, s * XI_PITCH, 16); if (lane < 32) __builtin_amdgcn_raw_buffer_store_b32(__float_as_uint(sq), ry, (rt * 40 + c) * 128 + l15 * 8 + lg * 4, s * XI_PITCH, 16); } };
    auto gate = [&](int s, int slp) { wait_set(rfl, F_PX + ((s + QA) * 64 + (lane & 31) * 2 + rt) * FLP, hop, slp); };
    auto req = [&](int s) { const int ln = lane_id();
        rr = __builtin_amdgcn_raw_buffer_load_b64(rx, xo, s * XI_PITCH, 16); SBAR();
#pragma unroll
        for (int q = 0; q < 40; ++q) { p[q] = __builtin_amdgcn_raw_buffer_load_b128(rst, ln * 16 + (q & 3) * 1024, ST_PX + ((s & (PR - 1)) * 2 + rt) * 40960 + (q < 32 ? ((q >> 2) + cr) & 7 : q >> 2) * 4096, 16); SBAR(); }
        CBAR(); };
    auto comp = [&](int s) { const int ln = lane_id();
        const unsigned char* xp = lds + (s & 3) * 32768 + ln * 16; const unsigned char* mp = lds + RO_UM + u3 * 8192 + ln * 16;
        u32x4 uf[8];
#pragma unroll
        for (int q = 0; q < 7; ++q) uf[q] = lds16(xp + (((q >> 2) + cr) & 7) * 4096 + (q & 3) * 1024);
        SBAR();
        f32x4 acc = (f32x4){lo2f(rr.x), hi2f(rr.x), lo2f(rr.y), hi2f(rr.y)};
#pragma unroll
        for (int q = 0; q < 40; ++q) { acc = dmf((TAIL == OLAG && q >= 32) ? um[q & 7] : uf[q & 7], p[q], acc); PIN(acc); SBAR();
            if (q + 7 < (TAIL == OLAG ? 32 : 40)) { uf[(q + 7) & 7] = q + 7 < 32 ? lds16(xp + ((((q + 7) >> 2) + cr) & 7) * 4096 + ((q + 7) & 3) * 1024) : lds16(mp + (q - 25) * 1024); SBAR(); } }
        o = (u32x2){pk2(acc[0], acc[1]), pk2(acc[2], acc[3])};
        sq = row_sums(o); };
    auto fin = [&](int s) { put(s); if (!layer) { VMDRAIN(); if (lane == 0) st16c(rfl, F_OF + (s * NR_ + r) * FLP, (u32x4){cP, 1u, 1u, tb}); } if (lane == 0 && r == 0) STAMP(300 + s - 56); if (lane == 0) STAMP(3000 + (s - 60) * 80 + r); };
    auto times = [&]() { const unsigned tpv = *reinterpret_cast<const unsigned*>(lds + RO_TIM + lane * 4); tb = __builtin_amdgcn_readlane(tpv, 0); cC1 = __builtin_amdgcn_readlane(tpv, 1); cP = __builtin_amdgcn_readlane(tpv, 2); };
    if (TAIL) {
        const int s = NSEG_ - 1 - OLAG + TAIL;
        for (int t = 0; t < NSEG_; ++t) {
            __builtin_amdgcn_s_barrier();
            if (TAIL == OLAG) __builtin_amdgcn_s_sleep(ASL);
            if (TAIL == OLAG && t > 0) { const unsigned sr = (unsigned)layer * (20u << 14) + (unsigned)((t + 2) & 3) * (5u << 14);
                __builtin_amdgcn_raw_buffer_store_b32(~0u, MKR(al2m(a.ws + WS_FR), FR_USED), (r < 64 ? 0 : 8192) + r * 128 + (lane >> 5) * 8192 + (lane & 31) * 4, sr, 16);
                __builtin_amdgcn_raw_buffer_store_b32(~0u, MKR(al2m(a.ws + WS_FR), FR_USED), (r % 5 == 0 && lane < 2 && t + 2 < NSEG_) ? M_STAMP + (r / 5) * 64 + lane * 8192 : OOB, sr + ((t + 2) >> 2) * 4, 16); }
            if (TAIL == OLAG && (t >= 2 || layer)) {
#pragma unroll
                for (int q = 0; q < 4; ++q) st16s((u32x4){~0u, ~0u, ~0u, ~0u}, MKR(al2m(a.ws + WS_FR), FR_USED), uo + q * 1024, (((t + 2) & 3) << 14) + rt * 4096); }
            if (TAIL == OLAG) VMDRAIN();
            __builtin_amdgcn_s_barrier(); }
        CBAR();
        u3 = s % 3;
        if (TAIL == OLAG) {
            const rsrc_t rhx = MKR(al2m((a.ws + WS_FR)), FR_USED);
            times(); rt_wait(tb + cC1);
            for (unsigned sp = 0;; ++sp) { unsigned m = 0; CBAR();
#pragma unroll
                for (int q = 0; q < 8; ++q) um[q] = __builtin_amdgcn_raw_buffer_load_b128(rhx, uo + (q & 3) * 1024, ((s & 3) << 14) + (q >> 2) * 4096, 16);
#pragma unroll
                for (int q = 0; q < 8; ++q) m = max(m, umax4(um[q]));
                if (__all(m != 0xffffffffu)) break;
                if (sp > SPIN_CAP_W) { set_err(hop); break; }
                __builtin_amdgcn_s_sleep(8); }
            rt_wait(tb + TT_TL);
        }
        gate(s, 0); req(s); comp(s); fin(s);
    } else {
#pragma unroll 1
    for (int t = 0; t < NSEG_; ++t) {
        const int s = t - OLAG;
        ATV;
        LBAR();
        if (s >= 0) {
            const unsigned to_ = max((int)(tb + cP + TT_BD - rt_now()), 0) + rt_now() + TT_OD;
            gate(s, 0);
            if (s > 0) put(s - 1);
            rt_wait(to_);
            AT(0);
            req(s);
            AT(1);
            if (lane == 0 && r == 0 && s > 56) STAMP(300 + s - 57);
        }
        LBAR();
        times();
        if (!layer && s > 0) { PIN(rr); if (lane == 0) st16c(rfl, F_OF + ((s - 1) * NR_ + r) * FLP, (u32x4){cP, 1u, 1u, tb}); }
        if (s >= 0) { __builtin_amdgcn_s_sleep(OSL); comp(s); u3 = u3 == 2 ? 0 : u3 + 1; AT(2); }
        ATD(2, t == 42 && r == 0);
    }
    fin(NSEG_ - 1 - OLAG);
    }
    LBAR();
    }
}
__device__ __forceinline__ void ro_fill(KA& a, unsigned char* lds, const int r) {
    constexpr int wave = 3;
    const int lane = lane_id();
#pragma unroll 1
    for (int layer = 0; layer < 2; ++layer) {
    unsigned char* hop = (a.ws + WS_HOP) + layer * HOP_BYTES;
    const rsrc_t rfl = MKR(hop, F_ERR);
    unsigned tb = 0, cC1 = 0, cP = 0;
#pragma unroll 1
    for (int t = 0; t < RO_END; ++t) {
        ATV;
        if (t < NSEG_) {
            if (t == 0) wait_set(rfl, F_XP + (r % NXP_) * FLP, hop, 4);
            if (t > 1) rt_wait(tb + cC1 + TT_FP);
            wait_set(rfl, (r >= 40 && r < 68 && t > 2) ? F_UW + (t * 8 + (lane & 7)) * FLP : (r < 64 || (t == 0 && lane >= 32)) ? F_UX + (t * 32 + (lane & 31)) * FLP : F_PX + (t * 64 + (lane & 31) * 2 + ((r >> 1) & 1)) * FLP, hop, t > 2 ? 0 : RSL);
        }
        LBAR();
        LBAR();
        if (t < NSEG_) { const unsigned tpv = *reinterpret_cast<const unsigned*>(lds + RO_TIM + lane * 4); tb = __builtin_amdgcn_readlane(tpv, 0); cC1 = __builtin_amdgcn_readlane(tpv, 1); cP = __builtin_amdgcn_readlane(tpv, 2); }
        ATD(2, t == 42 && r == 0);
    }
    LBAR();
    if (lane == 0) STAMP(512 + blockIdx.x);
    }
}
__device__ __forceinline__ void ro_part(KA& a, unsigned char* lds, const int r, const int wave) {
    if (wave == 0) { lds_u32* tim = (lds_u32*)(lds + RO_TIM + 256); tim[0] = 0; tim[1] = 0; tim[2] = 0; }
    if (wave == 0) { __builtin_amdgcn_s_setprio(3); ro_state(a, lds, r, page_pol(a)); }
    else if (wave == 1) ro_time(a, lds, r, page_pol(a));
    else if (wave == 2) ro_out<0>(a, lds, r, 0u);
    else if (wave == 3) { init_wait(a); ro_fill(a, lds, r); }
    else if (wave == 4) ro_out<1>(a, lds, r, 0u);
    else if (wave == 7) ro_out<2>(a, lds, r, 0u);
    else if (wave == 6) { const unsigned pol = page_pol(a); ro_out<3>(a, lds, r, um_base(um_sides(a), pol, r >> 1) + lane_id() * 16); }
    else {
        const rsrc_t rz = MKR((a.ws + WS_HOP) + HOP_BYTES, HOP_BYTES);
        for (unsigned o = r * 1024 + lane_id() * 16; o < HOP_BYTES; o += NR_ * 1024) st16c(rz, o, (u32x4){0u, 0u, 0u, 0u});
        VMDRAIN();
        if (r == 0) ro_master(a); else for (int q = 0; q < 4 * NSEG_ + 2; ++q) __builtin_amdgcn_s_barrier(); }
}

#define KVS 1
#define AW1 1
#define AWS 0
#define QE 3
#define XKB 4
#define XSL 0
#define VMKNOWN5() __builtin_amdgcn_s_waitcnt(0x0F75)
#define PIM 60
#define XNP 3
constexpr int XB = 40960;
struct XWt { u32x4 w[2][20]; f32x4 cs[2]; };
template <int NB, int NR, class G, class H> __device__ __forceinline__ void x_load(XWt& x, unsigned char* lds, const int wave, const int pw, G&& src, H&& more) {
    const unsigned ldsb = LDSB(), lane = lane_id(), l15 = lane & 15, lg = lane >> 4;
    load_b<NB, 4, NR, 0, 0>(ldsb, wave, src, [&](int rd, int base) { if (rd >= 10) more(rd, base); else if (pw >= 0) {
#pragma unroll
        for (int u = 0; u < 2; ++u)
#pragma unroll
            for (int k = 0; k < 2; ++k) x.w[u][(2 * rd + k) % 20] = ld_frag(lds + base + (pw >> 1) * 8192, 32 * k + 8 * lg, 4, (pw & 1) * 2 + u, l15); } }, [](int) {});
    if (pw >= 0) {
        const u32x4 one = (u32x4){0x3F803F80u, 0x3F803F80u, 0x3F803F80u, 0x3F803F80u};
        x.cs[0] = Z4; x.cs[1] = Z4;
#pragma unroll
        for (int k = 0; k < 20; ++k) { x.cs[0] = dmf(x.w[0][k], one, x.cs[0]); x.cs[1] = dmf(x.w[1][k], one, x.cs[1]); }
        PIN(x.cs[0]); PIN(x.cs[1]);
    }
}
__device__ __forceinline__ void c_load(u32x2 (&sv)[4], rsrc_t rx, int t) {
    const int lane = lane_id();
    asm volatile("s_nop 4\n\tbuffer_load_dwordx2 %0, %4, %5, %6 offen sc1\n\tbuffer_load_dwordx2 %1, %4, %5, %6 offen offset:128 sc1\n\tbuffer_load_dwordx2 %2, %4, %5, %6 offen offset:256 sc1\n\tbuffer_load_dwordx2 %3, %4, %5, %6 offen offset:384 sc1"
        : "=&v"(sv[0]), "=&v"(sv[1]), "=&v"(sv[2]), "=&v"(sv[3]) : "v"((lane >> 4) * 512 + (lane & 15) * 8), "s"(rx), "s"(t * XI_PITCH + XI_S8) : "memory");
}
__device__ __forceinline__ void c_got(u32x2 (&sv)[4]) { asm volatile("" : "+v"(sv[0]), "+v"(sv[1]), "+v"(sv[2]), "+v"(sv[3])); }
__device__ __forceinline__ void c_put(const u32x2 (&sv)[4], unsigned char* dst) {
    float s[2];
#pragma unroll
    for (int q = 0; q < 2; ++q) s[q] = adds(adds(__uint_as_float(sv[0][q]), __uint_as_float(sv[1][q])), adds(__uint_as_float(sv[2][q]), __uint_as_float(sv[3][q])));
    asm volatile("s_nop 1" : "+v"(s[0]), "+v"(s[1]));
    const auto a1 = __builtin_amdgcn_permlane16_swap(__float_as_uint(s[0]), __float_as_uint(s[0]), false, false), a2 = __builtin_amdgcn_permlane16_swap(__float_as_uint(s[1]), __float_as_uint(s[1]), false, false);
    float m, i; ln_consts(adds(__uint_as_float(a1[0]), __uint_as_float(a1[1])), __uint_as_float(a2[0]) + __uint_as_float(a2[1]), m, i);
    const int lane = lane_id();
    *reinterpret_cast<f32x2*>(dst + (lane >> 5) * 128 + (lane & 15) * 8) = (f32x2){-m, i};
}
template <class F> __device__ __forceinline__ void x_proj(const XWt& x, const unsigned char* xb, const unsigned char* cb, u32x4 (&o)[2], F&& mid) {
    const int lane = lane_id();
    unsigned l16 = lane * 16, c8 = (lane & 15) * 8; PIN(l16); PIN(c8);
    const unsigned char* xp = xb + l16;
    f32x4 acc[2][2] = {{Z4, Z4}, {Z4, Z4}};
    u32x4 xf[3][2]; f32x2 ct[2];
#pragma unroll
    for (int k = 0; k < 2; ++k) { xf[k][0] = lds16(xp + k * 1024); SBAR(); xf[k][1] = lds16(xp + 20480 + k * 1024); SBAR(); }
#pragma unroll
    for (int k = 0; k < 20; ++k) {
#pragma unroll
        for (int j = 0; j < 4; ++j) { acc[j >> 1][j & 1] = dmf(x.w[j >> 1][k], xf[k % 3][j & 1], acc[j >> 1][j & 1]); PIN(acc[j >> 1][j & 1]); SBAR();
            if ((j == 1 || j == 2) && k + 2 < 20) { xf[(k + 2) % 3][j - 1] = lds16(xp + (j - 1) * 20480 + (k + 2) * 1024); SBAR(); }
            if (j == 1 && k == 18) { ct[0] = *reinterpret_cast<const f32x2*>(cb + c8); ct[1] = *reinterpret_cast<const f32x2*>(cb + c8 + 128); SBAR(); } }
        mid(k, l16);
    }
#pragma unroll
    for (int rt = 0; rt < 2; ++rt)
#pragma unroll
        for (int q = 0; q < 4; ++q) { const int u = q >> 1, e = 2 * (q & 1);
            o[rt][q] = pk2(__builtin_fmaf(ct[rt][0], x.cs[u][e], acc[u][rt][e]) * ct[rt][1], __builtin_fmaf(ct[rt][0], x.cs[u][e + 1], acc[u][rt][e + 1]) * ct[rt][1]); }
}
__device__ __forceinline__ void kv_part(KA& a, unsigned char* lds, const int i, const int wave) {
    const int lane = lane_id(), l15 = lane & 15, lg = lane >> 4;
    const rsrc_t rk = MKR(a.ws + WS_KVP, 32 * 16 * 2048), ro = MKR(a.ws + WS_KVF + i * 2048, 2048);
    if (wave == 5) st8c(ro, (lane & 31) <= 20 ? (lane >> 5) * 1024 + (lane & 31) * 48 + 40 : OOB, (u32x2){0u, 0u});
    if (wave == 7) wait_set(MKR(a.ws + WS_HOP, F_ERR), F_QK + (l15 * 8 + (i >> 1)) * FLP, a.ws + WS_HOP, KVS);
    LBAR();
    if (wave == 0 && lane == 0) { const int layer = 0; STAMP(1600 + i); }
    u32x4 v[8];
#pragma unroll
    for (int q = 0; q < 8; ++q) v[q] = __builtin_amdgcn_raw_buffer_load_b128(rk, (q & 1) && lane >= 56 ? OOB : lane * 16 + (q & 3) * 1024, (i * 16 + wave * 2 + (q >> 2)) * 4096, 16);
#pragma unroll
    for (int q = 0; q < 2; ++q) *reinterpret_cast<f32x4*>(lds + 65536 + ((wave >> 2) * 8 + q * 4 + (wave & 3)) * 1024 + lane * 16) = (__builtin_bit_cast(f32x4, v[q]) + __builtin_bit_cast(f32x4, v[q + 2])) + (__builtin_bit_cast(f32x4, v[q + 4]) + __builtin_bit_cast(f32x4, v[q + 6]));
    LBAR();
    if (wave < 4) { const f32x4* p = reinterpret_cast<const f32x4*>(lds + 65536 + wave * 4096 + lane * 16);
        const f32x4 s = (p[0] + p[64]) + (p[128] + p[192]);
        const int e = !(wave & 1) ? l15 : (lane < 32 ? 16 + (lane & 7) : lane - 32), d = !(wave & 1) ? 4 * lg : (lane < 32 ? 4 * (lane >> 3) : 16);
        st8c(ro, e <= 20 ? (wave >> 1) * 1024 + e * 48 + d * 2 : OOB, (u32x2){min(pk2(s[0], s[1]), 0xfffffffeu), min(pk2(s[2], s[3]), 0xfffffffeu)}); }
    if (wave == 0 && lane == 0) { const int layer = 0; STAMP(1620 + i); }
}
__device__ __forceinline__ void xp_part(KA& a, unsigned char* lds, const int j, const int wave) {
    const int lane = lane_id(), tid = wave * 64 + lane;
    const unsigned ldsb = LDSB();
    const int hp = j / 3, kind = j - 3 * hp;
    XWt x;
    {
        const rsrc_t rk = MKR(psel(a.wk, a.wv, kind >> 1), D_ * HE_ * 2), rv = MKR(a.wv, D_ * HE_ * 2);
        const unsigned row = 8 * wave + (lane >> 3), vo = row * (HE_ * 2) + ((lane & 7) ^ (wsw(row) << 1)) * 16 + hp * 768;
        x_load<4, 10>(x, lds, wave, wave, [&](int rd, int q, unsigned dst) { dma16(q < 3 ? rk : rv, vo, rd * (64 * HE_ * 2) + (kind < 2 ? kind * 384 + (q % 3) * 128 : (q >> 1) * 384 + 128 + (q & 1) * 128), dst); }, [](int, int) {});
    }
    const unsigned oo = kind < 2 ? (wave < 6 ? ST_KX + wave * 1024 : ST_VX + (wave - 6) * 1024) + (2 * hp + kind) * 12288 : ST_VX + (2 * hp + (wave >> 2)) * 12288 + (2 + (wave & 3)) * 1024;
    const rsrc_t rst = MKR((a.ws + WS_ST), ST_BYTES), rx = MKR((a.ws + WS_X), XI_BYTES);
    const int wr_ = (wave + (blockIdx.x >> 3) * ROT) & 7;
#pragma unroll 1
    for (int layer = 0; layer < 2; ++layer) {
    unsigned char* hop = (a.ws + WS_HOP) + layer * HOP_BYTES;
    const rsrc_t rfl = MKR(hop, F_ERR);
    auto piece = [&](int t, int b, int k, unsigned l16) { const unsigned o = (wr_ * 5 + k) * 1024; dma16c(rx, l16, (unsigned)t * XI_PITCH + XI_X + o, ldsb + b * XB + o); };
    auto x_dma = [&](int t, int b) {
#pragma unroll
        for (int k = 0; k < 5; ++k) piece(t, b, k, lane * 16); };
    if (tid == 0 && j == 0) STAMP(7);
    if (wave == 2) { if (!layer) { init_wait(a); wait_set(rfl, F_PQ + (64 + j) * FLP, hop, 4); } wait_set(rfl, F_AF + (lane & 15) * FLP, hop, XSL); }
    LBAR();
    if (tid == 0 && j == 0) STAMP(8);
    { u32x2 sv[4]; if (wave == 1) c_load(sv, rx, 0);
      x_dma(0, 0);
      if (wave == 1) { VMW(5); c_got(sv); c_put(sv, lds + 3 * XB); } }
    VMKNOWN(); LBAR();
    unsigned tnext = rt_now(); int b = 0;
#pragma unroll 1
    for (int t = 0; t < NSEG_; ++t) {
        ATV;
        AT(0);
        u32x4 fl; u32x2 sv[4];
        const unsigned fo = (lane < 16 && t >= XLD) ? F_KM + ((t - XLD) * 32 + 2 * hp) * FLP : F_AF + (min(t + (lane < 16 ? 1 : 2), NSEG_ - 1) * 16 + (lane & 15)) * FLP;
        if (wave == 2) { if (t > XLD) rt_wait(tnext);
            asm volatile("buffer_load_dwordx4 %0, %1, %2, 0 offen sc1" : "=&v"(fl) : "v"(fo), "s"(rfl) : "memory"); }
        const int bn = b == 0 ? 2 : b - 1;
        u32x4 o[2];
        x_proj(x, lds + b * XB, lds + 3 * XB + (t & 1) * 256, o, [&](int k, unsigned l16) {
            if (k == XKB) { if (wave == 2) VMW(1); else VMDRAIN();
                __builtin_amdgcn_s_barrier(); CBAR();
                if (wave == 0 && t > 0 && l16 == 0) { st4c(rfl, F_XP + ((t - 1) * NXP_ + j) * FLP, 1u); if (j == 0 && t < 4) STAMP(t); if (j == 7 && t < 12) STAMP(2100 + t); }
                AT(2); SBAR(); }
            if (k == XKB + 3) {
                if (wave == 2) { VMDRAIN(); asm volatile("" : "+v"(fl));
                    if (__builtin_expect(__all(fl.x != 0u), 1)) tnext = __builtin_amdgcn_readfirstlane(fl.w) + XNP * __builtin_amdgcn_readfirstlane(fl.x) + TT_KD; else { wait_timed(rfl, fo, hop, tnext, XNP, TT_KD, t < XLD); VMKNOWN(); } }
                __builtin_amdgcn_s_barrier(); CBAR();
                if (wave == 1) c_load(sv, rx, min(t + 1, NSEG_ - 1));
                if (t == 0) x_dma(1, 1);
                SBAR(); }
            if (k > XKB + 3 && k <= XKB + 13 && ((k - XKB) & 1) == 0 && t + 2 < NSEG_) { piece(t + 2, bn, (k - XKB - 4) >> 1, l16); SBAR(); } });
        const int ln = lane_id();
        st16s(o[0], rst, ln * 16, oo + (t & (KXR - 1)) * (32 * 12288)); st16s(o[1], rst, ln * 16, oo + (t & (KXR - 1)) * (32 * 12288) + 6144);
        AT(1);
        if (wave == 1) { if (t + 2 < NSEG_) VMW(7); else VMW(2); c_got(sv); c_put(sv, lds + 3 * XB + ((t + 1) & 1) * 256); asm volatile("s_waitcnt lgkmcnt(0)" ::: "memory"); }
        if (t == 0) { VMW(7); LBAR(); }
        ATD(3, t == 48 && j == 0);
        b = b == 2 ? 0 : b + 1;
    }
    VMDRAIN(); LBAR();
    if (tid == 0) st4c(rfl, F_XP + ((NSEG_ - 1) * NXP_ + j) * FLP, 1u);
    if (tid == 0) STAMP(512 + blockIdx.x);
    }
}

template <int AT_> __device__ __forceinline__ void xq_run(KA& a, unsigned char* lds, const int h, const int wave) {
    constexpr bool ATT = AT_ >= 0; constexpr int qt = ATT ? AT_ : 0;
    const int lane = lane_id(), tid = wave * 64 + lane;
    const unsigned ldsb = LDSB();
    constexpr int L_C = 2 * XB, L_Q = L_C + 512, L_VX = L_Q + (QA + 1) * 12288;
    const int pw = AW1 == 1 ? wave - 2 : wave - 1 - (wave >> 2);
    XWt x; u32x4 wo[4][6];
    {
        const rsrc_t rq = MKR(a.wq, D_ * HE_ * 2), ro = MKR(a.wo, HE_ * D_ * 2);
        const unsigned row = 8 * wave + (lane >> 3), ch = ((lane & 7) ^ (wsw(row) << 1)) * 16, vo = row * (HE_ * 2) + ch + h * 384, oo = (h * 192 + row) * (D_ * 2) + ch;
        x_load<3, 12>(x, lds, wave, ATT ? -1 : pw, [&](int rd, int q, unsigned dst) { if (rd < 10) dma16(rq, vo, rd * (64 * HE_ * 2) + q * 128, dst); else dma16(ro, oo, q * (64 * D_ * 2) + (rd - 2) * 128, dst); },
            [&](int rd, int base) { if (ATT) {
#pragma unroll
                for (int q = 0; q < 12; ++q) wo[2 * (rd & 1) + q / 6][q % 6] = ld_frag(lds + base + ((q % 6) >> 1) * 8192, (q & 1) * 32 + 4 * (lane >> 4), 16, qt + 2 * (q / 6), lane & 15); } });
    }
    const rsrc_t rst = MKR((a.ws + WS_ST), ST_BYTES), rx = MKR((a.ws + WS_X), XI_BYTES);
    const int wr_ = (pw + (blockIdx.x >> 3)) % 6, xj = xp_of(h), xj2 = 3 * (h >> 1) + 2;
    const int l15 = lane & 15, lg = lane >> 4;
    const bool cm[4] = {4 * lg <= l15, 4 * lg + 1 <= l15, 4 * lg + 2 <= l15, 4 * lg + 3 <= l15};
#pragma unroll 1
    for (int layer = 0; layer < 2; ++layer) {
    unsigned char* hop = (a.ws + WS_HOP) + layer * HOP_BYTES;
    const rsrc_t rfl = MKR(hop, F_ERR);
    auto piece = [&](int t, int k, unsigned l16) { const unsigned o = min(wr_ * 7 + k, 39) * 1024; dma16c(rx, l16, (unsigned)t * XI_PITCH + XI_X + o, ldsb + (t & 1) * XB + o); };
    if (wave == 0) { if (!layer) init_wait(a); wait_set(rfl, F_AF + (lane & 31) * FLP, hop, 2); }
    LBAR();
    if (!ATT) {
#pragma unroll
        for (int k = 0; k < 7; ++k) piece(0, k, lane * 16); }
    u32x4 kf[2][6];
    if (AT_ == 0) { u32x2 sv[4]; c_load(sv, rx, 0); VMDRAIN(); c_got(sv); c_put(sv, lds + L_C); }
    VMKNOWN();
    unsigned tnext = rt_now(); int qs = 0; bool rdy = false, owe = false;
#pragma unroll 1
    for (int i = 0; i < NSEG_ + QA; ++i) {
        ATV;
        if (i <= NSEG_) LBAR();
        AT(0);
        if (AT_ == 1 && h == 5 && i < 10 && lane == 0) STAMP(2000 + i * 4);
        if (!ATT) {
            if (i < NSEG_) {
                u32x4 o[2];
                x_proj(x, lds + (i & 1) * XB, lds + L_C + (i & 1) * 256, o, [&](int k, unsigned l16) { if ((k & 1) && k < 14 && i + 1 < NSEG_) { piece(i + 1, k >> 1, l16); SBAR(); } });
                unsigned char* qp = lds + L_Q + qs * 12288 + pw * 1024 + lane_id() * 16;
                *reinterpret_cast<u32x4*>(qp) = o[0]; *reinterpret_cast<u32x4*>(qp + 6144) = o[1];
                AT(1);
                VMDRAIN();
            }
        } else {
            const int t = i - QA;
            const unsigned fa = F_AF + (min(i + 2, NSEG_ - 1) * 16 + l15) * FLP, fx = F_XP + ((lane & 1) ? xj : xj2) * FLP, ub = ST_U + (((i & (URING - 1)) * 40 + 32 + qt) * 32 + h) * 1024;
            unsigned l16 = lane_id() * 16; PIN(l16);
            auto v_dma = [&]() { dma16cn<6>(rst, l16, ST_VX + (((i + 1) & (KXR - 1)) * 32 + h) * 12288 + qt * 6144, ldsb + L_VX + ((i + 1) & 1) * 12288 + qt * 6144); };
            auto next = [&]() {
                const unsigned kb = ST_KX + (((t + 1) & (KXR - 1)) * 32 + h) * 12288;
#pragma unroll
                for (int m = 0; m < (qt ? 12 : 6); ++m) asm volatile("buffer_load_dwordx4 %0, %1, %2, %3 offen offset:%4 sc1" : "=&v"(kf[m / 6][m % 6]) : "v"(l16), "s"(rst), "s"(kb + (m >> 2) * 4096), "i"((m & 3) * 1024) : "memory"); };
            auto got = [&]() {
#pragma unroll
                for (int m = 0; m < (qt ? 12 : 6); ++m) PIN(kf[m / 6][m % 6]); };
            u32x4 vf[12]; u32x2 sv[4];
            auto u_from = [&](int u0) {
                f32x4 acc[4][2];
#pragma unroll
                for (int j = 0; j < 48; ++j) { const int k = j >> 3, u = (j >> 1) & 3, r = j & 1; if (u >= u0) acc[u][r] = dmf(vf[r * 6 + k], wo[u][k], k ? acc[u][r] : Z4); }
                SBAR();
#pragma unroll
                for (int u = u0; u < 4; ++u) st16s((u32x4){pk2(acc[u][0][0], acc[u][1][0]), pk2(acc[u][0][2], acc[u][1][2]), pk2(acc[u][0][1], acc[u][1][1]), pk2(acc[u][0][3], acc[u][1][3])}, rst, l16, ub + u * 65536);
                SBAR(); };
            auto out_then = [&](int n) {
                const unsigned v = ld4c(rfl, fx + n * (NXP_ * FLP)); VMDRAIN();
                if (l16 == 0) st4c(rfl, F_PX + (i * 64 + h * 2 + qt) * FLP, 1u);
                if (!__all(v != 0u)) wait_set(rfl, fx + n * (NXP_ * FLP), hop, 0); };
            if (t < 0) {
                if (!qt) c_load(sv, rx, i + 1);
                wait_set(rfl, lane < 16 ? fx + i * (NXP_ * FLP) : fa, hop, 1);
#pragma unroll
                for (int m = 0; m < 12; ++m) vf[m] = __builtin_amdgcn_raw_buffer_load_b128(rst, l16 + (m & 3) * 1024, ST_VX + (i * 32 + h) * 12288 + (m >> 2) * 4096, 16);
                SBAR();
                if (!qt) { c_got(sv); c_put(sv, lds + L_C + ((i + 1) & 1) * 256); SBAR(); }
                u_from(0);
                next();
                out_then(t == -1 ? i + 1 : i);
                if (t == -1) { v_dma(); VMDRAIN(); }
                got();
            } else {
                const unsigned char* qp = lds + L_Q + (qs == QA ? 0 : qs + 1) * 12288 + qt * 6144 + l16;
                u32x4 qf[6], km[6], fl;
                const bool uu = i < NSEG_;
                if (!qt) c_load(sv, rx, min(i + 1, NSEG_ - 1));
                const bool er = !rdy;
                if (uu && !er) { v_dma(); VMW(qt ? 6 : 10); } else VMW(qt ? 0 : 4);
                got();
                __builtin_amdgcn_raw_buffer_store_b32(1u, rfl, owe ? l16 << 20 : OOB, F_PX + ((i - 1) * 64 + h * 2 + qt) * FLP, 16);
                if (l16 == 0 && h == 0 && qt == 1 && t > 56) STAMP(340 + t - 57);
                AT(1);
                const unsigned fo = lane < 16 ? F_KM + (t * 32 + h) * FLP : (lane < 32 ? fx + min(i + 2, NSEG_ - 1) * (NXP_ * FLP) : fa);
                const int dl = t + 5 >= NSEG_ ? TT_QN : TT_QD;
#pragma unroll
                for (int m = 0; m < 6; ++m) qf[m] = lds16(qp + m * 1024);
                rt_wait(tnext);
                AT(5);
                asm volatile("buffer_load_dwordx4 %0, %1, %2, 0 offen sc1" : "=&v"(fl) : "v"(fo), "s"(rfl) : "memory");
                f32x4 sc[3] = {Z4, Z4, Z4};
#pragma unroll
                for (int m = 0; m < 6; ++m) { sc[0] = dmf(kf[0][m], qf[m], sc[0]); if (qt) sc[1] = dmf(kf[1][m], qf[m], sc[1]); }
                float s2[10];
#pragma unroll
                for (int q = 0; q < 8; ++q) s2[q] = (q >> 2) > qt ? 0.f : (((q >> 2) < qt || cm[q & 3]) ? sc[q >> 2][q & 3] : -3.0e38f);
                float mx = max3f(max3f(s2[0], s2[1], s2[2]), s2[3], qt ? max3f(max3f(s2[4], s2[5], s2[6]), s2[7], s2[7]) : s2[3]);
#pragma unroll
                for (int q = 0; q < (qt ? 8 : 4); ++q) PIN(s2[q]);
                PIN(mx);
                if (!qt && uu) { if (er) VMW(2); else VMW(8); c_got(sv); c_put(sv, lds + L_C + ((i + 1) & 1) * 256); }
#pragma unroll
                for (int m = 0; m < 12; ++m) vf[m] = lds16(lds + L_VX + (i & 1) * 12288 + l16 + m * 1024);
                SBAR();
                asm volatile("s_waitcnt vmcnt(0)" : "+v"(fl) :: "memory");
                const unsigned long long fz = __ballot(fl.x == 0u);
                rdy = !(fz & 0xffff0000ull);
                if (!(fz & ~0xffff0000ull)) tnext = __builtin_amdgcn_readfirstlane(fl.w) + 2u * __builtin_amdgcn_readfirstlane(fl.x) + dl; else wait_timed(rfl, lg == 1 ? fa : fo, hop, tnext, 2, dl, t + 4 >= NSEG_ || t < QE);
                AT(2);
                if (l16 == 0 && h == 0 && qt == 0 && t >= 56) STAMP(360 + t - 56);
                const unsigned mo = (lg * 8 + (((l15 >> 2) * 2 + (l15 & 1) + (lg & 1) * 4) & 7)) * 16;
#pragma unroll
                for (int m = 0; m < 6; ++m) asm volatile("buffer_load_dwordx4 %0, %1, %2, %3 offen offset:%4 sc1" : "=&v"(km[m]) : "v"(mo), "s"(rst), "s"(ST_KM + ((t & (PR - 1)) * 32 + h) * 3072), "i"(m * 512) : "memory");
                if (uu) { u_from(1); VMW(3); } else VMDRAIN();
                asm volatile("" : "+v"(km[0]), "+v"(km[1]), "+v"(km[2]), "+v"(km[3]), "+v"(km[4]), "+v"(km[5]));
#pragma unroll
                for (int m = 0; m < 6; ++m) sc[2] = dmf(km[m], qf[m], sc[2]);
                s2[8] = sc[2][0]; s2[9] = sc[2][1]; mx = max3f(mx, s2[8], s2[9]);
                constexpr float c1 = 0.07216878364870322f * 1.4426950408889634f;
                mx = xg_max(mx) * -c1;
                float sum = 0.f, su1 = 0.f;
#pragma unroll
                for (int q = 0; q < 10; ++q) { if (qt == 0 && q >= 4 && q < 8) continue; s2[q] = __builtin_amdgcn_exp2f(__builtin_fmaf(s2[q], c1, mx)); if (q & 1) su1 += s2[q]; else sum += s2[q]; }
                float inv = __builtin_amdgcn_rcpf(xg_sum(sum + su1));
                asm volatile("s_nop 0" : "+v"(inv));
#pragma unroll
                for (int q = 0; q < 10; ++q) { if (qt == 0 && q >= 4 && q < 8) continue; s2[q] = muls(s2[q], inv); }
                const unsigned pb = ST_PX + ((t & (PR - 1)) * 2 + qt) * 40960;
                st16s((u32x4){pk2(s2[0], s2[4]), pk2(s2[2], s2[6]), pk2(s2[1], s2[5]), pk2(s2[3], s2[7])}, rst, l16, pb + h * 1024);
                __builtin_amdgcn_raw_buffer_store_b32(pk2(s2[8], s2[9]), rst, l15 * 16 + (lg & 1) * 8 + (lg >> 1) * 4, pb + 32768 + h * 256, 16);
                AT(3);
                if (t + 1 == NSEG_) { VMDRAIN(); if (l16 == 0) st4c(rfl, F_PX + (i * 64 + h * 2 + qt) * FLP, 1u); if (l16 == 0 && h == 0 && qt == 1) STAMP(347); }
                owe = !(er && !rdy);
                if (!owe) out_then(min(i + 1, NSEG_ - 1));
                if (er) v_dma();
                next();
                if (er) VMW(qt ? 12 : 6);
                AT(4);
            }
        }
        ATD(4, i == 44 && h == 0);
        qs = qs == QA ? 0 : qs + 1;
    }
    VMDRAIN(); LBAR();
    if (tid == 0) STAMP(512 + blockIdx.x);
    }
}
__device__ __forceinline__ void xq_part(KA& a, unsigned char* lds, const int h, const int wave) {
    if (wave == 0 || wave == AW1) __builtin_amdgcn_s_setprio(1);
    if (wave == (AWS ? AW1 : 0)) xq_run<0>(a, lds, h, wave); else if (wave == (AWS ? 0 : AW1)) xq_run<1>(a, lds, h, wave); else xq_run<-1>(a, lds, h, wave);
}

__device__ __forceinline__ void q_part(KA& a, unsigned char* lds, const int rb, const int g, const int wave) {
    constexpr int STG = 26624, NS = 5, layer = 0;
    const int lane = lane_id(), l15 = lane & 15, lg = lane >> 4, rp = wave & 1, cg = wave >> 1;
    const unsigned ldsb = LDSB();
    const rsrc_t rw = MKR(a.wqkv, D_ * 1920 * 2), rx = MKR(a.x0, S_ * D_ * 2);
    unsigned vo[4];
    vo[0] = (rb * 128 + wave * 16 + (lane >> 2)) * (D_ * 2) + ((lane ^ -lg) & 3) * 16;
#pragma unroll
    for (int u = 0; u < 3; ++u) { const int p = wave + 8 * u, c = p * 16 + (lane >> 2), row = c / 9, e = c - row * 9, sg = e / 3;
        vo[u + 1] = p < 18 ? row * 3840 + sg * 1280 + ((160 * g) & ~63) + (e - sg * 3) * 64 + ((lane & 3) ^ ((row >> 2) & 2)) * 16 : OOB; }
    const unsigned m3 = wave < 2 ? ~0u : 0u, d3 = wave < 2 ? 24576 : NS * STG;
    const int rot = (rb + (g & 1) * 5) % 10;
    auto piece = [&](int st, unsigned sl, int j) { dma16(j ? rw : rx, vo[j], st * (j ? 32 * 3840 : 64), ldsb + wave * 1024 + (j < 3 ? sl + j * 8192 : (sl & m3) + d3)); };
#pragma unroll 1
    for (int st = 0; st < NS - 1; ++st) {
#pragma unroll
        for (int j = 0; j < 4; ++j) piece((st + 2 * rot) % 20, st * STG, j); }
    if (wave == 0 && lane == 0 && rb == 0 && g == 0) STAMP(17);
    f32x4 acc[4][4], gr = Z4, cs[4] = {Z4, Z4, Z4, Z4};
#pragma unroll
    for (int i = 0; i < 16; ++i) acc[i >> 2][i & 3] = Z4;
    u32x4 one = (u32x4){0x3F803F80u, 0x3F803F80u, 0x3F803F80u, 0x3F803F80u}; PIN(one);
    unsigned wp[4]; const unsigned xp = (rp * 4 + cg) * 1024 + l15 * 64 + ((lg ^ -(l15 >> 2)) & 3) * 16;
#pragma unroll
    for (int u = 0; u < 4; ++u) { const int T = min(4 * u + cg, 14); wp[u] = 8192 + (8 * lg + (l15 >> 2)) * 576 + (((T / 5) * 192 + (T % 5 + (g & 1)) * 32) ^ ((lg & 1) * 32)) + (l15 & 3) * 8; }
    const unsigned w3 = cg == 3 ? 0u : ~0u;
    if (cg == 3) { wp[3] = NS * STG + 12288; sts16a(wp[3], one); sts16a(wp[3] + 4 * 576, one); }
    u32x4 xf[2][4], wf[2][4];
    auto rw_ = [&](int kk, unsigned sl, int u) { const unsigned char* p = lds + (u == 3 ? sl & w3 : sl) + wp[u]; const u32x2 x0 = ldstr(p), x1 = ldstr(p + 4 * 576); wf[kk][u] = (u32x4){x0.x, x0.y, x1.x, x1.y}; SBAR(); };
    auto rx_ = [&](int kk, unsigned sl, int i) { xf[kk][i] = lds16(lds + sl + (xp ^ (i * 1024))); SBAR(); };
    VMW(12);
    LBAR();
    if (wave == 0 && lane == 0 && rb == 0 && g == 0) STAMP(14);
#pragma unroll
    for (int j = 0; j < 8; ++j) { if (j < 4) rx_(0, 0, j); else rw_(0, 0, j - 4); }
    unsigned sn = STG, sf = (NS - 1) * STG;
    int cf = (rot + 2) % 10; const int cb = (rb + 10 - rot) % 10 + (rb < 10 ? 0 : 16);
#pragma unroll 1
    for (int ch = 0; ch < 10; ++ch) {
#pragma unroll
        for (int kk = 0; kk < 2; ++kk) {
            AT(3 * kk);
            VMW(8);
            __builtin_amdgcn_s_barrier(); CBAR();
            AT(3 * kk + 1);
            gr = dmf(xf[kk][0], xf[kk][0], gr); PIN(gr); SBAR();
#pragma unroll
            for (int j = 0; j < 16; ++j) { const int u = j >> 2, i = j & 3;
                acc[i][u] = dmf(wf[kk][u], xf[kk][i], acc[i][u]); PIN(acc[i][u]); SBAR();
                if (!(j & 1)) { if (j < 8) rx_(kk ^ 1, sn, j >> 1); else rw_(kk ^ 1, sn, (j >> 1) - 4); }
                if (i == 1) { piece(2 * cf + kk, sf, u); SBAR(); } }
            if (rp) { cs[0] = dmf(wf[kk][2], one, cs[0]); cs[1] = dmf(wf[kk][3], one, cs[1]); } else { cs[0] = dmf(wf[kk][0], one, cs[0]); cs[1] = dmf(wf[kk][1], one, cs[1]); }
            if (__builtin_expect(ch == cb && kk == rp, 0)) {
#pragma unroll
                for (int u = 0; u < 4; ++u) { const int T = 4 * u + cg; st16s(wf[kk][u], MKR(a.ws + WS_WF_AQKV, D_ * 1920 * 2), T < 15 ? lane * 16 : OOB, (((T / 5) * 40 + 5 * g + T % 5) * 20 + 2 * rb + kk) * 1024); } }
            SBAR();
            sn = sn == (NS - 1) * STG ? 0 : sn + STG; sf = sf == (NS - 1) * STG ? 0 : sf + STG;
        }
        cf = cf == 9 ? 0 : cf + 1;
        if (ch == 7) { vo[0] = vo[1] = vo[2] = vo[3] = OOB; }
        AT(6);
        ATD(6, ch == 5 && rb == 0 && g == 0);
    }
    if (wave == 0 && lane == 0 && rb == 0 && g == 0) STAMP(15);
    constexpr int KP = 176, L_K = 0, L_V = L_K + 128 * KP, L_Q = L_V + 128 * KP + 64, L_S = NS * STG + 8192, L_CS = L_S + 1024;
    const int row = rp * 64 + cg * 16 + l15;
    if (lg == (l15 >> 2)) *reinterpret_cast<float*>(lds + L_S + row * 8 + 4) = (l15 & 2) ? ((l15 & 1) ? gr[3] : gr[2]) : ((l15 & 1) ? gr[1] : gr[0]);
    if (cg == 3) *reinterpret_cast<float*>(lds + L_S + (row ^ (lg * 16)) * 8) = lg < 2 ? (lg ? acc[1][3][0] : acc[0][3][0]) : (lg == 2 ? acc[2][3][0] : acc[3][3][0]);
    if (l15 < 2) *reinterpret_cast<f32x4*>(lds + L_CS + wave * 128 + l15 * 64 + lg * 16) = l15 ? cs[1] : cs[0];
    VMDRAIN(); LBAR();
    f32x2 ct[4];
    {
        const f32x2 sv = *reinterpret_cast<const f32x2*>(lds + L_S + (row ^ (lg * 16)) * 8);
#pragma unroll
        for (int u = 0; u < 4; ++u) cs[u] = *reinterpret_cast<const f32x4*>(lds + L_CS + cg * 256 + u * 64 + lg * 16);
        float m, iv; ln_consts(sv[0], sv[1], m, iv); m = -m;
        const auto m2 = __builtin_amdgcn_permlane32_swap(__float_as_uint(m), __float_as_uint(m), false, false), i2 = __builtin_amdgcn_permlane32_swap(__float_as_uint(iv), __float_as_uint(iv), false, false);
#pragma unroll
        for (int z = 0; z < 2; ++z) { const auto x = __builtin_amdgcn_permlane16_swap(m2[z], m2[z], false, false), y = __builtin_amdgcn_permlane16_swap(i2[z], i2[z], false, false);
            ct[2 * z] = (f32x2){__uint_as_float(x[0]), __uint_as_float(y[0])}; ct[2 * z + 1] = (f32x2){__uint_as_float(x[1]), __uint_as_float(y[1])}; }
    }
    if (wave == 0 && lane == 0 && rb == 0 && g == 0) STAMP(18);
    auto tile = [&](int u, bool ph, unsigned char* dst, int pitch) {
#pragma unroll
        for (int i = 0; i < 4; ++i) { f32x4 y = acc[i][u] + ct[i][0] * cs[u];
            if (ph) { y = y * ct[i][1] + 1e-6f; const f32x4 z = y * 1.4426950408889634f - 1.4426950408889634e-6f;
#pragma unroll
                for (int r = 0; r < 4; ++r) y[r] = __builtin_amdgcn_exp2f(__builtin_amdgcn_fmed3f(z[r], 0.f, -3.0e38f)) + __builtin_amdgcn_fmed3f(y[r], 1e-6f, 3.0e38f); }
            else y *= ct[i][1];
            *reinterpret_cast<u32x2*>(dst + (i ^ cg) * pitch) = (u32x2){pk2(y[0], y[1]), pk2(y[2], y[3])}; } };
    unsigned char* kp = lds + L_K + (rp * 64 + l15) * KP + 8 * lg + cg * 32;
    if (cg > 0) tile(1, true, kp - 32, 16 * KP);
    if (cg < 2) tile(2, true, kp + 96, 16 * KP); else tile(2, false, kp + (L_V - L_K) - 64, 16 * KP);
    if (cg < 3) tile(3, false, kp + (L_V - L_K) + 64, 16 * KP);
    LBAR();
    if (wave == 0 && lane == 0 && rb == 0 && g == 0) STAMP(19);
    if (wave >= 4) {
        f32x4 d4[4] = {Z4, Z4, Z4, Z4};
#pragma unroll
        for (int ks = 0; ks < 4; ++ks) {
            u32x4 A[2], B[2];
#pragma unroll
            for (int t = 0; t < 2; ++t) { const unsigned char* p = lds + L_K + (32 * ks + 8 * lg + (l15 >> 2)) * KP + 40 * (wave & 3) + 32 * t + 8 * (l15 & 3);
                const u32x2 a0 = ldstr(p), a1 = ldstr(p + 4 * KP), b0 = ldstr(p + (L_V - L_K)), b1 = ldstr(p + (L_V - L_K) + 4 * KP);
                A[t] = (u32x4){a0.x, a0.y, a1.x, a1.y}; B[t] = (u32x4){b0.x, b0.y, b1.x, b1.y}; }
            if (l15 >= 4) B[1] = one;
#pragma unroll
            for (int q = 0; q < 4; ++q) d4[q] = dmf(A[q >> 1], B[q & 1], d4[q]); }
        const rsrc_t rk = MKR(a.ws + WS_KVP, 32 * 16 * 2048);
#pragma unroll
        for (int q = 0; q < 4; ++q) st16c(rk, ((q < 2 || lg == 0) && (!(q & 1) || l15 < 8)) ? ((4 * g + (wave & 3)) * 16 + rb) * 2048 + (q == 0 ? lg * 256 : (q == 1 ? 1024 + lg * 128 : 1024 + q * 256)) + l15 * 16 : OOB, __builtin_bit_cast(u32x4, d4[q]));
    }
    auto qdst = [&](int T) { const int gi = 4 * T + lg, hd = gi / 5, pt = gi - 5 * hd; return lds + L_Q + rp * 10240 + hd * 640 + (pt < 4 ? 128 + l15 * 32 + pt * 8 : l15 * 8); };
    tile(0, true, qdst(cg), 2560);
    if (cg == 0) tile(1, true, qdst(4), 2560);
    if (wave == 3) boot_wait(a);
    VMDRAIN(); LBAR();
    const rsrc_t rfl = MKR(a.ws + WS_HOP, F_ERR), rq = MKR(a.ws + WS_PQ, S_ * D_ * 2);
    if (wave == 3 && lane == 0) { st4c(rfl, F_QK + (rb * 8 + g) * FLP, 1u); if (rb == 0 && g == 0) STAMP(16); STAMP(1400 + rb * 8 + g); }
#pragma unroll
    for (int j = 0; j < 3; ++j) st16c(rq, (j < 2 || lane < 32) ? (rb * 8 + wave) * 20480 + g * 2560 + j * 1024 + lane * 16 : OOB, lds16(lds + L_Q + wave * 2560 + j * 1024 + lane * 16));
    VMDRAIN(); LBAR();
    if (wave == 3 && lane == 0) st4c(rfl, F_PQ + (rb * 8 + g) * FLP, 1u);
}
__device__ __forceinline__ void boot_part(KA& a, unsigned char* lds, const int i, const int wave) {
    const int lane = lane_id(), l15 = lane & 15, lg = lane >> 4, w = i * 8 + wave;
    const rsrc_t rb = MKR((a.ws + WS_HOP) - 2048, 2048), rz = MKR((a.ws + WS_HOP), F_AF), rh = MKR(al2m((a.ws + WS_FR)), FR_FILL), rf = MKR((a.ws + WS_WF_AO), D_ * D_ * 2);
    const u32x2 tag = ld8c(rb, 0);
    {
        const unsigned c = (w & 7) * 64 + lane, b = ((w >> 4) & 1) * 8 + (c >> 6), m = c & 15u;
        u32x4 v = (u32x4){~0u, ~0u, ~0u, ~0u};
        if (b < 10u) { const unsigned* mp = reinterpret_cast<const unsigned*>(a.mem0 + (2 * (m >> 2) + (m & 1)) * D_ + b * 64 + ((c >> 4) & 3u) * 16 + (m & 2));
            v = (u32x4){min(mp[0], 0xfffffffeu), min(mp[2], 0xfffffffeu), min(mp[4], 0xfffffffeu), min(mp[6], 0xfffffffeu)}; }
        else if (!(c & 3u)) v.x = c == 196u ? TT_E1 + TT_E2 + 40u : TT_E1 + 20u;
        st16c(rh, (w >> 5) * (20u << 14) + (w & 31) * 1024 + lane * 16, v);
    }
    st16c(rz, w * 1024 + lane * 16, (u32x4){0u, 0u, 0u, 0u});
    st16c(MKR(a.ws + WS_KVF, 65536), w * 1024 + lane * 16, (u32x4){~0u, ~0u, ~0u, ~0u});
    VMDRAIN(); LBAR();
    if (wave == 0 && lane == 0) st8c(rb, 128 + i * 128, (u32x2){tag.x + 1u, ~tag.y});
    if (w == 0 && lane == 0) { const int layer = 0; STAMP(20); }
    u32x4 v[4][4];
#pragma unroll
    for (int q = 0; q < 16; ++q) { const int u = min(w + 64 * (q >> 2), 199), kk = u / 10; v[q >> 2][q & 3] = *reinterpret_cast<const u32x4*>(a.wao + (kk * 32 + (q & 3) * 8 + (lane >> 3)) * D_ + (u - kk * 10) * 64 + (lane & 7) * 8); }
    unsigned char* t = lds + wave * 16384;
#pragma unroll
    for (int q = 0; q < 16; ++q) *reinterpret_cast<u32x4*>(t + q * 1024 + lane * 16) = v[q >> 2][q & 3];
    CBAR();
#pragma unroll
    for (int q = 0; q < 16; ++q) { const int u = w + 64 * (q >> 2), kk = u / 10; const unsigned char* p = t + (q >> 2) * 4096 + (8 * lg + (l15 >> 2)) * 128 + (q & 3) * 32 + (l15 & 3) * 8; const u32x2 x0 = ldstr(p), y0 = ldstr(p + 512);
        if (u < 200) st16c(rf, ((((u - kk * 10) * 4 + (q & 3)) * 20 + kk) * 64 + lane) * 16, (u32x4){x0.x, x0.y, y0.x, y0.y}); }
    boot_wait(a);
    VMDRAIN(); LBAR();
    if (wave == 0 && lane == 0) st4c(rz, F_WO + i * FLP, 1u);
    if (w == 1) { wait_set(rz, F_ZR + lane * FLP, a.ws + WS_HOP, 2); wait_set(rz, F_ZR + (48 + lane) * FLP, a.ws + WS_HOP, 2); if (lane < 8) st4c(rz, F_IN + lane * FLP, 1u); }
    if (w == 0 && lane == 0) { const int layer = 0; STAMP(21); }
    LBAR();
}

__device__ __forceinline__ void zero_part(KA& a, const int i, const int wave) {
    const unsigned lane = lane_id(), w = i * 8 + wave, o = w * 1024 + lane * 16;
    const u32x4 z = (u32x4){0u, 0u, 0u, 0u};
    const rsrc_t rz = MKR(a.ws + WS_HOP + F_AF, HOP_BYTES - F_AF);
#pragma unroll
    for (int j = 0; j < 4; ++j) st16c(rz, o + j * (896 * 1024), z);
    static_assert(HOP_BYTES - F_AF <= 4u * 896 * 1024, "flags");
    st16c(MKR(al2m(a.ws + WS_FR), FR_FILL), (w >> 4) % 20u >= 2u ? o : OOB, (u32x4){~0u, ~0u, ~0u, ~0u});
    if (wave == 0) boot_wait(a);
    VMDRAIN(); LBAR();
    if (wave == 0 && lane == 0) st4c(MKR(a.ws + WS_HOP, F_AF), F_ZR + i * FLP, 1u);
}

#define PQS 2
#define KSL1 2
#define KSL0 16
#define KSL 6
#define AU0 72
#define TT_AW 950
#define ATS 62
#define ATU 0
#define AIM 0
template <int K, bool W3, class F> __device__ __forceinline__ void a_proj(KA& a, unsigned char* lds, const int h, const int wave, F&& pre) {
    constexpr int YB = 51200, L_SK = 3 * YB, L_C = L_SK + 6144 + 64, L_KN = L_C + 256, KP = 48, layer = 0;
    const int lane = lane_id(), l15 = lane & 15, lg = lane >> 4, hd = 2 * h + (wave & 1);
    const unsigned ldsb = LDSB();
    unsigned char* hop = (a.ws + WS_HOP);
    const rsrc_t rfl = MKR(hop, F_ERR), rp = MKR((a.ws + WS_PQ), S_ * D_ * 2), rxa = MKR(a.ws + WS_Y, XI_BYTES);
    u32x4 wq[2][20]; f32x4 cs[2] = {Z4, Z4}, d4[4] = {Z4, Z4, Z4, Z4};
    if (K) {
        const rsrc_t rw = MKR((a.ws + WS_WF_AQKV), 640 * 1920 * 2);
#pragma unroll
        for (int u = 0; u < 2; ++u) { const int c = 20 * hd + (K == 2 ? (1 + u) * 640 + l15 : (u ? min(l15 >> 2, 2) * 640 + 16 + (l15 & 3) : l15));
#pragma unroll
            for (int q = 0; q < 20; ++q) wq[u][q] = __builtin_amdgcn_raw_buffer_load_b128(rw, ((c >> 4) * 20 * 64 + lg * 16 + (c & 15)) * 16, q * 1024, 16); }
#pragma unroll
        for (int q = 0; q < 40; ++q) cs[q & 1] = dmf(wq[q & 1][q >> 1], (u32x4){0x3F803F80u, 0x3F803F80u, 0x3F803F80u, 0x3F803F80u}, cs[q & 1]);
        PIN(cs[0]); PIN(cs[1]); VMKNOWN();
    }
    const unsigned f0 = F_OF + lane * FLP, f1 = F_OF + (64 + l15 + (lane & 16) * 5) * FLP;
    unsigned tnext = rt_now(), per = 0;
    auto kv = [&]() {
        { u32x4 A[2], B[2]; const int z = wave & 1;
#pragma unroll
            for (int q = 0; q < 2; ++q) { const unsigned char* p = lds + L_SK + z * 64 * KP + (8 * lg + (l15 >> 2)) * KP + 32 * q + 8 * (l15 & 3);
                const u32x2 a0 = ldstr(p), a1 = ldstr(p + 4 * KP), b0 = ldstr(p + 32 * KP), b1 = ldstr(p + 36 * KP);
                A[q] = (u32x4){a0.x, a0.y, a1.x, a1.y}; B[q] = (u32x4){b0.x, b0.y, b1.x, b1.y}; }
            if (l15 >= 4) { A[1] = (u32x4){0u, 0u, 0u, 0u}; B[1] = (u32x4){0x3F803F80u, 0x3F803F80u, 0x3F803F80u, 0x3F803F80u}; }
#pragma unroll
            for (int q = 0; q < 4; ++q) d4[q] = dmf(A[q >> 1], B[q & 1], d4[q]); } };
    auto fill = [&](int n, unsigned dst, int g0) {
#pragma unroll
        for (int g = 0; g < 2; ++g) { const unsigned z = (g0 + g + (h >> 2) * AROT) & 3, o = (z * 13 - (z + 1) / 4 * 2) * 1024; dma16cn<13>(rxa, lane * 16, (unsigned)n * XI_PITCH + o, dst + o); } };
    LBAR();
    if (!K) __builtin_amdgcn_s_setprio(2);
    int nx = 0, kn = 0, yb = 0;
    auto need = [&](int t, unsigned dst) {
        if (kn == t) {
            if (t < TF) rt_wait(tnext);
            for (unsigned sp = 0;; ++sp) { CBAR(); const u32x4 v = ld16c(rfl, f0 + t * (NR_ * FLP)); const unsigned w = ld4c(rfl, F_OF + (64 + l15 + t * NR_) * FLP);
                if (__all(v.x != 0u && w != 0u)) { per = __builtin_amdgcn_readfirstlane(v.x); tnext = __builtin_amdgcn_readfirstlane(v.w) + per + (t + 1 < TF - 6 ? TT_AD : TT_AN); break; }
                if (t < TF) __builtin_amdgcn_s_sleep(12); else __builtin_amdgcn_s_sleep(3);
                if (sp > SPIN_CAP_W) { set_err(hop); break; } }
            ++kn; }
        fill(t, dst, 0); fill(t, dst, 2);
        if (kn >= TF && kn < NSEG_) { const int k1 = min(kn + 1, NSEG_ - 1);
            const unsigned x = ld4c(rfl, f0 + kn * (NR_ * FLP)), y = ld4c(rfl, f0 + k1 * (NR_ * FLP)), z = ld4c(rfl, F_OF + (64 + l15 + ((lane & 16) ? k1 : kn) * NR_) * FLP);
            const unsigned long long zz = __ballot(z == 0u);
            if (__all(x != 0u) && !(zz & 0xffffull)) kn = (__all(y != 0u) && !zz) ? k1 + 1 : kn + 1; }
        VMKNOWN(); };
    if (W3) need(0, ldsb);
    nx = kn = 1;
    LBAR();
    auto seg = [&](const int t) {
        AT(0);
        if (!K && !W3 && wave && t) kv();
        AT(1);
        if (lane == 0 && wave == 4 && h == 0 && t >= 56) STAMP(330 + t - 56);
        const int up = min(kn, t + 3), n = nx + ((nx ^ wave) & 1);
        const unsigned nb = ldsb + (n == t + 1 ? (yb == 2 * YB ? 0 : yb + YB) : (yb == 0 ? 2 * YB : yb - YB));
        u32x4 fa, fb; unsigned fc; int pr = 0; f32x4 acc[2][2] = {{Z4, Z4}, {Z4, Z4}};
        if (K) {
            const unsigned char* yp = lds + yb + XI_X + lane * 16;
            u32x4 yf[4];
#pragma unroll
            for (int q = 0; q < 3; ++q) { yf[q] = lds16(yp + q * 1024); SBAR(); }
            f32x2 ct; float y[8], e[8]; u32x2 o0, o1;
            auto epi = [&](int rt, int z) {
                constexpr int NP = 8 / (K ? K : 1);
                if (z == 0) ct = *reinterpret_cast<const f32x2*>(lds + L_C + rt * 128 + l15 * 8);
                if (z >= 2 && z < 10) { y[z - 2] = __builtin_fmaf(ct[0], cs[(z - 2) >> 2][(z - 2) & 3], acc[rt][(z - 2) >> 2][(z - 2) & 3]); PIN(y[z - 2]); if (z > 2) { y[z - 3] *= ct[1]; PIN(y[z - 3]); } }
                if (z == 10) { y[7] *= ct[1]; PIN(y[7]); }
                if (z >= 10 && z < 10 + NP) { e[z - 10] = __builtin_amdgcn_fmed3f(y[z - 10], 0.f, -3.0e38f); PIN(e[z - 10]); }
                if (z >= 11 && z < 11 + NP) { e[z - 11] *= 1.4426950408889634f; PIN(e[z - 11]); }
                if (z >= 19 && z < 19 + NP) { e[z - 19] = __builtin_amdgcn_exp2f(e[z - 19]); PIN(e[z - 19]); }
                if (z >= 20 && z < 20 + NP) { const float m = __builtin_amdgcn_fmed3f(y[z - 20], 0.f, 3.0e38f); e[z - 20] = (e[z - 20] + 1e-6f) + m; PIN(e[z - 20]); }
                if (z >= 28 && z < 32) { e[z - 24] = (K == 2 || lg >= 2) ? y[z - 24] : e[z - 24]; PIN(e[z - 24]); }
                if (z == 30) { o0 = (u32x2){pk2(e[0], e[1]), pk2(e[2], e[3])}; PIN(o0); }
                if (z == 32) { o1 = (u32x2){pk2(e[4], e[5]), pk2(e[6], e[7])}; PIN(o1); }
                unsigned char* sk = lds + L_SK + (wave & 1) * 64 * KP + (rt * 16 + l15) * KP;
                if (z == 33) { if (K == 2) *reinterpret_cast<u32x2*>(sk + 8 * lg) = o0; else __builtin_amdgcn_raw_buffer_store_b64(o0, rp, 128 + l15 * 32 + lg * 8, (t * 2 + rt) * 20480 + hd * 640, 16); }
                if (z == 34) *reinterpret_cast<u32x2*>(sk + (K == 2 ? 32 * KP + 8 * lg : (lg >= 2 ? 32 * KP + 32 : 40 - 8 * lg))) = o1;
                if (z == 35 && K == 1) __builtin_amdgcn_raw_buffer_store_b64(o1, rp, lg ? OOB : l15 * 8, (t * 2 + rt) * 20480 + hd * 640, 16);
                SBAR(); };
#pragma unroll
            for (int q = 0; q < 40; ++q) { acc[q / 20][0] = dmf(wq[0][q % 20], yf[q & 3], acc[q / 20][0]); PIN(acc[q / 20][0]); SBAR(); if (q + 3 < 40) { yf[(q + 3) & 3] = lds16(yp + (q + 3) * 1024); SBAR(); }
                if (q > 21) epi(0, 2 * q - 44);
                acc[q / 20][1] = dmf(wq[1][q % 20], yf[q & 3], acc[q / 20][1]); PIN(acc[q / 20][1]); SBAR();
                if (q > 21) epi(0, 2 * q - 43);
                if (q == 21) { CBAR(); __builtin_amdgcn_s_barrier(); CBAR(); SBAR(); } }
#pragma unroll
            for (int z = 0; z < 36; ++z) epi(1, z);
            if (K == 1 && t == 8) VMDRAIN();
        } else if (!W3 && wave == 0) {
            const f32x2* sp = reinterpret_cast<const f32x2*>(lds + yb + lg * 2560 + l15 * 8);
            f32x2 sv[20];
#pragma unroll
            for (int q = 0; q < 20; ++q) sv[q] = sp[q * 16];
#pragma unroll
            for (int q = 0; q < 16; ++q) { sv[q & 3][0] = adds(sv[q & 3][0], sv[q + 4][0]); sv[q & 3][1] = adds(sv[q & 3][1], sv[q + 4][1]); }
            const u32x2 s4[4] = {__builtin_bit_cast(u32x2, sv[0]), __builtin_bit_cast(u32x2, sv[1]), __builtin_bit_cast(u32x2, sv[2]), __builtin_bit_cast(u32x2, sv[3])};
            c_put(s4, lds + L_C);
        } else if (W3) {
            const int d = (int)(rt_now() - tnext);
            pr = kn >= NSEG_ ? 0 : (kn >= TF || d >= (int)per ? 2 : (d >= 0 ? 1 : 0));
            if (kn + 1 >= NSEG_) pr = min(pr, 1);
            fa = __builtin_amdgcn_raw_buffer_load_b128(rfl, pr ? f0 : OOB, kn * (NR_ * FLP), 16); fb = __builtin_amdgcn_raw_buffer_load_b128(rfl, pr > 1 ? f0 : OOB, (kn + 1) * (NR_ * FLP), 16);
            fc = __builtin_amdgcn_raw_buffer_load_b32(rfl, pr > ((lane >> 4) & 1) ? f1 : OOB, kn * (NR_ * FLP), 16); SBAR();
        } else if (n < up) fill(n, nb, 0);
        AT(2);
        if (!K) LBAR();
        AT(3);
        if (K) {
        } else if (W3) {
            {
                const unsigned long long z = __ballot(fc == 0u);
                if (__all(fa.x != 0u) && !(z & 0xffffull)) { const bool two = __all(fb.x != 0u) && !z; kn += two ? 2 : 1;
                    per = __builtin_amdgcn_readfirstlane(two ? fb.x : fa.x); tnext = __builtin_amdgcn_readfirstlane(two ? fb.w : fa.w) + per + (kn < TF - 6 ? TT_AD : TT_AN); } }
            if (up <= t + 1 && t + 1 < NSEG_) need(t + 1, ldsb + (yb == 2 * YB ? 0 : yb + YB));
            *reinterpret_cast<int*>(lds + L_KN + (t & 1) * 4) = kn;
            if (t == 40) wait_set(rfl, F_AQ + (lane & 15) * 4, hop, 4);
        } else if (wave) { if (n < up) fill(n, nb, 2); if (((t + 1) ^ wave) & 1) { } else VMDRAIN(); }
        nx = max(up, t + 2);
        AT(4);
        LBAR();
        AT(5);
        kn = __builtin_amdgcn_readfirstlane(*reinterpret_cast<const int*>(lds + L_KN + (t & 1) * 4));
        if (W3 && t == 8 && lane == 0) st4c(rfl, F_AQ + h * 4, 1u);
        yb = yb == 2 * YB ? 0 : yb + YB;
        AT(6); AT(7); ATD(5, t == ATS && h == 0);
    };
#pragma unroll 1
    for (int t = 0; t < 50; ++t) seg(t);
    pre();
#pragma unroll 1
    for (int t = 50; t < NSEG_; ++t) seg(t);
    __builtin_amdgcn_s_setprio(0);
    if (!K && !W3 && wave) {
        kv();
        const rsrc_t rkv = MKR(a.ws + WS_KVF + 32768, 32768);
#pragma unroll
        for (int q = 0; q < 4; ++q) { const int e = 16 * (q & 1) + l15, d = 8 * (q & 2) + 4 * lg;
            st8c(rkv, (d < 24 && e <= 20) ? hd * 1024 + e * 48 + d * 2 : OOB, (u32x2){min(pk2(d4[q][0], d4[q][1]), 0xfffffffeu), min(pk2(d4[q][2], d4[q][3]), 0xfffffffeu)}); }
    }
}
__device__ __forceinline__ void a_part(KA& a, unsigned char* lds, const int ai, const int wave) {
    const int lane = lane_id(), tid = wave * 64 + lane;
    constexpr int L_ST = 20480;
    const int cb = ai & 7, rep = ai >> 3, h = ai;
    const int nt = wave < 5 ? wave : 0;
    u32x4 b[20], tq[4]; u32x2 rres;
    const int ct = 5 * cb + nt;
    const rsrc_t rp = MKR((a.ws + WS_PQ), S_ * D_ * 2), rxa = MKR((a.ws + WS_X), XI_BYTES);
    auto fetch = [&](int u) { const int lane = lane_id(), l15 = lane & 15, lg = lane >> 4;
#pragma unroll
        for (int q = 0; q < 4; ++q) tq[q] = __builtin_amdgcn_raw_buffer_load_b128(rp, (lg < 2 ? 128 + l15 * 32 + lg * 16 : (lg == 2 ? l15 * 8 : OOB)) + q * 640, u * 20480 + wave * 2560, 16); };
    auto x_off = [&]() { const int lane = lane_id(), l15 = lane & 15, lg = lane >> 4; return XI_X + (ct >> 1) * 1024 + ((ct & 1) * 2 + (lg >> 1)) * 256 + l15 * 16 + (lg & 1) * 8; };
    auto b_load = [&]() { const int lane = lane_id();
#pragma unroll
        for (int q = 0; q < 20; ++q) b[q] = __builtin_amdgcn_raw_buffer_load_b128(MKR((a.ws + WS_WF_AO), D_ * D_ * 2), wave < 5 ? lane * 16 + (q & 3) * 1024 : OOB, ct * 20480 + (q >> 2) * 4096, 16); };
    auto outp = [&](const int layer) {
        const int lane = lane_id(), l15 = lane & 15, lg = lane >> 4;
        unsigned char* hop = (a.ws + WS_HOP) + layer * HOP_BYTES;
        const rsrc_t rfl = MKR(hop, F_ERR), rr_ = MKR(psel(a.x0, a.ws + WS_Y, layer), layer ? XI_BYTES : S_ * D_ * 2);
        const unsigned oo = x_off(), ro = layer ? oo : (unsigned)(l15 * D_ + 16 * ct + 4 * lg) * 2;
        auto res = [&](int u) { rres = __builtin_amdgcn_raw_buffer_load_b64(rr_, ro, (u >> 1) * (layer ? XI_PITCH : 40960) + (u & 1) * 20480, 16); };
        if (!layer) { init_wait(a); wait_set(rfl, F_WO + (lane & 7) * FLP, hop, 1); b_load(); res(rep); if (ai < 16) { VMKNOWN(); kv_part(a, lds, ai, wave); } if (wave >= 6) wait_set(rfl, F_PQ + ((wave & 1) * 64 + lane) * FLP, hop, PQS); LBAR(); fetch(rep); }
        unsigned so[5];
#pragma unroll
        for (int q = 0; q < 5; ++q) { const unsigned c = 80 * wave + (q < 4 ? 20 * q + 4 * lg : 20 * lg + 16); so[q] = (c >> 3) * 256 + l15 * 16 + (c & 4) * 2; }
        u32x4 k0[4], k1[4], kz[4];
        {
            const rsrc_t rkv = MKR(a.ws + WS_KVF + layer * 32768, 32768);
            const unsigned char* kp = lds + 32768 + wave * 4096 + (lg < 3 ? lg : 0) * 16;
            if (tid == 0) STAMP(1680 + ai);
            if (layer) __builtin_amdgcn_s_sleep(KSL0); else __builtin_amdgcn_s_sleep(KSL1);
            for (unsigned sp = 0; sp < SPIN_CAP_W; ++sp) {
                dma16cn<4>(rkv, lane * 16, wave * 4096, 32768 + wave * 4096); VMDRAIN();
                unsigned mx = 0u;
#pragma unroll
                for (int q = 0; q < 4; ++q) { k0[q] = lds16(kp + l15 * 48 + q * 1024); kz[q] = lds16(kp + 960 + q * 1024); }
                const u32x4 kx = lds16(kp + (16 + (l15 & 3)) * 48 + (l15 >> 2) * 1024);
#pragma unroll
                for (int q = 0; q < 4; ++q) { mx = max(mx, max(umax4(k0[q]), umax4(kz[q]))); k1[q] = (l15 >> 2) == q ? kx : (u32x4){0u, 0u, 0u, 0u}; }
                mx = max(mx, umax4(kx));
                if (__all(mx != 0xffffffffu)) break;
                __builtin_amdgcn_s_sleep(KSL);
            }
            VMKNOWN();
            if (tid == 0 && ai == 0) STAMP(4);
            if (lane == 0 && ai < 4) STAMP(1640 + ai * 8 + wave);
        }
        const int ue = (!layer && rep < 2) ? AU0 : 2 * NSEG_; int up = -1;
#pragma unroll 1
        for (int u = rep, un; u < ue; up = u, u = un) {
            un = u + ((!layer && u + 1 >= AU0) ? 1 : 3);
            if (layer && u >= 9 && u < 12) wait_set(rfl, F_AQ + (lane & 15) * 4, hop, 1);
            if (layer) AT(0);
            {
                f32x4 dz[4], d0[4], d1 = Z4;
#pragma unroll
                for (int q = 0; q < 4; ++q) dz[q] = dmf(kz[q], tq[q], Z4);
#pragma unroll
                for (int q = 0; q < 4; ++q) { d0[q] = dmf(k0[q], tq[q], Z4); d1 = dmf(k1[q], tq[q], d1); }
                SBAR(); fetch(min(un, 2 * NSEG_ - 1)); SBAR();
                float inv[4];
#pragma unroll
                for (int q = 0; q < 4; ++q) inv[q] = __builtin_amdgcn_rcpf(dz[q][0] + 1e-6f);
#pragma unroll
                for (int q = 0; q < 4; ++q) { d0[q] *= inv[q]; *reinterpret_cast<u32x2*>(lds + so[q]) = (u32x2){pk2(d0[q][0], d0[q][1]), pk2(d0[q][2], d0[q][3])}; }
                d1 *= lg < 2 ? (lg == 0 ? inv[0] : inv[1]) : (lg == 2 ? inv[2] : inv[3]);
                *reinterpret_cast<u32x2*>(lds + so[4]) = (u32x2){pk2(d1[0], d1[1]), pk2(d1[2], d1[3])};
            }
            f32x4 acc = (f32x4){lo2f(rres.x), hi2f(rres.x), lo2f(rres.y), hi2f(rres.y)}; PIN(acc);
            if (layer) AT(1);
            LBAR();
            if (layer) AT(2);
            if (tid == 384 && up >= AIM) { st4c(rfl, F_AF + (up * 8 + cb) * FLP, 1u); if (up < 3) STAMP(1710 + ai); if (ai == 0 && up == 0) STAMP(5); }
            if (layer && u == rep && tid == 448 && ai < 16) st4c(rfl, F_AQ + h * 4, 1u);
            if (wave < 5) {
                const unsigned char* ar = lds + lane * 16;
                u32x4 af[8];
#pragma unroll
                for (int q = 0; q < 5; ++q) { af[q] = lds16(ar + 1024 * q); SBAR(); }
#pragma unroll
                for (int q = 0; q < 20; ++q) { acc = dmf(b[q], af[q & 7], acc); PIN(acc); SBAR();
                    if (q + 5 < 20) { af[(q + 5) & 7] = lds16(ar + 1024 * (q + 5)); SBAR(); } }
                const u32x2 o = (u32x2){pk2(acc[0], acc[1]), pk2(acc[2], acc[3])};
                __builtin_amdgcn_raw_buffer_store_b64(o, rxa, oo, (u >> 1) * XI_PITCH + (u & 1) * 20480, 16);
                *reinterpret_cast<float*>(lds + L_ST + wave * 128 + l15 * 8 + (lg & 1) * 4) = row_sums(o);
            }
            if (layer) AT(3);
            LBAR();
            if (layer) AT(4);
            if (wave == 0) {
                if (lane < 32) { const float* sp = reinterpret_cast<const float*>(lds + L_ST + lane * 4); const float v = ((sp[0] + sp[32]) + (sp[64] + sp[96])) + sp[128];
                    __builtin_amdgcn_raw_buffer_store_b32(__float_as_uint(v), rxa, XI_S8 + cb * 128 + lane * 4, (u >> 1) * XI_PITCH + (u & 1) * 1024, 16); } }
            if (u < AIM || un >= ue) { VMKNOWN(); LBAR(); if (tid == 0) st4c(rfl, F_AF + (u * 8 + cb) * FLP, 1u); }
            if (AIM && tid == 0 && ai == 0 && u == 0) STAMP(5);
            if (tid == 0 && ai == 16 && un >= ue) STAMP(6);
            if (layer) { AT(5); AT(6); AT(7); }
            ATD(5, layer && u == ATU && ai == 0);
            CBAR(); res(min(un, 2 * NSEG_ - 1));
        }
    };
    outp(0);
    VMKNOWN();
    auto pre = [&]() { fetch(rep); rres = __builtin_amdgcn_raw_buffer_load_b64(MKR(a.ws + WS_Y, XI_BYTES), x_off(), (rep >> 1) * XI_PITCH + (rep & 1) * 20480, 16); VMKNOWN(); };
    if (wave >= 4 && ai < 16) { if (wave >= 6) a_proj<2, false>(a, lds, h, wave, pre); else a_proj<1, false>(a, lds, h, wave, pre); b_load(); outp(1); }
    else { if (ai >= 16) {
            if (wave == 0) { const rsrc_t rfl = MKR(a.ws + WS_HOP, F_ERR); unsigned tn = rt_now();
                wait_set(rfl, F_AQ + (lane & 15) * 4, a.ws + WS_HOP, 60); wait_timed(rfl, F_KM + (50 * 32 + ai) * FLP, a.ws + WS_HOP, tn, 13, TT_AW, false); rt_wait(tn); }
            LBAR(); pre(); }
        else if (wave == 3) a_proj<0, true>(a, lds, h, wave, pre); else a_proj<0, false>(a, lds, h, wave, pre); outp(1); }
}

#define QPF 1
#define ZDL 250
#define CPF 150
#define MAPV 2
#define WDL 550
#define WDL2 900
#define WDL3 500
#define XPERM 0x76543210u
__global__ void __launch_bounds__(512, 2) KATTR bmega_kernel(MegaArgs) {
    ATI();
    extern __shared__ __attribute__((aligned(16))) unsigned char lds[];
    const int c = blockIdx.x, x = (XPERM >> ((c & 7) * 4)) & 7, rr = c >> 3, q = x >> 1, wave = __builtin_amdgcn_readfirstlane(threadIdx.x >> 6);
    if (LDSB() != 0u) return;
    int role, id, qt = -1;
    const bool ev = (x & 1) == 0;
    if (rr < 8) { role = ev ? 0 : 1; id = q * 8 + rr; }
    else if (rr >= 28) { if (ev == (MAPV == 1)) { role = rr < 30 ? 5 : 6; id = (rr < 30 ? 16 : 0) + 2 * q + (rr & 1); } else { role = 5; id = (rr - 28) * 4 + q; } }
    else if (ev) { role = 2; id = q * 20 + rr - 8; } else if (rr < 20) { role = 3; id = q * 12 + rr - 8; } else { role = 4; id = q * 8 + rr - 20; }
    if (role == 2 || role == 4 || role == 6 || (role == 5 && id >= 16)) qt = (role == 4 ? (rr + 4) >> 1 : (rr - 8) >> 1) * 8 + 2 * q + (rr & 1);
    if (role == 0 || role == 1 || role == 3) {
        const unsigned t0 = rt_now();
        const int zi = role == 3 ? 64 + id : role * 32 + id;
        if (QPF) {
            const rsrc_t rx = MKR(args().x0, S_ * D_ * 2), rw = MKR(args().wqkv, D_ * 1920 * 2); const unsigned o = (zi * 8 + wave) * 1024 + lane_id() * 16;
#pragma unroll
            for (int j = 0; j < 3; ++j) { dma16(rw, o + j * (896 * 1024), 0, wave * 1024); dma16(rx, o + j * (896 * 1024), 0, wave * 1024); } }
        if (CPF && rr < 4) { unsigned long long pc; asm volatile("s_getpc_b64 %0" : "=s"(pc)); const rsrc_t rc = MKR(pc, CPF * 1024);
#pragma unroll
            for (int j = 0; j < 6; ++j) dma16(rc, lane_id() * 16 + (j * 32 + rr * 8 + wave) * 1024, 0, wave * 1024); }
        rt_wait(t0 + ZDL);
        zero_part(args(), zi, wave);
        if (role == 0 && !(id & 3)) pol_vote(args(), id * 2 + wave);
        rt_wait(t0 + (role == 0 ? WDL : (role == 1 ? WDL2 : WDL3))); }
    if (role == 0) s_part(args(), lds, id, wave);
    else if (role == 1) { if (wave < 4) v_run<true>(args(), lds, id, wave); else v_run<false>(args(), lds, id, wave); }
    else if (role == 3) xp_part(args(), lds, id, wave);
    else {
        if (qt >= 0) q_part(args(), lds, qt >> 3, qt & 7, wave); else if (id >= 8) boot_part(args(), lds, id - 8, wave);
        if (role == 5) a_part(args(), lds, id, wave);
        else if (role == 6) w_part(args(), lds, id, wave);
        else if (role == 4) xq_part(args(), lds, id, wave);
        else {
            if (wave == 0 && !(id & 1) && lane_id() == 0) st4c(MKR(args().ws + WS_HOP, F_ERR), F_SD + (id >> 1) * 4, 1u + my_side());
            ro_part(args(), lds, id, wave); }
    }
}

extern "C" void kernel_launch(void* const* d_in, const int* in_sizes, int n_in, void* d_out, int out_size, void* d_ws, size_t ws_size, hipStream_t stream) {
    (void)hipFuncSetAttribute((const void*)bmega_kernel, hipFuncAttributeMaxDynamicSharedMemorySize, MG_LDS);
    MegaArgs ma; ma.wq = (const bf16_t*)d_in[5]; ma.wk = (const bf16_t*)d_in[6]; ma.wv = (const bf16_t*)d_in[7]; ma.wo = (const bf16_t*)d_in[8];
    ma.wqkv = (const bf16_t*)d_in[1]; ma.wao = (const bf16_t*)d_in[2]; ma.mem0 = (const bf16_t*)d_in[4];
    ma.x0 = (const bf16_t*)d_in[0]; ma.out = (bf16_t*)d_out; ma.ws = (unsigned char*)d_ws;
    bmega_kernel<<<256, 512, MG_LDS, stream>>>(ma);
}
```
